```python
import torch

def _softmax(x, axis=-1):
    return torch.softmax(x, dim=axis)
def _gelu(x):
    return torch.nn.functional.gelu(x, approximate="tanh")
def _silu(x):
    return torch.nn.functional.silu(x)
def _sigmoid(x):
    return torch.sigmoid(x)
def _layernorm(x, axis=-1, eps=1e-5):
    m = x.mean(dim=axis, keepdim=True)
    v = x.var(dim=axis, keepdim=True, unbiased=False)
    return (x - m) / torch.sqrt(v + eps)
def _scatter_add(tgt, idx, src, axis=0):
    return tgt.clone().index_add_(axis, idx, src)
def _segment_sum(src, seg, n):
    out = torch.zeros((n,) + src.shape[1:], dtype=src.dtype)
    return out.index_add_(0, seg, src)
def _pad(x, pw):
    flat = tuple(v for pair in reversed(pw) for v in pair)
    return torch.nn.functional.pad(x, flat)

kernel_name = 'recurrent_scan__mha_block_cached__glu_gated_swiglu__prompt'

def setup_inputs():

    _w = (lambda a: (lambda t: t.to(torch.bfloat16) if t.is_floating_point() else t)(torch.as_tensor(a)))
    def _A_setup():
        B, Sq, D = 1, 2048, 448
        H, Sk, Dh = 16, 2048, 256
        return {
            "x": _w((lambda *s: torch.randn(*s, dtype=torch.bfloat16) * 0.05)(B, Sq, D)),
            "k_cache": _w((lambda *s: torch.randn(*s, dtype=torch.bfloat16) * 1.6)(B, H, Sk, Dh)),
            "v_cache": _w((lambda *s: torch.randn(*s, dtype=torch.bfloat16) * 1.6)(B, H, Sk, Dh)),
            "wq": _w((lambda *s: torch.randn(*s, dtype=torch.bfloat16) * 1.6)(D, H * Dh) * (D ** -0.5)),
            "wo": _w((lambda *s: torch.randn(*s, dtype=torch.bfloat16) * 1.6)(H * Dh, D) * ((H * Dh) ** -0.5)),
        }
    _A_in = _A_setup()

    def _B_setup():
        B, S, D, F = 1, 2048, 448, 1792
        return {
            "x": _w((lambda *s: torch.randn(*s, dtype=torch.bfloat16) * 0.05)(B, S, D)),
            "w1": _w((lambda *s: torch.randn(*s, dtype=torch.bfloat16) * 0.2)(D, F) * (2.0 / D) ** 0.5),
            "w3": _w((lambda *s: torch.randn(*s, dtype=torch.bfloat16) * 0.2)(D, F) * (2.0 / D) ** 0.5),
            "w2": _w((lambda *s: torch.randn(*s, dtype=torch.bfloat16) * 0.2)(F, D) * (2.0 / F) ** 0.5),
        }
    _B_in = _B_setup()

    return {
        "A_x": _A_in["x"],
        "A_k_cache": _A_in["k_cache"],
        "A_v_cache": _A_in["v_cache"],
        "A_wq": _A_in["wq"],
        "A_wo": _A_in["wo"],
        "B_x": _B_in["x"],
        "B_w1": _B_in["w1"],
        "B_w3": _B_in["w3"],
        "B_w2": _B_in["w2"],
    }

def reference(A_x, A_k_cache, A_v_cache, A_wq, A_wo, B_x, B_w1, B_w3, B_w2):
    def _A_ref(x, k_cache, v_cache, wq, wo):
        B, Sq, D = x.shape
        H, Dh = 16, 256
        q = (lambda x, *d: x.permute(*d))((x @ wq).reshape(B, Sq, H, Dh), 0, 2, 1, 3)
        scores = torch.einsum("bhqd,bhkd->bhqk", q, k_cache) * (Dh ** -0.5)
        p = _softmax(scores, axis=-1)
        o = torch.einsum("bhqk,bhkd->bhqd", p, v_cache)
        return (lambda x, *d: x.permute(*d))(o, 0, 2, 1, 3).reshape(B, Sq, H * Dh) @ wo

    def _B_ref(x, w1, w3, w2):
        gate = torch.einsum("bsd,df->bsf", x, w1)
        up = torch.einsum("bsd,df->bsf", x, w3)
        h = _silu(gate) * up
        out = torch.einsum("bsf,fd->bsd", h, w2)
        return out

    x = A_x
    xn = _layernorm(x, axis=-1)
    s = _A_ref(xn, A_k_cache, A_v_cache, A_wq, A_wo)
    g = _silu(_B_ref(xn, B_w1, B_w3, B_w2))
    return x + s * g
```

```cpp
#include <hip/hip_runtime.h>

__global__ __launch_bounds__(256, 1) void fused_main(const void* p_x, const void* p_k, const void* p_v, const void* p_wq, const void* p_wo, const void* p_w1, const void* p_w3, const void* p_w2, const void* p_ws, const void* p_out) {
  __shared__ __attribute__((aligned(1024))) char lds[163840];
  asm volatile(
".Lentry_%=:\n"
"s_load_dwordx16 s[8:23], %[ka], 0x0\n"
"s_load_dwordx4 s[24:27], %[ka], 0x40\n"
"v_lshrrev_b32 v254, 6, %[tid]\n"
"v_and_b32 v255, 63, %[tid]\n"
"v_readfirstlane_b32 s84, v254\n"
"s_nop 3\n"
"s_getpc_b64 s[90:91]\n"
".Lwpc_1_%=:\n"
"s_lshr_b32 s92, %[bid], 3\n"
"s_lshl_b32 s92, s92, 2\n"
"s_add_u32 s92, s92, s84\n"
"s_mul_i32 s92, s92, 640\n"
"v_lshlrev_b32 v2, 7, v255\n"
"v_min_u32 v2, 512, v2\n"
"v_add_u32 v2, s92, v2\n"
"s_mov_b32 s92, .Lend_%=-.Lwpc_1_%=-4\n"
"v_min_u32 v2, s92, v2\n"
"global_load_dword a255, v2, s[90:91]\n"
"s_nop 3\n"
"s_getpc_b64 s[90:91]\n"
".Lwpc_2_%=:\n"
"s_mul_i32 s92, s84, 2048\n"
"v_lshlrev_b32 v2, 7, v255\n"
"v_min_u32 v2, 1920, v2\n"
"v_add_u32 v2, s92, v2\n"
"s_mov_b32 s92, .Lend_%=-.Lwpc_2_%=-4\n"
"v_min_u32 v2, s92, v2\n"
"global_load_dword a255, v2, s[90:91]\n"
"s_bfe_u32 s87, %[bid], 0x20001\n"
"s_lshl_b32 s87, s87, 2\n"
"s_bfe_u32 s90, %[bid], 0x20003\n"
"s_add_u32 s87, s87, s90\n"
"s_lshr_b32 s89, %[bid], 5\n"
"s_and_b32 s90, %[bid], 1\n"
"s_lshl_b32 s90, s90, 3\n"
"s_add_u32 s89, s89, s90\n"
"s_lshl_b32 s90, s89, 2\n"
"s_add_u32 s90, s90, s84\n"
"s_mul_i32 s92, s90, 28672\n"
"v_lshrrev_b32 v2, 3, v255\n"
"v_and_b32 v3, 7, v255\n"
"v_lshrrev_b32 v4, 1, v2\n"
"v_xor_b32 v5, v4, v3\n"
"v_mul_u32_u24 v6, 896, v2\n"
"v_lshl_add_u32 v0, v5, 4, v6\n"
"v_xor_b32 v5, 4, v5\n"
"v_lshl_add_u32 v1, v5, 4, v6\n"
"s_min_u32 s91, s84, 1\n"
"s_add_u32 s91, s91, s84\n"
"s_lshl_b32 s44, s91, 15\n"
"s_mov_b32 m0, s44\n"
"s_and_b32 s86, s87, 0\n"
"s_mul_i32 s86, s86, 14336\n"
"s_lshr_b32 s48, %[bid], 5\n"
"s_lshl_b32 s48, s48, 2\n"
"s_add_u32 s48, s48, s84\n"
"s_mul_i32 s93, s48, 14\n"
"v_and_b32 v7, 3, v255\n"
"v_lshlrev_b32 v7, 7, v7\n"
"v_min_u32 v8, 252, v7\n"
"v_lshrrev_b32 v9, 2, v255\n"
"v_min_u32 v9, 13, v9\n"
"v_add_u32 v9, s93, v9\n"
"v_mul_u32_u24 v230, 3584, v9\n"
"v_add_u32 v230, v230, v8\n"
"v_lshl_add_u32 v231, v9, 13, v7\n"
"s_mul_i32 s46, s87, 224\n"
"s_and_b32 s46, s46, 0xffffffc0\n"
"s_sub_u32 s47, 1605632, s46\n"
"s_waitcnt lgkmcnt(0)\n"
"s_add_u32 s64, s18, s46\n"
"s_addc_u32 s65, s19, 0\n"
"s_and_b32 s65, s65, 0xffff\n"
"s_mov_b32 s66, s47\n"
"s_mov_b32 s67, 0x20000\n"
"s_add_u32 s68, s20, s46\n"
"s_addc_u32 s69, s21, 0\n"
"s_and_b32 s69, s69, 0xffff\n"
"s_mov_b32 s70, s47\n"
"s_mov_b32 s71, 0x20000\n"
"s_add_u32 s56, s8, s92\n"
"s_addc_u32 s57, s9, 0\n"
"s_and_b32 s57, s57, 0xffff\n"
"s_mov_b32 s58, 28672\n"
"s_mov_b32 s59, 0x20000\n"
"s_add_u32 s85, s86, 0\n"
"s_nop 0\n"
"s_nop 0\n"
"buffer_load_dwordx4 v0, s[56:59], s85 offen offset:0 lds\n"
"s_mul_i32 s91, s90, 458752\n"
"s_lshl_b32 s92, s87, 10\n"
"s_add_u32 s91, s91, s92\n"
"s_add_u32 s80, s24, s91\n"
"s_addc_u32 s81, s25, 0\n"
"s_and_b32 s81, s81, 0xffff\n"
"s_mov_b32 s82, 0x70000\n"
"s_add_u32 m0, m0, 896\n"
"s_nop 0\n"
"buffer_load_dwordx4 v0, s[56:59], s85 offen offset:128 lds\n"
"s_mov_b32 s83, 0x20000\n"
"s_lshl_b32 s91, s89, 12\n"
"s_add_u32 s91, s91, 0x4000000\n"
"s_add_u32 s94, s24, s91\n"
"s_addc_u32 s95, s25, 0\n"
"s_load_dwordx2 s[96:97], s[94:95], 0x80000\n"
"s_mov_b32 s32, s8\n"
"s_add_u32 m0, m0, 896\n"
"s_nop 0\n"
"buffer_load_dwordx4 v0, s[56:59], s85 offen offset:256 lds\n"
"s_mov_b32 s33, s9\n"
"s_mov_b32 s34, s26\n"
"s_mov_b32 s35, s27\n"
"s_and_b32 s90, s87, 3\n"
"s_mul_i32 s90, s90, 224\n"
"s_and_b32 s90, s90, 0xffffffc0\n"
"s_sub_u32 s91, 401408, s90\n"
"s_add_u32 m0, m0, 896\n"
"s_nop 0\n"
"buffer_load_dwordx4 v0, s[56:59], s85 offen offset:384 lds\n"
"s_lshr_b32 s92, s87, 2\n"
"s_mul_i32 s92, s92, 401408\n"
"s_add_u32 s90, s90, s92\n"
"s_add_u32 s72, s22, s90\n"
"s_addc_u32 s73, s23, 0\n"
"s_and_b32 s73, s73, 0xffff\n"
"s_mov_b32 s74, s91\n"
"s_add_u32 m0, m0, 896\n"
"s_nop 0\n"
"buffer_load_dwordx4 v0, s[56:59], s85 offen offset:512 lds\n"
"s_mov_b32 s75, 0x20000\n"
"v_lshlrev_b32 v4, 1, v4\n"
"v_xor_b32 v4, v4, v3\n"
"v_lshlrev_b32 v4, 4, v4\n"
"s_lshl_b32 s90, s84, 3\n"
"v_add_u32 v5, s90, v2\n"
"v_mul_u32_u24 v6, 3584, v5\n"
"s_add_u32 m0, m0, 896\n"
"s_nop 0\n"
"buffer_load_dwordx4 v0, s[56:59], s85 offen offset:640 lds\n"
"v_add_u32 v237, v6, v4\n"
"v_lshl_add_u32 v236, v5, 13, v4\n"
"v_lshl_add_u32 v253, v5, 9, v4\n"
"s_and_b32 s90, s84, 1\n"
"s_lshl_b32 s90, s90, 2\n"
"v_lshrrev_b32 v6, 1, v2\n"
"v_add_u32 v6, s90, v6\n"
"s_add_u32 m0, m0, 896\n"
"s_nop 0\n"
"buffer_load_dwordx4 v0, s[56:59], s85 offen offset:768 lds\n"
"v_xor_b32 v6, v6, v3\n"
"v_lshlrev_b32 v6, 4, v6\n"
"v_lshl_add_u32 v252, v5, 9, v6\n"
"s_lshl_b32 s90, s87, 9\n"
"s_sub_u32 s91, 3670016, s90\n"
"s_add_u32 s60, s14, s90\n"
"s_addc_u32 s61, s15, 0\n"
"s_add_u32 s85, s86, 7168\n"
"s_add_u32 m0, m0, 1792\n"
"s_nop 0\n"
"buffer_load_dwordx4 v1, s[56:59], s85 offen offset:0 lds\n"
"s_and_b32 s61, s61, 0xffff\n"
"s_mov_b32 s62, s91\n"
"s_mov_b32 s63, 0x20000\n"
"s_mul_i32 s90, s87, 229376\n"
"s_add_u32 s76, s16, s90\n"
"s_addc_u32 s77, s17, 0\n"
"s_and_b32 s77, s77, 0xffff\n"
"s_add_u32 m0, m0, 896\n"
"s_nop 0\n"
"buffer_load_dwordx4 v1, s[56:59], s85 offen offset:128 lds\n"
"s_mov_b32 s78, 229376\n"
"s_mov_b32 s79, 0x20000\n"
"s_lshl_b32 s90, s87, 20\n"
"s_add_u32 s36, s10, s90\n"
"s_addc_u32 s37, s11, 0\n"
"s_and_b32 s37, s37, 0xffff\n"
"s_mov_b32 s38, 0x100000\n"
"s_add_u32 m0, m0, 896\n"
"s_nop 0\n"
"buffer_load_dwordx4 v1, s[56:59], s85 offen offset:256 lds\n"
"s_mov_b32 s39, 0x20000\n"
"s_add_u32 s40, s12, s90\n"
"s_addc_u32 s41, s13, 0\n"
"s_and_b32 s41, s41, 0xffff\n"
"s_mov_b32 s42, 0x100000\n"
"s_mov_b32 s43, 0x20000\n"
"s_mov_b32 s88, %[bid]\n"
"s_add_u32 m0, m0, 896\n"
"s_nop 0\n"
"buffer_load_dwordx4 v1, s[56:59], s85 offen offset:384 lds\n"
"s_mov_b32 s54, 0x5d800000\n"
"s_mov_b32 s49, 0\n"
"s_mov_b32 s50, 0x41800000\n"
"s_lshl_b32 s51, s89, 4\n"
"s_add_u32 s51, s51, s87\n"
"s_lshl_b32 s51, s51, 2\n"
"s_add_u32 s51, s51, s84\n"
"s_add_u32 m0, m0, 896\n"
"s_nop 0\n"
"buffer_load_dwordx4 v1, s[56:59], s85 offen offset:512 lds\n"
"s_lshl_b32 s51, s51, 10\n"
"s_add_u32 s51, s51, 0x100000\n"
"v_lshlrev_b32 v239, 4, v255\n"
"v_and_b32 v2, 15, v255\n"
"v_lshrrev_b32 v3, 3, v2\n"
"v_mul_u32_u24 v3, 7168, v3\n"
"v_and_b32 v4, 7, v2\n"
"s_add_u32 m0, m0, 896\n"
"s_nop 0\n"
"buffer_load_dwordx4 v1, s[56:59], s85 offen offset:640 lds\n"
"v_lshl_add_u32 v3, v4, 7, v3\n"
"v_bfe_u32 v4, v255, 4, 1\n"
"v_lshl_add_u32 v3, v4, 3, v3\n"
"v_add_u32 v3, s44, v3\n"
"v_lshrrev_b32 v2, 1, v2\n"
"v_lshrrev_b32 v4, 5, v255\n"
"v_xor_b32 v2, v2, v4\n"
"s_add_u32 m0, m0, 896\n"
"s_nop 0\n"
"buffer_load_dwordx4 v1, s[56:59], s85 offen offset:768 lds\n"
"v_xor_b32 v4, 0, v2\n"
"v_lshl_add_u32 v246, v4, 4, v3\n"
"v_xor_b32 v4, 2, v2\n"
"v_lshl_add_u32 v247, v4, 4, v3\n"
"v_xor_b32 v4, 4, v2\n"
"v_lshl_add_u32 v248, v4, 4, v3\n"
"v_xor_b32 v4, 6, v2\n"
"s_sub_u32 s85, 14336, s86\n"
"s_add_u32 m0, m0, 1792\n"
"s_nop 0\n"
"buffer_load_dwordx4 v0, s[56:59], s85 offen offset:0 lds\n"
"v_lshl_add_u32 v249, v4, 4, v3\n"
"s_mov_b32 s44, 0\n"
"s_mov_b32 s45, 0x4000\n"
"s_mov_b32 s46, 0\n"
"s_mov_b32 s47, 0x4000\n"
"v_bfe_u32 v2, v255, 2, 3\n"
"v_lshrrev_b32 v3, 5, v255\n"
"s_add_u32 m0, m0, 896\n"
"s_nop 0\n"
"buffer_load_dwordx4 v0, s[56:59], s85 offen offset:128 lds\n"
"v_lshlrev_b32 v3, 12, v3\n"
"v_lshl_add_u32 v3, v2, 7, v3\n"
"v_and_b32 v4, 3, v255\n"
"v_lshl_add_u32 v3, v4, 3, v3\n"
"v_lshrrev_b32 v2, 1, v2\n"
"s_and_b32 s93, s84, 1\n"
"s_lshl_b32 s90, s93, 1\n"
"s_add_u32 m0, m0, 896\n"
"s_nop 0\n"
"buffer_load_dwordx4 v0, s[56:59], s85 offen offset:256 lds\n"
"v_xor_b32 v4, s90, v2\n"
"v_lshl_add_u32 v234, v4, 5, v3\n"
"v_xor_b32 v235, 32, v234\n"
"s_and_b32 s90, s87, 1\n"
"s_mul_i32 s91, s93, 3\n"
"s_add_u32 s90, s90, s91\n"
"s_add_u32 s91, s90, 0\n"
"s_add_u32 m0, m0, 896\n"
"s_nop 0\n"
"buffer_load_dwordx4 v0, s[56:59], s85 offen offset:384 lds\n"
"s_and_b32 s92, s91, 3\n"
"s_lshr_b32 s91, s91, 2\n"
"s_lshl_b32 s91, s91, 10\n"
"v_xor_b32 v4, s92, v2\n"
"v_lshl_add_u32 v4, v4, 5, v3\n"
"v_add_u32 v224, s91, v4\n"
"s_and_b32 s90, s87, 1\n"
"s_add_u32 m0, m0, 896\n"
"s_nop 0\n"
"buffer_load_dwordx4 v0, s[56:59], s85 offen offset:512 lds\n"
"s_mul_i32 s91, s93, 3\n"
"s_add_u32 s90, s90, s91\n"
"s_add_u32 s91, s90, 1\n"
"s_and_b32 s92, s91, 3\n"
"s_lshr_b32 s91, s91, 2\n"
"s_lshl_b32 s91, s91, 10\n"
"v_xor_b32 v4, s92, v2\n"
"s_add_u32 m0, m0, 896\n"
"s_nop 0\n"
"buffer_load_dwordx4 v0, s[56:59], s85 offen offset:640 lds\n"
"v_lshl_add_u32 v4, v4, 5, v3\n"
"v_add_u32 v225, s91, v4\n"
"s_and_b32 s90, s87, 1\n"
"s_mul_i32 s91, s93, 3\n"
"s_add_u32 s90, s90, s91\n"
"s_add_u32 s91, s90, 2\n"
"s_and_b32 s92, s91, 3\n"
"s_add_u32 m0, m0, 896\n"
"s_nop 0\n"
"buffer_load_dwordx4 v0, s[56:59], s85 offen offset:768 lds\n"
"s_lshr_b32 s91, s91, 2\n"
"s_lshl_b32 s91, s91, 10\n"
"v_xor_b32 v4, s92, v2\n"
"v_lshl_add_u32 v4, v4, 5, v3\n"
"v_add_u32 v226, s91, v4\n"
"s_and_b32 s90, s87, 1\n"
"s_add_u32 s91, s90, 6\n"
"s_sub_u32 s85, 21504, s86\n"
"s_add_u32 m0, m0, 1792\n"
"s_nop 0\n"
"buffer_load_dwordx4 v1, s[56:59], s85 offen offset:0 lds\n"
"s_and_b32 s92, s91, 3\n"
"s_lshr_b32 s91, s91, 2\n"
"s_lshl_b32 s91, s91, 10\n"
"v_xor_b32 v4, s92, v2\n"
"v_lshl_add_u32 v4, v4, 5, v3\n"
"v_add_u32 v227, s91, v4\n"
"s_lshl_b32 s90, s84, 12\n"
"s_add_u32 m0, m0, 896\n"
"s_nop 0\n"
"buffer_load_dwordx4 v1, s[56:59], s85 offen offset:128 lds\n"
"s_add_u32 s90, s90, 98304\n"
"v_add_u32 v244, s90, v239\n"
"v_xor_b32 v245, 0x1000, v244\n"
"v_mov_b32 v176, 0x3f803f80\n"
"v_mov_b32 v177, 0x3f803f80\n"
"v_mov_b32 v178, 0x3f803f80\n"
"v_mov_b32 v179, 0x3f803f80\n"
"s_add_u32 m0, m0, 896\n"
"s_nop 0\n"
"buffer_load_dwordx4 v1, s[56:59], s85 offen offset:256 lds\n"
"s_add_u32 m0, m0, 896\n"
"s_nop 0\n"
"buffer_load_dwordx4 v1, s[56:59], s85 offen offset:384 lds\n"
"s_add_u32 m0, m0, 896\n"
"s_nop 0\n"
"buffer_load_dwordx4 v1, s[56:59], s85 offen offset:512 lds\n"
"s_add_u32 m0, m0, 896\n"
"s_nop 0\n"
"buffer_load_dwordx4 v1, s[56:59], s85 offen offset:640 lds\n"
"s_add_u32 m0, m0, 896\n"
"s_nop 0\n"
"buffer_load_dwordx4 v1, s[56:59], s85 offen offset:768 lds\n"
"s_lshl_b32 s90, s84, 12\n"
"s_mov_b32 m0, s90\n"
"s_waitcnt lgkmcnt(0)\n"
"s_add_u32 s96, s96, 0x7f4a7c15\n"
"s_addc_u32 s97, s97, 0x9e3779b9\n"
"s_lshl_b32 s98, s86, 4\n"
"s_sub_u32 s99, 229376, s98\n"
"s_waitcnt vmcnt(14)\n"
"ds_read_b64 v[0:1], v246 offset:0\n"
"ds_read_b64 v[2:3], v247 offset:0\n"
"ds_read_b64 v[8:9], v248 offset:0\n"
"ds_read_b64 v[10:11], v249 offset:0\n"
"ds_read_b64 v[16:17], v246 offset:1024\n"
"ds_read_b64 v[18:19], v247 offset:1024\n"
"ds_read_b64 v[24:25], v248 offset:1024\n"
"ds_read_b64 v[26:27], v249 offset:1024\n"
"ds_read_b64 v[32:33], v246 offset:2048\n"
"ds_read_b64 v[34:35], v247 offset:2048\n"
"ds_read_b64 v[40:41], v248 offset:2048\n"
"ds_read_b64 v[42:43], v249 offset:2048\n"
"ds_read_b64 v[48:49], v246 offset:3072\n"
"ds_read_b64 v[50:51], v247 offset:3072\n"
"s_add_u32 m0, m0, 0x8000\n"
"s_mov_b32 s85, 0x0\n"
"buffer_load_dwordx4 v237, s[64:67], s85 offen offset:0 lds\n"
"s_waitcnt lgkmcnt(12)\n"
"v_mfma_f32_16x16x32_bf16 v[180:183], v[176:179], v[0:3], 0\n"
"v_mfma_f32_16x16x32_bf16 v[188:191], v[0:3], v[0:3], 0\n"
"ds_read_b64 v[56:57], v248 offset:3072\n"
"ds_read_b64 v[58:59], v249 offset:3072\n"
"s_add_u32 m0, m0, 0x380\n"
"s_mov_b32 s85, 0x0\n"
"buffer_load_dwordx4 v237, s[64:67], s85 offen offset:128 lds\n"
"s_waitcnt lgkmcnt(12)\n"
"v_mfma_f32_16x16x32_bf16 v[180:183], v[176:179], v[8:11], v[180:183]\n"
"v_mfma_f32_16x16x32_bf16 v[188:191], v[8:11], v[8:11], v[188:191]\n"
"ds_read_b64 v[64:65], v246 offset:4096\n"
"ds_read_b64 v[66:67], v247 offset:4096\n"
"s_add_u32 m0, m0, 0x480\n"
"s_mov_b32 s85, 0x0\n"
"buffer_load_dwordx4 v237, s[68:71], s85 offen offset:0 lds\n"
"s_waitcnt lgkmcnt(12)\n"
"v_mfma_f32_16x16x32_bf16 v[180:183], v[176:179], v[16:19], v[180:183]\n"
"v_mfma_f32_16x16x32_bf16 v[188:191], v[16:19], v[16:19], v[188:191]\n"
"ds_read_b64 v[72:73], v248 offset:4096\n"
"ds_read_b64 v[74:75], v249 offset:4096\n"
"s_add_u32 m0, m0, 0x380\n"
"s_mov_b32 s85, 0x0\n"
"buffer_load_dwordx4 v237, s[68:71], s85 offen offset:128 lds\n"
"s_waitcnt lgkmcnt(12)\n"
"v_mfma_f32_16x16x32_bf16 v[180:183], v[176:179], v[24:27], v[180:183]\n"
"v_mfma_f32_16x16x32_bf16 v[188:191], v[24:27], v[24:27], v[188:191]\n"
"ds_read_b64 v[80:81], v246 offset:5120\n"
"ds_read_b64 v[82:83], v247 offset:5120\n"
"s_add_u32 m0, m0, 0x3480\n"
"s_mov_b32 s85, 0x1c000\n"
"buffer_load_dwordx4 v237, s[64:67], s85 offen offset:0 lds\n"
"s_waitcnt lgkmcnt(12)\n"
"v_mfma_f32_16x16x32_bf16 v[180:183], v[176:179], v[32:35], v[180:183]\n"
"v_mfma_f32_16x16x32_bf16 v[188:191], v[32:35], v[32:35], v[188:191]\n"
"ds_read_b64 v[88:89], v248 offset:5120\n"
"ds_read_b64 v[90:91], v249 offset:5120\n"
"s_add_u32 m0, m0, 0x380\n"
"s_mov_b32 s85, 0x1c000\n"
"buffer_load_dwordx4 v237, s[64:67], s85 offen offset:128 lds\n"
"s_waitcnt lgkmcnt(12)\n"
"v_mfma_f32_16x16x32_bf16 v[180:183], v[176:179], v[40:43], v[180:183]\n"
"v_mfma_f32_16x16x32_bf16 v[188:191], v[40:43], v[40:43], v[188:191]\n"
"ds_read_b64 v[96:97], v246 offset:6144\n"
"ds_read_b64 v[98:99], v247 offset:6144\n"
"s_add_u32 m0, m0, 0x480\n"
"s_mov_b32 s85, 0x1c000\n"
"buffer_load_dwordx4 v237, s[68:71], s85 offen offset:0 lds\n"
"s_waitcnt lgkmcnt(12)\n"
"v_mfma_f32_16x16x32_bf16 v[180:183], v[176:179], v[48:51], v[180:183]\n"
"v_mfma_f32_16x16x32_bf16 v[188:191], v[48:51], v[48:51], v[188:191]\n"
"ds_read_b64 v[104:105], v248 offset:6144\n"
"ds_read_b64 v[106:107], v249 offset:6144\n"
"s_add_u32 m0, m0, 0x380\n"
"s_mov_b32 s85, 0x1c000\n"
"buffer_load_dwordx4 v237, s[68:71], s85 offen offset:128 lds\n"
"s_waitcnt lgkmcnt(12)\n"
"v_mfma_f32_16x16x32_bf16 v[180:183], v[176:179], v[56:59], v[180:183]\n"
"v_mfma_f32_16x16x32_bf16 v[188:191], v[56:59], v[56:59], v[188:191]\n"
"s_waitcnt lgkmcnt(10)\n"
"v_mfma_f32_16x16x32_bf16 v[180:183], v[176:179], v[64:67], v[180:183]\n"
"v_mfma_f32_16x16x32_bf16 v[188:191], v[64:67], v[64:67], v[188:191]\n"
"s_waitcnt lgkmcnt(8)\n"
"v_mfma_f32_16x16x32_bf16 v[180:183], v[176:179], v[72:75], v[180:183]\n"
"v_mfma_f32_16x16x32_bf16 v[188:191], v[72:75], v[72:75], v[188:191]\n"
"s_waitcnt lgkmcnt(6)\n"
"v_mfma_f32_16x16x32_bf16 v[180:183], v[176:179], v[80:83], v[180:183]\n"
"v_mfma_f32_16x16x32_bf16 v[188:191], v[80:83], v[80:83], v[188:191]\n"
"s_waitcnt lgkmcnt(4)\n"
"v_mfma_f32_16x16x32_bf16 v[180:183], v[176:179], v[88:91], v[180:183]\n"
"v_mfma_f32_16x16x32_bf16 v[188:191], v[88:91], v[88:91], v[188:191]\n"
"s_waitcnt lgkmcnt(2)\n"
"v_mfma_f32_16x16x32_bf16 v[180:183], v[176:179], v[96:99], v[180:183]\n"
"v_mfma_f32_16x16x32_bf16 v[188:191], v[96:99], v[96:99], v[188:191]\n"
"s_waitcnt lgkmcnt(0)\n"
"v_mfma_f32_16x16x32_bf16 v[180:183], v[176:179], v[104:107], v[180:183]\n"
"v_mfma_f32_16x16x32_bf16 v[188:191], v[104:107], v[104:107], v[188:191]\n"
"buffer_load_dword a255, v230, s[64:67], 0 offen\n"
"buffer_load_dword a255, v230, s[68:71], 0 offen\n"
"s_waitcnt vmcnt(10)\n"
"ds_read_b64 v[4:5], v246 offset:14336\n"
"ds_read_b64 v[6:7], v247 offset:14336\n"
"ds_read_b64 v[12:13], v248 offset:14336\n"
"ds_read_b64 v[14:15], v249 offset:14336\n"
"ds_read_b64 v[20:21], v246 offset:15360\n"
"ds_read_b64 v[22:23], v247 offset:15360\n"
"ds_read_b64 v[28:29], v248 offset:15360\n"
"ds_read_b64 v[30:31], v249 offset:15360\n"
"ds_read_b64 v[36:37], v246 offset:16384\n"
"ds_read_b64 v[38:39], v247 offset:16384\n"
"ds_read_b64 v[44:45], v248 offset:16384\n"
"ds_read_b64 v[46:47], v249 offset:16384\n"
"ds_read_b64 v[52:53], v246 offset:17408\n"
"ds_read_b64 v[54:55], v247 offset:17408\n"
"s_waitcnt lgkmcnt(12)\n"
"v_mfma_f32_16x16x32_bf16 v[184:187], v[176:179], v[4:7], 0\n"
"v_mfma_f32_16x16x32_bf16 v[192:195], v[4:7], v[4:7], 0\n"
"ds_read_b64 v[60:61], v248 offset:17408\n"
"ds_read_b64 v[62:63], v249 offset:17408\n"
"s_waitcnt lgkmcnt(12)\n"
"v_mfma_f32_16x16x32_bf16 v[184:187], v[176:179], v[12:15], v[184:187]\n"
"v_mfma_f32_16x16x32_bf16 v[192:195], v[12:15], v[12:15], v[192:195]\n"
"ds_read_b64 v[68:69], v246 offset:18432\n"
"ds_read_b64 v[70:71], v247 offset:18432\n"
"s_waitcnt lgkmcnt(12)\n"
"v_mfma_f32_16x16x32_bf16 v[184:187], v[176:179], v[20:23], v[184:187]\n"
"v_mfma_f32_16x16x32_bf16 v[192:195], v[20:23], v[20:23], v[192:195]\n"
"ds_read_b64 v[76:77], v248 offset:18432\n"
"ds_read_b64 v[78:79], v249 offset:18432\n"
"s_waitcnt lgkmcnt(12)\n"
"v_mfma_f32_16x16x32_bf16 v[184:187], v[176:179], v[28:31], v[184:187]\n"
"v_mfma_f32_16x16x32_bf16 v[192:195], v[28:31], v[28:31], v[192:195]\n"
"ds_read_b64 v[84:85], v246 offset:19456\n"
"ds_read_b64 v[86:87], v247 offset:19456\n"
"s_waitcnt lgkmcnt(12)\n"
"v_mfma_f32_16x16x32_bf16 v[184:187], v[176:179], v[36:39], v[184:187]\n"
"v_mfma_f32_16x16x32_bf16 v[192:195], v[36:39], v[36:39], v[192:195]\n"
"ds_read_b64 v[92:93], v248 offset:19456\n"
"ds_read_b64 v[94:95], v249 offset:19456\n"
"s_waitcnt lgkmcnt(12)\n"
"v_mfma_f32_16x16x32_bf16 v[184:187], v[176:179], v[44:47], v[184:187]\n"
"v_mfma_f32_16x16x32_bf16 v[192:195], v[44:47], v[44:47], v[192:195]\n"
"ds_read_b64 v[100:101], v246 offset:20480\n"
"ds_read_b64 v[102:103], v247 offset:20480\n"
"s_waitcnt lgkmcnt(12)\n"
"v_mfma_f32_16x16x32_bf16 v[184:187], v[176:179], v[52:55], v[184:187]\n"
"v_mfma_f32_16x16x32_bf16 v[192:195], v[52:55], v[52:55], v[192:195]\n"
"ds_read_b64 v[108:109], v248 offset:20480\n"
"ds_read_b64 v[110:111], v249 offset:20480\n"
"s_waitcnt lgkmcnt(12)\n"
"v_mfma_f32_16x16x32_bf16 v[184:187], v[176:179], v[60:63], v[184:187]\n"
"v_mfma_f32_16x16x32_bf16 v[192:195], v[60:63], v[60:63], v[192:195]\n"
"s_waitcnt lgkmcnt(10)\n"
"v_mfma_f32_16x16x32_bf16 v[184:187], v[176:179], v[68:71], v[184:187]\n"
"v_mfma_f32_16x16x32_bf16 v[192:195], v[68:71], v[68:71], v[192:195]\n"
"s_waitcnt lgkmcnt(8)\n"
"v_mfma_f32_16x16x32_bf16 v[184:187], v[176:179], v[76:79], v[184:187]\n"
"v_mfma_f32_16x16x32_bf16 v[192:195], v[76:79], v[76:79], v[192:195]\n"
"s_waitcnt lgkmcnt(6)\n"
"v_mfma_f32_16x16x32_bf16 v[184:187], v[176:179], v[84:87], v[184:187]\n"
"v_mfma_f32_16x16x32_bf16 v[192:195], v[84:87], v[84:87], v[192:195]\n"
"s_waitcnt lgkmcnt(4)\n"
"v_mfma_f32_16x16x32_bf16 v[184:187], v[176:179], v[92:95], v[184:187]\n"
"v_mfma_f32_16x16x32_bf16 v[192:195], v[92:95], v[92:95], v[192:195]\n"
"s_waitcnt lgkmcnt(2)\n"
"v_mfma_f32_16x16x32_bf16 v[184:187], v[176:179], v[100:103], v[184:187]\n"
"v_mfma_f32_16x16x32_bf16 v[192:195], v[100:103], v[100:103], v[192:195]\n"
"s_waitcnt lgkmcnt(0)\n"
"v_mfma_f32_16x16x32_bf16 v[184:187], v[176:179], v[108:111], v[184:187]\n"
"v_mfma_f32_16x16x32_bf16 v[192:195], v[108:111], v[108:111], v[192:195]\n"
"s_barrier\n"
"s_add_u32 m0, m0, 0x3480\n"
"s_mov_b32 s85, 0x38000\n"
"buffer_load_dwordx4 v237, s[64:67], s85 offen offset:0 lds\n"
"s_add_u32 m0, m0, 0x380\n"
"s_mov_b32 s85, 0x38000\n"
"buffer_load_dwordx4 v237, s[64:67], s85 offen offset:128 lds\n"
"s_mov_b32 s90, 0x100001\n"
"s_mov_b32 s91, 0x10000100\n"
"v_cndmask_b32 v196, 0, v188, s[90:91]\n"
"v_cndmask_b32 v197, 0, v192, s[90:91]\n"
"s_lshl_b64 s[90:91], s[90:91], 1\n"
"v_cndmask_b32 v196, v196, v189, s[90:91]\n"
"v_cndmask_b32 v197, v197, v193, s[90:91]\n"
"s_lshl_b64 s[90:91], s[90:91], 1\n"
"v_cndmask_b32 v196, v196, v190, s[90:91]\n"
"v_cndmask_b32 v197, v197, v194, s[90:91]\n"
"s_lshl_b64 s[90:91], s[90:91], 1\n"
"v_cndmask_b32 v196, v196, v191, s[90:91]\n"
"v_cndmask_b32 v197, v197, v195, s[90:91]\n"
"v_mov_b32 v198, v196\n"
"s_nop 1\n"
"v_permlane32_swap_b32 v196, v198\n"
"s_nop 1\n"
"v_add_f32 v196, v196, v198\n"
"v_mov_b32 v198, v196\n"
"s_nop 1\n"
"v_permlane16_swap_b32 v196, v198\n"
"s_nop 1\n"
"v_add_f32 v196, v196, v198\n"
"v_mul_f32 v199, 0x3b124925, v180\n"
"v_mul_f32 v196, 0x3b124925, v196\n"
"v_fma_f32 v196, -v199, v199, v196\n"
"v_max_f32 v196, 0, v196\n"
"v_add_f32 v196, 0x3727c5ac, v196\n"
"v_rsq_f32 v196, v196\n"
"s_nop 0\n"
"v_mul_f32 v240, 0x3db8aa3b, v196\n"
"v_mul_f32_e64 v242, -v199, v240\n"
"s_add_u32 m0, m0, 0x480\n"
"s_mov_b32 s85, 0x38000\n"
"buffer_load_dwordx4 v237, s[68:71], s85 offen offset:0 lds\n"
"v_mov_b32 v198, v197\n"
"s_nop 1\n"
"v_permlane32_swap_b32 v197, v198\n"
"s_nop 1\n"
"v_add_f32 v197, v197, v198\n"
"v_mov_b32 v198, v197\n"
"s_nop 1\n"
"v_permlane16_swap_b32 v197, v198\n"
"s_nop 1\n"
"v_add_f32 v197, v197, v198\n"
"v_mul_f32 v199, 0x3b124925, v184\n"
"v_mul_f32 v197, 0x3b124925, v197\n"
"v_fma_f32 v197, -v199, v199, v197\n"
"v_max_f32 v197, 0, v197\n"
"v_add_f32 v197, 0x3727c5ac, v197\n"
"v_rsq_f32 v197, v197\n"
"s_nop 0\n"
"v_mul_f32 v241, 0x3db8aa3b, v197\n"
"v_mul_f32_e64 v243, -v199, v241\n"
"s_add_u32 m0, m0, 0x380\n"
"s_mov_b32 s85, 0x38000\n"
"buffer_load_dwordx4 v237, s[68:71], s85 offen offset:128 lds\n"
"v_lshlrev_b32 v200, 16, v0\n"
"v_and_b32 v201, 0xffff0000, v0\n"
"v_lshlrev_b32 v202, 16, v1\n"
"v_and_b32 v203, 0xffff0000, v1\n"
"v_lshlrev_b32 v204, 16, v2\n"
"v_and_b32 v205, 0xffff0000, v2\n"
"v_lshlrev_b32 v206, 16, v3\n"
"v_and_b32 v207, 0xffff0000, v3\n"
"s_add_u32 m0, m0, 0x3480\n"
"s_mov_b32 s85, 0x54000\n"
"buffer_load_dwordx4 v237, s[64:67], s85 offen offset:0 lds\n"
"v_pk_fma_f32 v[200:201], v[200:201], v[240:241], v[242:243] op_sel:[0,0,0] op_sel_hi:[1,0,0]\n"
"v_pk_fma_f32 v[202:203], v[202:203], v[240:241], v[242:243] op_sel:[0,0,0] op_sel_hi:[1,0,0]\n"
"v_pk_fma_f32 v[204:205], v[204:205], v[240:241], v[242:243] op_sel:[0,0,0] op_sel_hi:[1,0,0]\n"
"v_pk_fma_f32 v[206:207], v[206:207], v[240:241], v[242:243] op_sel:[0,0,0] op_sel_hi:[1,0,0]\n"
"v_cvt_pk_bf16_f32 v0, v200, v201\n"
"v_cvt_pk_bf16_f32 v1, v202, v203\n"
"v_cvt_pk_bf16_f32 v2, v204, v205\n"
"v_cvt_pk_bf16_f32 v3, v206, v207\n"
"ds_write_b128 v244, v[0:3] offset:0\n"
"v_lshlrev_b32 v208, 16, v4\n"
"v_and_b32 v209, 0xffff0000, v4\n"
"v_lshlrev_b32 v210, 16, v5\n"
"v_and_b32 v211, 0xffff0000, v5\n"
"v_lshlrev_b32 v212, 16, v6\n"
"v_and_b32 v213, 0xffff0000, v6\n"
"v_lshlrev_b32 v214, 16, v7\n"
"v_and_b32 v215, 0xffff0000, v7\n"
"s_add_u32 m0, m0, 0x380\n"
"s_mov_b32 s85, 0x54000\n"
"buffer_load_dwordx4 v237, s[64:67], s85 offen offset:128 lds\n"
"v_pk_fma_f32 v[208:209], v[208:209], v[240:241], v[242:243] op_sel:[0,1,1] op_sel_hi:[1,1,1]\n"
"v_pk_fma_f32 v[210:211], v[210:211], v[240:241], v[242:243] op_sel:[0,1,1] op_sel_hi:[1,1,1]\n"
"v_pk_fma_f32 v[212:213], v[212:213], v[240:241], v[242:243] op_sel:[0,1,1] op_sel_hi:[1,1,1]\n"
"v_pk_fma_f32 v[214:215], v[214:215], v[240:241], v[242:243] op_sel:[0,1,1] op_sel_hi:[1,1,1]\n"
"v_cvt_pk_bf16_f32 v4, v208, v209\n"
"v_cvt_pk_bf16_f32 v5, v210, v211\n"
"v_cvt_pk_bf16_f32 v6, v212, v213\n"
"v_cvt_pk_bf16_f32 v7, v214, v215\n"
"ds_write_b128 v244, v[4:7] offset:1024\n"
"v_lshlrev_b32 v200, 16, v8\n"
"v_and_b32 v201, 0xffff0000, v8\n"
"v_lshlrev_b32 v202, 16, v9\n"
"v_and_b32 v203, 0xffff0000, v9\n"
"v_lshlrev_b32 v204, 16, v10\n"
"v_and_b32 v205, 0xffff0000, v10\n"
"v_lshlrev_b32 v206, 16, v11\n"
"v_and_b32 v207, 0xffff0000, v11\n"
"s_add_u32 m0, m0, 0x480\n"
"s_mov_b32 s85, 0x54000\n"
"buffer_load_dwordx4 v237, s[68:71], s85 offen offset:0 lds\n"
"v_pk_fma_f32 v[200:201], v[200:201], v[240:241], v[242:243] op_sel:[0,0,0] op_sel_hi:[1,0,0]\n"
"v_pk_fma_f32 v[202:203], v[202:203], v[240:241], v[242:243] op_sel:[0,0,0] op_sel_hi:[1,0,0]\n"
"v_pk_fma_f32 v[204:205], v[204:205], v[240:241], v[242:243] op_sel:[0,0,0] op_sel_hi:[1,0,0]\n"
"v_pk_fma_f32 v[206:207], v[206:207], v[240:241], v[242:243] op_sel:[0,0,0] op_sel_hi:[1,0,0]\n"
"v_cvt_pk_bf16_f32 v8, v200, v201\n"
"v_cvt_pk_bf16_f32 v9, v202, v203\n"
"v_cvt_pk_bf16_f32 v10, v204, v205\n"
"v_cvt_pk_bf16_f32 v11, v206, v207\n"
"ds_write_b128 v244, v[8:11] offset:2048\n"
"v_lshlrev_b32 v208, 16, v12\n"
"v_and_b32 v209, 0xffff0000, v12\n"
"v_lshlrev_b32 v210, 16, v13\n"
"v_and_b32 v211, 0xffff0000, v13\n"
"v_lshlrev_b32 v212, 16, v14\n"
"v_and_b32 v213, 0xffff0000, v14\n"
"v_lshlrev_b32 v214, 16, v15\n"
"v_and_b32 v215, 0xffff0000, v15\n"
"s_add_u32 m0, m0, 0x380\n"
"s_mov_b32 s85, 0x54000\n"
"buffer_load_dwordx4 v237, s[68:71], s85 offen offset:128 lds\n"
"v_pk_fma_f32 v[208:209], v[208:209], v[240:241], v[242:243] op_sel:[0,1,1] op_sel_hi:[1,1,1]\n"
"v_pk_fma_f32 v[210:211], v[210:211], v[240:241], v[242:243] op_sel:[0,1,1] op_sel_hi:[1,1,1]\n"
"v_pk_fma_f32 v[212:213], v[212:213], v[240:241], v[242:243] op_sel:[0,1,1] op_sel_hi:[1,1,1]\n"
"v_pk_fma_f32 v[214:215], v[214:215], v[240:241], v[242:243] op_sel:[0,1,1] op_sel_hi:[1,1,1]\n"
"v_cvt_pk_bf16_f32 v12, v208, v209\n"
"v_cvt_pk_bf16_f32 v13, v210, v211\n"
"v_cvt_pk_bf16_f32 v14, v212, v213\n"
"v_cvt_pk_bf16_f32 v15, v214, v215\n"
"ds_write_b128 v244, v[12:15] offset:3072\n"
"v_lshlrev_b32 v200, 16, v16\n"
"v_and_b32 v201, 0xffff0000, v16\n"
"v_lshlrev_b32 v202, 16, v17\n"
"v_and_b32 v203, 0xffff0000, v17\n"
"v_lshlrev_b32 v204, 16, v18\n"
"v_and_b32 v205, 0xffff0000, v18\n"
"v_lshlrev_b32 v206, 16, v19\n"
"v_and_b32 v207, 0xffff0000, v19\n"
"v_pk_fma_f32 v[200:201], v[200:201], v[240:241], v[242:243] op_sel:[0,0,0] op_sel_hi:[1,0,0]\n"
"v_pk_fma_f32 v[202:203], v[202:203], v[240:241], v[242:243] op_sel:[0,0,0] op_sel_hi:[1,0,0]\n"
"v_pk_fma_f32 v[204:205], v[204:205], v[240:241], v[242:243] op_sel:[0,0,0] op_sel_hi:[1,0,0]\n"
"v_pk_fma_f32 v[206:207], v[206:207], v[240:241], v[242:243] op_sel:[0,0,0] op_sel_hi:[1,0,0]\n"
"v_cvt_pk_bf16_f32 v16, v200, v201\n"
"v_cvt_pk_bf16_f32 v17, v202, v203\n"
"v_cvt_pk_bf16_f32 v18, v204, v205\n"
"v_cvt_pk_bf16_f32 v19, v206, v207\n"
"ds_write_b128 v244, v[16:19] offset:16384\n"
"v_lshlrev_b32 v208, 16, v20\n"
"v_and_b32 v209, 0xffff0000, v20\n"
"v_lshlrev_b32 v210, 16, v21\n"
"v_and_b32 v211, 0xffff0000, v21\n"
"v_lshlrev_b32 v212, 16, v22\n"
"v_and_b32 v213, 0xffff0000, v22\n"
"v_lshlrev_b32 v214, 16, v23\n"
"v_and_b32 v215, 0xffff0000, v23\n"
"v_pk_fma_f32 v[208:209], v[208:209], v[240:241], v[242:243] op_sel:[0,1,1] op_sel_hi:[1,1,1]\n"
"v_pk_fma_f32 v[210:211], v[210:211], v[240:241], v[242:243] op_sel:[0,1,1] op_sel_hi:[1,1,1]\n"
"v_pk_fma_f32 v[212:213], v[212:213], v[240:241], v[242:243] op_sel:[0,1,1] op_sel_hi:[1,1,1]\n"
"v_pk_fma_f32 v[214:215], v[214:215], v[240:241], v[242:243] op_sel:[0,1,1] op_sel_hi:[1,1,1]\n"
"v_cvt_pk_bf16_f32 v20, v208, v209\n"
"v_cvt_pk_bf16_f32 v21, v210, v211\n"
"v_cvt_pk_bf16_f32 v22, v212, v213\n"
"v_cvt_pk_bf16_f32 v23, v214, v215\n"
"ds_write_b128 v244, v[20:23] offset:17408\n"
"v_lshlrev_b32 v200, 16, v24\n"
"v_and_b32 v201, 0xffff0000, v24\n"
"v_lshlrev_b32 v202, 16, v25\n"
"v_and_b32 v203, 0xffff0000, v25\n"
"v_lshlrev_b32 v204, 16, v26\n"
"v_and_b32 v205, 0xffff0000, v26\n"
"v_lshlrev_b32 v206, 16, v27\n"
"v_and_b32 v207, 0xffff0000, v27\n"
"v_pk_fma_f32 v[200:201], v[200:201], v[240:241], v[242:243] op_sel:[0,0,0] op_sel_hi:[1,0,0]\n"
"v_pk_fma_f32 v[202:203], v[202:203], v[240:241], v[242:243] op_sel:[0,0,0] op_sel_hi:[1,0,0]\n"
"v_pk_fma_f32 v[204:205], v[204:205], v[240:241], v[242:243] op_sel:[0,0,0] op_sel_hi:[1,0,0]\n"
"v_pk_fma_f32 v[206:207], v[206:207], v[240:241], v[242:243] op_sel:[0,0,0] op_sel_hi:[1,0,0]\n"
"v_cvt_pk_bf16_f32 v24, v200, v201\n"
"v_cvt_pk_bf16_f32 v25, v202, v203\n"
"v_cvt_pk_bf16_f32 v26, v204, v205\n"
"v_cvt_pk_bf16_f32 v27, v206, v207\n"
"ds_write_b128 v244, v[24:27] offset:18432\n"
"v_lshlrev_b32 v208, 16, v28\n"
"v_and_b32 v209, 0xffff0000, v28\n"
"v_lshlrev_b32 v210, 16, v29\n"
"v_and_b32 v211, 0xffff0000, v29\n"
"v_lshlrev_b32 v212, 16, v30\n"
"v_and_b32 v213, 0xffff0000, v30\n"
"v_lshlrev_b32 v214, 16, v31\n"
"v_and_b32 v215, 0xffff0000, v31\n"
"v_pk_fma_f32 v[208:209], v[208:209], v[240:241], v[242:243] op_sel:[0,1,1] op_sel_hi:[1,1,1]\n"
"v_pk_fma_f32 v[210:211], v[210:211], v[240:241], v[242:243] op_sel:[0,1,1] op_sel_hi:[1,1,1]\n"
"v_pk_fma_f32 v[212:213], v[212:213], v[240:241], v[242:243] op_sel:[0,1,1] op_sel_hi:[1,1,1]\n"
"v_pk_fma_f32 v[214:215], v[214:215], v[240:241], v[242:243] op_sel:[0,1,1] op_sel_hi:[1,1,1]\n"
"v_cvt_pk_bf16_f32 v28, v208, v209\n"
"v_cvt_pk_bf16_f32 v29, v210, v211\n"
"v_cvt_pk_bf16_f32 v30, v212, v213\n"
"v_cvt_pk_bf16_f32 v31, v214, v215\n"
"ds_write_b128 v244, v[28:31] offset:19456\n"
"v_lshlrev_b32 v200, 16, v32\n"
"v_and_b32 v201, 0xffff0000, v32\n"
"v_lshlrev_b32 v202, 16, v33\n"
"v_and_b32 v203, 0xffff0000, v33\n"
"v_lshlrev_b32 v204, 16, v34\n"
"v_and_b32 v205, 0xffff0000, v34\n"
"v_lshlrev_b32 v206, 16, v35\n"
"v_and_b32 v207, 0xffff0000, v35\n"
"v_pk_fma_f32 v[200:201], v[200:201], v[240:241], v[242:243] op_sel:[0,0,0] op_sel_hi:[1,0,0]\n"
"v_pk_fma_f32 v[202:203], v[202:203], v[240:241], v[242:243] op_sel:[0,0,0] op_sel_hi:[1,0,0]\n"
"v_pk_fma_f32 v[204:205], v[204:205], v[240:241], v[242:243] op_sel:[0,0,0] op_sel_hi:[1,0,0]\n"
"v_pk_fma_f32 v[206:207], v[206:207], v[240:241], v[242:243] op_sel:[0,0,0] op_sel_hi:[1,0,0]\n"
"v_cvt_pk_bf16_f32 v32, v200, v201\n"
"v_cvt_pk_bf16_f32 v33, v202, v203\n"
"v_cvt_pk_bf16_f32 v34, v204, v205\n"
"v_cvt_pk_bf16_f32 v35, v206, v207\n"
"v_lshlrev_b32 v208, 16, v36\n"
"v_and_b32 v209, 0xffff0000, v36\n"
"v_lshlrev_b32 v210, 16, v37\n"
"v_and_b32 v211, 0xffff0000, v37\n"
"v_lshlrev_b32 v212, 16, v38\n"
"v_and_b32 v213, 0xffff0000, v38\n"
"v_lshlrev_b32 v214, 16, v39\n"
"v_and_b32 v215, 0xffff0000, v39\n"
"v_pk_fma_f32 v[208:209], v[208:209], v[240:241], v[242:243] op_sel:[0,1,1] op_sel_hi:[1,1,1]\n"
"v_pk_fma_f32 v[210:211], v[210:211], v[240:241], v[242:243] op_sel:[0,1,1] op_sel_hi:[1,1,1]\n"
"v_pk_fma_f32 v[212:213], v[212:213], v[240:241], v[242:243] op_sel:[0,1,1] op_sel_hi:[1,1,1]\n"
"v_pk_fma_f32 v[214:215], v[214:215], v[240:241], v[242:243] op_sel:[0,1,1] op_sel_hi:[1,1,1]\n"
"v_cvt_pk_bf16_f32 v36, v208, v209\n"
"v_cvt_pk_bf16_f32 v37, v210, v211\n"
"v_cvt_pk_bf16_f32 v38, v212, v213\n"
"v_cvt_pk_bf16_f32 v39, v214, v215\n"
"v_lshlrev_b32 v200, 16, v40\n"
"v_and_b32 v201, 0xffff0000, v40\n"
"v_lshlrev_b32 v202, 16, v41\n"
"v_and_b32 v203, 0xffff0000, v41\n"
"v_lshlrev_b32 v204, 16, v42\n"
"v_and_b32 v205, 0xffff0000, v42\n"
"v_lshlrev_b32 v206, 16, v43\n"
"v_and_b32 v207, 0xffff0000, v43\n"
"v_pk_fma_f32 v[200:201], v[200:201], v[240:241], v[242:243] op_sel:[0,0,0] op_sel_hi:[1,0,0]\n"
"v_pk_fma_f32 v[202:203], v[202:203], v[240:241], v[242:243] op_sel:[0,0,0] op_sel_hi:[1,0,0]\n"
"v_pk_fma_f32 v[204:205], v[204:205], v[240:241], v[242:243] op_sel:[0,0,0] op_sel_hi:[1,0,0]\n"
"v_pk_fma_f32 v[206:207], v[206:207], v[240:241], v[242:243] op_sel:[0,0,0] op_sel_hi:[1,0,0]\n"
"v_cvt_pk_bf16_f32 v40, v200, v201\n"
"v_cvt_pk_bf16_f32 v41, v202, v203\n"
"v_cvt_pk_bf16_f32 v42, v204, v205\n"
"v_cvt_pk_bf16_f32 v43, v206, v207\n"
"v_lshlrev_b32 v208, 16, v44\n"
"v_and_b32 v209, 0xffff0000, v44\n"
"v_lshlrev_b32 v210, 16, v45\n"
"v_and_b32 v211, 0xffff0000, v45\n"
"v_lshlrev_b32 v212, 16, v46\n"
"v_and_b32 v213, 0xffff0000, v46\n"
"v_lshlrev_b32 v214, 16, v47\n"
"v_and_b32 v215, 0xffff0000, v47\n"
"v_pk_fma_f32 v[208:209], v[208:209], v[240:241], v[242:243] op_sel:[0,1,1] op_sel_hi:[1,1,1]\n"
"v_pk_fma_f32 v[210:211], v[210:211], v[240:241], v[242:243] op_sel:[0,1,1] op_sel_hi:[1,1,1]\n"
"v_pk_fma_f32 v[212:213], v[212:213], v[240:241], v[242:243] op_sel:[0,1,1] op_sel_hi:[1,1,1]\n"
"v_pk_fma_f32 v[214:215], v[214:215], v[240:241], v[242:243] op_sel:[0,1,1] op_sel_hi:[1,1,1]\n"
"v_cvt_pk_bf16_f32 v44, v208, v209\n"
"v_cvt_pk_bf16_f32 v45, v210, v211\n"
"v_cvt_pk_bf16_f32 v46, v212, v213\n"
"v_cvt_pk_bf16_f32 v47, v214, v215\n"
"s_nop 0\n"
"s_add_u32 m0, m0, 0xb480\n"
"s_mov_b32 s85, 0x70000\n"
"v_lshlrev_b32 v246, 16, v48\n"
"v_and_b32 v247, 0xffff0000, v48\n"
"v_lshlrev_b32 v248, 16, v49\n"
"v_and_b32 v249, 0xffff0000, v49\n"
"s_nop 0\n"
"s_waitcnt vmcnt(10) lgkmcnt(4)\n"
"s_barrier\n"
"ds_read_b64_tr_b16 a[128:129], v227 offset:32768\n"
"v_fma_f32 v246, v246, v240, v242\n"
"v_fma_f32 v247, v247, v240, v242\n"
"s_nop 0\n"
"ds_read_b64_tr_b16 a[130:131], v227 offset:40960\n"
"v_cvt_pk_bf16_f32 v48, v246, v247\n"
"v_fma_f32 v248, v248, v240, v242\n"
"v_fma_f32 v249, v249, v240, v242\n"
"v_lshlrev_b32 v250, 16, v50\n"
"s_nop 0\n"
"ds_read_b64_tr_b16 a[132:133], v227 offset:34816\n"
"v_cvt_pk_bf16_f32 v49, v248, v249\n"
"v_and_b32 v251, 0xffff0000, v50\n"
"v_fma_f32 v250, v250, v240, v242\n"
"v_lshlrev_b32 v254, 16, v51\n"
"s_nop 0\n"
"ds_read_b64_tr_b16 a[134:135], v227 offset:43008\n"
"v_fma_f32 v251, v251, v240, v242\n"
"v_and_b32 v255, 0xffff0000, v51\n"
"v_fma_f32 v254, v254, v240, v242\n"
"v_lshlrev_b32 v246, 16, v52\n"
"s_nop 0\n"
"buffer_load_dwordx4 v237, s[64:67], s85 offen offset:0 lds\n"
"s_add_u32 m0, m0, 0x380\n"
"v_cvt_pk_bf16_f32 v50, v250, v251\n"
"v_fma_f32 v255, v255, v240, v242\n"
"v_and_b32 v247, 0xffff0000, v52\n"
"s_nop 0\n"
"ds_read_b128 v[112:115], v245 offset:0\n"
"v_cvt_pk_bf16_f32 v51, v254, v255\n"
"v_fma_f32 v246, v246, v241, v243\n"
"v_fma_f32 v247, v247, v241, v243\n"
"v_lshlrev_b32 v248, 16, v53\n"
"s_nop 0\n"
"ds_read_b64_tr_b16 a[136:137], v224 offset:32768\n"
"v_cvt_pk_bf16_f32 v52, v246, v247\n"
"v_and_b32 v249, 0xffff0000, v53\n"
"v_fma_f32 v248, v248, v241, v243\n"
"v_lshlrev_b32 v250, 16, v54\n"
"s_nop 0\n"
"ds_read_b64_tr_b16 a[138:139], v224 offset:40960\n"
"v_fma_f32 v249, v249, v241, v243\n"
"v_and_b32 v251, 0xffff0000, v54\n"
"v_fma_f32 v250, v250, v241, v243\n"
"v_lshlrev_b32 v254, 16, v55\n"
"s_nop 0\n"
"ds_read_b128 v[116:119], v245 offset:1024\n"
"v_cvt_pk_bf16_f32 v53, v248, v249\n"
"v_fma_f32 v251, v251, v241, v243\n"
"v_and_b32 v255, 0xffff0000, v55\n"
"v_fma_f32 v254, v254, v241, v243\n"
"s_nop 0\n"
"buffer_load_dwordx4 v237, s[64:67], s85 offen offset:128 lds\n"
"s_add_u32 m0, m0, 0x480\n"
"v_cvt_pk_bf16_f32 v54, v250, v251\n"
"v_fma_f32 v255, v255, v241, v243\n"
"v_lshlrev_b32 v246, 16, v56\n"
"s_nop 0\n"
"ds_read_b64_tr_b16 a[140:141], v224 offset:34816\n"
"v_cvt_pk_bf16_f32 v55, v254, v255\n"
"v_and_b32 v247, 0xffff0000, v56\n"
"v_fma_f32 v246, v246, v240, v242\n"
"v_lshlrev_b32 v248, 16, v57\n"
"s_nop 0\n"
"ds_read_b64_tr_b16 a[142:143], v224 offset:43008\n"
"v_fma_f32 v247, v247, v240, v242\n"
"v_and_b32 v249, 0xffff0000, v57\n"
"v_fma_f32 v248, v248, v240, v242\n"
"v_lshlrev_b32 v250, 16, v58\n"
"s_nop 0\n"
"ds_read_b64_tr_b16 a[144:145], v225 offset:32768\n"
"v_cvt_pk_bf16_f32 v56, v246, v247\n"
"v_fma_f32 v249, v249, v240, v242\n"
"v_and_b32 v251, 0xffff0000, v58\n"
"v_fma_f32 v250, v250, v240, v242\n"
"s_nop 0\n"
"ds_read_b64_tr_b16 a[146:147], v225 offset:40960\n"
"v_cvt_pk_bf16_f32 v57, v248, v249\n"
"v_fma_f32 v251, v251, v240, v242\n"
"v_lshlrev_b32 v254, 16, v59\n"
"v_and_b32 v255, 0xffff0000, v59\n"
"s_nop 0\n"
"buffer_load_dwordx4 v237, s[68:71], s85 offen offset:0 lds\n"
"s_add_u32 m0, m0, 0x380\n"
"v_cvt_pk_bf16_f32 v58, v250, v251\n"
"v_fma_f32 v254, v254, v240, v242\n"
"v_fma_f32 v255, v255, v240, v242\n"
"s_nop 0\n"
"ds_read_b64_tr_b16 a[148:149], v225 offset:34816\n"
"v_cvt_pk_bf16_f32 v59, v254, v255\n"
"v_lshlrev_b32 v246, 16, v60\n"
"v_and_b32 v247, 0xffff0000, v60\n"
"v_lshlrev_b32 v248, 16, v61\n"
"s_nop 0\n"
"s_waitcnt lgkmcnt(9)\n"
"ds_read_b64_tr_b16 a[150:151], v225 offset:43008\n"
"v_fma_f32 v246, v246, v241, v243\n"
"v_fma_f32 v247, v247, v241, v243\n"
"v_and_b32 v249, 0xffff0000, v61\n"
"s_nop 0\n"
"ds_read_b64_tr_b16 a[152:153], v226 offset:32768\n"
"v_cvt_pk_bf16_f32 v60, v246, v247\n"
"v_fma_f32 v248, v248, v241, v243\n"
"v_fma_f32 v249, v249, v241, v243\n"
"v_lshlrev_b32 v250, 16, v62\n"
"s_nop 0\n"
"ds_read_b128 v[120:123], v245 offset:2048\n"
"v_cvt_pk_bf16_f32 v61, v248, v249\n"
"v_and_b32 v251, 0xffff0000, v62\n"
"v_fma_f32 v250, v250, v241, v243\n"
"v_lshlrev_b32 v254, 16, v63\n"
"v_mfma_f32_16x16x32_bf16 a[224:227], a[128:131], v[0:3], 0\n"
"buffer_load_dwordx4 v237, s[68:71], s85 offen offset:128 lds\n"
"s_add_u32 m0, m0, 0x3480\n"
"v_mfma_f32_16x16x32_bf16 a[232:235], a[128:131], v[4:7], 0\n"
"s_mov_b32 s85, 0x8c000\n"
"ds_read_b64_tr_b16 a[154:155], v226 offset:40960\n"
"v_mfma_f32_16x16x32_bf16 a[236:239], a[132:135], v[4:7], 0\n"
"s_waitcnt lgkmcnt(7)\n"
"ds_read_b128 v[124:127], v245 offset:3072\n"
"v_mfma_f32_16x16x32_bf16 a[228:231], a[132:135], v[0:3], 0\n"
"s_waitcnt lgkmcnt(10)\n"
"ds_read_b64_tr_b16 a[156:157], v226 offset:34816\n"
"v_mfma_f32_16x16x32_bf16 a[8:11], a[136:139], v[0:3], 0\n"
"ds_read_b64_tr_b16 a[158:159], v226 offset:43008\n"
"v_fma_f32 v251, v251, v241, v243\n"
"v_mfma_f32_16x16x32_bf16 a[40:43], a[136:139], v[4:7], 0\n"
"buffer_load_dwordx4 v237, s[64:67], s85 offen offset:0 lds\n"
"s_add_u32 m0, m0, 0x380\n"
"v_mfma_f32_16x16x32_bf16 a[72:75], a[136:139], v[112:115], 0\n"
"ds_read_b64_tr_b16 a[128:129], v227 offset:49152\n"
"v_cvt_pk_bf16_f32 v62, v250, v251\n"
"v_mfma_f32_16x16x32_bf16 a[104:107], a[136:139], v[116:119], 0\n"
"ds_read_b64_tr_b16 a[130:131], v227 offset:57344\n"
"v_and_b32 v255, 0xffff0000, v63\n"
"v_mfma_f32_16x16x32_bf16 a[108:111], a[140:143], v[116:119], 0\n"
"ds_read_b64_tr_b16 a[132:133], v227 offset:51200\n"
"v_fma_f32 v254, v254, v241, v243\n"
"v_mfma_f32_16x16x32_bf16 a[76:79], a[140:143], v[112:115], 0\n"
"s_waitcnt lgkmcnt(9)\n"
"ds_read_b64_tr_b16 a[134:135], v227 offset:59392\n"
"v_mfma_f32_16x16x32_bf16 a[44:47], a[140:143], v[4:7], 0\n"
"buffer_load_dwordx4 v237, s[64:67], s85 offen offset:128 lds\n"
"s_add_u32 m0, m0, 0x480\n"
"v_mfma_f32_16x16x32_bf16 a[12:15], a[140:143], v[0:3], 0\n"
"ds_read_b64_tr_b16 a[136:137], v224 offset:49152\n"
"v_add_u32 v227, 0x10000, v227\n"
"v_mfma_f32_16x16x32_bf16 a[16:19], a[144:147], v[0:3], 0\n"
"ds_read_b64_tr_b16 a[138:139], v224 offset:57344\n"
"v_fma_f32 v255, v255, v241, v243\n"
"v_mfma_f32_16x16x32_bf16 a[48:51], a[144:147], v[4:7], 0\n"
"ds_read_b64_tr_b16 a[140:141], v224 offset:51200\n"
"v_cvt_pk_bf16_f32 v63, v254, v255\n"
"v_mfma_f32_16x16x32_bf16 a[80:83], a[144:147], v[112:115], 0\n"
"ds_read_b64_tr_b16 a[142:143], v224 offset:59392\n"
"v_add_u32 v224, 0x10000, v224\n"
"v_mfma_f32_16x16x32_bf16 a[112:115], a[144:147], v[116:119], 0\n"
"buffer_load_dwordx4 v237, s[68:71], s85 offen offset:0 lds\n"
"s_add_u32 m0, m0, 0x380\n"
"v_mfma_f32_16x16x32_bf16 a[116:119], a[148:151], v[116:119], 0\n"
"ds_read_b64_tr_b16 a[144:145], v225 offset:49152\n"
"v_lshlrev_b32 v246, 16, v64\n"
"v_mfma_f32_16x16x32_bf16 a[84:87], a[148:151], v[112:115], 0\n"
"s_waitcnt lgkmcnt(9)\n"
"ds_read_b64_tr_b16 a[146:147], v225 offset:57344\n"
"v_mfma_f32_16x16x32_bf16 a[52:55], a[148:151], v[4:7], 0\n"
"v_and_b32 v247, 0xffff0000, v64\n"
"v_fma_f32 v246, v246, v240, v242\n"
"v_mfma_f32_16x16x32_bf16 a[20:23], a[148:151], v[0:3], 0\n"
"ds_read_b64_tr_b16 a[148:149], v225 offset:51200\n"
"v_fma_f32 v247, v247, v240, v242\n"
"v_mfma_f32_16x16x32_bf16 a[24:27], a[152:155], v[0:3], 0\n"
"buffer_load_dwordx4 v237, s[68:71], s85 offen offset:128 lds\n"
"s_add_u32 m0, m0, 0xfffdb480\n"
"v_mfma_f32_16x16x32_bf16 a[56:59], a[152:155], v[4:7], 0\n"
"s_mov_b32 s85, 0xa8000\n"
"ds_read_b64_tr_b16 a[150:151], v225 offset:59392\n"
"v_mfma_f32_16x16x32_bf16 a[88:91], a[152:155], v[112:115], 0\n"
"v_add_u32 v225, 0x10000, v225\n"
"v_cvt_pk_bf16_f32 v64, v246, v247\n"
"v_mfma_f32_16x16x32_bf16 a[120:123], a[152:155], v[116:119], 0\n"
"ds_read_b64_tr_b16 a[152:153], v226 offset:49152\n"
"v_lshlrev_b32 v248, 16, v65\n"
"v_mfma_f32_16x16x32_bf16 a[124:127], a[156:159], v[116:119], 0\n"
"ds_read_b64_tr_b16 a[154:155], v226 offset:57344\n"
"v_and_b32 v249, 0xffff0000, v65\n"
"v_mfma_f32_16x16x32_bf16 a[92:95], a[156:159], v[112:115], 0\n"
"buffer_load_dwordx4 v237, s[64:67], s85 offen offset:0 lds\n"
"s_add_u32 m0, m0, 0x380\n"
"v_mfma_f32_16x16x32_bf16 a[60:63], a[156:159], v[4:7], 0\n"
"s_waitcnt lgkmcnt(10)\n"
"v_fma_f32 v248, v248, v240, v242\n"
"v_fma_f32 v249, v249, v240, v242\n"
"v_mfma_f32_16x16x32_bf16 a[28:31], a[156:159], v[0:3], 0\n"
"ds_read_b64_tr_b16 a[156:157], v226 offset:51200\n"
"v_cvt_pk_bf16_f32 v65, v248, v249\n"
"v_mfma_f32_16x16x32_bf16 a[224:227], a[128:131], v[8:11], a[224:227]\n"
"ds_read_b64_tr_b16 a[158:159], v226 offset:59392\n"
"v_add_u32 v226, 0x10000, v226\n"
"v_mfma_f32_16x16x32_bf16 a[232:235], a[128:131], v[12:15], a[232:235]\n"
"s_waitcnt lgkmcnt(8)\n"
"v_lshlrev_b32 v250, 16, v66\n"
"v_and_b32 v251, 0xffff0000, v66\n"
"v_mfma_f32_16x16x32_bf16 a[236:239], a[132:135], v[12:15], a[236:239]\n"
"buffer_load_dwordx4 v237, s[64:67], s85 offen offset:128 lds\n"
"s_add_u32 m0, m0, 0x480\n"
"v_mfma_f32_16x16x32_bf16 a[228:231], a[132:135], v[8:11], a[228:231]\n"
"s_waitcnt lgkmcnt(15)\n"
"v_fma_f32 v250, v250, v240, v242\n"
"v_fma_f32 v251, v251, v240, v242\n"
"v_mfma_f32_16x16x32_bf16 a[8:11], a[136:139], v[8:11], a[8:11]\n"
"v_cvt_pk_bf16_f32 v66, v250, v251\n"
"v_lshlrev_b32 v254, 16, v67\n"
"v_mfma_f32_16x16x32_bf16 a[40:43], a[136:139], v[12:15], a[40:43]\n"
"v_and_b32 v255, 0xffff0000, v67\n"
"v_fma_f32 v254, v254, v240, v242\n"
"v_mfma_f32_16x16x32_bf16 a[72:75], a[136:139], v[120:123], a[72:75]\n"
"v_fma_f32 v255, v255, v240, v242\n"
"v_lshlrev_b32 v246, 16, v68\n"
"v_mfma_f32_16x16x32_bf16 a[104:107], a[136:139], v[124:127], a[104:107]\n"
"buffer_load_dwordx4 v237, s[68:71], s85 offen offset:0 lds\n"
"s_add_u32 m0, m0, 0x380\n"
"v_mfma_f32_16x16x32_bf16 a[108:111], a[140:143], v[124:127], a[108:111]\n"
"v_cvt_pk_bf16_f32 v67, v254, v255\n"
"v_and_b32 v247, 0xffff0000, v68\n"
"v_mfma_f32_16x16x32_bf16 a[76:79], a[140:143], v[120:123], a[76:79]\n"
"s_waitcnt vmcnt(11) lgkmcnt(15)\n"
"s_barrier\n"
"s_waitcnt lgkmcnt(4)\n"
"v_mfma_f32_16x16x32_bf16 a[44:47], a[140:143], v[12:15], a[44:47]\n"
"ds_read_b64_tr_b16 a[128:129], v227 offset:0\n"
"v_fma_f32 v246, v246, v241, v243\n"
"v_mfma_f32_16x16x32_bf16 a[12:15], a[140:143], v[8:11], a[12:15]\n"
"ds_read_b64_tr_b16 a[130:131], v227 offset:8192\n"
"v_fma_f32 v247, v247, v241, v243\n"
"v_mfma_f32_16x16x32_bf16 a[16:19], a[144:147], v[8:11], a[16:19]\n"
"buffer_load_dwordx4 v237, s[68:71], s85 offen offset:128 lds\n"
"s_add_u32 m0, m0, 0x3480\n"
"v_mfma_f32_16x16x32_bf16 a[48:51], a[144:147], v[12:15], a[48:51]\n"
"s_mov_b32 s85, 0xc4000\n"
"ds_read_b128 v[128:131], v245 offset:16384\n"
"v_mfma_f32_16x16x32_bf16 a[80:83], a[144:147], v[120:123], a[80:83]\n"
"ds_read_b64_tr_b16 a[132:133], v227 offset:2048\n"
"v_cvt_pk_bf16_f32 v68, v246, v247\n"
"v_mfma_f32_16x16x32_bf16 a[112:115], a[144:147], v[124:127], a[112:115]\n"
"buffer_load_dword a255, v231, s[60:63], 0 offen\n"
"v_mfma_f32_16x16x32_bf16 a[116:119], a[148:151], v[124:127], a[116:119]\n"
"ds_read_b64_tr_b16 a[134:135], v227 offset:10240\n"
"v_lshlrev_b32 v248, 16, v69\n"
"v_mfma_f32_16x16x32_bf16 a[84:87], a[148:151], v[120:123], a[84:87]\n"
"s_waitcnt lgkmcnt(5)\n"
"ds_read_b128 v[132:135], v245 offset:17408\n"
"v_mfma_f32_16x16x32_bf16 a[52:55], a[148:151], v[12:15], a[52:55]\n"
"buffer_load_dwordx4 v237, s[64:67], s85 offen offset:0 lds\n"
"s_add_u32 m0, m0, 0x380\n"
"v_mfma_f32_16x16x32_bf16 a[20:23], a[148:151], v[8:11], a[20:23]\n"
"ds_read_b64_tr_b16 a[136:137], v224 offset:0\n"
"v_and_b32 v249, 0xffff0000, v69\n"
"v_mfma_f32_16x16x32_bf16 a[24:27], a[152:155], v[8:11], a[24:27]\n"
"ds_read_b64_tr_b16 a[138:139], v224 offset:8192\n"
"v_fma_f32 v248, v248, v241, v243\n"
"v_mfma_f32_16x16x32_bf16 a[56:59], a[152:155], v[12:15], a[56:59]\n"
"ds_read_b64_tr_b16 a[140:141], v224 offset:2048\n"
"ds_write_b128 v244, v[32:35] offset:0\n"
"v_mfma_f32_16x16x32_bf16 a[88:91], a[152:155], v[120:123], a[88:91]\n"
"ds_read_b64_tr_b16 a[142:143], v224 offset:10240\n"
"v_fma_f32 v249, v249, v241, v243\n"
"v_mfma_f32_16x16x32_bf16 a[120:123], a[152:155], v[124:127], a[120:123]\n"
"buffer_load_dwordx4 v237, s[64:67], s85 offen offset:128 lds\n"
"s_add_u32 m0, m0, 0x480\n"
"v_mfma_f32_16x16x32_bf16 a[124:127], a[156:159], v[124:127], a[124:127]\n"
"ds_read_b64_tr_b16 a[144:145], v225 offset:0\n"
"v_cvt_pk_bf16_f32 v69, v248, v249\n"
"v_mfma_f32_16x16x32_bf16 a[92:95], a[156:159], v[120:123], a[92:95]\n"
"s_waitcnt lgkmcnt(7)\n"
"ds_read_b64_tr_b16 a[146:147], v225 offset:8192\n"
"v_mfma_f32_16x16x32_bf16 a[60:63], a[156:159], v[12:15], a[60:63]\n"
"ds_read_b64_tr_b16 a[148:149], v225 offset:2048\n"
"v_lshlrev_b32 v250, 16, v70\n"
"v_mfma_f32_16x16x32_bf16 a[28:31], a[156:159], v[8:11], a[28:31]\n"
"ds_read_b64_tr_b16 a[150:151], v225 offset:10240\n"
"v_and_b32 v251, 0xffff0000, v70\n"
"v_mfma_f32_16x16x32_bf16 a[224:227], a[128:131], v[16:19], a[224:227]\n"
"buffer_load_dwordx4 v237, s[68:71], s85 offen offset:0 lds\n"
"s_add_u32 m0, m0, 0x380\n"
"v_mfma_f32_16x16x32_bf16 a[232:235], a[128:131], v[20:23], a[232:235]\n"
"s_waitcnt lgkmcnt(4)\n"
"ds_read_b128 v[136:139], v245 offset:18432\n"
"v_mfma_f32_16x16x32_bf16 a[236:239], a[132:135], v[20:23], a[236:239]\n"
"s_waitcnt lgkmcnt(10)\n"
"ds_read_b64_tr_b16 a[152:153], v226 offset:0\n"
"v_mfma_f32_16x16x32_bf16 a[228:231], a[132:135], v[16:19], a[228:231]\n"
"ds_write_b128 v244, v[36:39] offset:1024\n"
"v_mfma_f32_16x16x32_bf16 a[8:11], a[136:139], v[16:19], a[8:11]\n"
"ds_read_b64_tr_b16 a[154:155], v226 offset:8192\n"
"v_fma_f32 v250, v250, v241, v243\n"
"v_mfma_f32_16x16x32_bf16 a[40:43], a[136:139], v[20:23], a[40:43]\n"
"ds_read_b128 v[140:143], v245 offset:19456\n"
"v_fma_f32 v251, v251, v241, v243\n"
"v_mfma_f32_16x16x32_bf16 a[72:75], a[136:139], v[128:131], a[72:75]\n"
"buffer_load_dwordx4 v237, s[68:71], s85 offen offset:128 lds\n"
"s_add_u32 m0, m0, 0x3480\n"
"v_mfma_f32_16x16x32_bf16 a[104:107], a[136:139], v[132:135], a[104:107]\n"
"s_mov_b32 s85, 0xe0000\n"
"ds_read_b64_tr_b16 a[156:157], v226 offset:2048\n"
"v_mfma_f32_16x16x32_bf16 a[108:111], a[140:143], v[132:135], a[108:111]\n"
"ds_read_b64_tr_b16 a[158:159], v226 offset:10240\n"
"v_cvt_pk_bf16_f32 v70, v250, v251\n"
"v_mfma_f32_16x16x32_bf16 a[76:79], a[140:143], v[128:131], a[76:79]\n"
"s_waitcnt lgkmcnt(7)\n"
"ds_read_b64_tr_b16 a[128:129], v227 offset:16384\n"
"v_mfma_f32_16x16x32_bf16 a[44:47], a[140:143], v[20:23], a[44:47]\n"
"ds_read_b64_tr_b16 a[130:131], v227 offset:24576\n"
"v_lshlrev_b32 v254, 16, v71\n"
"v_mfma_f32_16x16x32_bf16 a[12:15], a[140:143], v[16:19], a[12:15]\n"
"ds_read_b64_tr_b16 a[132:133], v227 offset:18432\n"
"v_and_b32 v255, 0xffff0000, v71\n"
"v_mfma_f32_16x16x32_bf16 a[16:19], a[144:147], v[16:19], a[16:19]\n"
"ds_read_b64_tr_b16 a[134:135], v227 offset:26624\n"
"v_add_u32 v227, 0x10000, v227\n"
"v_mfma_f32_16x16x32_bf16 a[48:51], a[144:147], v[20:23], a[48:51]\n"
"ds_write_b128 v244, v[40:43] offset:2048\n"
"v_mfma_f32_16x16x32_bf16 a[80:83], a[144:147], v[128:131], a[80:83]\n"
"ds_read_b64_tr_b16 a[136:137], v224 offset:16384\n"
"v_fma_f32 v254, v254, v241, v243\n"
"v_mfma_f32_16x16x32_bf16 a[112:115], a[144:147], v[132:135], a[112:115]\n"
"ds_read_b64_tr_b16 a[138:139], v224 offset:24576\n"
"v_fma_f32 v255, v255, v241, v243\n"
"v_mfma_f32_16x16x32_bf16 a[116:119], a[148:151], v[132:135], a[116:119]\n"
"ds_read_b64_tr_b16 a[140:141], v224 offset:18432\n"
"v_cvt_pk_bf16_f32 v71, v254, v255\n"
"v_mfma_f32_16x16x32_bf16 a[84:87], a[148:151], v[128:131], a[84:87]\n"
"s_waitcnt lgkmcnt(8)\n"
"ds_read_b64_tr_b16 a[142:143], v224 offset:26624\n"
"v_mfma_f32_16x16x32_bf16 a[52:55], a[148:151], v[20:23], a[52:55]\n"
"ds_read_b64_tr_b16 a[144:145], v225 offset:16384\n"
"v_add_u32 v224, 0x10000, v224\n"
"v_mfma_f32_16x16x32_bf16 a[20:23], a[148:151], v[16:19], a[20:23]\n"
"ds_read_b64_tr_b16 a[146:147], v225 offset:24576\n"
"v_lshlrev_b32 v246, 16, v72\n"
"v_mfma_f32_16x16x32_bf16 a[24:27], a[152:155], v[16:19], a[24:27]\n"
"ds_write_b128 v244, v[44:47] offset:3072\n"
"v_mfma_f32_16x16x32_bf16 a[56:59], a[152:155], v[20:23], a[56:59]\n"
"ds_read_b64_tr_b16 a[148:149], v225 offset:18432\n"
"v_and_b32 v247, 0xffff0000, v72\n"
"v_mfma_f32_16x16x32_bf16 a[88:91], a[152:155], v[128:131], a[88:91]\n"
"ds_read_b64_tr_b16 a[150:151], v225 offset:26624\n"
"v_add_u32 v225, 0x10000, v225\n"
"v_mfma_f32_16x16x32_bf16 a[120:123], a[152:155], v[132:135], a[120:123]\n"
"ds_read_b64_tr_b16 a[152:153], v226 offset:16384\n"
"v_fma_f32 v246, v246, v240, v242\n"
"v_mfma_f32_16x16x32_bf16 a[124:127], a[156:159], v[132:135], a[124:127]\n"
"ds_read_b64_tr_b16 a[154:155], v226 offset:24576\n"
"v_fma_f32 v247, v247, v240, v242\n"
"v_mfma_f32_16x16x32_bf16 a[92:95], a[156:159], v[128:131], a[92:95]\n"
"s_waitcnt lgkmcnt(12)\n"
"v_cvt_pk_bf16_f32 v72, v246, v247\n"
"v_lshlrev_b32 v248, 16, v73\n"
"v_mfma_f32_16x16x32_bf16 a[60:63], a[156:159], v[20:23], a[60:63]\n"
"v_and_b32 v249, 0xffff0000, v73\n"
"v_fma_f32 v248, v248, v240, v242\n"
"v_mfma_f32_16x16x32_bf16 a[28:31], a[156:159], v[16:19], a[28:31]\n"
"ds_read_b64_tr_b16 a[156:157], v226 offset:18432\n"
"v_fma_f32 v249, v249, v240, v242\n"
"v_mfma_f32_16x16x32_bf16 a[224:227], a[128:131], v[24:27], a[224:227]\n"
"ds_read_b64_tr_b16 a[158:159], v226 offset:26624\n"
"v_add_u32 v226, 0x10000, v226\n"
"v_mfma_f32_16x16x32_bf16 a[232:235], a[128:131], v[28:31], a[232:235]\n"
"s_waitcnt lgkmcnt(9)\n"
"v_cvt_pk_bf16_f32 v73, v248, v249\n"
"v_lshlrev_b32 v250, 16, v74\n"
"v_mfma_f32_16x16x32_bf16 a[236:239], a[132:135], v[28:31], a[236:239]\n"
"s_waitcnt lgkmcnt(15)\n"
"v_and_b32 v251, 0xffff0000, v74\n"
"v_fma_f32 v250, v250, v240, v242\n"
"v_mfma_f32_16x16x32_bf16 a[228:231], a[132:135], v[24:27], a[228:231]\n"
"v_fma_f32 v251, v251, v240, v242\n"
"v_lshlrev_b32 v254, 16, v75\n"
"v_mfma_f32_16x16x32_bf16 a[8:11], a[136:139], v[24:27], a[8:11]\n"
"v_cvt_pk_bf16_f32 v74, v250, v251\n"
"v_and_b32 v255, 0xffff0000, v75\n"
"v_mfma_f32_16x16x32_bf16 a[40:43], a[136:139], v[28:31], a[40:43]\n"
"v_fma_f32 v254, v254, v240, v242\n"
"v_fma_f32 v255, v255, v240, v242\n"
"v_mfma_f32_16x16x32_bf16 a[72:75], a[136:139], v[136:139], a[72:75]\n"
"v_cvt_pk_bf16_f32 v75, v254, v255\n"
"v_lshlrev_b32 v246, 16, v76\n"
"v_mfma_f32_16x16x32_bf16 a[104:107], a[136:139], v[140:143], a[104:107]\n"
"v_and_b32 v247, 0xffff0000, v76\n"
"v_fma_f32 v246, v246, v241, v243\n"
"v_mfma_f32_16x16x32_bf16 a[108:111], a[140:143], v[140:143], a[108:111]\n"
"v_fma_f32 v247, v247, v241, v243\n"
"v_lshlrev_b32 v248, 16, v77\n"
"v_mfma_f32_16x16x32_bf16 a[76:79], a[140:143], v[136:139], a[76:79]\n"
"s_waitcnt vmcnt(9) lgkmcnt(6)\n"
"s_barrier\n"
"s_waitcnt lgkmcnt(4)\n"
"v_mfma_f32_16x16x32_bf16 a[44:47], a[140:143], v[28:31], a[44:47]\n"
"ds_read_b64_tr_b16 a[128:129], v227 offset:0\n"
"v_cvt_pk_bf16_f32 v76, v246, v247\n"
"v_mfma_f32_16x16x32_bf16 a[12:15], a[140:143], v[24:27], a[12:15]\n"
"ds_read_b64_tr_b16 a[130:131], v227 offset:8192\n"
"v_and_b32 v249, 0xffff0000, v77\n"
"v_mfma_f32_16x16x32_bf16 a[16:19], a[144:147], v[24:27], a[16:19]\n"
"ds_read_b64_tr_b16 a[132:133], v227 offset:2048\n"
"v_fma_f32 v248, v248, v241, v243\n"
"v_mfma_f32_16x16x32_bf16 a[48:51], a[144:147], v[28:31], a[48:51]\n"
"buffer_load_dwordx4 v237, s[64:67], s85 offen offset:0 lds\n"
"s_add_u32 m0, m0, 0x380\n"
"v_mfma_f32_16x16x32_bf16 a[80:83], a[144:147], v[136:139], a[80:83]\n"
"ds_read_b128 v[144:147], v245 offset:0\n"
"v_fma_f32 v249, v249, v241, v243\n"
"v_mfma_f32_16x16x32_bf16 a[112:115], a[144:147], v[140:143], a[112:115]\n"
"ds_read_b64_tr_b16 a[134:135], v227 offset:10240\n"
"v_cvt_pk_bf16_f32 v77, v248, v249\n"
"v_mfma_f32_16x16x32_bf16 a[116:119], a[148:151], v[140:143], a[116:119]\n"
"ds_read_b64_tr_b16 a[136:137], v224 offset:0\n"
"v_lshlrev_b32 v250, 16, v78\n"
"v_mfma_f32_16x16x32_bf16 a[84:87], a[148:151], v[136:139], a[84:87]\n"
"s_waitcnt lgkmcnt(6)\n"
"ds_read_b64_tr_b16 a[138:139], v224 offset:8192\n"
"v_mfma_f32_16x16x32_bf16 a[52:55], a[148:151], v[28:31], a[52:55]\n"
"ds_read_b128 v[148:151], v245 offset:1024\n"
"v_and_b32 v251, 0xffff0000, v78\n"
"v_mfma_f32_16x16x32_bf16 a[20:23], a[148:151], v[24:27], a[20:23]\n"
"buffer_load_dwordx4 v237, s[64:67], s85 offen offset:128 lds\n"
"s_add_u32 m0, m0, 0x480\n"
"v_mfma_f32_16x16x32_bf16 a[24:27], a[152:155], v[24:27], a[24:27]\n"
"ds_read_b64_tr_b16 a[140:141], v224 offset:2048\n"
"v_fma_f32 v250, v250, v241, v243\n"
"v_mfma_f32_16x16x32_bf16 a[56:59], a[152:155], v[28:31], a[56:59]\n"
"ds_write_b128 v244, v[48:51] offset:16384\n"
"v_mfma_f32_16x16x32_bf16 a[88:91], a[152:155], v[136:139], a[88:91]\n"
"ds_read_b64_tr_b16 a[142:143], v224 offset:10240\n"
"v_fma_f32 v251, v251, v241, v243\n"
"v_mfma_f32_16x16x32_bf16 a[120:123], a[152:155], v[140:143], a[120:123]\n"
"ds_read_b64_tr_b16 a[144:145], v225 offset:0\n"
"v_cvt_pk_bf16_f32 v78, v250, v251\n"
"v_mfma_f32_16x16x32_bf16 a[124:127], a[156:159], v[140:143], a[124:127]\n"
"buffer_load_dwordx4 v237, s[68:71], s85 offen offset:0 lds\n"
"s_add_u32 m0, m0, 0x380\n"
"v_mfma_f32_16x16x32_bf16 a[92:95], a[156:159], v[136:139], a[92:95]\n"
"s_waitcnt lgkmcnt(7)\n"
"ds_read_b64_tr_b16 a[146:147], v225 offset:8192\n"
"v_mfma_f32_16x16x32_bf16 a[60:63], a[156:159], v[28:31], a[60:63]\n"
"ds_read_b64_tr_b16 a[148:149], v225 offset:2048\n"
"v_lshlrev_b32 v254, 16, v79\n"
"v_mfma_f32_16x16x32_bf16 a[28:31], a[156:159], v[24:27], a[28:31]\n"
"ds_read_b64_tr_b16 a[150:151], v225 offset:10240\n"
"v_and_b32 v255, 0xffff0000, v79\n"
"v_mfma_f32_16x16x32_bf16 a[224:227], a[128:131], v[32:35], a[224:227]\n"
"ds_read_b64_tr_b16 a[152:153], v226 offset:0\n"
"v_fma_f32 v254, v254, v241, v243\n"
"v_mfma_f32_16x16x32_bf16 a[232:235], a[128:131], v[36:39], a[232:235]\n"
"s_waitcnt lgkmcnt(5)\n"
"ds_read_b128 v[152:155], v245 offset:2048\n"
"v_mfma_f32_16x16x32_bf16 a[236:239], a[132:135], v[36:39], a[236:239]\n"
"buffer_load_dwordx4 v237, s[68:71], s85 offen offset:128 lds\n"
"s_add_u32 m0, m0, 0x3480\n"
"v_mfma_f32_16x16x32_bf16 a[228:231], a[132:135], v[32:35], a[228:231]\n"
"ds_write_b128 v244, v[52:55] offset:17408\n"
"v_mfma_f32_16x16x32_bf16 a[8:11], a[136:139], v[32:35], a[8:11]\n"
"s_mov_b32 s85, 0xfc000\n"
"ds_read_b64_tr_b16 a[154:155], v226 offset:8192\n"
"v_mfma_f32_16x16x32_bf16 a[40:43], a[136:139], v[36:39], a[40:43]\n"
"s_waitcnt lgkmcnt(11)\n"
"ds_read_b128 v[156:159], v245 offset:3072\n"
"v_mfma_f32_16x16x32_bf16 a[72:75], a[136:139], v[144:147], a[72:75]\n"
"ds_read_b64_tr_b16 a[156:157], v226 offset:2048\n"
"v_fma_f32 v255, v255, v241, v243\n"
"v_mfma_f32_16x16x32_bf16 a[104:107], a[136:139], v[148:151], a[104:107]\n"
"buffer_load_dwordx4 v237, s[64:67], s85 offen offset:0 lds\n"
"s_add_u32 m0, m0, 0x380\n"
"v_mfma_f32_16x16x32_bf16 a[108:111], a[140:143], v[148:151], a[108:111]\n"
"ds_read_b64_tr_b16 a[158:159], v226 offset:10240\n"
"v_cvt_pk_bf16_f32 v79, v254, v255\n"
"v_mfma_f32_16x16x32_bf16 a[76:79], a[140:143], v[144:147], a[76:79]\n"
"s_waitcnt lgkmcnt(7)\n"
"ds_read_b64_tr_b16 a[128:129], v227 offset:16384\n"
"v_mfma_f32_16x16x32_bf16 a[44:47], a[140:143], v[36:39], a[44:47]\n"
"ds_read_b64_tr_b16 a[130:131], v227 offset:24576\n"
"v_lshlrev_b32 v246, 16, v80\n"
"v_mfma_f32_16x16x32_bf16 a[12:15], a[140:143], v[32:35], a[12:15]\n"
"ds_read_b64_tr_b16 a[132:133], v227 offset:18432\n"
"v_and_b32 v247, 0xffff0000, v80\n"
"v_mfma_f32_16x16x32_bf16 a[16:19], a[144:147], v[32:35], a[16:19]\n"
"buffer_load_dwordx4 v237, s[64:67], s85 offen offset:128 lds\n"
"s_add_u32 m0, m0, 0x480\n"
"v_mfma_f32_16x16x32_bf16 a[48:51], a[144:147], v[36:39], a[48:51]\n"
"ds_write_b128 v244, v[56:59] offset:18432\n"
"v_mfma_f32_16x16x32_bf16 a[80:83], a[144:147], v[144:147], a[80:83]\n"
"ds_read_b64_tr_b16 a[134:135], v227 offset:26624\n"
"v_fma_f32 v246, v246, v240, v242\n"
"v_mfma_f32_16x16x32_bf16 a[112:115], a[144:147], v[148:151], a[112:115]\n"
"ds_read_b64_tr_b16 a[136:137], v224 offset:16384\n"
"v_fma_f32 v247, v247, v240, v242\n"
"v_mfma_f32_16x16x32_bf16 a[116:119], a[148:151], v[148:151], a[116:119]\n"
"ds_read_b64_tr_b16 a[138:139], v224 offset:24576\n"
"v_cvt_pk_bf16_f32 v80, v246, v247\n"
"v_mfma_f32_16x16x32_bf16 a[84:87], a[148:151], v[144:147], a[84:87]\n"
"buffer_load_dwordx4 v237, s[68:71], s85 offen offset:0 lds\n"
"s_add_u32 m0, m0, 0x380\n"
"v_mfma_f32_16x16x32_bf16 a[52:55], a[148:151], v[36:39], a[52:55]\n"
"s_waitcnt lgkmcnt(7)\n"
"ds_read_b64_tr_b16 a[140:141], v224 offset:18432\n"
"v_mfma_f32_16x16x32_bf16 a[20:23], a[148:151], v[32:35], a[20:23]\n"
"ds_read_b64_tr_b16 a[142:143], v224 offset:26624\n"
"v_lshlrev_b32 v248, 16, v81\n"
"v_mfma_f32_16x16x32_bf16 a[24:27], a[152:155], v[32:35], a[24:27]\n"
"ds_read_b64_tr_b16 a[144:145], v225 offset:16384\n"
"v_add_u32 v224, 0xfffe0000, v224\n"
"v_mfma_f32_16x16x32_bf16 a[56:59], a[152:155], v[36:39], a[56:59]\n"
"ds_read_b64_tr_b16 a[146:147], v225 offset:24576\n"
"v_add_u32 v227, 0xfffe0000, v227\n"
"v_mfma_f32_16x16x32_bf16 a[88:91], a[152:155], v[144:147], a[88:91]\n"
"buffer_load_dwordx4 v237, s[68:71], s85 offen offset:128 lds\n"
"s_add_u32 m0, m0, 0x3480\n"
"v_mfma_f32_16x16x32_bf16 a[120:123], a[152:155], v[148:151], a[120:123]\n"
"ds_write_b128 v244, v[60:63] offset:19456\n"
"v_mfma_f32_16x16x32_bf16 a[124:127], a[156:159], v[148:151], a[124:127]\n"
"s_mov_b32 s85, 0x118000\n"
"ds_read_b64_tr_b16 a[148:149], v225 offset:18432\n"
"v_mfma_f32_16x16x32_bf16 a[92:95], a[156:159], v[144:147], a[92:95]\n"
"s_waitcnt lgkmcnt(8)\n"
"ds_read_b64_tr_b16 a[150:151], v225 offset:26624\n"
"v_mfma_f32_16x16x32_bf16 a[60:63], a[156:159], v[36:39], a[60:63]\n"
"ds_read_b64_tr_b16 a[152:153], v226 offset:16384\n"
"v_add_u32 v225, 0xfffe0000, v225\n"
"v_mfma_f32_16x16x32_bf16 a[28:31], a[156:159], v[32:35], a[28:31]\n"
"ds_read_b64_tr_b16 a[154:155], v226 offset:24576\n"
"v_and_b32 v249, 0xffff0000, v81\n"
"v_mfma_f32_16x16x32_bf16 a[224:227], a[128:131], v[40:43], a[224:227]\n"
"ds_read_b64_tr_b16 a[156:157], v226 offset:18432\n"
"v_fma_f32 v248, v248, v240, v242\n"
"v_mfma_f32_16x16x32_bf16 a[232:235], a[128:131], v[44:47], a[232:235]\n"
"s_waitcnt lgkmcnt(8)\n"
"ds_read_b64_tr_b16 a[158:159], v226 offset:26624\n"
"v_mfma_f32_16x16x32_bf16 a[236:239], a[132:135], v[44:47], a[236:239]\n"
"s_waitcnt lgkmcnt(15)\n"
"v_add_u32 v226, 0xfffe0000, v226\n"
"v_fma_f32 v249, v249, v240, v242\n"
"v_mfma_f32_16x16x32_bf16 a[228:231], a[132:135], v[40:43], a[228:231]\n"
"v_cvt_pk_bf16_f32 v81, v248, v249\n"
"v_lshlrev_b32 v250, 16, v82\n"
"v_mfma_f32_16x16x32_bf16 a[8:11], a[136:139], v[40:43], a[8:11]\n"
"v_and_b32 v251, 0xffff0000, v82\n"
"v_fma_f32 v250, v250, v240, v242\n"
"v_mfma_f32_16x16x32_bf16 a[40:43], a[136:139], v[44:47], a[40:43]\n"
"v_fma_f32 v251, v251, v240, v242\n"
"v_lshlrev_b32 v254, 16, v83\n"
"v_mfma_f32_16x16x32_bf16 a[72:75], a[136:139], v[152:155], a[72:75]\n"
"v_cvt_pk_bf16_f32 v82, v250, v251\n"
"v_and_b32 v255, 0xffff0000, v83\n"
"v_mfma_f32_16x16x32_bf16 a[104:107], a[136:139], v[156:159], a[104:107]\n"
"v_fma_f32 v254, v254, v240, v242\n"
"v_fma_f32 v255, v255, v240, v242\n"
"v_mfma_f32_16x16x32_bf16 a[108:111], a[140:143], v[156:159], a[108:111]\n"
"v_cvt_pk_bf16_f32 v83, v254, v255\n"
"v_lshlrev_b32 v246, 16, v84\n"
"v_mfma_f32_16x16x32_bf16 a[76:79], a[140:143], v[152:155], a[76:79]\n"
"s_waitcnt vmcnt(8) lgkmcnt(6)\n"
"s_barrier\n"
"s_waitcnt lgkmcnt(4)\n"
"v_mfma_f32_16x16x32_bf16 a[44:47], a[140:143], v[44:47], a[44:47]\n"
"ds_read_b64_tr_b16 a[128:129], v227 offset:0\n"
"v_and_b32 v247, 0xffff0000, v84\n"
"v_mfma_f32_16x16x32_bf16 a[12:15], a[140:143], v[40:43], a[12:15]\n"
"ds_read_b64_tr_b16 a[130:131], v227 offset:8192\n"
"v_fma_f32 v246, v246, v241, v243\n"
"v_mfma_f32_16x16x32_bf16 a[16:19], a[144:147], v[40:43], a[16:19]\n"
"ds_read_b64_tr_b16 a[132:133], v227 offset:2048\n"
"v_fma_f32 v247, v247, v241, v243\n"
"v_mfma_f32_16x16x32_bf16 a[48:51], a[144:147], v[44:47], a[48:51]\n"
"buffer_load_dwordx4 v237, s[64:67], s85 offen offset:0 lds\n"
"s_add_u32 m0, m0, 0x380\n"
"v_mfma_f32_16x16x32_bf16 a[80:83], a[144:147], v[152:155], a[80:83]\n"
"ds_read_b128 v[160:163], v245 offset:16384\n"
"v_cvt_pk_bf16_f32 v84, v246, v247\n"
"v_mfma_f32_16x16x32_bf16 a[112:115], a[144:147], v[156:159], a[112:115]\n"
"ds_read_b64_tr_b16 a[134:135], v227 offset:10240\n"
"v_lshlrev_b32 v248, 16, v85\n"
"v_mfma_f32_16x16x32_bf16 a[116:119], a[148:151], v[156:159], a[116:119]\n"
"ds_read_b64_tr_b16 a[136:137], v224 offset:0\n"
"v_and_b32 v249, 0xffff0000, v85\n"
"v_mfma_f32_16x16x32_bf16 a[84:87], a[148:151], v[152:155], a[84:87]\n"
"s_waitcnt lgkmcnt(6)\n"
"ds_read_b64_tr_b16 a[138:139], v224 offset:8192\n"
"v_mfma_f32_16x16x32_bf16 a[52:55], a[148:151], v[44:47], a[52:55]\n"
"ds_read_b128 v[164:167], v245 offset:17408\n"
"v_fma_f32 v248, v248, v241, v243\n"
"v_mfma_f32_16x16x32_bf16 a[20:23], a[148:151], v[40:43], a[20:23]\n"
"buffer_load_dwordx4 v237, s[64:67], s85 offen offset:128 lds\n"
"s_add_u32 m0, m0, 0x480\n"
"v_mfma_f32_16x16x32_bf16 a[24:27], a[152:155], v[40:43], a[24:27]\n"
"ds_read_b64_tr_b16 a[140:141], v224 offset:2048\n"
"v_fma_f32 v249, v249, v241, v243\n"
"v_mfma_f32_16x16x32_bf16 a[56:59], a[152:155], v[44:47], a[56:59]\n"
"ds_write_b128 v244, v[64:67] offset:0\n"
"v_mfma_f32_16x16x32_bf16 a[88:91], a[152:155], v[152:155], a[88:91]\n"
"ds_read_b64_tr_b16 a[142:143], v224 offset:10240\n"
"v_cvt_pk_bf16_f32 v85, v248, v249\n"
"v_mfma_f32_16x16x32_bf16 a[120:123], a[152:155], v[156:159], a[120:123]\n"
"ds_read_b64_tr_b16 a[144:145], v225 offset:0\n"
"v_lshlrev_b32 v250, 16, v86\n"
"v_mfma_f32_16x16x32_bf16 a[124:127], a[156:159], v[156:159], a[124:127]\n"
"buffer_load_dwordx4 v237, s[68:71], s85 offen offset:0 lds\n"
"s_add_u32 m0, m0, 0x380\n"
"v_mfma_f32_16x16x32_bf16 a[92:95], a[156:159], v[152:155], a[92:95]\n"
"s_waitcnt lgkmcnt(7)\n"
"ds_read_b64_tr_b16 a[146:147], v225 offset:8192\n"
"v_mfma_f32_16x16x32_bf16 a[60:63], a[156:159], v[44:47], a[60:63]\n"
"ds_read_b64_tr_b16 a[148:149], v225 offset:2048\n"
"v_and_b32 v251, 0xffff0000, v86\n"
"v_mfma_f32_16x16x32_bf16 a[28:31], a[156:159], v[40:43], a[28:31]\n"
"ds_read_b64_tr_b16 a[150:151], v225 offset:10240\n"
"v_fma_f32 v250, v250, v241, v243\n"
"v_mfma_f32_16x16x32_bf16 a[224:227], a[128:131], v[48:51], a[224:227]\n"
"ds_read_b64_tr_b16 a[152:153], v226 offset:0\n"
"v_fma_f32 v251, v251, v241, v243\n"
"v_mfma_f32_16x16x32_bf16 a[232:235], a[128:131], v[52:55], a[232:235]\n"
"s_waitcnt lgkmcnt(5)\n"
"ds_read_b128 v[168:171], v245 offset:18432\n"
"v_mfma_f32_16x16x32_bf16 a[236:239], a[132:135], v[52:55], a[236:239]\n"
"buffer_load_dwordx4 v237, s[68:71], s85 offen offset:128 lds\n"
"s_add_u32 m0, m0, 0x3480\n"
"v_mfma_f32_16x16x32_bf16 a[228:231], a[132:135], v[48:51], a[228:231]\n"
"ds_write_b128 v244, v[68:71] offset:1024\n"
"v_mfma_f32_16x16x32_bf16 a[8:11], a[136:139], v[48:51], a[8:11]\n"
"s_mov_b32 s85, 0x134000\n"
"ds_read_b64_tr_b16 a[154:155], v226 offset:8192\n"
"v_mfma_f32_16x16x32_bf16 a[40:43], a[136:139], v[52:55], a[40:43]\n"
"s_waitcnt lgkmcnt(11)\n"
"ds_read_b128 v[172:175], v245 offset:19456\n"
"v_mfma_f32_16x16x32_bf16 a[72:75], a[136:139], v[160:163], a[72:75]\n"
"ds_read_b64_tr_b16 a[156:157], v226 offset:2048\n"
"v_cvt_pk_bf16_f32 v86, v250, v251\n"
"v_mfma_f32_16x16x32_bf16 a[104:107], a[136:139], v[164:167], a[104:107]\n"
"buffer_load_dwordx4 v237, s[64:67], s85 offen offset:0 lds\n"
"s_add_u32 m0, m0, 0x380\n"
"v_mfma_f32_16x16x32_bf16 a[108:111], a[140:143], v[164:167], a[108:111]\n"
"ds_read_b64_tr_b16 a[158:159], v226 offset:10240\n"
"v_lshlrev_b32 v254, 16, v87\n"
"v_mfma_f32_16x16x32_bf16 a[76:79], a[140:143], v[160:163], a[76:79]\n"
"s_waitcnt lgkmcnt(7)\n"
"ds_read_b64_tr_b16 a[128:129], v227 offset:16384\n"
"v_mfma_f32_16x16x32_bf16 a[44:47], a[140:143], v[52:55], a[44:47]\n"
"ds_read_b64_tr_b16 a[130:131], v227 offset:24576\n"
"v_and_b32 v255, 0xffff0000, v87\n"
"v_mfma_f32_16x16x32_bf16 a[12:15], a[140:143], v[48:51], a[12:15]\n"
"ds_read_b64_tr_b16 a[132:133], v227 offset:18432\n"
"v_fma_f32 v254, v254, v241, v243\n"
"v_mfma_f32_16x16x32_bf16 a[16:19], a[144:147], v[48:51], a[16:19]\n"
"buffer_load_dwordx4 v237, s[64:67], s85 offen offset:128 lds\n"
"s_add_u32 m0, m0, 0x480\n"
"v_mfma_f32_16x16x32_bf16 a[48:51], a[144:147], v[52:55], a[48:51]\n"
"ds_write_b128 v244, v[72:75] offset:2048\n"
"v_mfma_f32_16x16x32_bf16 a[80:83], a[144:147], v[160:163], a[80:83]\n"
"ds_read_b64_tr_b16 a[134:135], v227 offset:26624\n"
"v_fma_f32 v255, v255, v241, v243\n"
"v_mfma_f32_16x16x32_bf16 a[112:115], a[144:147], v[164:167], a[112:115]\n"
"ds_read_b64_tr_b16 a[136:137], v224 offset:16384\n"
"v_cvt_pk_bf16_f32 v87, v254, v255\n"
"v_mfma_f32_16x16x32_bf16 a[116:119], a[148:151], v[164:167], a[116:119]\n"
"ds_read_b64_tr_b16 a[138:139], v224 offset:24576\n"
"v_lshlrev_b32 v246, 16, v88\n"
"v_mfma_f32_16x16x32_bf16 a[84:87], a[148:151], v[160:163], a[84:87]\n"
"buffer_load_dwordx4 v237, s[68:71], s85 offen offset:0 lds\n"
"s_add_u32 m0, m0, 0x380\n"
"v_mfma_f32_16x16x32_bf16 a[52:55], a[148:151], v[52:55], a[52:55]\n"
"s_waitcnt lgkmcnt(7)\n"
"ds_read_b64_tr_b16 a[140:141], v224 offset:18432\n"
"v_mfma_f32_16x16x32_bf16 a[20:23], a[148:151], v[48:51], a[20:23]\n"
"ds_read_b64_tr_b16 a[142:143], v224 offset:26624\n"
"v_and_b32 v247, 0xffff0000, v88\n"
"v_mfma_f32_16x16x32_bf16 a[24:27], a[152:155], v[48:51], a[24:27]\n"
"ds_read_b64_tr_b16 a[144:145], v225 offset:16384\n"
"v_fma_f32 v246, v246, v240, v242\n"
"v_mfma_f32_16x16x32_bf16 a[56:59], a[152:155], v[52:55], a[56:59]\n"
"ds_read_b64_tr_b16 a[146:147], v225 offset:24576\n"
"v_fma_f32 v247, v247, v240, v242\n"
"v_mfma_f32_16x16x32_bf16 a[88:91], a[152:155], v[160:163], a[88:91]\n"
"buffer_load_dwordx4 v237, s[68:71], s85 offen offset:128 lds\n"
"s_add_u32 m0, m0, 0xb480\n"
"v_mfma_f32_16x16x32_bf16 a[120:123], a[152:155], v[164:167], a[120:123]\n"
"ds_write_b128 v244, v[76:79] offset:3072\n"
"v_mfma_f32_16x16x32_bf16 a[124:127], a[156:159], v[164:167], a[124:127]\n"
"s_mov_b32 s85, 0x150000\n"
"ds_read_b64_tr_b16 a[148:149], v225 offset:18432\n"
"v_mfma_f32_16x16x32_bf16 a[92:95], a[156:159], v[160:163], a[92:95]\n"
"ds_read_b64_tr_b16 a[150:151], v225 offset:26624\n"
"v_cvt_pk_bf16_f32 v88, v246, v247\n"
"v_mfma_f32_16x16x32_bf16 a[60:63], a[156:159], v[52:55], a[60:63]\n"
"ds_read_b64_tr_b16 a[152:153], v226 offset:16384\n"
"v_lshlrev_b32 v248, 16, v89\n"
"v_mfma_f32_16x16x32_bf16 a[28:31], a[156:159], v[48:51], a[28:31]\n"
"s_waitcnt lgkmcnt(10)\n"
"ds_read_b64_tr_b16 a[154:155], v226 offset:24576\n"
"v_mfma_f32_16x16x32_bf16 a[224:227], a[128:131], v[56:59], a[224:227]\n"
"ds_read_b64_tr_b16 a[156:157], v226 offset:18432\n"
"v_and_b32 v249, 0xffff0000, v89\n"
"v_mfma_f32_16x16x32_bf16 a[232:235], a[128:131], v[60:63], a[232:235]\n"
"ds_read_b64_tr_b16 a[158:159], v226 offset:26624\n"
"v_fma_f32 v248, v248, v240, v242\n"
"v_mfma_f32_16x16x32_bf16 a[236:239], a[132:135], v[60:63], a[236:239]\n"
"s_waitcnt lgkmcnt(9)\n"
"v_fma_f32 v249, v249, v240, v242\n"
"v_lshlrev_b32 v250, 16, v90\n"
"v_mfma_f32_16x16x32_bf16 a[228:231], a[132:135], v[56:59], a[228:231]\n"
"s_waitcnt lgkmcnt(15)\n"
"v_cvt_pk_bf16_f32 v89, v248, v249\n"
"v_and_b32 v251, 0xffff0000, v90\n"
"v_mfma_f32_16x16x32_bf16 a[8:11], a[136:139], v[56:59], a[8:11]\n"
"v_fma_f32 v250, v250, v240, v242\n"
"v_fma_f32 v251, v251, v240, v242\n"
"v_mfma_f32_16x16x32_bf16 a[40:43], a[136:139], v[60:63], a[40:43]\n"
"v_cvt_pk_bf16_f32 v90, v250, v251\n"
"v_lshlrev_b32 v254, 16, v91\n"
"v_mfma_f32_16x16x32_bf16 a[72:75], a[136:139], v[168:171], a[72:75]\n"
"v_and_b32 v255, 0xffff0000, v91\n"
"v_fma_f32 v254, v254, v240, v242\n"
"v_mfma_f32_16x16x32_bf16 a[104:107], a[136:139], v[172:175], a[104:107]\n"
"v_fma_f32 v255, v255, v240, v242\n"
"v_lshlrev_b32 v246, 16, v92\n"
"v_mfma_f32_16x16x32_bf16 a[108:111], a[140:143], v[172:175], a[108:111]\n"
"v_cvt_pk_bf16_f32 v91, v254, v255\n"
"v_and_b32 v247, 0xffff0000, v92\n"
"v_mfma_f32_16x16x32_bf16 a[76:79], a[140:143], v[168:171], a[76:79]\n"
"s_waitcnt vmcnt(8) lgkmcnt(6)\n"
"s_barrier\n"
"s_waitcnt lgkmcnt(4)\n"
"v_mfma_f32_16x16x32_bf16 a[44:47], a[140:143], v[60:63], a[44:47]\n"
"ds_read_b64_tr_b16 a[128:129], v227 offset:32768\n"
"v_fma_f32 v246, v246, v241, v243\n"
"v_mfma_f32_16x16x32_bf16 a[12:15], a[140:143], v[56:59], a[12:15]\n"
"ds_read_b64_tr_b16 a[130:131], v227 offset:40960\n"
"v_fma_f32 v247, v247, v241, v243\n"
"v_mfma_f32_16x16x32_bf16 a[16:19], a[144:147], v[56:59], a[16:19]\n"
"ds_read_b64_tr_b16 a[132:133], v227 offset:34816\n"
"v_cvt_pk_bf16_f32 v92, v246, v247\n"
"v_mfma_f32_16x16x32_bf16 a[48:51], a[144:147], v[60:63], a[48:51]\n"
"buffer_load_dwordx4 v237, s[64:67], s85 offen offset:0 lds\n"
"s_add_u32 m0, m0, 0x380\n"
"v_mfma_f32_16x16x32_bf16 a[80:83], a[144:147], v[168:171], a[80:83]\n"
"ds_read_b128 v[176:179], v245 offset:0\n"
"v_lshlrev_b32 v248, 16, v93\n"
"v_mfma_f32_16x16x32_bf16 a[112:115], a[144:147], v[172:175], a[112:115]\n"
"ds_read_b64_tr_b16 a[134:135], v227 offset:43008\n"
"v_and_b32 v249, 0xffff0000, v93\n"
"v_mfma_f32_16x16x32_bf16 a[116:119], a[148:151], v[172:175], a[116:119]\n"
"ds_read_b64_tr_b16 a[136:137], v224 offset:32768\n"
"v_fma_f32 v248, v248, v241, v243\n"
"v_mfma_f32_16x16x32_bf16 a[84:87], a[148:151], v[168:171], a[84:87]\n"
"ds_read_b64_tr_b16 a[138:139], v224 offset:40960\n"
"v_fma_f32 v249, v249, v241, v243\n"
"v_mfma_f32_16x16x32_bf16 a[52:55], a[148:151], v[60:63], a[52:55]\n"
"s_waitcnt lgkmcnt(7)\n"
"ds_read_b128 v[180:183], v245 offset:1024\n"
"v_mfma_f32_16x16x32_bf16 a[20:23], a[148:151], v[56:59], a[20:23]\n"
"buffer_load_dwordx4 v237, s[64:67], s85 offen offset:128 lds\n"
"s_add_u32 m0, m0, 0x480\n"
"v_mfma_f32_16x16x32_bf16 a[24:27], a[152:155], v[56:59], a[24:27]\n"
"ds_read_b64_tr_b16 a[140:141], v224 offset:34816\n"
"v_cvt_pk_bf16_f32 v93, v248, v249\n"
"v_mfma_f32_16x16x32_bf16 a[56:59], a[152:155], v[60:63], a[56:59]\n"
"ds_write_b128 v244, v[80:83] offset:16384\n"
"v_mfma_f32_16x16x32_bf16 a[88:91], a[152:155], v[168:171], a[88:91]\n"
"ds_read_b64_tr_b16 a[142:143], v224 offset:43008\n"
"v_lshlrev_b32 v250, 16, v94\n"
"v_mfma_f32_16x16x32_bf16 a[120:123], a[152:155], v[172:175], a[120:123]\n"
"ds_read_b64_tr_b16 a[144:145], v225 offset:32768\n"
"v_and_b32 v251, 0xffff0000, v94\n"
"v_mfma_f32_16x16x32_bf16 a[124:127], a[156:159], v[172:175], a[124:127]\n"
"buffer_load_dwordx4 v237, s[68:71], s85 offen offset:0 lds\n"
"s_add_u32 m0, m0, 0x380\n"
"v_mfma_f32_16x16x32_bf16 a[92:95], a[156:159], v[168:171], a[92:95]\n"
"ds_read_b64_tr_b16 a[146:147], v225 offset:40960\n"
"v_fma_f32 v250, v250, v241, v243\n"
"v_mfma_f32_16x16x32_bf16 a[60:63], a[156:159], v[60:63], a[60:63]\n"
"ds_read_b64_tr_b16 a[148:149], v225 offset:34816\n"
"v_fma_f32 v251, v251, v241, v243\n"
"v_mfma_f32_16x16x32_bf16 a[28:31], a[156:159], v[56:59], a[28:31]\n"
"s_waitcnt lgkmcnt(9)\n"
"ds_read_b64_tr_b16 a[150:151], v225 offset:43008\n"
"v_mfma_f32_16x16x32_bf16 a[224:227], a[128:131], v[64:67], a[224:227]\n"
"ds_read_b64_tr_b16 a[152:153], v226 offset:32768\n"
"v_cvt_pk_bf16_f32 v94, v250, v251\n"
"v_mfma_f32_16x16x32_bf16 a[232:235], a[128:131], v[68:71], a[232:235]\n"
"ds_read_b128 v[184:187], v245 offset:2048\n"
"v_lshlrev_b32 v254, 16, v95\n"
"v_mfma_f32_16x16x32_bf16 a[236:239], a[132:135], v[68:71], a[236:239]\n"
"buffer_load_dwordx4 v237, s[68:71], s85 offen offset:128 lds\n"
"s_add_u32 m0, m0, 0x3480\n"
"v_mfma_f32_16x16x32_bf16 a[228:231], a[132:135], v[64:67], a[228:231]\n"
"s_waitcnt lgkmcnt(6)\n"
"ds_write_b128 v244, v[84:87] offset:17408\n"
"v_mfma_f32_16x16x32_bf16 a[8:11], a[136:139], v[64:67], a[8:11]\n"
"s_mov_b32 s85, 0x16c000\n"
"ds_read_b64_tr_b16 a[154:155], v226 offset:40960\n"
"v_mfma_f32_16x16x32_bf16 a[40:43], a[136:139], v[68:71], a[40:43]\n"
"s_waitcnt lgkmcnt(11)\n"
"ds_read_b128 v[188:191], v245 offset:3072\n"
"v_mfma_f32_16x16x32_bf16 a[72:75], a[136:139], v[176:179], a[72:75]\n"
"ds_read_b64_tr_b16 a[156:157], v226 offset:34816\n"
"v_and_b32 v255, 0xffff0000, v95\n"
"v_mfma_f32_16x16x32_bf16 a[104:107], a[136:139], v[180:183], a[104:107]\n"
"buffer_load_dwordx4 v237, s[64:67], s85 offen offset:0 lds\n"
"s_add_u32 m0, m0, 0x380\n"
"v_mfma_f32_16x16x32_bf16 a[108:111], a[140:143], v[180:183], a[108:111]\n"
"ds_read_b64_tr_b16 a[158:159], v226 offset:43008\n"
"v_fma_f32 v254, v254, v241, v243\n"
"v_mfma_f32_16x16x32_bf16 a[76:79], a[140:143], v[176:179], a[76:79]\n"
"ds_read_b64_tr_b16 a[128:129], v227 offset:49152\n"
"v_fma_f32 v255, v255, v241, v243\n"
"v_mfma_f32_16x16x32_bf16 a[44:47], a[140:143], v[68:71], a[44:47]\n"
"ds_read_b64_tr_b16 a[130:131], v227 offset:57344\n"
"v_cvt_pk_bf16_f32 v95, v254, v255\n"
"v_mfma_f32_16x16x32_bf16 a[12:15], a[140:143], v[64:67], a[12:15]\n"
"s_waitcnt lgkmcnt(9)\n"
"ds_read_b64_tr_b16 a[132:133], v227 offset:51200\n"
"v_mfma_f32_16x16x32_bf16 a[16:19], a[144:147], v[64:67], a[16:19]\n"
"buffer_load_dwordx4 v237, s[64:67], s85 offen offset:128 lds\n"
"s_add_u32 m0, m0, 0x480\n"
"v_mfma_f32_16x16x32_bf16 a[48:51], a[144:147], v[68:71], a[48:51]\n"
"ds_write_b128 v244, v[88:91] offset:18432\n"
"v_mfma_f32_16x16x32_bf16 a[80:83], a[144:147], v[176:179], a[80:83]\n"
"ds_read_b64_tr_b16 a[134:135], v227 offset:59392\n"
"v_lshlrev_b32 v246, 16, v96\n"
"v_mfma_f32_16x16x32_bf16 a[112:115], a[144:147], v[180:183], a[112:115]\n"
"ds_read_b64_tr_b16 a[136:137], v224 offset:49152\n"
"v_and_b32 v247, 0xffff0000, v96\n"
"v_mfma_f32_16x16x32_bf16 a[116:119], a[148:151], v[180:183], a[116:119]\n"
"ds_read_b64_tr_b16 a[138:139], v224 offset:57344\n"
"v_fma_f32 v246, v246, v240, v242\n"
"v_mfma_f32_16x16x32_bf16 a[84:87], a[148:151], v[176:179], a[84:87]\n"
"buffer_load_dwordx4 v237, s[68:71], s85 offen offset:0 lds\n"
"s_add_u32 m0, m0, 0x380\n"
"v_mfma_f32_16x16x32_bf16 a[52:55], a[148:151], v[68:71], a[52:55]\n"
"ds_read_b64_tr_b16 a[140:141], v224 offset:51200\n"
"v_fma_f32 v247, v247, v240, v242\n"
"v_mfma_f32_16x16x32_bf16 a[20:23], a[148:151], v[64:67], a[20:23]\n"
"s_waitcnt lgkmcnt(8)\n"
"ds_read_b64_tr_b16 a[142:143], v224 offset:59392\n"
"v_mfma_f32_16x16x32_bf16 a[24:27], a[152:155], v[64:67], a[24:27]\n"
"ds_read_b64_tr_b16 a[144:145], v225 offset:49152\n"
"v_cvt_pk_bf16_f32 v96, v246, v247\n"
"v_mfma_f32_16x16x32_bf16 a[56:59], a[152:155], v[68:71], a[56:59]\n"
"ds_read_b64_tr_b16 a[146:147], v225 offset:57344\n"
"v_lshlrev_b32 v248, 16, v97\n"
"v_mfma_f32_16x16x32_bf16 a[88:91], a[152:155], v[176:179], a[88:91]\n"
"buffer_load_dwordx4 v237, s[68:71], s85 offen offset:128 lds\n"
"s_add_u32 m0, m0, 0xfffdb480\n"
"v_mfma_f32_16x16x32_bf16 a[120:123], a[152:155], v[180:183], a[120:123]\n"
"ds_write_b128 v244, v[92:95] offset:19456\n"
"v_and_b32 v249, 0xffff0000, v97\n"
"v_mfma_f32_16x16x32_bf16 a[124:127], a[156:159], v[180:183], a[124:127]\n"
"s_mov_b32 s85, 0x0\n"
"ds_read_b64_tr_b16 a[148:149], v225 offset:51200\n"
"v_fma_f32 v248, v248, v240, v242\n"
"v_mfma_f32_16x16x32_bf16 a[92:95], a[156:159], v[176:179], a[92:95]\n"
"ds_read_b64_tr_b16 a[150:151], v225 offset:59392\n"
"v_fma_f32 v249, v249, v240, v242\n"
"v_mfma_f32_16x16x32_bf16 a[60:63], a[156:159], v[68:71], a[60:63]\n"
"ds_read_b64_tr_b16 a[152:153], v226 offset:49152\n"
"v_cvt_pk_bf16_f32 v97, v248, v249\n"
"v_lshlrev_b32 v250, 16, v98\n"
"v_mfma_f32_16x16x32_bf16 a[28:31], a[156:159], v[64:67], a[28:31]\n"
"s_waitcnt lgkmcnt(10)\n"
"ds_read_b64_tr_b16 a[154:155], v226 offset:57344\n"
"v_and_b32 v251, 0xffff0000, v98\n"
"v_mfma_f32_16x16x32_bf16 a[224:227], a[128:131], v[72:75], a[224:227]\n"
"ds_read_b64_tr_b16 a[156:157], v226 offset:51200\n"
"v_fma_f32 v250, v250, v240, v242\n"
"v_mfma_f32_16x16x32_bf16 a[232:235], a[128:131], v[76:79], a[232:235]\n"
"ds_read_b64_tr_b16 a[158:159], v226 offset:59392\n"
"v_fma_f32 v251, v251, v240, v242\n"
"v_mfma_f32_16x16x32_bf16 a[236:239], a[132:135], v[76:79], a[236:239]\n"
"s_waitcnt lgkmcnt(9)\n"
"v_cvt_pk_bf16_f32 v98, v250, v251\n"
"v_lshlrev_b32 v254, 16, v99\n"
"v_mfma_f32_16x16x32_bf16 a[228:231], a[132:135], v[72:75], a[228:231]\n"
"s_waitcnt lgkmcnt(15)\n"
"v_and_b32 v255, 0xffff0000, v99\n"
"v_fma_f32 v254, v254, v240, v242\n"
"v_mfma_f32_16x16x32_bf16 a[8:11], a[136:139], v[72:75], a[8:11]\n"
"v_fma_f32 v255, v255, v240, v242\n"
"v_lshlrev_b32 v246, 16, v100\n"
"v_mfma_f32_16x16x32_bf16 a[40:43], a[136:139], v[76:79], a[40:43]\n"
"v_cvt_pk_bf16_f32 v99, v254, v255\n"
"v_and_b32 v247, 0xffff0000, v100\n"
"v_mfma_f32_16x16x32_bf16 a[72:75], a[136:139], v[184:187], a[72:75]\n"
"v_fma_f32 v246, v246, v241, v243\n"
"v_fma_f32 v247, v247, v241, v243\n"
"v_mfma_f32_16x16x32_bf16 a[104:107], a[136:139], v[188:191], a[104:107]\n"
"v_cvt_pk_bf16_f32 v100, v246, v247\n"
"v_lshlrev_b32 v248, 16, v101\n"
"v_mfma_f32_16x16x32_bf16 a[108:111], a[140:143], v[188:191], a[108:111]\n"
"v_and_b32 v249, 0xffff0000, v101\n"
"v_fma_f32 v248, v248, v241, v243\n"
"v_mfma_f32_16x16x32_bf16 a[76:79], a[140:143], v[184:187], a[76:79]\n"
"s_waitcnt vmcnt(8) lgkmcnt(6)\n"
"s_barrier\n"
"v_fma_f32 v249, v249, v241, v243\n"
"v_mfma_f32_16x16x32_bf16 a[44:47], a[140:143], v[76:79], a[44:47]\n"
"ds_read_b128 v[192:195], v245 offset:16384\n"
"v_cvt_pk_bf16_f32 v101, v248, v249\n"
"v_mfma_f32_16x16x32_bf16 a[12:15], a[140:143], v[72:75], a[12:15]\n"
"s_waitcnt lgkmcnt(5)\n"
"ds_read_b128 v[196:199], v245 offset:17408\n"
"v_mfma_f32_16x16x32_bf16 a[16:19], a[144:147], v[72:75], a[16:19]\n"
"v_lshlrev_b32 v250, 16, v102\n"
"v_and_b32 v251, 0xffff0000, v102\n"
"v_mfma_f32_16x16x32_bf16 a[48:51], a[144:147], v[76:79], a[48:51]\n"
"buffer_load_dwordx4 v236, s[60:63], s85 offen offset:0 lds\n"
"s_add_u32 m0, m0, 0x4000\n"
"v_mfma_f32_16x16x32_bf16 a[80:83], a[144:147], v[184:187], a[80:83]\n"
"s_mov_b32 s85, 0x40000\n"
"v_add_u32 v227, 0x10000, v227\n"
"v_fma_f32 v250, v250, v241, v243\n"
"v_mfma_f32_16x16x32_bf16 a[112:115], a[144:147], v[188:191], a[112:115]\n"
"ds_read_b64_tr_b16 a[128:129], v227 offset:0\n"
"v_fma_f32 v251, v251, v241, v243\n"
"v_mfma_f32_16x16x32_bf16 a[116:119], a[148:151], v[188:191], a[116:119]\n"
"ds_read_b64_tr_b16 a[130:131], v227 offset:8192\n"
"v_cvt_pk_bf16_f32 v102, v250, v251\n"
"v_mfma_f32_16x16x32_bf16 a[84:87], a[148:151], v[184:187], a[84:87]\n"
"ds_read_b64_tr_b16 a[132:133], v227 offset:2048\n"
"v_lshlrev_b32 v254, 16, v103\n"
"v_mfma_f32_16x16x32_bf16 a[52:55], a[148:151], v[76:79], a[52:55]\n"
"ds_read_b64_tr_b16 a[134:135], v227 offset:10240\n"
"v_add_u32 v224, 0x10000, v224\n"
"v_mfma_f32_16x16x32_bf16 a[20:23], a[148:151], v[72:75], a[20:23]\n"
"s_waitcnt lgkmcnt(6)\n"
"ds_read_b64_tr_b16 a[136:137], v224 offset:0\n"
"v_mfma_f32_16x16x32_bf16 a[24:27], a[152:155], v[72:75], a[24:27]\n"
"ds_read_b64_tr_b16 a[138:139], v224 offset:8192\n"
"v_and_b32 v255, 0xffff0000, v103\n"
"v_mfma_f32_16x16x32_bf16 a[56:59], a[152:155], v[76:79], a[56:59]\n"
"ds_read_b64_tr_b16 a[140:141], v224 offset:2048\n"
"buffer_load_dwordx4 v236, s[60:63], s85 offen offset:0 lds\n"
"v_mfma_f32_16x16x32_bf16 a[88:91], a[152:155], v[184:187], a[88:91]\n"
"ds_read_b64_tr_b16 a[142:143], v224 offset:10240\n"
"ds_write_b128 v244, v[96:99] offset:0\n"
"v_mfma_f32_16x16x32_bf16 a[120:123], a[152:155], v[188:191], a[120:123]\n"
"s_add_u32 m0, m0, 0xffffc400\n"
"ds_read_b128 v[200:203], v245 offset:18432\n"
"v_mfma_f32_16x16x32_bf16 a[124:127], a[156:159], v[188:191], a[124:127]\n"
"s_mov_b32 s85, 0x80000\n"
"ds_read_b128 v[204:207], v245 offset:19456\n"
"v_fma_f32 v254, v254, v241, v243\n"
"v_mfma_f32_16x16x32_bf16 a[92:95], a[156:159], v[184:187], a[92:95]\n"
"s_waitcnt lgkmcnt(7)\n"
"v_add_u32 v225, 0x10000, v225\n"
"v_add_u32 v226, 0x10000, v226\n"
"v_fma_f32 v255, v255, v241, v243\n"
"v_mfma_f32_16x16x32_bf16 a[60:63], a[156:159], v[76:79], a[60:63]\n"
"buffer_load_dwordx4 v236, s[60:63], s85 offen offset:0 lds\n"
"s_add_u32 m0, m0, 0x4000\n"
"v_cvt_pk_bf16_f32 v103, v254, v255\n"
"v_mfma_f32_16x16x32_bf16 a[28:31], a[156:159], v[72:75], a[28:31]\n"
"s_mov_b32 s85, 0xc0000\n"
"ds_read_b64_tr_b16 a[144:145], v225 offset:0\n"
"v_mfma_f32_16x16x32_bf16 a[224:227], a[128:131], v[80:83], a[224:227]\n"
"ds_read_b64_tr_b16 a[146:147], v225 offset:8192\n"
"v_lshlrev_b32 v246, 16, v104\n"
"v_mfma_f32_16x16x32_bf16 a[232:235], a[128:131], v[84:87], a[232:235]\n"
"ds_read_b64_tr_b16 a[148:149], v225 offset:2048\n"
"v_and_b32 v247, 0xffff0000, v104\n"
"v_mfma_f32_16x16x32_bf16 a[236:239], a[132:135], v[84:87], a[236:239]\n"
"ds_read_b64_tr_b16 a[150:151], v225 offset:10240\n"
"v_fma_f32 v246, v246, v240, v242\n"
"v_mfma_f32_16x16x32_bf16 a[228:231], a[132:135], v[80:83], a[228:231]\n"
"s_waitcnt lgkmcnt(7)\n"
"ds_write_b128 v244, v[100:103] offset:1024\n"
"v_mfma_f32_16x16x32_bf16 a[8:11], a[136:139], v[80:83], a[8:11]\n"
"buffer_load_dwordx4 v236, s[60:63], s85 offen offset:0 lds\n"
"s_add_u32 m0, m0, 0xffffc400\n"
"v_mfma_f32_16x16x32_bf16 a[40:43], a[136:139], v[84:87], a[40:43]\n"
"s_waitcnt lgkmcnt(15)\n"
"ds_read_b64_tr_b16 a[152:153], v226 offset:0\n"
"v_fma_f32 v247, v247, v240, v242\n"
"v_mfma_f32_16x16x32_bf16 a[72:75], a[136:139], v[192:195], a[72:75]\n"
"s_mov_b32 s85, 0x100000\n"
"ds_read_b64_tr_b16 a[154:155], v226 offset:8192\n"
"v_cvt_pk_bf16_f32 v104, v246, v247\n"
"v_mfma_f32_16x16x32_bf16 a[104:107], a[136:139], v[196:199], a[104:107]\n"
"ds_read_b64_tr_b16 a[156:157], v226 offset:2048\n"
"v_lshlrev_b32 v248, 16, v105\n"
"v_and_b32 v249, 0xffff0000, v105\n"
"v_mfma_f32_16x16x32_bf16 a[108:111], a[140:143], v[196:199], a[108:111]\n"
"ds_read_b64_tr_b16 a[158:159], v226 offset:10240\n"
"v_fma_f32 v248, v248, v240, v242\n"
"v_mfma_f32_16x16x32_bf16 a[76:79], a[140:143], v[192:195], a[76:79]\n"
"buffer_load_dwordx4 v236, s[60:63], s85 offen offset:0 lds\n"
"s_add_u32 m0, m0, 0x4000\n"
"v_fma_f32 v249, v249, v240, v242\n"
"v_mfma_f32_16x16x32_bf16 a[44:47], a[140:143], v[84:87], a[44:47]\n"
"s_mov_b32 s85, 0x140000\n"
"ds_read_b64_tr_b16 a[128:129], v227 offset:16384\n"
"v_cvt_pk_bf16_f32 v105, v248, v249\n"
"v_mfma_f32_16x16x32_bf16 a[12:15], a[140:143], v[80:83], a[12:15]\n"
"s_waitcnt lgkmcnt(6)\n"
"ds_read_b64_tr_b16 a[130:131], v227 offset:24576\n"
"v_lshlrev_b32 v250, 16, v106\n"
"v_mfma_f32_16x16x32_bf16 a[16:19], a[144:147], v[80:83], a[16:19]\n"
"ds_read_b64_tr_b16 a[132:133], v227 offset:18432\n"
"v_and_b32 v251, 0xffff0000, v106\n"
"v_fma_f32 v250, v250, v240, v242\n"
"v_mfma_f32_16x16x32_bf16 a[48:51], a[144:147], v[84:87], a[48:51]\n"
"ds_read_b64_tr_b16 a[134:135], v227 offset:26624\n"
"v_fma_f32 v251, v251, v240, v242\n"
"v_mfma_f32_16x16x32_bf16 a[80:83], a[144:147], v[192:195], a[80:83]\n"
"buffer_load_dwordx4 v236, s[60:63], s85 offen offset:0 lds\n"
"s_add_u32 m0, m0, 0xffffc400\n"
"v_cvt_pk_bf16_f32 v106, v250, v251\n"
"v_mfma_f32_16x16x32_bf16 a[112:115], a[144:147], v[196:199], a[112:115]\n"
"s_mov_b32 s85, 0x180000\n"
"ds_read_b64_tr_b16 a[136:137], v224 offset:16384\n"
"v_lshlrev_b32 v254, 16, v107\n"
"v_mfma_f32_16x16x32_bf16 a[116:119], a[148:151], v[196:199], a[116:119]\n"
"ds_read_b64_tr_b16 a[138:139], v224 offset:24576\n"
"v_and_b32 v255, 0xffff0000, v107\n"
"v_fma_f32 v254, v254, v240, v242\n"
"v_mfma_f32_16x16x32_bf16 a[84:87], a[148:151], v[192:195], a[84:87]\n"
"ds_read_b64_tr_b16 a[140:141], v224 offset:18432\n"
"v_fma_f32 v255, v255, v240, v242\n"
"v_mfma_f32_16x16x32_bf16 a[52:55], a[148:151], v[84:87], a[52:55]\n"
"ds_read_b64_tr_b16 a[142:143], v224 offset:26624\n"
"v_cvt_pk_bf16_f32 v107, v254, v255\n"
"v_mfma_f32_16x16x32_bf16 a[20:23], a[148:151], v[80:83], a[20:23]\n"
"s_waitcnt lgkmcnt(8)\n"
"buffer_load_dwordx4 v236, s[60:63], s85 offen offset:0 lds\n"
"v_mfma_f32_16x16x32_bf16 a[24:27], a[152:155], v[80:83], a[24:27]\n"
"s_add_u32 m0, m0, 0x7400\n"
"ds_read_b64_tr_b16 a[144:145], v225 offset:16384\n"
"v_mfma_f32_16x16x32_bf16 a[56:59], a[152:155], v[84:87], a[56:59]\n"
"s_mov_b32 s85, 0x1c0000\n"
"ds_read_b64_tr_b16 a[146:147], v225 offset:24576\n"
"v_mfma_f32_16x16x32_bf16 a[88:91], a[152:155], v[192:195], a[88:91]\n"
"ds_write_b128 v244, v[104:107] offset:2048\n"
"v_mfma_f32_16x16x32_bf16 a[120:123], a[152:155], v[196:199], a[120:123]\n"
"ds_read_b64_tr_b16 a[148:149], v225 offset:18432\n"
"v_lshlrev_b32 v246, 16, v108\n"
"v_mfma_f32_16x16x32_bf16 a[124:127], a[156:159], v[196:199], a[124:127]\n"
"ds_read_b64_tr_b16 a[150:151], v225 offset:26624\n"
"v_and_b32 v247, 0xffff0000, v108\n"
"v_mfma_f32_16x16x32_bf16 a[92:95], a[156:159], v[192:195], a[92:95]\n"
"ds_read_b64_tr_b16 a[152:153], v226 offset:16384\n"
"v_fma_f32 v246, v246, v241, v243\n"
"v_mfma_f32_16x16x32_bf16 a[60:63], a[156:159], v[84:87], a[60:63]\n"
"ds_read_b64_tr_b16 a[154:155], v226 offset:24576\n"
"v_fma_f32 v247, v247, v241, v243\n"
"v_mfma_f32_16x16x32_bf16 a[28:31], a[156:159], v[80:83], a[28:31]\n"
"s_waitcnt lgkmcnt(11)\n"
"ds_read_b64_tr_b16 a[156:157], v226 offset:18432\n"
"v_cvt_pk_bf16_f32 v108, v246, v247\n"
"v_mfma_f32_16x16x32_bf16 a[224:227], a[128:131], v[88:91], a[224:227]\n"
"ds_read_b64_tr_b16 a[158:159], v226 offset:26624\n"
"v_lshlrev_b32 v248, 16, v109\n"
"v_and_b32 v249, 0xffff0000, v109\n"
"v_mfma_f32_16x16x32_bf16 a[232:235], a[128:131], v[92:95], a[232:235]\n"
"s_waitcnt lgkmcnt(9)\n"
"v_fma_f32 v248, v248, v241, v243\n"
"v_fma_f32 v249, v249, v241, v243\n"
"v_mfma_f32_16x16x32_bf16 a[236:239], a[132:135], v[92:95], a[236:239]\n"
"v_lshlrev_b32 v250, 16, v110\n"
"v_and_b32 v251, 0xffff0000, v110\n"
"v_lshlrev_b32 v254, 16, v111\n"
"v_and_b32 v255, 0xffff0000, v111\n"
"v_mfma_f32_16x16x32_bf16 a[228:231], a[132:135], v[88:91], a[228:231]\n"
"v_fma_f32 v250, v250, v241, v243\n"
"v_fma_f32 v251, v251, v241, v243\n"
"v_fma_f32 v254, v254, v241, v243\n"
"v_fma_f32 v255, v255, v241, v243\n"
"v_cvt_pk_bf16_f32 v109, v248, v249\n"
"v_mfma_f32_16x16x32_bf16 a[8:11], a[136:139], v[88:91], a[8:11]\n"
"s_waitcnt lgkmcnt(15)\n"
"v_cvt_pk_bf16_f32 v110, v250, v251\n"
"v_cvt_pk_bf16_f32 v111, v254, v255\n"
"v_mfma_f32_16x16x32_bf16 a[40:43], a[136:139], v[92:95], a[40:43]\n"
"ds_write_b128 v244, v[108:111] offset:3072\n"
"v_mfma_f32_16x16x32_bf16 a[72:75], a[136:139], v[200:203], a[72:75]\n"
"v_add_u32 v224, 0x10000, v224\n"
"v_add_u32 v225, 0x10000, v225\n"
"v_mfma_f32_16x16x32_bf16 a[104:107], a[136:139], v[204:207], a[104:107]\n"
"v_add_u32 v226, 0x10000, v226\n"
"v_add_u32 v227, 0x10000, v227\n"
"v_mfma_f32_16x16x32_bf16 a[108:111], a[140:143], v[204:207], a[108:111]\n"
"v_mfma_f32_16x16x32_bf16 a[76:79], a[140:143], v[200:203], a[76:79]\n"
"s_waitcnt vmcnt(7) lgkmcnt(0)\n"
"s_barrier\n"
"s_waitcnt lgkmcnt(5)\n"
"v_mfma_f32_16x16x32_bf16 a[44:47], a[140:143], v[92:95], a[44:47]\n"
"s_mov_b32 s93, 0x3c053526\n"
"ds_read_b64_tr_b16 a[128:129], v227 offset:0\n"
"v_mfma_f32_16x16x32_bf16 a[12:15], a[140:143], v[88:91], a[12:15]\n"
"s_lshr_b32 s89, s88, 5\n"
"ds_read_b64_tr_b16 a[130:131], v227 offset:8192\n"
"v_mfma_f32_16x16x32_bf16 a[16:19], a[144:147], v[88:91], a[16:19]\n"
"s_and_b32 s90, s88, 1\n"
"ds_read_b64_tr_b16 a[132:133], v227 offset:2048\n"
"v_mfma_f32_16x16x32_bf16 a[48:51], a[144:147], v[92:95], a[48:51]\n"
"buffer_load_dwordx4 v236, s[60:63], s85 offen offset:0 lds\n"
"s_add_u32 m0, m0, 0x4000\n"
"v_mfma_f32_16x16x32_bf16 a[80:83], a[144:147], v[200:203], a[80:83]\n"
"s_mov_b32 s85, 0x200000\n"
"ds_read_b128 v[208:211], v245 offset:0\n"
"v_mfma_f32_16x16x32_bf16 a[112:115], a[144:147], v[204:207], a[112:115]\n"
"s_lshl_b32 s90, s90, 3\n"
"ds_read_b64_tr_b16 a[134:135], v227 offset:10240\n"
"v_mfma_f32_16x16x32_bf16 a[116:119], a[148:151], v[204:207], a[116:119]\n"
"s_add_u32 s89, s89, s90\n"
"ds_read_b64_tr_b16 a[136:137], v224 offset:0\n"
"v_mfma_f32_16x16x32_bf16 a[84:87], a[148:151], v[200:203], a[84:87]\n"
"s_mul_i32 s90, s89, 458752\n"
"ds_read_b64_tr_b16 a[138:139], v224 offset:8192\n"
"v_mfma_f32_16x16x32_bf16 a[52:55], a[148:151], v[92:95], a[52:55]\n"
"s_lshl_b32 s58, s89, 12\n"
"ds_read_b128 v[212:215], v245 offset:1024\n"
"v_mfma_f32_16x16x32_bf16 a[20:23], a[148:151], v[88:91], a[20:23]\n"
"s_waitcnt lgkmcnt(9)\n"
"buffer_load_dwordx4 v236, s[60:63], s85 offen offset:0 lds\n"
"v_mfma_f32_16x16x32_bf16 a[24:27], a[152:155], v[88:91], a[24:27]\n"
"s_add_u32 m0, m0, 0xffffc400\n"
"ds_read_b64_tr_b16 a[140:141], v224 offset:2048\n"
"v_mfma_f32_16x16x32_bf16 a[56:59], a[152:155], v[92:95], a[56:59]\n"
"s_mov_b32 s85, 0x240000\n"
"ds_read_b64_tr_b16 a[142:143], v224 offset:10240\n"
"v_mfma_f32_16x16x32_bf16 a[88:91], a[152:155], v[200:203], a[88:91]\n"
"s_add_u32 s58, s58, 0x2000000\n"
"ds_read_b64_tr_b16 a[144:145], v225 offset:0\n"
"v_mfma_f32_16x16x32_bf16 a[120:123], a[152:155], v[204:207], a[120:123]\n"
"s_sub_u32 s90, s58, s90\n"
"ds_read_b64_tr_b16 a[146:147], v225 offset:8192\n"
"v_mfma_f32_16x16x32_bf16 a[124:127], a[156:159], v[204:207], a[124:127]\n"
"buffer_load_dwordx4 v236, s[60:63], s85 offen offset:0 lds\n"
"s_add_u32 m0, m0, 0x4000\n"
"v_mfma_f32_16x16x32_bf16 a[92:95], a[156:159], v[200:203], a[92:95]\n"
"s_mov_b32 s85, 0x280000\n"
"ds_read_b64_tr_b16 a[148:149], v225 offset:2048\n"
"v_mfma_f32_16x16x32_bf16 a[60:63], a[156:159], v[92:95], a[60:63]\n"
"s_sub_u32 s56, s94, s90\n"
"s_subb_u32 s57, s95, 0\n"
"ds_read_b64_tr_b16 a[150:151], v225 offset:10240\n"
"v_mfma_f32_16x16x32_bf16 a[28:31], a[156:159], v[88:91], a[28:31]\n"
"s_waitcnt lgkmcnt(9)\n"
"v_lshrrev_b32 v232, 13, v231\n"
"v_mul_u32_u24 v232, 896, v232\n"
"v_and_b32 v233, 0x1ff, v231\n"
"v_min_u32 v233, 252, v233\n"
"v_add_u32 v232, v232, v233\n"
"buffer_load_dword a255, v232, s[72:75], 0 offen\n"
"v_mfma_f32_16x16x32_bf16 a[224:227], a[128:131], v[96:99], a[224:227]\n"
"s_and_b32 s57, s57, 0xffff\n"
"ds_read_b64_tr_b16 a[152:153], v226 offset:0\n"
"v_mfma_f32_16x16x32_bf16 a[232:235], a[128:131], v[100:103], a[232:235]\n"
"s_mov_b32 s58, 458752\n"
"ds_read_b128 v[216:219], v245 offset:2048\n"
"v_mfma_f32_16x16x32_bf16 a[236:239], a[132:135], v[100:103], a[236:239]\n"
"buffer_load_dwordx4 v236, s[60:63], s85 offen offset:0 lds\n"
"s_add_u32 m0, m0, 0xffffc400\n"
"v_mfma_f32_16x16x32_bf16 a[228:231], a[132:135], v[96:99], a[228:231]\n"
"s_waitcnt lgkmcnt(6)\n"
"ds_read_b64_tr_b16 a[154:155], v226 offset:8192\n"
"v_mfma_f32_16x16x32_bf16 a[8:11], a[136:139], v[96:99], a[8:11]\n"
"s_mov_b32 s85, 0x2c0000\n"
"ds_read_b128 v[220:223], v245 offset:3072\n"
"v_mfma_f32_16x16x32_bf16 a[40:43], a[136:139], v[100:103], a[40:43]\n"
"s_waitcnt lgkmcnt(10)\n"
"ds_read_b64_tr_b16 a[156:157], v226 offset:2048\n"
"v_mfma_f32_16x16x32_bf16 a[72:75], a[136:139], v[208:211], a[72:75]\n"
"s_mov_b32 s59, 0x20000\n"
"ds_read_b64_tr_b16 a[158:159], v226 offset:10240\n"
"v_mfma_f32_16x16x32_bf16 a[104:107], a[136:139], v[212:215], a[104:107]\n"
"buffer_load_dwordx4 v236, s[60:63], s85 offen offset:0 lds\n"
"s_add_u32 m0, m0, 0x4000\n"
"v_mfma_f32_16x16x32_bf16 a[108:111], a[140:143], v[212:215], a[108:111]\n"
"s_mov_b32 s85, 0x300000\n"
"ds_read_b64_tr_b16 a[128:129], v227 offset:16384\n"
"v_mfma_f32_16x16x32_bf16 a[76:79], a[140:143], v[208:211], a[76:79]\n"
"s_mul_i32 s89, s87, 7168\n"
"ds_read_b64_tr_b16 a[130:131], v227 offset:24576\n"
"v_mfma_f32_16x16x32_bf16 a[44:47], a[140:143], v[100:103], a[44:47]\n"
"s_mul_i32 s90, s84, 114688\n"
"ds_read_b64_tr_b16 a[132:133], v227 offset:18432\n"
"v_mfma_f32_16x16x32_bf16 a[12:15], a[140:143], v[96:99], a[12:15]\n"
"s_waitcnt lgkmcnt(9)\n"
"ds_read_b64_tr_b16 a[134:135], v227 offset:26624\n"
"v_mfma_f32_16x16x32_bf16 a[16:19], a[144:147], v[96:99], a[16:19]\n"
"buffer_load_dwordx4 v236, s[60:63], s85 offen offset:0 lds\n"
"s_add_u32 m0, m0, 0xffffc400\n"
"v_mfma_f32_16x16x32_bf16 a[48:51], a[144:147], v[100:103], a[48:51]\n"
"s_mov_b32 s85, 0x340000\n"
"ds_read_b64_tr_b16 a[136:137], v224 offset:16384\n"
"v_mfma_f32_16x16x32_bf16 a[80:83], a[144:147], v[208:211], a[80:83]\n"
"s_add_u32 s70, s89, s90\n"
"ds_read_b64_tr_b16 a[138:139], v224 offset:24576\n"
"v_mfma_f32_16x16x32_bf16 a[112:115], a[144:147], v[212:215], a[112:115]\n"
"s_add_u32 s70, s70, 6144\n"
"ds_read_b64_tr_b16 a[140:141], v224 offset:18432\n"
"v_mfma_f32_16x16x32_bf16 a[116:119], a[148:151], v[212:215], a[116:119]\n"
"s_and_b32 s91, s84, 1\n"
"ds_read_b64_tr_b16 a[142:143], v224 offset:26624\n"
"v_mfma_f32_16x16x32_bf16 a[84:87], a[148:151], v[208:211], a[84:87]\n"
"buffer_load_dwordx4 v236, s[60:63], s85 offen offset:0 lds\n"
"s_add_u32 m0, m0, 0x7400\n"
"v_mfma_f32_16x16x32_bf16 a[52:55], a[148:151], v[100:103], a[52:55]\n"
"s_mul_i32 s86, s48, 2048\n"
"v_lshlrev_b32 v232, 3, v239\n"
"v_min_u32 v232, 1920, v232\n"
"buffer_load_dword a255, v232, s[36:39], s86 offen\n"
"v_mfma_f32_16x16x32_bf16 a[20:23], a[148:151], v[96:99], a[20:23]\n"
"s_waitcnt lgkmcnt(8)\n"
"s_mov_b32 s85, 0x80\n"
"s_mul_i32 s91, s91, 3072\n"
"v_mfma_f32_16x16x32_bf16 a[24:27], a[152:155], v[96:99], a[24:27]\n"
"s_add_u32 s89, s89, s91\n"
"ds_read_b64_tr_b16 a[144:145], v225 offset:16384\n"
"v_mfma_f32_16x16x32_bf16 a[56:59], a[152:155], v[100:103], a[56:59]\n"
"s_add_u32 s68, s89, s90\n"
"ds_read_b64_tr_b16 a[146:147], v225 offset:24576\n"
"v_mfma_f32_16x16x32_bf16 a[88:91], a[152:155], v[208:211], a[88:91]\n"
"s_xor_b32 s90, s84, 1\n"
"ds_read_b64_tr_b16 a[148:149], v225 offset:18432\n"
"v_mfma_f32_16x16x32_bf16 a[120:123], a[152:155], v[212:215], a[120:123]\n"
"s_mul_i32 s90, s90, 114688\n"
"ds_read_b64_tr_b16 a[150:151], v225 offset:26624\n"
"v_mfma_f32_16x16x32_bf16 a[124:127], a[156:159], v[212:215], a[124:127]\n"
"s_add_u32 s69, s89, s90\n"
"ds_read_b64_tr_b16 a[152:153], v226 offset:16384\n"
"v_mfma_f32_16x16x32_bf16 a[92:95], a[156:159], v[208:211], a[92:95]\n"
"s_waitcnt lgkmcnt(9)\n"
"ds_read_b64_tr_b16 a[154:155], v226 offset:24576\n"
"v_mfma_f32_16x16x32_bf16 a[60:63], a[156:159], v[100:103], a[60:63]\n"
"v_mfma_f32_16x16x32_bf16 a[28:31], a[156:159], v[96:99], a[28:31]\n"
"ds_read_b64_tr_b16 a[156:157], v226 offset:18432\n"
"v_mfma_f32_16x16x32_bf16 a[224:227], a[128:131], v[104:107], a[224:227]\n"
"ds_read_b64_tr_b16 a[158:159], v226 offset:26624\n"
"v_mfma_f32_16x16x32_bf16 a[232:235], a[128:131], v[108:111], a[232:235]\n"
"s_waitcnt lgkmcnt(8)\n"
"v_mfma_f32_16x16x32_bf16 a[236:239], a[132:135], v[108:111], a[236:239]\n"
"s_waitcnt lgkmcnt(15)\n"
"v_mfma_f32_16x16x32_bf16 a[228:231], a[132:135], v[104:107], a[228:231]\n"
"v_mfma_f32_16x16x32_bf16 a[8:11], a[136:139], v[104:107], a[8:11]\n"
"v_mfma_f32_16x16x32_bf16 a[40:43], a[136:139], v[108:111], a[40:43]\n"
"v_mfma_f32_16x16x32_bf16 a[72:75], a[136:139], v[216:219], a[72:75]\n"
"v_mfma_f32_16x16x32_bf16 a[104:107], a[136:139], v[220:223], a[104:107]\n"
"v_mfma_f32_16x16x32_bf16 a[108:111], a[140:143], v[220:223], a[108:111]\n"
"s_mul_i32 s86, s48, 2048\n"
"v_lshlrev_b32 v232, 3, v239\n"
"v_min_u32 v232, 1920, v232\n"
"buffer_load_dword a255, v232, s[40:43], s86 offen\n"
"v_mfma_f32_16x16x32_bf16 a[76:79], a[140:143], v[216:219], a[76:79]\n"
"s_waitcnt vmcnt(10)\n"
"s_barrier\n"
"v_and_b32 v228, 0xf0, v239\n"
"v_mfma_f32_16x16x32_bf16 a[44:47], a[140:143], v[108:111], a[44:47]\n"
"ds_read_b64_tr_b16 a[128:129], v234 offset:0\n"
"v_lshrrev_b32 v229, 9, v239\n"
"v_mfma_f32_16x16x32_bf16 a[12:15], a[140:143], v[104:107], a[12:15]\n"
"s_waitcnt lgkmcnt(5)\n"
"ds_read_b64_tr_b16 a[130:131], v234 offset:8192\n"
"v_mfma_f32_16x16x32_bf16 a[16:19], a[144:147], v[104:107], a[16:19]\n"
"ds_read_b64_tr_b16 a[132:133], v235 offset:0\n"
"v_lshl_add_u32 v228, v229, 8, v228\n"
"v_mfma_f32_16x16x32_bf16 a[48:51], a[144:147], v[108:111], a[48:51]\n"
"buffer_load_dwordx4 v236, s[60:63], s85 offen offset:0 lds\n"
"s_add_u32 m0, m0, 0x4000\n"
"v_mfma_f32_16x16x32_bf16 a[80:83], a[144:147], v[216:219], a[80:83]\n"
"s_mov_b32 s85, 0x40080\n"
"ds_read_b64_tr_b16 a[134:135], v235 offset:8192\n"
"v_mfma_f32_16x16x32_bf16 a[112:115], a[144:147], v[220:223], a[112:115]\n"
"ds_read_b64_tr_b16 a[136:137], v234 offset:16384\n"
"v_bfe_u32 v229, v239, 8, 1\n"
"v_mfma_f32_16x16x32_bf16 a[116:119], a[148:151], v[220:223], a[116:119]\n"
"ds_read_b64_tr_b16 a[138:139], v234 offset:24576\n"
"v_lshl_add_u32 v228, v229, 3, v228\n"
"v_mfma_f32_16x16x32_bf16 a[84:87], a[148:151], v[216:219], a[84:87]\n"
"s_and_b32 s90, s84, 1\n"
"ds_read_b64_tr_b16 a[140:141], v235 offset:16384\n"
"v_mfma_f32_16x16x32_bf16 a[52:55], a[148:151], v[108:111], a[52:55]\n"
"buffer_load_dwordx4 v236, s[60:63], s85 offen offset:0 lds\n"
"s_add_u32 m0, m0, 0xffffc400\n"
"v_mfma_f32_16x16x32_bf16 a[20:23], a[148:151], v[104:107], a[20:23]\n"
"s_waitcnt lgkmcnt(7)\n"
"ds_read_b64_tr_b16 a[142:143], v235 offset:24576\n"
"v_mfma_f32_16x16x32_bf16 a[24:27], a[152:155], v[104:107], a[24:27]\n"
"s_mov_b32 s85, 0x80080\n"
"ds_read_b64_tr_b16 a[144:145], v234 offset:1024\n"
"v_mfma_f32_16x16x32_bf16 a[56:59], a[152:155], v[108:111], a[56:59]\n"
"s_lshl_b32 s90, s90, 11\n"
"ds_read_b64_tr_b16 a[146:147], v234 offset:9216\n"
"v_mfma_f32_16x16x32_bf16 a[88:91], a[152:155], v[216:219], a[88:91]\n"
"s_add_u32 s90, s90, 98304\n"
"ds_read_b64_tr_b16 a[148:149], v235 offset:1024\n"
"v_mfma_f32_16x16x32_bf16 a[120:123], a[152:155], v[220:223], a[120:123]\n"
"buffer_load_dwordx4 v236, s[60:63], s85 offen offset:0 lds\n"
"s_add_u32 m0, m0, 0x4000\n"
"v_mfma_f32_16x16x32_bf16 a[124:127], a[156:159], v[220:223], a[124:127]\n"
"s_mov_b32 s85, 0xc0080\n"
"ds_read_b64_tr_b16 a[150:151], v235 offset:9216\n"
"v_mfma_f32_16x16x32_bf16 a[92:95], a[156:159], v[216:219], a[92:95]\n"
"s_lshl_b32 s89, s84, 12\n"
"ds_read_b64_tr_b16 a[152:153], v234 offset:17408\n"
"v_mfma_f32_16x16x32_bf16 a[60:63], a[156:159], v[108:111], a[60:63]\n"
"s_add_u32 s89, s89, s90\n"
"ds_read_b64_tr_b16 a[154:155], v234 offset:25600\n"
"v_mfma_f32_16x16x32_bf16 a[28:31], a[156:159], v[104:107], a[28:31]\n"
"s_waitcnt lgkmcnt(10)\n"
"v_add_u32 v238, s89, v228\n"
"v_mfma_f32_16x16x32_bf16 a[160:163], a[128:131], v[0:3], 0\n"
"buffer_load_dwordx4 v236, s[60:63], s85 offen offset:0 lds\n"
"v_mfma_f32_16x16x32_bf16 a[164:167], a[128:131], v[4:7], 0\n"
"s_add_u32 m0, m0, 0xffffc400\n"
"ds_read_b64_tr_b16 a[156:157], v235 offset:17408\n"
"v_mfma_f32_16x16x32_bf16 a[168:171], a[128:131], v[112:115], 0\n"
"s_mov_b32 s85, 0x100080\n"
"ds_read_b64_tr_b16 a[158:159], v235 offset:25600\n"
"v_mfma_f32_16x16x32_bf16 a[172:175], a[128:131], v[116:119], 0\n"
"v_xor_b32 v237, 0x800, v238\n"
"s_and_b32 s89, s84, 2\n"
"v_mfma_f32_16x16x32_bf16 a[188:191], a[132:135], v[116:119], 0\n"
"s_lshl_b32 s89, s89, 12\n"
"ds_read_b64_tr_b16 a[128:129], v234 offset:2048\n"
"v_mfma_f32_16x16x32_bf16 a[184:187], a[132:135], v[112:115], 0\n"
"buffer_load_dwordx4 v236, s[60:63], s85 offen offset:0 lds\n"
"s_add_u32 m0, m0, 0x4000\n"
"v_mfma_f32_16x16x32_bf16 a[180:183], a[132:135], v[4:7], 0\n"
"s_mov_b32 s85, 0x140080\n"
"ds_read_b64_tr_b16 a[130:131], v234 offset:10240\n"
"v_mfma_f32_16x16x32_bf16 a[176:179], a[132:135], v[0:3], 0\n"
"s_waitcnt lgkmcnt(10)\n"
"s_add_u32 s89, s89, s90\n"
"v_add_u32 v244, s89, v239\n"
"v_mfma_f32_16x16x32_bf16 a[160:163], a[136:139], v[8:11], a[160:163]\n"
"ds_read_b64_tr_b16 a[132:133], v235 offset:2048\n"
"v_xor_b32 v245, 0x800, v244\n"
"v_mfma_f32_16x16x32_bf16 a[164:167], a[136:139], v[12:15], a[164:167]\n"
"ds_read_b64_tr_b16 a[134:135], v235 offset:10240\n"
"v_accvgpr_read_b32 v240, a8\n"
"v_mfma_f32_16x16x32_bf16 a[168:171], a[136:139], v[120:123], a[168:171]\n"
"buffer_load_dwordx4 v236, s[60:63], s85 offen offset:0 lds\n"
"s_add_u32 m0, m0, 0xffffc400\n"
"v_mfma_f32_16x16x32_bf16 a[172:175], a[136:139], v[124:127], a[172:175]\n"
"s_mov_b32 s85, 0x180080\n"
"v_accvgpr_read_b32 v241, a12\n"
"v_accvgpr_read_b32 v242, a9\n"
"v_mfma_f32_16x16x32_bf16 a[188:191], a[140:143], v[124:127], a[188:191]\n"
"ds_read_b64_tr_b16 a[136:137], v234 offset:18432\n"
"v_mul_f32 v241, v240, v241\n"
"v_mfma_f32_16x16x32_bf16 a[184:187], a[140:143], v[120:123], a[184:187]\n"
"ds_read_b64_tr_b16 a[138:139], v234 offset:26624\n"
"v_mul_f32 v240, 0xc1800000, v240\n"
"v_mfma_f32_16x16x32_bf16 a[180:183], a[140:143], v[12:15], a[180:183]\n"
"s_waitcnt lgkmcnt(10)\n"
"v_exp_f32 v240, v240\n"
"v_mfma_f32_16x16x32_bf16 a[176:179], a[140:143], v[8:11], a[176:179]\n"
"buffer_load_dwordx4 v236, s[60:63], s85 offen offset:0 lds\n"
"s_add_u32 m0, m0, 0xf400\n"
"v_mfma_f32_16x16x32_bf16 a[160:163], a[144:147], v[16:19], a[160:163]\n"
"s_mov_b32 s85, 0x1c0080\n"
"ds_read_b64_tr_b16 a[140:141], v235 offset:18432\n"
"v_mfma_f32_16x16x32_bf16 a[164:167], a[144:147], v[20:23], a[164:167]\n"
"ds_read_b64_tr_b16 a[142:143], v235 offset:26624\n"
"v_accvgpr_read_b32 v243, a13\n"
"v_mfma_f32_16x16x32_bf16 a[168:171], a[144:147], v[128:131], a[168:171]\n"
"v_mul_f32 v243, v242, v243\n"
"v_mul_f32 v242, 0xc1800000, v242\n"
"v_mfma_f32_16x16x32_bf16 a[172:175], a[144:147], v[132:135], a[172:175]\n"
"v_accvgpr_read_b32 v246, a10\n"
"v_accvgpr_read_b32 v247, a14\n"
"v_mfma_f32_16x16x32_bf16 a[188:191], a[148:151], v[132:135], a[188:191]\n"
"v_exp_f32 v242, v242\n"
"v_mfma_f32_16x16x32_bf16 a[184:187], a[148:151], v[128:131], a[184:187]\n"
"ds_read_b64_tr_b16 a[144:145], v234 offset:3072\n"
"v_fma_f32 v240, v240, s93, s93\n"
"v_mfma_f32_16x16x32_bf16 a[180:183], a[148:151], v[20:23], a[180:183]\n"
"ds_read_b64_tr_b16 a[146:147], v234 offset:11264\n"
"v_mul_f32 v247, v246, v247\n"
"v_mfma_f32_16x16x32_bf16 a[176:179], a[148:151], v[16:19], a[176:179]\n"
"s_waitcnt lgkmcnt(10)\n"
"v_mul_f32 v246, 0xc1800000, v246\n"
"v_accvgpr_read_b32 v248, a11\n"
"v_mfma_f32_16x16x32_bf16 a[160:163], a[152:155], v[24:27], a[160:163]\n"
"v_rcp_f32 v240, v240\n"
"v_mfma_f32_16x16x32_bf16 a[164:167], a[152:155], v[28:31], a[164:167]\n"
"ds_read_b64_tr_b16 a[148:149], v235 offset:3072\n"
"v_fma_f32 v242, v242, s93, s93\n"
"v_mfma_f32_16x16x32_bf16 a[168:171], a[152:155], v[136:139], a[168:171]\n"
"v_exp_f32 v246, v246\n"
"v_mfma_f32_16x16x32_bf16 a[172:175], a[152:155], v[140:143], a[172:175]\n"
"ds_read_b64_tr_b16 a[150:151], v235 offset:11264\n"
"v_accvgpr_read_b32 v249, a15\n"
"v_mfma_f32_16x16x32_bf16 a[188:191], a[156:159], v[140:143], a[188:191]\n"
"v_mul_f32 v249, v248, v249\n"
"v_mul_f32 v241, v241, v240\n"
"v_mfma_f32_16x16x32_bf16 a[184:187], a[156:159], v[136:139], a[184:187]\n"
"s_waitcnt lgkmcnt(8)\n"
"v_rcp_f32 v242, v242\n"
"v_mfma_f32_16x16x32_bf16 a[180:183], a[156:159], v[28:31], a[180:183]\n"
"v_mul_f32 v248, 0xc1800000, v248\n"
"v_accvgpr_read_b32 v250, a40\n"
"v_mfma_f32_16x16x32_bf16 a[176:179], a[156:159], v[24:27], a[176:179]\n"
"v_fma_f32 v246, v246, s93, s93\n"
"v_accvgpr_read_b32 v251, a44\n"
"v_mfma_f32_16x16x32_bf16 a[160:163], a[128:131], v[32:35], a[160:163]\n"
"v_exp_f32 v248, v248\n"
"v_mfma_f32_16x16x32_bf16 a[164:167], a[128:131], v[36:39], a[164:167]\n"
"v_mul_f32 v243, v243, v242\n"
"v_mul_f32 v251, v250, v251\n"
"v_mfma_f32_16x16x32_bf16 a[168:171], a[128:131], v[144:147], a[168:171]\n"
"v_accvgpr_read_b32 v254, a41\n"
"v_cvt_pk_bf16_f32 v230, v241, v243\n"
"v_mfma_f32_16x16x32_bf16 a[172:175], a[128:131], v[148:151], a[172:175]\n"
"v_rcp_f32 v246, v246\n"
"v_mfma_f32_16x16x32_bf16 a[188:191], a[132:135], v[148:151], a[188:191]\n"
"v_mul_f32 v250, 0xc1800000, v250\n"
"v_accvgpr_read_b32 v255, a45\n"
"v_mfma_f32_16x16x32_bf16 a[184:187], a[132:135], v[144:147], a[184:187]\n"
"s_waitcnt vmcnt(9)\n"
"s_barrier\n"
"v_fma_f32 v248, v248, s93, s93\n"
"v_mul_f32 v255, v254, v255\n"
"v_mfma_f32_16x16x32_bf16 a[180:183], a[132:135], v[36:39], a[180:183]\n"
"s_waitcnt lgkmcnt(4)\n"
"v_exp_f32 v250, v250\n"
"v_mul_f32 v247, v247, v246\n"
"v_mfma_f32_16x16x32_bf16 a[176:179], a[132:135], v[32:35], a[176:179]\n"
"ds_read_b64_tr_b16 a[152:153], v234 offset:32768\n"
"v_mul_f32 v254, 0xc1800000, v254\n"
"v_accvgpr_read_b32 v240, a42\n"
"v_mfma_f32_16x16x32_bf16 a[160:163], a[136:139], v[40:43], a[160:163]\n"
"v_rcp_f32 v248, v248\n"
"v_accvgpr_read_b32 v241, a46\n"
"v_mfma_f32_16x16x32_bf16 a[164:167], a[136:139], v[44:47], a[164:167]\n"
"buffer_load_dwordx4 v236, s[60:63], s85 offen offset:0 lds\n"
"s_add_u32 m0, m0, 0x4000\n"
"v_fma_f32 v250, v250, s93, s93\n"
"v_mfma_f32_16x16x32_bf16 a[168:171], a[136:139], v[152:155], a[168:171]\n"
"s_mov_b32 s85, 0x200080\n"
"v_exp_f32 v254, v254\n"
"v_mul_f32 v241, v240, v241\n"
"v_mfma_f32_16x16x32_bf16 a[172:175], a[136:139], v[156:159], a[172:175]\n"
"ds_read_b64_tr_b16 a[154:155], v234 offset:40960\n"
"v_mul_f32 v249, v249, v248\n"
"v_mul_f32 v240, 0xc1800000, v240\n"
"v_mfma_f32_16x16x32_bf16 a[188:191], a[140:143], v[156:159], a[188:191]\n"
"ds_read_b64_tr_b16 a[156:157], v235 offset:32768\n"
"v_accvgpr_read_b32 v242, a43\n"
"v_cvt_pk_bf16_f32 v231, v247, v249\n"
"v_mfma_f32_16x16x32_bf16 a[184:187], a[140:143], v[152:155], a[184:187]\n"
"s_waitcnt lgkmcnt(3)\n"
"v_rcp_f32 v250, v250\n"
"v_fma_f32 v254, v254, s93, s93\n"
"v_mfma_f32_16x16x32_bf16 a[180:183], a[140:143], v[44:47], a[180:183]\n"
"ds_read_b64_tr_b16 a[158:159], v235 offset:40960\n"
"v_accvgpr_read_b32 v243, a47\n"
"v_exp_f32 v240, v240\n"
"v_mfma_f32_16x16x32_bf16 a[176:179], a[140:143], v[40:43], a[176:179]\n"
"ds_read_b64_tr_b16 a[128:129], v234 offset:49152\n"
"v_mul_f32 v243, v242, v243\n"
"v_mul_f32 v251, v251, v250\n"
"v_mfma_f32_16x16x32_bf16 a[160:163], a[144:147], v[48:51], a[160:163]\n"
"v_rcp_f32 v254, v254\n"
"v_mul_f32 v242, 0xc1800000, v242\n"
"v_mfma_f32_16x16x32_bf16 a[164:167], a[144:147], v[52:55], a[164:167]\n"
"buffer_load_dwordx4 v236, s[60:63], s85 offen offset:0 lds\n"
"s_add_u32 m0, m0, 0xffffc400\n"
"v_accvgpr_read_b32 v246, a72\n"
"v_mfma_f32_16x16x32_bf16 a[168:171], a[144:147], v[160:163], a[168:171]\n"
"s_mov_b32 s85, 0x240080\n"
"ds_read_b64_tr_b16 a[130:131], v234 offset:57344\n"
"v_fma_f32 v240, v240, s93, s93\n"
"v_mfma_f32_16x16x32_bf16 a[172:175], a[144:147], v[164:167], a[172:175]\n"
"ds_read_b64_tr_b16 a[132:133], v235 offset:49152\n"
"v_accvgpr_read_b32 v247, a76\n"
"v_mul_f32 v255, v255, v254\n"
"v_mfma_f32_16x16x32_bf16 a[188:191], a[148:151], v[164:167], a[188:191]\n"
"v_exp_f32 v242, v242\n"
"v_mul_f32 v247, v246, v247\n"
"v_mfma_f32_16x16x32_bf16 a[184:187], a[148:151], v[160:163], a[184:187]\n"
"ds_read_b64_tr_b16 a[134:135], v235 offset:57344\n"
"v_accvgpr_read_b32 v248, a73\n"
"v_cvt_pk_bf16_f32 v232, v251, v255\n"
"v_mfma_f32_16x16x32_bf16 a[180:183], a[148:151], v[52:55], a[180:183]\n"
"s_waitcnt lgkmcnt(4)\n"
"v_rcp_f32 v240, v240\n"
"v_mul_f32 v246, 0xc1800000, v246\n"
"v_mfma_f32_16x16x32_bf16 a[176:179], a[148:151], v[48:51], a[176:179]\n"
"ds_read_b64_tr_b16 a[136:137], v234 offset:33792\n"
"v_accvgpr_read_b32 v249, a77\n"
"v_fma_f32 v242, v242, s93, s93\n"
"v_mfma_f32_16x16x32_bf16 a[160:163], a[152:155], v[56:59], a[160:163]\n"
"ds_read_b64_tr_b16 a[138:139], v234 offset:41984\n"
"v_mul_f32 v249, v248, v249\n"
"v_mul_f32 v241, v241, v240\n"
"v_mfma_f32_16x16x32_bf16 a[164:167], a[152:155], v[60:63], a[164:167]\n"
"buffer_load_dwordx4 v236, s[60:63], s85 offen offset:0 lds\n"
"s_add_u32 m0, m0, 0x4000\n"
"v_exp_f32 v246, v246\n"
"v_mfma_f32_16x16x32_bf16 a[168:171], a[152:155], v[168:171], a[168:171]\n"
"s_mov_b32 s85, 0x280080\n"
"ds_read_b64_tr_b16 a[140:141], v235 offset:33792\n"
"v_mul_f32 v248, 0xc1800000, v248\n"
"v_mfma_f32_16x16x32_bf16 a[172:175], a[152:155], v[172:175], a[172:175]\n"
"v_rcp_f32 v242, v242\n"
"v_accvgpr_read_b32 v250, a74\n"
"v_mfma_f32_16x16x32_bf16 a[188:191], a[156:159], v[172:175], a[188:191]\n"
"ds_read_b64_tr_b16 a[142:143], v235 offset:41984\n"
"v_accvgpr_read_b32 v251, a78\n"
"v_fma_f32 v246, v246, s93, s93\n"
"v_mfma_f32_16x16x32_bf16 a[184:187], a[156:159], v[168:171], a[184:187]\n"
"s_waitcnt lgkmcnt(4)\n"
"v_exp_f32 v248, v248\n"
"v_mul_f32 v251, v250, v251\n"
"v_mfma_f32_16x16x32_bf16 a[180:183], a[156:159], v[60:63], a[180:183]\n"
"ds_read_b64_tr_b16 a[144:145], v234 offset:50176\n"
"v_mul_f32 v243, v243, v242\n"
"v_mul_f32 v250, 0xc1800000, v250\n"
"v_mfma_f32_16x16x32_bf16 a[176:179], a[156:159], v[56:59], a[176:179]\n"
"ds_read_b64_tr_b16 a[146:147], v234 offset:58368\n"
"v_accvgpr_read_b32 v254, a75\n"
"v_cvt_pk_bf16_f32 v233, v241, v243\n"
"v_mfma_f32_16x16x32_bf16 a[160:163], a[128:131], v[64:67], a[160:163]\n"
"v_rcp_f32 v246, v246\n"
"v_fma_f32 v248, v248, s93, s93\n"
"v_mfma_f32_16x16x32_bf16 a[164:167], a[128:131], v[68:71], a[164:167]\n"
"buffer_load_dwordx4 v236, s[60:63], s85 offen offset:0 lds\n"
"s_add_u32 m0, m0, 0xffffc400\n"
"v_accvgpr_read_b32 v255, a79\n"
"v_mfma_f32_16x16x32_bf16 a[168:171], a[128:131], v[176:179], a[168:171]\n"
"s_mov_b32 s85, 0x2c0080\n"
"buffer_store_dwordx4 v[230:233], v239, s[56:59], s68 offen offset:0 sc1\n"
"v_exp_f32 v250, v250\n"
"v_mfma_f32_16x16x32_bf16 a[172:175], a[128:131], v[180:183], a[172:175]\n"
"ds_read_b64_tr_b16 a[148:149], v235 offset:50176\n"
"v_mul_f32 v255, v254, v255\n"
"v_mul_f32 v247, v247, v246\n"
"v_mfma_f32_16x16x32_bf16 a[188:191], a[132:135], v[180:183], a[188:191]\n"
"v_rcp_f32 v248, v248\n"
"v_mul_f32 v254, 0xc1800000, v254\n"
"v_mfma_f32_16x16x32_bf16 a[184:187], a[132:135], v[176:179], a[184:187]\n"
"ds_read_b64_tr_b16 a[150:151], v235 offset:58368\n"
"v_accvgpr_read_b32 v240, a104\n"
"v_fma_f32 v250, v250, s93, s93\n"
"v_mfma_f32_16x16x32_bf16 a[180:183], a[132:135], v[68:71], a[180:183]\n"
"buffer_load_dwordx4 v236, s[60:63], s85 offen offset:0 lds\n"
"s_add_u32 m0, m0, 0x4000\n"
"v_accvgpr_read_b32 v241, a108\n"
"v_mfma_f32_16x16x32_bf16 a[176:179], a[132:135], v[64:67], a[176:179]\n"
"s_waitcnt lgkmcnt(4)\n"
"v_exp_f32 v254, v254\n"
"v_mul_f32 v249, v249, v248\n"
"v_mfma_f32_16x16x32_bf16 a[160:163], a[136:139], v[72:75], a[160:163]\n"
"s_mov_b32 s85, 0x300080\n"
"ds_read_b64_tr_b16 a[152:153], v234 offset:34816\n"
"v_mul_f32 v241, v240, v241\n"
"v_mfma_f32_16x16x32_bf16 a[164:167], a[136:139], v[76:79], a[164:167]\n"
"ds_read_b64_tr_b16 a[154:155], v234 offset:43008\n"
"v_accvgpr_read_b32 v242, a105\n"
"v_cvt_pk_bf16_f32 v230, v247, v249\n"
"v_mfma_f32_16x16x32_bf16 a[168:171], a[136:139], v[184:187], a[168:171]\n"
"v_rcp_f32 v250, v250\n"
"v_mul_f32 v240, 0xc1800000, v240\n"
"v_accvgpr_read_b32 v243, a109\n"
"v_mfma_f32_16x16x32_bf16 a[172:175], a[136:139], v[188:191], a[172:175]\n"
"buffer_load_dwordx4 v236, s[60:63], s85 offen offset:0 lds\n"
"s_add_u32 m0, m0, 0xffffc400\n"
"v_fma_f32 v254, v254, s93, s93\n"
"v_mul_f32 v243, v242, v243\n"
"v_mfma_f32_16x16x32_bf16 a[188:191], a[140:143], v[188:191], a[188:191]\n"
"s_mov_b32 s85, 0x340080\n"
"v_exp_f32 v240, v240\n"
"v_mul_f32 v251, v251, v250\n"
"v_mul_f32 v242, 0xc1800000, v242\n"
"v_mfma_f32_16x16x32_bf16 a[184:187], a[140:143], v[184:187], a[184:187]\n"
"ds_read_b64_tr_b16 a[156:157], v235 offset:34816\n"
"v_accvgpr_read_b32 v246, a106\n"
"v_rcp_f32 v254, v254\n"
"v_accvgpr_read_b32 v247, a110\n"
"v_mfma_f32_16x16x32_bf16 a[180:183], a[140:143], v[76:79], a[180:183]\n"
"ds_read_b64_tr_b16 a[158:159], v235 offset:43008\n"
"v_fma_f32 v240, v240, s93, s93\n"
"v_exp_f32 v242, v242\n"
"v_mul_f32 v247, v246, v247\n"
"v_mfma_f32_16x16x32_bf16 a[176:179], a[140:143], v[72:75], a[176:179]\n"
"s_waitcnt lgkmcnt(4)\n"
"ds_read_b64_tr_b16 a[128:129], v234 offset:51200\n"
"v_mul_f32 v255, v255, v254\n"
"v_mul_f32 v246, 0xc1800000, v246\n"
"v_mfma_f32_16x16x32_bf16 a[160:163], a[144:147], v[80:83], a[160:163]\n"
"buffer_load_dwordx4 v236, s[60:63], s85 offen offset:0 lds\n"
"s_add_u32 m0, m0, 0xfffdf400\n"
"v_accvgpr_read_b32 v248, a107\n"
"v_cvt_pk_bf16_f32 v231, v251, v255\n"
"v_mfma_f32_16x16x32_bf16 a[164:167], a[144:147], v[84:87], a[164:167]\n"
"s_mov_b32 s85, 0x100\n"
"v_rcp_f32 v240, v240\n"
"v_fma_f32 v242, v242, s93, s93\n"
"v_accvgpr_read_b32 v249, a111\n"
"v_mfma_f32_16x16x32_bf16 a[168:171], a[144:147], v[192:195], a[168:171]\n"
"v_exp_f32 v246, v246\n"
"v_mul_f32 v249, v248, v249\n"
"v_mul_f32 v241, v241, v240\n"
"v_mfma_f32_16x16x32_bf16 a[172:175], a[144:147], v[196:199], a[172:175]\n"
"v_rcp_f32 v242, v242\n"
"v_mul_f32 v248, 0xc1800000, v248\n"
"v_accvgpr_read_b32 v250, a16\n"
"v_mfma_f32_16x16x32_bf16 a[188:191], a[148:151], v[196:199], a[188:191]\n"
"ds_read_b64_tr_b16 a[130:131], v234 offset:59392\n"
"v_fma_f32 v246, v246, s93, s93\n"
"v_accvgpr_read_b32 v251, a20\n"
"v_mul_f32 v243, v243, v242\n"
"v_mfma_f32_16x16x32_bf16 a[184:187], a[148:151], v[192:195], a[184:187]\n"
"ds_read_b64_tr_b16 a[132:133], v235 offset:51200\n"
"v_mul_f32 v251, v250, v251\n"
"v_exp_f32 v248, v248\n"
"v_accvgpr_read_b32 v254, a17\n"
"v_mfma_f32_16x16x32_bf16 a[180:183], a[148:151], v[84:87], a[180:183]\n"
"ds_read_b64_tr_b16 a[134:135], v235 offset:59392\n"
"v_cvt_pk_bf16_f32 v232, v241, v243\n"
"v_rcp_f32 v246, v246\n"
"v_mul_f32 v250, 0xc1800000, v250\n"
"v_mfma_f32_16x16x32_bf16 a[176:179], a[148:151], v[80:83], a[176:179]\n"
"s_waitcnt lgkmcnt(4)\n"
"ds_read_b64_tr_b16 a[136:137], v234 offset:35840\n"
"v_accvgpr_read_b32 v255, a21\n"
"v_fma_f32 v248, v248, s93, s93\n"
"v_mfma_f32_16x16x32_bf16 a[160:163], a[152:155], v[88:91], a[160:163]\n"
"ds_read_b64_tr_b16 a[138:139], v234 offset:44032\n"
"v_mul_f32 v255, v254, v255\n"
"v_mul_f32 v247, v247, v246\n"
"v_exp_f32 v250, v250\n"
"v_mfma_f32_16x16x32_bf16 a[164:167], a[152:155], v[92:95], a[164:167]\n"
"ds_read_b64_tr_b16 a[140:141], v235 offset:35840\n"
"v_mul_f32 v254, 0xc1800000, v254\n"
"v_accvgpr_read_b32 v240, a18\n"
"v_rcp_f32 v248, v248\n"
"v_mfma_f32_16x16x32_bf16 a[168:171], a[152:155], v[200:203], a[168:171]\n"
"ds_read_b64_tr_b16 a[142:143], v235 offset:44032\n"
"v_accvgpr_read_b32 v241, a22\n"
"v_fma_f32 v250, v250, s93, s93\n"
"v_exp_f32 v254, v254\n"
"v_mfma_f32_16x16x32_bf16 a[172:175], a[152:155], v[204:207], a[172:175]\n"
"v_mul_f32 v241, v240, v241\n"
"v_mul_f32 v249, v249, v248\n"
"v_accvgpr_read_b32 v242, a19\n"
"v_rcp_f32 v250, v250\n"
"v_mfma_f32_16x16x32_bf16 a[188:191], a[156:159], v[204:207], a[188:191]\n"
"v_mul_f32 v240, 0xc1800000, v240\n"
"v_cvt_pk_bf16_f32 v233, v247, v249\n"
"v_fma_f32 v254, v254, s93, s93\n"
"v_accvgpr_read_b32 v243, a23\n"
"v_mfma_f32_16x16x32_bf16 a[184:187], a[156:159], v[200:203], a[184:187]\n"
"s_waitcnt vmcnt(8)\n"
"s_barrier\n"
"v_mul_f32 v243, v242, v243\n"
"v_exp_f32 v240, v240\n"
"v_mul_f32 v251, v251, v250\n"
"v_mfma_f32_16x16x32_bf16 a[180:183], a[156:159], v[92:95], a[180:183]\n"
"s_waitcnt lgkmcnt(4)\n"
"buffer_store_dwordx4 v[230:233], v239, s[56:59], s69 offen offset:0 sc1\n"
"v_rcp_f32 v254, v254\n"
"v_mul_f32 v242, 0xc1800000, v242\n"
"v_mfma_f32_16x16x32_bf16 a[176:179], a[156:159], v[88:91], a[176:179]\n"
"v_accvgpr_read_b32 v246, a48\n"
"v_fma_f32 v240, v240, s93, s93\n"
"v_accvgpr_read_b32 v247, a52\n"
"v_mul_f32 v255, v255, v254\n"
"v_mfma_f32_16x16x32_bf16 a[160:163], a[128:131], v[96:99], a[160:163]\n"
"v_exp_f32 v242, v242\n"
"v_mul_f32 v247, v246, v247\n"
"v_accvgpr_read_b32 v248, a49\n"
"v_mfma_f32_16x16x32_bf16 a[164:167], a[128:131], v[100:103], a[164:167]\n"
"buffer_load_dwordx4 v236, s[60:63], s85 offen offset:0 lds\n"
"s_add_u32 m0, m0, 0x4000\n"
"v_cvt_pk_bf16_f32 v230, v251, v255\n"
"v_rcp_f32 v240, v240\n"
"v_mfma_f32_16x16x32_bf16 a[168:171], a[128:131], v[208:211], a[168:171]\n"
"s_mov_b32 s85, 0x40100\n"
"v_add_u32 v234, 0x10000, v234\n"
"v_mul_f32 v246, 0xc1800000, v246\n"
"v_accvgpr_read_b32 v249, a53\n"
"v_fma_f32 v242, v242, s93, s93\n"
"v_mfma_f32_16x16x32_bf16 a[172:175], a[128:131], v[212:215], a[172:175]\n"
"ds_read_b64_tr_b16 a[144:145], v234 offset:0\n"
"v_mul_f32 v249, v248, v249\n"
"v_mul_f32 v241, v241, v240\n"
"v_exp_f32 v246, v246\n"
"v_mfma_f32_16x16x32_bf16 a[188:191], a[132:135], v[212:215], a[188:191]\n"
"ds_read_b64_tr_b16 a[146:147], v234 offset:8192\n"
"v_add_u32 v235, 0x10000, v235\n"
"v_mul_f32 v248, 0xc1800000, v248\n"
"v_accvgpr_read_b32 v250, a50\n"
"v_mfma_f32_16x16x32_bf16 a[184:187], a[132:135], v[208:211], a[184:187]\n"
"ds_read_b64_tr_b16 a[148:149], v235 offset:0\n"
"v_accvgpr_read_b32 v251, a54\n"
"v_rcp_f32 v242, v242\n"
"v_fma_f32 v246, v246, s93, s93\n"
"v_mfma_f32_16x16x32_bf16 a[180:183], a[132:135], v[100:103], a[180:183]\n"
"ds_read_b64_tr_b16 a[150:151], v235 offset:8192\n"
"v_mul_f32 v251, v250, v251\n"
"v_exp_f32 v248, v248\n"
"v_mul_f32 v243, v243, v242\n"
"v_mfma_f32_16x16x32_bf16 a[176:179], a[132:135], v[96:99], a[176:179]\n"
"s_waitcnt lgkmcnt(4)\n"
"ds_read_b64_tr_b16 a[152:153], v234 offset:16384\n"
"v_mul_f32 v250, 0xc1800000, v250\n"
"v_accvgpr_read_b32 v254, a51\n"
"v_mfma_f32_16x16x32_bf16 a[160:163], a[136:139], v[104:107], a[160:163]\n"
"v_rcp_f32 v246, v246\n"
"v_cvt_pk_bf16_f32 v231, v241, v243\n"
"v_fma_f32 v248, v248, s93, s93\n"
"v_mfma_f32_16x16x32_bf16 a[164:167], a[136:139], v[108:111], a[164:167]\n"
"buffer_load_dwordx4 v236, s[60:63], s85 offen offset:0 lds\n"
"s_add_u32 m0, m0, 0xffffc400\n"
"v_accvgpr_read_b32 v255, a55\n"
"v_exp_f32 v250, v250\n"
"v_mfma_f32_16x16x32_bf16 a[168:171], a[136:139], v[216:219], a[168:171]\n"
"s_mov_b32 s85, 0x80100\n"
"ds_read_b64_tr_b16 a[154:155], v234 offset:24576\n"
"v_mul_f32 v255, v254, v255\n"
"v_mul_f32 v247, v247, v246\n"
"v_mfma_f32_16x16x32_bf16 a[172:175], a[136:139], v[220:223], a[172:175]\n"
"v_rcp_f32 v248, v248\n"
"v_mul_f32 v254, 0xc1800000, v254\n"
"v_accvgpr_read_b32 v240, a80\n"
"v_mfma_f32_16x16x32_bf16 a[188:191], a[140:143], v[220:223], a[188:191]\n"
"ds_read_b64_tr_b16 a[156:157], v235 offset:16384\n"
"v_fma_f32 v250, v250, s93, s93\n"
"v_accvgpr_read_b32 v241, a84\n"
"v_mul_f32 v249, v249, v248\n"
"v_mfma_f32_16x16x32_bf16 a[184:187], a[140:143], v[216:219], a[184:187]\n"
"s_waitcnt lgkmcnt(3)\n"
"v_exp_f32 v254, v254\n"
"v_mul_f32 v241, v240, v241\n"
"v_accvgpr_read_b32 v242, a81\n"
"v_mfma_f32_16x16x32_bf16 a[180:183], a[140:143], v[108:111], a[180:183]\n"
"ds_read_b64_tr_b16 a[158:159], v235 offset:24576\n"
"v_cvt_pk_bf16_f32 v232, v247, v249\n"
"v_rcp_f32 v250, v250\n"
"v_mul_f32 v240, 0xc1800000, v240\n"
"v_mfma_f32_16x16x32_bf16 a[176:179], a[140:143], v[104:107], a[176:179]\n"
"ds_read_b64_tr_b16 a[128:129], v234 offset:1024\n"
"v_accvgpr_read_b32 v243, a85\n"
"v_fma_f32 v254, v254, s93, s93\n"
"v_mul_f32 v251, v251, v250\n"
"v_mfma_f32_16x16x32_bf16 a[192:195], a[144:147], v[0:3], 0\n"
"v_exp_f32 v240, v240\n"
"v_mul_f32 v243, v242, v243\n"
"v_accvgpr_read_b32 v246, a82\n"
"v_mfma_f32_16x16x32_bf16 a[196:199], a[144:147], v[4:7], 0\n"
"buffer_load_dwordx4 v236, s[60:63], s85 offen offset:0 lds\n"
"s_add_u32 m0, m0, 0x4000\n"
"v_mul_f32 v242, 0xc1800000, v242\n"
"v_rcp_f32 v254, v254\n"
"v_mfma_f32_16x16x32_bf16 a[200:203], a[144:147], v[112:115], 0\n"
"s_mov_b32 s85, 0xc0100\n"
"ds_read_b64_tr_b16 a[130:131], v234 offset:9216\n"
"v_accvgpr_read_b32 v247, a86\n"
"v_fma_f32 v240, v240, s93, s93\n"
"v_mfma_f32_16x16x32_bf16 a[204:207], a[144:147], v[116:119], 0\n"
"v_exp_f32 v242, v242\n"
"v_mul_f32 v247, v246, v247\n"
"v_mul_f32 v255, v255, v254\n"
"v_mfma_f32_16x16x32_bf16 a[220:223], a[148:151], v[116:119], 0\n"
"ds_read_b64_tr_b16 a[132:133], v235 offset:1024\n"
"v_mul_f32 v246, 0xc1800000, v246\n"
"v_accvgpr_read_b32 v248, a83\n"
"v_cvt_pk_bf16_f32 v233, v251, v255\n"
"v_mfma_f32_16x16x32_bf16 a[216:219], a[148:151], v[112:115], 0\n"
"v_rcp_f32 v240, v240\n"
"v_fma_f32 v242, v242, s93, s93\n"
"v_accvgpr_read_b32 v249, a87\n"
"v_mfma_f32_16x16x32_bf16 a[212:215], a[148:151], v[4:7], 0\n"
"ds_read_b64_tr_b16 a[134:135], v235 offset:9216\n"
"v_mul_f32 v249, v248, v249\n"
"v_exp_f32 v246, v246\n"
"v_mul_f32 v241, v241, v240\n"
"v_mfma_f32_16x16x32_bf16 a[208:211], a[148:151], v[0:3], 0\n"
"s_waitcnt lgkmcnt(4)\n"
"buffer_store_dwordx4 v[230:233], v239, s[56:59], s68 offen offset:1024 sc1\n"
"v_rcp_f32 v242, v242\n"
"v_mul_f32 v248, 0xc1800000, v248\n"
"v_mfma_f32_16x16x32_bf16 a[192:195], a[152:155], v[8:11], a[192:195]\n"
"ds_read_b64_tr_b16 a[136:137], v234 offset:17408\n"
"v_accvgpr_read_b32 v250, a112\n"
"v_fma_f32 v246, v246, s93, s93\n"
"v_accvgpr_read_b32 v251, a116\n"
"v_mfma_f32_16x16x32_bf16 a[196:199], a[152:155], v[12:15], a[196:199]\n"
"buffer_load_dwordx4 v236, s[60:63], s85 offen offset:0 lds\n"
"s_add_u32 m0, m0, 0xffffc400\n"
"v_mul_f32 v243, v243, v242\n"
"v_exp_f32 v248, v248\n"
"v_mfma_f32_16x16x32_bf16 a[200:203], a[152:155], v[120:123], a[200:203]\n"
"s_mov_b32 s85, 0x100100\n"
"ds_read_b64_tr_b16 a[138:139], v234 offset:25600\n"
"v_mul_f32 v251, v250, v251\n"
"v_accvgpr_read_b32 v254, a113\n"
"v_mfma_f32_16x16x32_bf16 a[204:207], a[152:155], v[124:127], a[204:207]\n"
"v_rcp_f32 v246, v246\n"
"v_cvt_pk_bf16_f32 v230, v241, v243\n"
"v_mul_f32 v250, 0xc1800000, v250\n"
"v_mfma_f32_16x16x32_bf16 a[220:223], a[156:159], v[124:127], a[220:223]\n"
"ds_read_b64_tr_b16 a[140:141], v235 offset:17408\n"
"v_accvgpr_read_b32 v255, a117\n"
"v_fma_f32 v248, v248, s93, s93\n"
"v_mul_f32 v247, v247, v246\n"
"v_mfma_f32_16x16x32_bf16 a[216:219], a[156:159], v[120:123], a[216:219]\n"
"v_exp_f32 v250, v250\n"
"v_mul_f32 v255, v254, v255\n"
"v_accvgpr_read_b32 v240, a114\n"
"v_mfma_f32_16x16x32_bf16 a[212:215], a[156:159], v[12:15], a[212:215]\n"
"ds_read_b64_tr_b16 a[142:143], v235 offset:25600\n"
"v_mul_f32 v254, 0xc1800000, v254\n"
"v_rcp_f32 v248, v248\n"
"v_accvgpr_read_b32 v241, a118\n"
"v_mfma_f32_16x16x32_bf16 a[208:211], a[156:159], v[8:11], a[208:211]\n"
"s_waitcnt lgkmcnt(4)\n"
"v_exp_f32 v254, v254\n"
"v_fma_f32 v250, v250, s93, s93\n"
"v_mul_f32 v241, v240, v241\n"
"v_mfma_f32_16x16x32_bf16 a[192:195], a[128:131], v[16:19], a[192:195]\n"
"ds_read_b64_tr_b16 a[144:145], v234 offset:2048\n"
"v_mul_f32 v249, v249, v248\n"
"v_mul_f32 v240, 0xc1800000, v240\n"
"v_accvgpr_read_b32 v242, a115\n"
"v_mfma_f32_16x16x32_bf16 a[196:199], a[128:131], v[20:23], a[196:199]\n"
"buffer_load_dwordx4 v236, s[60:63], s85 offen offset:0 lds\n"
"s_add_u32 m0, m0, 0x4000\n"
"v_cvt_pk_bf16_f32 v231, v247, v249\n"
"v_rcp_f32 v250, v250\n"
"v_mfma_f32_16x16x32_bf16 a[200:203], a[128:131], v[128:131], a[200:203]\n"
"s_mov_b32 s85, 0x140100\n"
"ds_read_b64_tr_b16 a[146:147], v234 offset:10240\n"
"v_fma_f32 v254, v254, s93, s93\n"
"v_accvgpr_read_b32 v243, a119\n"
"v_mfma_f32_16x16x32_bf16 a[204:207], a[128:131], v[132:135], a[204:207]\n"
"v_exp_f32 v240, v240\n"
"v_mul_f32 v243, v242, v243\n"
"v_mul_f32 v251, v251, v250\n"
"v_mfma_f32_16x16x32_bf16 a[220:223], a[132:135], v[132:135], a[220:223]\n"
"v_rcp_f32 v254, v254\n"
"v_mul_f32 v242, 0xc1800000, v242\n"
"v_accvgpr_read_b32 v246, a24\n"
"v_mfma_f32_16x16x32_bf16 a[216:219], a[132:135], v[128:131], a[216:219]\n"
"ds_read_b64_tr_b16 a[148:149], v235 offset:2048\n"
"v_fma_f32 v240, v240, s93, s93\n"
"v_accvgpr_read_b32 v247, a28\n"
"v_mul_f32 v255, v255, v254\n"
"v_mfma_f32_16x16x32_bf16 a[212:215], a[132:135], v[20:23], a[212:215]\n"
"ds_read_b64_tr_b16 a[150:151], v235 offset:10240\n"
"v_mul_f32 v247, v246, v247\n"
"v_exp_f32 v242, v242\n"
"v_accvgpr_read_b32 v248, a25\n"
"v_mfma_f32_16x16x32_bf16 a[208:211], a[132:135], v[16:19], a[208:211]\n"
"s_waitcnt lgkmcnt(4)\n"
"buffer_load_dwordx4 v236, s[60:63], s85 offen offset:0 lds\n"
"v_cvt_pk_bf16_f32 v232, v251, v255\n"
"v_rcp_f32 v240, v240\n"
"v_mfma_f32_16x16x32_bf16 a[192:195], a[136:139], v[24:27], a[192:195]\n"
"s_add_u32 m0, m0, 0xffffc400\n"
"ds_read_b64_tr_b16 a[152:153], v234 offset:18432\n"
"v_mul_f32 v246, 0xc1800000, v246\n"
"v_accvgpr_read_b32 v249, a29\n"
"v_mfma_f32_16x16x32_bf16 a[196:199], a[136:139], v[28:31], a[196:199]\n"
"s_mov_b32 s85, 0x180100\n"
"ds_read_b64_tr_b16 a[154:155], v234 offset:26624\n"
"v_fma_f32 v242, v242, s93, s93\n"
"v_mul_f32 v249, v248, v249\n"
"v_mfma_f32_16x16x32_bf16 a[200:203], a[136:139], v[136:139], a[200:203]\n"
"v_exp_f32 v246, v246\n"
"v_mul_f32 v241, v241, v240\n"
"v_mul_f32 v248, 0xc1800000, v248\n"
"v_mfma_f32_16x16x32_bf16 a[204:207], a[136:139], v[140:143], a[204:207]\n"
"v_rcp_f32 v242, v242\n"
"v_accvgpr_read_b32 v250, a26\n"
"v_accvgpr_read_b32 v251, a30\n"
"v_mfma_f32_16x16x32_bf16 a[220:223], a[140:143], v[140:143], a[220:223]\n"
"buffer_load_dwordx4 v236, s[60:63], s85 offen offset:0 lds\n"
"s_add_u32 m0, m0, 0x7400\n"
"v_fma_f32 v246, v246, s93, s93\n"
"v_exp_f32 v248, v248\n"
"v_mfma_f32_16x16x32_bf16 a[216:219], a[140:143], v[136:139], a[216:219]\n"
"s_mov_b32 s85, 0x1c0100\n"
"ds_read_b64_tr_b16 a[156:157], v235 offset:18432\n"
"v_mul_f32 v251, v250, v251\n"
"v_mul_f32 v243, v243, v242\n"
"v_mfma_f32_16x16x32_bf16 a[212:215], a[140:143], v[28:31], a[212:215]\n"
"ds_read_b64_tr_b16 a[158:159], v235 offset:26624\n"
"v_mul_f32 v250, 0xc1800000, v250\n"
"v_accvgpr_read_b32 v254, a27\n"
"v_cvt_pk_bf16_f32 v233, v241, v243\n"
"v_mfma_f32_16x16x32_bf16 a[208:211], a[140:143], v[24:27], a[208:211]\n"
"s_waitcnt lgkmcnt(4)\n"
"v_rcp_f32 v246, v246\n"
"v_fma_f32 v248, v248, s93, s93\n"
"v_accvgpr_read_b32 v255, a31\n"
"v_mfma_f32_16x16x32_bf16 a[192:195], a[144:147], v[32:35], a[192:195]\n"
"buffer_store_dwordx4 v[230:233], v239, s[56:59], s69 offen offset:1024 sc1\n"
"v_exp_f32 v250, v250\n"
"v_mul_f32 v255, v254, v255\n"
"v_mfma_f32_16x16x32_bf16 a[196:199], a[144:147], v[36:39], a[196:199]\n"
"v_rcp_f32 v248, v248\n"
"v_mul_f32 v247, v247, v246\n"
"v_mul_f32 v254, 0xc1800000, v254\n"
"v_mfma_f32_16x16x32_bf16 a[200:203], a[144:147], v[144:147], a[200:203]\n"
"ds_read_b64_tr_b16 a[128:129], v234 offset:3072\n"
"v_accvgpr_read_b32 v240, a56\n"
"v_fma_f32 v250, v250, s93, s93\n"
"v_accvgpr_read_b32 v241, a60\n"
"v_mfma_f32_16x16x32_bf16 a[204:207], a[144:147], v[148:151], a[204:207]\n"
"ds_read_b64_tr_b16 a[130:131], v234 offset:11264\n"
"v_mul_f32 v249, v249, v248\n"
"v_exp_f32 v254, v254\n"
"v_mul_f32 v241, v240, v241\n"
"v_mfma_f32_16x16x32_bf16 a[220:223], a[148:151], v[148:151], a[220:223]\n"
"ds_read_b64_tr_b16 a[132:133], v235 offset:3072\n"
"v_accvgpr_read_b32 v242, a57\n"
"v_cvt_pk_bf16_f32 v230, v247, v249\n"
"v_rcp_f32 v250, v250\n"
"v_mfma_f32_16x16x32_bf16 a[216:219], a[148:151], v[144:147], a[216:219]\n"
"ds_read_b64_tr_b16 a[134:135], v235 offset:11264\n"
"s_waitcnt vmcnt(10)\n"
"s_barrier\n"
"v_mul_f32 v240, 0xc1800000, v240\n"
"v_accvgpr_read_b32 v243, a61\n"
"v_mfma_f32_16x16x32_bf16 a[212:215], a[148:151], v[36:39], a[212:215]\n"
"v_fma_f32 v254, v254, s93, s93\n"
"v_mul_f32 v243, v242, v243\n"
"v_mul_f32 v251, v251, v250\n"
"v_exp_f32 v240, v240\n"
"v_mfma_f32_16x16x32_bf16 a[208:211], a[148:151], v[32:35], a[208:211]\n"
"s_waitcnt lgkmcnt(4)\n"
"v_mul_f32 v242, 0xc1800000, v242\n"
"v_accvgpr_read_b32 v246, a58\n"
"v_rcp_f32 v254, v254\n"
"v_accvgpr_read_b32 v247, a62\n"
"v_mfma_f32_16x16x32_bf16 a[192:195], a[152:155], v[40:43], a[192:195]\n"
"v_exp_f32 v242, v242\n"
"v_fma_f32 v240, v240, s93, s93\n"
"v_mul_f32 v247, v246, v247\n"
"v_mfma_f32_16x16x32_bf16 a[196:199], a[152:155], v[44:47], a[196:199]\n"
"buffer_load_dwordx4 v236, s[60:63], s85 offen offset:0 lds\n"
"s_add_u32 m0, m0, 0x4000\n"
"v_mul_f32 v255, v255, v254\n"
"v_mul_f32 v246, 0xc1800000, v246\n"
"v_mfma_f32_16x16x32_bf16 a[200:203], a[152:155], v[152:155], a[200:203]\n"
"s_mov_b32 s85, 0x200100\n"
"v_add_u32 v234, 0x10000, v234\n"
"v_accvgpr_read_b32 v248, a59\n"
"v_cvt_pk_bf16_f32 v231, v251, v255\n"
"v_rcp_f32 v240, v240\n"
"v_mfma_f32_16x16x32_bf16 a[204:207], a[152:155], v[156:159], a[204:207]\n"
"ds_read_b64_tr_b16 a[136:137], v234 offset:0\n"
"v_fma_f32 v242, v242, s93, s93\n"
"v_accvgpr_read_b32 v249, a63\n"
"v_exp_f32 v246, v246\n"
"v_mfma_f32_16x16x32_bf16 a[220:223], a[156:159], v[156:159], a[220:223]\n"
"ds_read_b64_tr_b16 a[138:139], v234 offset:8192\n"
"v_add_u32 v235, 0x10000, v235\n"
"v_mul_f32 v249, v248, v249\n"
"v_accvgpr_read_b32 v224, a160\n"
"v_mfma_f32_16x16x32_bf16 a[216:219], a[156:159], v[152:155], a[216:219]\n"
"ds_read_b64_tr_b16 a[140:141], v235 offset:0\n"
"v_accvgpr_read_b32 v225, a161\n"
"v_mul_f32 v241, v241, v240\n"
"v_rcp_f32 v242, v242\n"
"v_mfma_f32_16x16x32_bf16 a[212:215], a[156:159], v[44:47], a[212:215]\n"
"ds_read_b64_tr_b16 a[142:143], v235 offset:8192\n"
"v_mul_f32 v248, 0xc1800000, v248\n"
"v_accvgpr_read_b32 v250, a88\n"
"v_accvgpr_read_b32 v226, a162\n"
"v_mfma_f32_16x16x32_bf16 a[208:211], a[156:159], v[40:43], a[208:211]\n"
"s_waitcnt lgkmcnt(4)\n"
"ds_read_b64_tr_b16 a[144:145], v234 offset:16384\n"
"v_accvgpr_read_b32 v227, a163\n"
"v_fma_f32 v246, v246, s93, s93\n"
"v_mfma_f32_16x16x32_bf16 a[192:195], a[128:131], v[48:51], a[192:195]\n"
"ds_read_b64_tr_b16 a[146:147], v234 offset:24576\n"
"v_accvgpr_read_b32 v251, a92\n"
"v_cvt_pk_bf16_f32 v228, v224, v225\n"
"v_mul_f32 v243, v243, v242\n"
"v_mfma_f32_16x16x32_bf16 a[196:199], a[128:131], v[52:55], a[196:199]\n"
"buffer_load_dwordx4 v236, s[60:63], s85 offen offset:0 lds\n"
"s_add_u32 m0, m0, 0xffffc400\n"
"v_exp_f32 v248, v248\n"
"v_mul_f32 v251, v250, v251\n"
"v_mfma_f32_16x16x32_bf16 a[200:203], a[128:131], v[160:163], a[200:203]\n"
"s_mov_b32 s85, 0x240100\n"
"ds_read_b64_tr_b16 a[148:149], v235 offset:16384\n"
"v_accvgpr_read_b32 v254, a89\n"
"v_cvt_pk_bf16_f32 v229, v226, v227\n"
"v_mfma_f32_16x16x32_bf16 a[204:207], a[128:131], v[164:167], a[204:207]\n"
"v_rcp_f32 v246, v246\n"
"v_cvt_pk_bf16_f32 v232, v241, v243\n"
"v_mul_f32 v250, 0xc1800000, v250\n"
"v_mfma_f32_16x16x32_bf16 a[220:223], a[132:135], v[164:167], a[220:223]\n"
"ds_write_b64 v238, v[228:229] offset:0\n"
"v_accvgpr_read_b32 v255, a93\n"
"v_fma_f32 v248, v248, s93, s93\n"
"v_mfma_f32_16x16x32_bf16 a[216:219], a[132:135], v[160:163], a[216:219]\n"
"ds_read_b64_tr_b16 a[150:151], v235 offset:24576\n"
"v_mul_f32 v255, v254, v255\n"
"v_mul_f32 v247, v247, v246\n"
"v_exp_f32 v250, v250\n"
"v_mfma_f32_16x16x32_bf16 a[212:215], a[132:135], v[52:55], a[212:215]\n"
"ds_read_b64_tr_b16 a[152:153], v234 offset:1024\n"
"v_mul_f32 v254, 0xc1800000, v254\n"
"v_accvgpr_read_b32 v240, a90\n"
"v_rcp_f32 v248, v248\n"
"v_mfma_f32_16x16x32_bf16 a[208:211], a[132:135], v[48:51], a[208:211]\n"
"s_waitcnt lgkmcnt(6)\n"
"ds_read_b64_tr_b16 a[154:155], v234 offset:9216\n"
"v_accvgpr_read_b32 v241, a94\n"
"v_fma_f32 v250, v250, s93, s93\n"
"v_mfma_f32_16x16x32_bf16 a[192:195], a[136:139], v[56:59], a[192:195]\n"
"v_exp_f32 v254, v254\n"
"v_mul_f32 v241, v240, v241\n"
"v_mul_f32 v249, v249, v248\n"
"v_mfma_f32_16x16x32_bf16 a[196:199], a[136:139], v[60:63], a[196:199]\n"
"buffer_load_dwordx4 v236, s[60:63], s85 offen offset:0 lds\n"
"s_add_u32 m0, m0, 0x4000\n"
"v_mul_f32 v240, 0xc1800000, v240\n"
"v_accvgpr_read_b32 v242, a91\n"
"v_mfma_f32_16x16x32_bf16 a[200:203], a[136:139], v[168:171], a[200:203]\n"
"s_mov_b32 s85, 0x280100\n"
"v_rcp_f32 v250, v250\n"
"v_cvt_pk_bf16_f32 v233, v247, v249\n"
"v_fma_f32 v254, v254, s93, s93\n"
"v_mfma_f32_16x16x32_bf16 a[204:207], a[136:139], v[172:175], a[204:207]\n"
"ds_read_b64_tr_b16 a[156:157], v235 offset:1024\n"
"v_accvgpr_read_b32 v243, a95\n"
"v_accvgpr_read_b32 v224, a164\n"
"v_accvgpr_read_b32 v225, a165\n"
"v_mfma_f32_16x16x32_bf16 a[220:223], a[140:143], v[172:175], a[220:223]\n"
"buffer_store_dwordx4 v[230:233], v239, s[56:59], s68 offen offset:2048 sc1\n"
"v_exp_f32 v240, v240\n"
"v_mul_f32 v243, v242, v243\n"
"v_mfma_f32_16x16x32_bf16 a[216:219], a[140:143], v[168:171], a[216:219]\n"
"ds_read_b64_tr_b16 a[158:159], v235 offset:9216\n"
"v_accvgpr_read_b32 v226, a166\n"
"v_accvgpr_read_b32 v227, a167\n"
"v_mul_f32 v251, v251, v250\n"
"v_mfma_f32_16x16x32_bf16 a[212:215], a[140:143], v[60:63], a[212:215]\n"
"v_rcp_f32 v254, v254\n"
"v_mul_f32 v242, 0xc1800000, v242\n"
"v_accvgpr_read_b32 v246, a120\n"
"v_mfma_f32_16x16x32_bf16 a[208:211], a[140:143], v[56:59], a[208:211]\n"
"s_waitcnt lgkmcnt(4)\n"
"ds_read_b64_tr_b16 a[128:129], v234 offset:17408\n"
"v_cvt_pk_bf16_f32 v228, v224, v225\n"
"v_fma_f32 v240, v240, s93, s93\n"
"v_mfma_f32_16x16x32_bf16 a[192:195], a[144:147], v[64:67], a[192:195]\n"
"ds_read_b64_tr_b16 a[130:131], v234 offset:25600\n"
"v_accvgpr_read_b32 v247, a124\n"
"v_cvt_pk_bf16_f32 v229, v226, v227\n"
"v_mul_f32 v255, v255, v254\n"
"v_mfma_f32_16x16x32_bf16 a[196:199], a[144:147], v[68:71], a[196:199]\n"
"buffer_load_dwordx4 v236, s[60:63], s85 offen offset:0 lds\n"
"s_add_u32 m0, m0, 0xffffc400\n"
"v_exp_f32 v242, v242\n"
"v_mul_f32 v247, v246, v247\n"
"v_accvgpr_read_b32 v248, a121\n"
"v_mfma_f32_16x16x32_bf16 a[200:203], a[144:147], v[176:179], a[200:203]\n"
"ds_write_b64 v238, v[228:229] offset:1024\n"
"v_cvt_pk_bf16_f32 v230, v251, v255\n"
"v_rcp_f32 v240, v240\n"
"v_mul_f32 v246, 0xc1800000, v246\n"
"v_mfma_f32_16x16x32_bf16 a[204:207], a[144:147], v[180:183], a[204:207]\n"
"s_mov_b32 s85, 0x2c0100\n"
"ds_read_b64_tr_b16 a[132:133], v235 offset:17408\n"
"v_accvgpr_read_b32 v249, a125\n"
"v_fma_f32 v242, v242, s93, s93\n"
"v_mfma_f32_16x16x32_bf16 a[220:223], a[148:151], v[180:183], a[220:223]\n"
"ds_read_b64_tr_b16 a[134:135], v235 offset:25600\n"
"v_mul_f32 v249, v248, v249\n"
"v_mul_f32 v241, v241, v240\n"
"v_exp_f32 v246, v246\n"
"v_accvgpr_read_b32 v250, a122\n"
"v_mfma_f32_16x16x32_bf16 a[216:219], a[148:151], v[176:179], a[216:219]\n"
"v_rcp_f32 v242, v242\n"
"v_mul_f32 v248, 0xc1800000, v248\n"
"v_accvgpr_read_b32 v251, a126\n"
"v_mfma_f32_16x16x32_bf16 a[212:215], a[148:151], v[68:71], a[212:215]\n"
"v_exp_f32 v248, v248\n"
"v_fma_f32 v246, v246, s93, s93\n"
"v_mul_f32 v251, v250, v251\n"
"v_mfma_f32_16x16x32_bf16 a[208:211], a[148:151], v[64:67], a[208:211]\n"
"s_waitcnt lgkmcnt(5)\n"
"ds_read_b64_tr_b16 a[136:137], v234 offset:2048\n"
"v_mul_f32 v243, v243, v242\n"
"v_mul_f32 v250, 0xc1800000, v250\n"
"v_accvgpr_read_b32 v254, a123\n"
"v_mfma_f32_16x16x32_bf16 a[192:195], a[152:155], v[72:75], a[192:195]\n"
"v_rcp_f32 v246, v246\n"
"v_cvt_pk_bf16_f32 v231, v241, v243\n"
"v_fma_f32 v248, v248, s93, s93\n"
"v_accvgpr_read_b32 v255, a127\n"
"v_mfma_f32_16x16x32_bf16 a[196:199], a[152:155], v[76:79], a[196:199]\n"
"buffer_load_dwordx4 v236, s[60:63], s85 offen offset:0 lds\n"
"s_add_u32 m0, m0, 0x4000\n"
"v_accvgpr_read_b32 v224, a168\n"
"v_accvgpr_read_b32 v225, a169\n"
"v_exp_f32 v250, v250\n"
"v_mfma_f32_16x16x32_bf16 a[200:203], a[152:155], v[184:187], a[200:203]\n"
"s_mov_b32 s85, 0x300100\n"
"ds_read_b64_tr_b16 a[138:139], v234 offset:10240\n"
"v_mul_f32 v255, v254, v255\n"
"v_accvgpr_read_b32 v226, a170\n"
"v_accvgpr_read_b32 v227, a171\n"
"v_mfma_f32_16x16x32_bf16 a[204:207], a[152:155], v[188:191], a[204:207]\n"
"v_rcp_f32 v248, v248\n"
"v_mul_f32 v247, v247, v246\n"
"v_mul_f32 v254, 0xc1800000, v254\n"
"v_accvgpr_read_b32 v240, a224\n"
"v_mfma_f32_16x16x32_bf16 a[220:223], a[156:159], v[188:191], a[220:223]\n"
"ds_read_b64_tr_b16 a[140:141], v235 offset:2048\n"
"v_cvt_pk_bf16_f32 v228, v224, v225\n"
"v_fma_f32 v250, v250, s93, s93\n"
"v_accvgpr_read_b32 v241, a228\n"
"v_cvt_pk_bf16_f32 v229, v226, v227\n"
"v_mfma_f32_16x16x32_bf16 a[216:219], a[156:159], v[184:187], a[216:219]\n"
"v_exp_f32 v254, v254\n"
"v_mul_f32 v249, v249, v248\n"
"v_mul_f32 v241, v240, v241\n"
"v_accvgpr_read_b32 v242, a225\n"
"v_mfma_f32_16x16x32_bf16 a[212:215], a[156:159], v[76:79], a[212:215]\n"
"ds_read_b64_tr_b16 a[142:143], v235 offset:10240\n"
"v_cvt_pk_bf16_f32 v232, v247, v249\n"
"v_rcp_f32 v250, v250\n"
"v_mul_f32 v240, 0xc1800000, v240\n"
"v_accvgpr_read_b32 v243, a229\n"
"v_mfma_f32_16x16x32_bf16 a[208:211], a[156:159], v[72:75], a[208:211]\n"
"s_waitcnt lgkmcnt(4)\n"
"v_fma_f32 v254, v254, s93, s93\n"
"v_mul_f32 v243, v242, v243\n"
"v_mul_f32 v251, v251, v250\n"
"v_exp_f32 v240, v240\n"
"v_accvgpr_read_b32 v246, a226\n"
"v_mfma_f32_16x16x32_bf16 a[192:195], a[128:131], v[80:83], a[192:195]\n"
"ds_write_b64 v237, v[228:229] offset:0\n"
"v_mul_f32 v242, 0xc1800000, v242\n"
"v_rcp_f32 v254, v254\n"
"v_accvgpr_read_b32 v247, a230\n"
"v_mfma_f32_16x16x32_bf16 a[196:199], a[128:131], v[84:87], a[196:199]\n"
"buffer_load_dwordx4 v236, s[60:63], s85 offen offset:0 lds\n"
"s_add_u32 m0, m0, 0xffffc400\n"
"v_fma_f32 v240, v240, s93, s93\n"
"v_exp_f32 v242, v242\n"
"v_mul_f32 v247, v246, v247\n"
"v_mfma_f32_16x16x32_bf16 a[200:203], a[128:131], v[192:195], a[200:203]\n"
"s_mov_b32 s85, 0x340100\n"
"ds_read_b64_tr_b16 a[144:145], v234 offset:18432\n"
"v_mul_f32 v255, v255, v254\n"
"v_mul_f32 v246, 0xc1800000, v246\n"
"v_accvgpr_read_b32 v248, a227\n"
"v_mfma_f32_16x16x32_bf16 a[204:207], a[128:131], v[196:199], a[204:207]\n"
"ds_read_b64_tr_b16 a[146:147], v234 offset:26624\n"
"v_accvgpr_read_b32 v224, a172\n"
"v_accvgpr_read_b32 v225, a173\n"
"v_cvt_pk_bf16_f32 v233, v251, v255\n"
"v_rcp_f32 v240, v240\n"
"v_mfma_f32_16x16x32_bf16 a[220:223], a[132:135], v[196:199], a[220:223]\n"
"v_accvgpr_read_b32 v226, a174\n"
"v_accvgpr_read_b32 v227, a175\n"
"v_fma_f32 v242, v242, s93, s93\n"
"v_accvgpr_read_b32 v249, a231\n"
"v_exp_f32 v246, v246\n"
"v_mfma_f32_16x16x32_bf16 a[216:219], a[132:135], v[192:195], a[216:219]\n"
"ds_read_b64_tr_b16 a[148:149], v235 offset:18432\n"
"v_cvt_pk_bf16_f32 v228, v224, v225\n"
"v_cvt_pk_bf16_f32 v229, v226, v227\n"
"v_accvgpr_read_b32 v224, a176\n"
"v_accvgpr_read_b32 v225, a177\n"
"v_accvgpr_read_b32 v226, a178\n"
"v_accvgpr_read_b32 v227, a179\n"
"v_mul_f32 v249, v248, v249\n"
"v_mul_f32 v241, v241, v240\n"
"v_rcp_f32 v242, v242\n"
"v_mfma_f32_16x16x32_bf16 a[212:215], a[132:135], v[84:87], a[212:215]\n"
"ds_read_b64_tr_b16 a[150:151], v235 offset:26624\n"
"ds_write_b64 v237, v[228:229] offset:1024\n"
"v_cvt_pk_bf16_f32 v228, v224, v225\n"
"v_cvt_pk_bf16_f32 v229, v226, v227\n"
"v_accvgpr_read_b32 v224, a180\n"
"v_accvgpr_read_b32 v225, a181\n"
"v_accvgpr_read_b32 v226, a182\n"
"v_accvgpr_read_b32 v227, a183\n"
"v_mul_f32 v248, 0xc1800000, v248\n"
"v_accvgpr_read_b32 v250, a232\n"
"v_fma_f32 v246, v246, s93, s93\n"
"v_mfma_f32_16x16x32_bf16 a[208:211], a[132:135], v[80:83], a[208:211]\n"
"s_waitcnt lgkmcnt(6)\n"
"ds_write_b64 v238, v[228:229] offset:512\n"
"buffer_store_dwordx4 v[230:233], v239, s[56:59], s69 offen offset:2048 sc1\n"
"v_cvt_pk_bf16_f32 v228, v224, v225\n"
"v_cvt_pk_bf16_f32 v229, v226, v227\n"
"v_accvgpr_read_b32 v224, a184\n"
"v_accvgpr_read_b32 v225, a185\n"
"v_accvgpr_read_b32 v226, a186\n"
"v_accvgpr_read_b32 v227, a187\n"
"v_accvgpr_read_b32 v251, a236\n"
"v_mul_f32 v243, v243, v242\n"
"v_exp_f32 v248, v248\n"
"v_mfma_f32_16x16x32_bf16 a[192:195], a[136:139], v[88:91], a[192:195]\n"
"ds_write_b64 v238, v[228:229] offset:1536\n"
"v_cvt_pk_bf16_f32 v228, v224, v225\n"
"v_cvt_pk_bf16_f32 v229, v226, v227\n"
"v_accvgpr_read_b32 v224, a188\n"
"v_accvgpr_read_b32 v225, a189\n"
"v_accvgpr_read_b32 v226, a190\n"
"v_accvgpr_read_b32 v227, a191\n"
"v_mul_f32 v251, v250, v251\n"
"v_accvgpr_read_b32 v254, a233\n"
"v_rcp_f32 v246, v246\n"
"v_mfma_f32_16x16x32_bf16 a[196:199], a[136:139], v[92:95], a[196:199]\n"
"ds_write_b64 v237, v[228:229] offset:512\n"
"buffer_load_dwordx4 v236, s[60:63], s85 offen offset:0 lds\n"
"v_cvt_pk_bf16_f32 v228, v224, v225\n"
"v_cvt_pk_bf16_f32 v229, v226, v227\n"
"v_cvt_pk_bf16_f32 v230, v241, v243\n"
"v_mul_f32 v250, 0xc1800000, v250\n"
"v_accvgpr_read_b32 v255, a237\n"
"v_mfma_f32_16x16x32_bf16 a[200:203], a[136:139], v[200:203], a[200:203]\n"
"ds_read_b64_tr_b16 a[152:153], v234 offset:3072\n"
"ds_write_b64 v237, v[228:229] offset:1536\n"
"v_fma_f32 v248, v248, s93, s93\n"
"v_mul_f32 v255, v254, v255\n"
"v_mul_f32 v247, v247, v246\n"
"v_mfma_f32_16x16x32_bf16 a[204:207], a[136:139], v[204:207], a[204:207]\n"
"s_add_u32 m0, m0, 0x7400\n"
"ds_read_b64_tr_b16 a[154:155], v234 offset:11264\n"
"v_exp_f32 v250, v250\n"
"v_mul_f32 v254, 0xc1800000, v254\n"
"v_accvgpr_read_b32 v240, a234\n"
"v_mfma_f32_16x16x32_bf16 a[220:223], a[140:143], v[204:207], a[220:223]\n"
"s_mov_b32 s85, 0x180\n"
"ds_read_b64_tr_b16 a[156:157], v235 offset:3072\n"
"v_rcp_f32 v248, v248\n"
"v_accvgpr_read_b32 v241, a238\n"
"v_fma_f32 v250, v250, s93, s93\n"
"v_mfma_f32_16x16x32_bf16 a[216:219], a[140:143], v[200:203], a[216:219]\n"
"ds_read_b64_tr_b16 a[158:159], v235 offset:11264\n"
"s_waitcnt vmcnt(10) lgkmcnt(3)\n"
"s_barrier\n"
"v_exp_f32 v254, v254\n"
"v_mul_f32 v241, v240, v241\n"
"v_mul_f32 v249, v249, v248\n"
"v_mfma_f32_16x16x32_bf16 a[212:215], a[140:143], v[92:95], a[212:215]\n"
"ds_read_b128 a[0:3], v244 offset:0\n"
"v_mul_f32 v240, 0xc1800000, v240\n"
"v_accvgpr_read_b32 v242, a235\n"
"v_cvt_pk_bf16_f32 v231, v247, v249\n"
"v_rcp_f32 v250, v250\n"
"v_mfma_f32_16x16x32_bf16 a[208:211], a[140:143], v[88:91], a[208:211]\n"
"s_waitcnt lgkmcnt(10)\n"
"ds_read_b128 a[32:35], v244 offset:1024\n"
"v_fma_f32 v254, v254, s93, s93\n"
"v_accvgpr_read_b32 v243, a239\n"
"v_exp_f32 v240, v240\n"
"v_mfma_f32_16x16x32_bf16 a[192:195], a[144:147], v[96:99], a[192:195]\n"
"ds_read_b128 a[64:67], v245 offset:0\n"
"v_mul_f32 v243, v242, v243\n"
"v_mul_f32 v251, v251, v250\n"
"v_rcp_f32 v254, v254\n"
"v_mfma_f32_16x16x32_bf16 a[196:199], a[144:147], v[100:103], a[196:199]\n"
"buffer_load_dwordx4 v236, s[60:63], s85 offen offset:0 lds\n"
"s_add_u32 m0, m0, 0x4000\n"
"v_mul_f32 v242, 0xc1800000, v242\n"
"v_fma_f32 v240, v240, s93, s93\n"
"v_mfma_f32_16x16x32_bf16 a[200:203], a[144:147], v[208:211], a[200:203]\n"
"s_mov_b32 s85, 0x40180\n"
"v_add_u32 v234, 0xfffe0000, v234\n"
"v_mul_f32 v255, v255, v254\n"
"v_exp_f32 v242, v242\n"
"v_rcp_f32 v240, v240\n"
"v_mfma_f32_16x16x32_bf16 a[204:207], a[144:147], v[212:215], a[204:207]\n"
"ds_read_b64_tr_b16 a[128:129], v234 offset:0\n"
"v_cvt_pk_bf16_f32 v232, v251, v255\n"
"v_fma_f32 v242, v242, s93, s93\n"
"v_mul_f32 v241, v241, v240\n"
"v_mfma_f32_16x16x32_bf16 a[220:223], a[148:151], v[212:215], a[220:223]\n"
"ds_read_b64_tr_b16 a[130:131], v234 offset:8192\n"
"v_add_u32 v235, 0xfffe0000, v235\n"
"v_rcp_f32 v242, v242\n"
"v_mfma_f32_16x16x32_bf16 a[216:219], a[148:151], v[208:211], a[216:219]\n"
"ds_read_b64_tr_b16 a[132:133], v235 offset:0\n"
"v_mul_f32 v243, v243, v242\n"
"v_mfma_f32_16x16x32_bf16 a[212:215], a[148:151], v[100:103], a[212:215]\n"
"ds_read_b64_tr_b16 a[134:135], v235 offset:8192\n"
"v_cvt_pk_bf16_f32 v233, v241, v243\n"
"v_mfma_f32_16x16x32_bf16 a[208:211], a[148:151], v[96:99], a[208:211]\n"
"s_waitcnt lgkmcnt(7)\n"
"buffer_store_dwordx4 v[230:233], v239, s[56:59], s70 offen offset:0 sc1\n"
"v_mfma_f32_16x16x32_bf16 a[192:195], a[152:155], v[104:107], a[192:195]\n"
"buffer_load_dwordx4 v236, s[60:63], s85 offen offset:0 lds\n"
"s_add_u32 m0, m0, 0xffffc400\n"
"v_mfma_f32_16x16x32_bf16 a[196:199], a[152:155], v[108:111], a[196:199]\n"
"s_mov_b32 s85, 0x80180\n"
"ds_read_b64_tr_b16 a[136:137], v234 offset:16384\n"
"v_mfma_f32_16x16x32_bf16 a[200:203], a[152:155], v[216:219], a[200:203]\n"
"s_mov_b32 s89, 384\n"
"ds_read_b64_tr_b16 a[138:139], v234 offset:24576\n"
"v_mfma_f32_16x16x32_bf16 a[204:207], a[152:155], v[220:223], a[204:207]\n"
"ds_read_b64_tr_b16 a[140:141], v235 offset:16384\n"
"v_lshrrev_b32 v240, 9, v253\n"
"v_mfma_f32_16x16x32_bf16 a[220:223], a[156:159], v[220:223], a[220:223]\n"
"ds_read_b64_tr_b16 a[142:143], v235 offset:24576\n"
"v_mad_u32_u24 v240, v240, s89, v253\n"
"v_mfma_f32_16x16x32_bf16 a[216:219], a[156:159], v[216:219], a[216:219]\n"
"s_lshl_b32 s89, s84, 7\n"
"ds_read_b128 a[96:99], v245 offset:1024\n"
"v_mfma_f32_16x16x32_bf16 a[212:215], a[156:159], v[108:111], a[212:215]\n"
"buffer_load_dwordx4 v236, s[60:63], s85 offen offset:0 lds\n"
"s_add_u32 m0, m0, 0x4000\n"
"v_mfma_f32_16x16x32_bf16 a[208:211], a[156:159], v[104:107], a[208:211]\n"
"s_waitcnt lgkmcnt(5)\n"
"ds_read_b64_tr_b16 a[144:145], v234 offset:1024\n"
"v_mfma_f32_16x16x32_bf16 a[160:163], a[128:131], v[0:3], 0\n"
"s_mov_b32 s85, 0xc0180\n"
"s_lshl_b32 s90, s87, 3\n"
"v_mfma_f32_16x16x32_bf16 a[164:167], a[128:131], v[4:7], 0\n"
"s_add_u32 s89, s89, s90\n"
"ds_read_b128 a[4:7], v244 offset:4096\n"
"v_mfma_f32_16x16x32_bf16 a[168:171], a[128:131], v[112:115], 0\n"
"ds_read_b64_tr_b16 a[146:147], v234 offset:9216\n"
"v_mov_b32 v246, s89\n"
"v_mfma_f32_16x16x32_bf16 a[172:175], a[128:131], v[116:119], 0\n"
"buffer_load_dwordx4 v236, s[60:63], s85 offen offset:0 lds\n"
"s_add_u32 m0, m0, 0xffffc400\n"
"v_mfma_f32_16x16x32_bf16 a[188:191], a[132:135], v[116:119], 0\n"
"s_mov_b32 s85, 0x100180\n"
"ds_read_b64_tr_b16 a[148:149], v235 offset:1024\n"
"v_mfma_f32_16x16x32_bf16 a[184:187], a[132:135], v[112:115], 0\n"
"ds_read_b128 a[36:39], v244 offset:5120\n"
"v_mov_b32 v248, s96\n"
"v_mfma_f32_16x16x32_bf16 a[180:183], a[132:135], v[4:7], 0\n"
"ds_read_b64_tr_b16 a[150:151], v235 offset:9216\n"
"v_mov_b32 v249, s97\n"
"v_mfma_f32_16x16x32_bf16 a[176:179], a[132:135], v[0:3], 0\n"
"s_waitcnt lgkmcnt(7)\n"
"ds_read_b64_tr_b16 a[152:153], v234 offset:17408\n"
"v_mfma_f32_16x16x32_bf16 a[160:163], a[136:139], v[8:11], a[160:163]\n"
"buffer_load_dwordx4 v236, s[60:63], s85 offen offset:0 lds\n"
"s_add_u32 m0, m0, 0x4000\n"
"v_mfma_f32_16x16x32_bf16 a[164:167], a[136:139], v[12:15], a[164:167]\n"
"s_mov_b32 s85, 0x140180\n"
"ds_read_b128 a[68:71], v245 offset:4096\n"
"v_mfma_f32_16x16x32_bf16 a[168:171], a[136:139], v[120:123], a[168:171]\n"
"ds_read_b64_tr_b16 a[154:155], v234 offset:25600\n"
"v_mbcnt_lo_u32_b32 v247, -1, 0\n"
"v_mfma_f32_16x16x32_bf16 a[172:175], a[136:139], v[124:127], a[172:175]\n"
"ds_read_b64_tr_b16 a[156:157], v235 offset:17408\n"
"v_mbcnt_hi_u32_b32 v247, -1, v247\n"
"v_mfma_f32_16x16x32_bf16 a[188:191], a[140:143], v[124:127], a[188:191]\n"
"ds_read_b64_tr_b16 a[158:159], v235 offset:25600\n"
"v_and_b32 v247, 15, v247\n"
"v_mfma_f32_16x16x32_bf16 a[184:187], a[140:143], v[120:123], a[184:187]\n"
"s_lshr_b32 s89, s87, 2\n"
"ds_read_b128 a[100:103], v245 offset:5120\n"
"v_mfma_f32_16x16x32_bf16 a[180:183], a[140:143], v[12:15], a[180:183]\n"
"buffer_load_dwordx4 v236, s[60:63], s85 offen offset:0 lds\n"
"s_add_u32 m0, m0, 0xffffc400\n"
"v_mfma_f32_16x16x32_bf16 a[176:179], a[140:143], v[8:11], a[176:179]\n"
"s_waitcnt lgkmcnt(6)\n"
"ds_read_b64_tr_b16 a[128:129], v234 offset:2048\n"
"v_mfma_f32_16x16x32_bf16 a[160:163], a[144:147], v[16:19], a[160:163]\n"
"s_mov_b32 s85, 0x180180\n"
"ds_read_b64_tr_b16 a[130:131], v234 offset:10240\n"
"v_mfma_f32_16x16x32_bf16 a[164:167], a[144:147], v[20:23], a[164:167]\n"
"s_lshl_b32 s89, s89, 7\n"
"ds_read_b64_tr_b16 a[132:133], v235 offset:2048\n"
"v_mfma_f32_16x16x32_bf16 a[168:171], a[144:147], v[128:131], a[168:171]\n"
"ds_read_b64_tr_b16 a[134:135], v235 offset:10240\n"
"v_lshl_add_u32 v247, v247, 3, s89\n"
"v_mfma_f32_16x16x32_bf16 a[172:175], a[144:147], v[132:135], a[172:175]\n"
"buffer_load_dwordx4 v236, s[60:63], s85 offen offset:0 lds\n"
"s_add_u32 m0, m0, 0xf400\n"
"v_accvgpr_read_b32 v224, a192\n"
"v_mfma_f32_16x16x32_bf16 a[188:191], a[148:151], v[132:135], a[188:191]\n"
"s_mov_b32 s85, 0x1c0180\n"
"ds_read_b64_tr_b16 a[136:137], v234 offset:18432\n"
"v_accvgpr_read_b32 v225, a193\n"
"v_mfma_f32_16x16x32_bf16 a[184:187], a[148:151], v[128:131], a[184:187]\n"
"ds_read_b64_tr_b16 a[138:139], v234 offset:26624\n"
"v_mbcnt_lo_u32_b32 v247, -1, 0\n"
"v_accvgpr_read_b32 v226, a194\n"
"v_mfma_f32_16x16x32_bf16 a[180:183], a[148:151], v[20:23], a[180:183]\n"
"ds_read_b64_tr_b16 a[140:141], v235 offset:18432\n"
"v_mbcnt_hi_u32_b32 v247, -1, v247\n"
"v_accvgpr_read_b32 v227, a195\n"
"v_mfma_f32_16x16x32_bf16 a[176:179], a[148:151], v[16:19], a[176:179]\n"
"s_waitcnt lgkmcnt(8)\n"
"ds_read_b64_tr_b16 a[142:143], v235 offset:26624\n"
"v_cvt_pk_bf16_f32 v228, v224, v225\n"
"v_mfma_f32_16x16x32_bf16 a[160:163], a[152:155], v[24:27], a[160:163]\n"
"ds_read_b64_tr_b16 a[144:145], v234 offset:3072\n"
"v_bfe_u32 v250, v247, 2, 1\n"
"v_cvt_pk_bf16_f32 v229, v226, v227\n"
"v_mfma_f32_16x16x32_bf16 a[164:167], a[152:155], v[28:31], a[164:167]\n"
"ds_write_b64 v238, v[228:229] offset:16384\n"
"v_mfma_f32_16x16x32_bf16 a[168:171], a[152:155], v[136:139], a[168:171]\n"
"ds_read_b64_tr_b16 a[146:147], v234 offset:11264\n"
"v_and_b32 v247, 3, v247\n"
"v_mfma_f32_16x16x32_bf16 a[172:175], a[152:155], v[140:143], a[172:175]\n"
"ds_read_b64_tr_b16 a[148:149], v235 offset:3072\n"
"v_lshlrev_b32 v247, 3, v247\n"
"v_mfma_f32_16x16x32_bf16 a[188:191], a[156:159], v[140:143], a[188:191]\n"
"ds_read_b64_tr_b16 a[150:151], v235 offset:11264\n"
"v_lshl_add_u32 v247, v250, 7, v247\n"
"v_mfma_f32_16x16x32_bf16 a[184:187], a[156:159], v[136:139], a[184:187]\n"
"s_and_b32 s89, s84, 2\n"
"s_lshl_b32 s89, s89, 7\n"
"s_and_b32 s90, s87, 12\n"
"v_accvgpr_read_b32 v224, a196\n"
"v_mfma_f32_16x16x32_bf16 a[180:183], a[156:159], v[28:31], a[180:183]\n"
"s_lshl_b32 s90, s90, 3\n"
"s_add_u32 s89, s89, s90\n"
"v_add_u32 v247, s89, v247\n"
"v_accvgpr_read_b32 v225, a197\n"
"v_mfma_f32_16x16x32_bf16 a[176:179], a[156:159], v[24:27], a[176:179]\n"
"s_waitcnt lgkmcnt(9)\n"
"s_mul_i32 s89, s84, 114688\n"
"s_lshr_b32 s90, s87, 2\n"
"v_accvgpr_read_b32 v226, a198\n"
"v_mfma_f32_16x16x32_bf16 a[160:163], a[128:131], v[32:35], a[160:163]\n"
"s_mul_i32 s90, s90, 28672\n"
"s_add_u32 s68, s89, s90\n"
"v_accvgpr_read_b32 v227, a199\n"
"v_cvt_pk_bf16_f32 v228, v224, v225\n"
"v_mfma_f32_16x16x32_bf16 a[164:167], a[128:131], v[36:39], a[164:167]\n"
"v_cvt_pk_bf16_f32 v229, v226, v227\n"
"s_waitcnt vmcnt(6)\n"
"s_mov_b64 exec, 1\n"
"global_store_dwordx2 v246, v[248:249], s[94:95] offset:0 sc1\n"
"s_mov_b64 exec, -1\n"
"v_mfma_f32_16x16x32_bf16 a[168:171], a[128:131], v[144:147], a[168:171]\n"
"ds_write_b64 v238, v[228:229] offset:17408\n"
"v_mfma_f32_16x16x32_bf16 a[172:175], a[128:131], v[148:151], a[172:175]\n"
"v_accvgpr_read_b32 v224, a200\n"
"v_accvgpr_read_b32 v225, a201\n"
"v_mfma_f32_16x16x32_bf16 a[188:191], a[132:135], v[148:151], a[188:191]\n"
"v_mbcnt_lo_u32_b32 v254, -1, 0\n"
"v_accvgpr_read_b32 v226, a202\n"
"v_mfma_f32_16x16x32_bf16 a[184:187], a[132:135], v[144:147], a[184:187]\n"
"s_waitcnt vmcnt(9) lgkmcnt(14)\n"
"s_barrier\n"
"v_mbcnt_hi_u32_b32 v254, -1, v254\n"
"v_mfma_f32_16x16x32_bf16 a[180:183], a[132:135], v[36:39], a[180:183]\n"
"ds_read_b64_tr_b16 a[152:153], v234 offset:32768\n"
"v_bfe_u32 v248, v254, 2, 3\n"
"v_mfma_f32_16x16x32_bf16 a[176:179], a[132:135], v[32:35], a[176:179]\n"
"s_waitcnt lgkmcnt(7)\n"
"ds_read_b64_tr_b16 a[154:155], v234 offset:40960\n"
"v_mfma_f32_16x16x32_bf16 a[160:163], a[136:139], v[40:43], a[160:163]\n"
"ds_read_b64_tr_b16 a[156:157], v235 offset:32768\n"
"v_lshrrev_b32 v249, 5, v254\n"
"v_mfma_f32_16x16x32_bf16 a[164:167], a[136:139], v[44:47], a[164:167]\n"
"buffer_load_dwordx4 v236, s[60:63], s85 offen offset:0 lds\n"
"s_add_u32 m0, m0, 0x4000\n"
"v_accvgpr_read_b32 v227, a203\n"
"v_mfma_f32_16x16x32_bf16 a[168:171], a[136:139], v[152:155], a[168:171]\n"
"s_mov_b32 s85, 0x200180\n"
"ds_read_b64_tr_b16 a[158:159], v235 offset:40960\n"
"v_cvt_pk_bf16_f32 v228, v224, v225\n"
"v_mfma_f32_16x16x32_bf16 a[172:175], a[136:139], v[156:159], a[172:175]\n"
"ds_read_b64_tr_b16 a[128:129], v234 offset:49152\n"
"v_lshlrev_b32 v249, 12, v249\n"
"v_cvt_pk_bf16_f32 v229, v226, v227\n"
"v_mfma_f32_16x16x32_bf16 a[188:191], a[140:143], v[156:159], a[188:191]\n"
"ds_write_b64 v237, v[228:229] offset:16384\n"
"v_mfma_f32_16x16x32_bf16 a[184:187], a[140:143], v[152:155], a[184:187]\n"
"ds_read_b64_tr_b16 a[130:131], v234 offset:57344\n"
"v_lshl_add_u32 v249, v248, 7, v249\n"
"v_mfma_f32_16x16x32_bf16 a[180:183], a[140:143], v[44:47], a[180:183]\n"
"ds_read_b64_tr_b16 a[132:133], v235 offset:49152\n"
"v_and_b32 v255, 3, v254\n"
"v_mfma_f32_16x16x32_bf16 a[176:179], a[140:143], v[40:43], a[176:179]\n"
"s_waitcnt lgkmcnt(9)\n"
"ds_read_b64_tr_b16 a[134:135], v235 offset:57344\n"
"v_mfma_f32_16x16x32_bf16 a[160:163], a[144:147], v[48:51], a[160:163]\n"
"ds_read_b64_tr_b16 a[136:137], v234 offset:33792\n"
"v_lshl_add_u32 v249, v255, 3, v249\n"
"v_mfma_f32_16x16x32_bf16 a[164:167], a[144:147], v[52:55], a[164:167]\n"
"buffer_load_dwordx4 v236, s[60:63], s85 offen offset:0 lds\n"
"s_add_u32 m0, m0, 0xffffc400\n"
"v_mfma_f32_16x16x32_bf16 a[168:171], a[144:147], v[160:163], a[168:171]\n"
"s_mov_b32 s85, 0x240180\n"
"ds_read_b64_tr_b16 a[138:139], v234 offset:41984\n"
"v_accvgpr_read_b32 v224, a204\n"
"v_mfma_f32_16x16x32_bf16 a[172:175], a[144:147], v[164:167], a[172:175]\n"
"ds_read_b64_tr_b16 a[140:141], v235 offset:33792\n"
"v_lshrrev_b32 v248, 1, v248\n"
"v_accvgpr_read_b32 v225, a205\n"
"v_mfma_f32_16x16x32_bf16 a[188:191], a[148:151], v[164:167], a[188:191]\n"
"s_and_b32 s89, s87, 1\n"
"ds_read_b64_tr_b16 a[142:143], v235 offset:41984\n"
"v_accvgpr_read_b32 v226, a206\n"
"v_mfma_f32_16x16x32_bf16 a[184:187], a[148:151], v[160:163], a[184:187]\n"
"s_add_u32 s89, s89, 0\n"
"ds_read_b64_tr_b16 a[144:145], v234 offset:50176\n"
"v_accvgpr_read_b32 v227, a207\n"
"v_mfma_f32_16x16x32_bf16 a[180:183], a[148:151], v[52:55], a[180:183]\n"
"s_and_b32 s90, s89, 3\n"
"ds_read_b64_tr_b16 a[146:147], v234 offset:58368\n"
"v_cvt_pk_bf16_f32 v228, v224, v225\n"
"v_mfma_f32_16x16x32_bf16 a[176:179], a[148:151], v[48:51], a[176:179]\n"
"s_waitcnt lgkmcnt(11)\n"
"buffer_load_dwordx4 v236, s[60:63], s85 offen offset:0 lds\n"
"v_cvt_pk_bf16_f32 v229, v226, v227\n"
"v_mfma_f32_16x16x32_bf16 a[160:163], a[152:155], v[56:59], a[160:163]\n"
"ds_write_b64 v237, v[228:229] offset:17408\n"
"v_mfma_f32_16x16x32_bf16 a[164:167], a[152:155], v[60:63], a[164:167]\n"
"s_add_u32 m0, m0, 0x4000\n"
"ds_read_b64_tr_b16 a[148:149], v235 offset:50176\n"
"v_mfma_f32_16x16x32_bf16 a[168:171], a[152:155], v[168:171], a[168:171]\n"
"s_mov_b32 s85, 0x280180\n"
"ds_read_b64_tr_b16 a[150:151], v235 offset:58368\n"
"v_mfma_f32_16x16x32_bf16 a[172:175], a[152:155], v[172:175], a[172:175]\n"
"s_lshr_b32 s89, s89, 2\n"
"s_lshl_b32 s89, s89, 10\n"
"v_xor_b32 v255, s90, v248\n"
"v_mfma_f32_16x16x32_bf16 a[188:191], a[156:159], v[172:175], a[188:191]\n"
"buffer_load_dwordx4 v236, s[60:63], s85 offen offset:0 lds\n"
"s_add_u32 m0, m0, 0xffffc400\n"
"v_mfma_f32_16x16x32_bf16 a[184:187], a[156:159], v[168:171], a[184:187]\n"
"s_mov_b32 s85, 0x2c0180\n"
"ds_read_b64_tr_b16 a[152:153], v234 offset:34816\n"
"v_accvgpr_read_b32 v224, a208\n"
"v_mfma_f32_16x16x32_bf16 a[180:183], a[156:159], v[60:63], a[180:183]\n"
"ds_read_b64_tr_b16 a[154:155], v234 offset:43008\n"
"v_lshl_add_u32 v255, v255, 5, v249\n"
"v_accvgpr_read_b32 v225, a209\n"
"v_mfma_f32_16x16x32_bf16 a[176:179], a[156:159], v[56:59], a[176:179]\n"
"s_waitcnt lgkmcnt(11)\n"
"v_add_u32 v230, s89, v255\n"
"s_and_b32 s89, s87, 1\n"
"v_accvgpr_read_b32 v226, a210\n"
"v_mfma_f32_16x16x32_bf16 a[160:163], a[128:131], v[64:67], a[160:163]\n"
"s_add_u32 s89, s89, 1\n"
"ds_read_b64_tr_b16 a[156:157], v235 offset:34816\n"
"v_accvgpr_read_b32 v227, a211\n"
"v_mfma_f32_16x16x32_bf16 a[164:167], a[128:131], v[68:71], a[164:167]\n"
"buffer_load_dwordx4 v236, s[60:63], s85 offen offset:0 lds\n"
"s_add_u32 m0, m0, 0x4000\n"
"v_cvt_pk_bf16_f32 v228, v224, v225\n"
"v_mfma_f32_16x16x32_bf16 a[168:171], a[128:131], v[176:179], a[168:171]\n"
"s_mov_b32 s85, 0x300180\n"
"ds_read_b64_tr_b16 a[158:159], v235 offset:43008\n"
"v_cvt_pk_bf16_f32 v229, v226, v227\n"
"v_mfma_f32_16x16x32_bf16 a[172:175], a[128:131], v[180:183], a[172:175]\n"
"ds_write_b64 v238, v[228:229] offset:16896\n"
"v_mfma_f32_16x16x32_bf16 a[188:191], a[132:135], v[180:183], a[188:191]\n"
"s_and_b32 s90, s89, 3\n"
"ds_read_b64_tr_b16 a[128:129], v234 offset:51200\n"
"v_mfma_f32_16x16x32_bf16 a[184:187], a[132:135], v[176:179], a[184:187]\n"
"s_lshr_b32 s89, s89, 2\n"
"ds_read_b64_tr_b16 a[130:131], v234 offset:59392\n"
"v_mfma_f32_16x16x32_bf16 a[180:183], a[132:135], v[68:71], a[180:183]\n"
"buffer_load_dwordx4 v236, s[60:63], s85 offen offset:0 lds\n"
"s_add_u32 m0, m0, 0xffffc400\n"
"v_mfma_f32_16x16x32_bf16 a[176:179], a[132:135], v[64:67], a[176:179]\n"
"s_waitcnt lgkmcnt(12)\n"
"s_mov_b32 s85, 0x340180\n"
"s_lshl_b32 s89, s89, 10\n"
"v_accvgpr_read_b32 v224, a212\n"
"v_mfma_f32_16x16x32_bf16 a[160:163], a[136:139], v[72:75], a[160:163]\n"
"ds_read_b64_tr_b16 a[132:133], v235 offset:51200\n"
"v_xor_b32 v255, s90, v248\n"
"v_accvgpr_read_b32 v225, a213\n"
"v_mfma_f32_16x16x32_bf16 a[164:167], a[136:139], v[76:79], a[164:167]\n"
"ds_read_b64_tr_b16 a[134:135], v235 offset:59392\n"
"v_lshl_add_u32 v255, v255, 5, v249\n"
"v_accvgpr_read_b32 v226, a214\n"
"v_mfma_f32_16x16x32_bf16 a[168:171], a[136:139], v[184:187], a[168:171]\n"
"v_add_u32 v231, s89, v255\n"
"s_and_b32 s89, s87, 1\n"
"v_accvgpr_read_b32 v227, a215\n"
"v_mfma_f32_16x16x32_bf16 a[172:175], a[136:139], v[188:191], a[172:175]\n"
"buffer_load_dwordx4 v236, s[60:63], s85 offen offset:0 lds\n"
"s_add_u32 m0, m0, 0xfffdf400\n"
"v_cvt_pk_bf16_f32 v228, v224, v225\n"
"v_mfma_f32_16x16x32_bf16 a[188:191], a[140:143], v[188:191], a[188:191]\n"
"s_mov_b32 s85, 0x0\n"
"ds_read_b64_tr_b16 a[136:137], v234 offset:35840\n"
"v_cvt_pk_bf16_f32 v229, v226, v227\n"
"v_mfma_f32_16x16x32_bf16 a[184:187], a[140:143], v[184:187], a[184:187]\n"
"ds_write_b64 v238, v[228:229] offset:17920\n"
"v_mfma_f32_16x16x32_bf16 a[180:183], a[140:143], v[76:79], a[180:183]\n"
"s_add_u32 s89, s89, 2\n"
"ds_read_b64_tr_b16 a[138:139], v234 offset:44032\n"
"v_mfma_f32_16x16x32_bf16 a[176:179], a[140:143], v[72:75], a[176:179]\n"
"s_waitcnt lgkmcnt(12)\n"
"s_and_b32 s90, s89, 3\n"
"s_lshr_b32 s89, s89, 2\n"
"v_mfma_f32_16x16x32_bf16 a[160:163], a[144:147], v[80:83], a[160:163]\n"
"s_lshl_b32 s89, s89, 10\n"
"ds_read_b64_tr_b16 a[140:141], v235 offset:35840\n"
"v_mfma_f32_16x16x32_bf16 a[164:167], a[144:147], v[84:87], a[164:167]\n"
"ds_read_b64_tr_b16 a[142:143], v235 offset:44032\n"
"v_xor_b32 v255, s90, v248\n"
"v_mfma_f32_16x16x32_bf16 a[168:171], a[144:147], v[192:195], a[168:171]\n"
"v_lshl_add_u32 v255, v255, 5, v249\n"
"v_accvgpr_read_b32 v224, a216\n"
"v_accvgpr_read_b32 v225, a217\n"
"v_mfma_f32_16x16x32_bf16 a[172:175], a[144:147], v[196:199], a[172:175]\n"
"v_add_u32 v232, s89, v255\n"
"s_and_b32 s89, s87, 1\n"
"v_accvgpr_read_b32 v226, a218\n"
"v_mfma_f32_16x16x32_bf16 a[188:191], a[148:151], v[196:199], a[188:191]\n"
"s_add_u32 s89, s89, 3\n"
"s_and_b32 s90, s89, 3\n"
"s_lshr_b32 s89, s89, 2\n"
"v_accvgpr_read_b32 v227, a219\n"
"v_mfma_f32_16x16x32_bf16 a[184:187], a[148:151], v[192:195], a[184:187]\n"
"s_lshl_b32 s89, s89, 10\n"
"v_xor_b32 v255, s90, v248\n"
"v_cvt_pk_bf16_f32 v228, v224, v225\n"
"v_cvt_pk_bf16_f32 v229, v226, v227\n"
"v_mfma_f32_16x16x32_bf16 a[180:183], a[148:151], v[84:87], a[180:183]\n"
"ds_write_b64 v237, v[228:229] offset:16896\n"
"v_mfma_f32_16x16x32_bf16 a[176:179], a[148:151], v[80:83], a[176:179]\n"
"s_waitcnt lgkmcnt(11)\n"
"v_lshl_add_u32 v255, v255, 5, v249\n"
"v_accvgpr_read_b32 v224, a220\n"
"v_mfma_f32_16x16x32_bf16 a[160:163], a[152:155], v[88:91], a[160:163]\n"
"v_accvgpr_read_b32 v225, a221\n"
"v_accvgpr_read_b32 v226, a222\n"
"v_accvgpr_read_b32 v227, a223\n"
"v_mfma_f32_16x16x32_bf16 a[164:167], a[152:155], v[92:95], a[164:167]\n"
"v_cvt_pk_bf16_f32 v228, v224, v225\n"
"v_cvt_pk_bf16_f32 v229, v226, v227\n"
"v_mfma_f32_16x16x32_bf16 a[168:171], a[152:155], v[200:203], a[168:171]\n"
"ds_write_b64 v237, v[228:229] offset:17920\n"
"v_mfma_f32_16x16x32_bf16 a[172:175], a[152:155], v[204:207], a[172:175]\n"
"v_add_u32 v233, s89, v255\n"
"s_and_b32 s89, s87, 1\n"
"v_mfma_f32_16x16x32_bf16 a[188:191], a[156:159], v[204:207], a[188:191]\n"
"s_add_u32 s89, s89, 4\n"
"s_and_b32 s90, s89, 3\n"
"s_lshr_b32 s89, s89, 2\n"
"v_mfma_f32_16x16x32_bf16 a[184:187], a[156:159], v[200:203], a[184:187]\n"
"s_waitcnt vmcnt(8) lgkmcnt(0)\n"
"s_barrier\n"
"s_lshl_b32 s89, s89, 10\n"
"v_mfma_f32_16x16x32_bf16 a[180:183], a[156:159], v[92:95], a[180:183]\n"
"ds_read_b128 a[8:11], v244 offset:16384\n"
"v_xor_b32 v255, s90, v248\n"
"v_mfma_f32_16x16x32_bf16 a[176:179], a[156:159], v[88:91], a[176:179]\n"
"s_waitcnt lgkmcnt(8)\n"
"ds_read_b128 a[40:43], v244 offset:17408\n"
"v_mfma_f32_16x16x32_bf16 a[160:163], a[128:131], v[96:99], a[160:163]\n"
"ds_read_b128 a[72:75], v245 offset:16384\n"
"v_lshl_add_u32 v255, v255, 5, v249\n"
"v_mfma_f32_16x16x32_bf16 a[164:167], a[128:131], v[100:103], a[164:167]\n"
"buffer_load_dwordx4 v240, s[72:75], s85 offen offset:0 lds\n"
"s_add_u32 m0, m0, 0x380\n"
"v_mfma_f32_16x16x32_bf16 a[168:171], a[128:131], v[208:211], a[168:171]\n"
"v_add_u32 v234, 0x10000, v234\n"
"v_add_u32 v241, s89, v255\n"
"v_mfma_f32_16x16x32_bf16 a[172:175], a[128:131], v[212:215], a[172:175]\n"
"s_and_b32 s89, s87, 1\n"
"ds_read_b64_tr_b16 a[144:145], v234 offset:0\n"
"v_mfma_f32_16x16x32_bf16 a[188:191], a[132:135], v[212:215], a[188:191]\n"
"ds_read_b64_tr_b16 a[146:147], v234 offset:8192\n"
"v_add_u32 v235, 0x10000, v235\n"
"v_mfma_f32_16x16x32_bf16 a[184:187], a[132:135], v[208:211], a[184:187]\n"
"s_add_u32 s89, s89, 5\n"
"ds_read_b64_tr_b16 a[148:149], v235 offset:0\n"
"v_mfma_f32_16x16x32_bf16 a[180:183], a[132:135], v[100:103], a[180:183]\n"
"s_and_b32 s90, s89, 3\n"
"ds_read_b64_tr_b16 a[150:151], v235 offset:8192\n"
"v_mfma_f32_16x16x32_bf16 a[176:179], a[132:135], v[96:99], a[176:179]\n"
"s_waitcnt lgkmcnt(9)\n"
"buffer_load_dwordx4 v240, s[72:75], s85 offen offset:128 lds\n"
"v_mfma_f32_16x16x32_bf16 a[160:163], a[136:139], v[104:107], a[160:163]\n"
"s_add_u32 m0, m0, 0x3c80\n"
"ds_read_b64_tr_b16 a[152:153], v234 offset:16384\n"
"v_mfma_f32_16x16x32_bf16 a[164:167], a[136:139], v[108:111], a[164:167]\n"
"s_mov_b32 s85, 0x7000\n"
"ds_read_b64_tr_b16 a[154:155], v234 offset:24576\n"
"v_mfma_f32_16x16x32_bf16 a[168:171], a[136:139], v[216:219], a[168:171]\n"
"s_lshr_b32 s89, s89, 2\n"
"ds_read_b64_tr_b16 a[156:157], v235 offset:16384\n"
"v_mfma_f32_16x16x32_bf16 a[172:175], a[136:139], v[220:223], a[172:175]\n"
"s_lshl_b32 s89, s89, 10\n"
"ds_read_b64_tr_b16 a[158:159], v235 offset:24576\n"
"v_mfma_f32_16x16x32_bf16 a[188:191], a[140:143], v[220:223], a[188:191]\n"
"buffer_load_dwordx4 v240, s[72:75], s85 offen offset:0 lds\n"
"s_add_u32 m0, m0, 0x380\n"
"v_mfma_f32_16x16x32_bf16 a[184:187], a[140:143], v[216:219], a[184:187]\n"
"ds_read_b128 a[104:107], v245 offset:17408\n"
"v_xor_b32 v255, s90, v248\n"
"v_mfma_f32_16x16x32_bf16 a[180:183], a[140:143], v[108:111], a[180:183]\n"
"ds_read_b64_tr_b16 a[128:129], v234 offset:1024\n"
"v_lshl_add_u32 v255, v255, 5, v249\n"
"v_mfma_f32_16x16x32_bf16 a[176:179], a[140:143], v[104:107], a[176:179]\n"
"s_waitcnt lgkmcnt(6)\n"
"ds_read_b64_tr_b16 a[130:131], v234 offset:9216\n"
"v_mfma_f32_16x16x32_bf16 a[192:195], a[144:147], v[0:3], 0\n"
"ds_read_b64_tr_b16 a[132:133], v235 offset:1024\n"
"v_add_u32 v242, s89, v255\n"
"v_mfma_f32_16x16x32_bf16 a[196:199], a[144:147], v[4:7], 0\n"
"s_and_b32 s89, s87, 1\n"
"ds_read_b128 a[12:15], v244 offset:20480\n"
"v_mfma_f32_16x16x32_bf16 a[200:203], a[144:147], v[112:115], 0\n"
"buffer_load_dwordx4 v240, s[72:75], s85 offen offset:128 lds\n"
"s_add_u32 m0, m0, 0xffffc480\n"
"v_mfma_f32_16x16x32_bf16 a[204:207], a[144:147], v[116:119], 0\n"
"s_mov_b32 s85, 0xe000\n"
"ds_read_b64_tr_b16 a[134:135], v235 offset:9216\n"
"v_mfma_f32_16x16x32_bf16 a[220:223], a[148:151], v[116:119], 0\n"
"s_add_u32 s89, s89, 6\n"
"ds_read_b64_tr_b16 a[136:137], v234 offset:17408\n"
"v_mfma_f32_16x16x32_bf16 a[216:219], a[148:151], v[112:115], 0\n"
"s_and_b32 s90, s89, 3\n"
"ds_read_b128 a[44:47], v244 offset:21504\n"
"v_mfma_f32_16x16x32_bf16 a[212:215], a[148:151], v[4:7], 0\n"
"s_lshr_b32 s89, s89, 2\n"
"ds_read_b64_tr_b16 a[138:139], v234 offset:25600\n"
"v_mfma_f32_16x16x32_bf16 a[208:211], a[148:151], v[0:3], 0\n"
"s_waitcnt lgkmcnt(9)\n"
"buffer_load_dwordx4 v240, s[72:75], s85 offen offset:0 lds\n"
"v_mfma_f32_16x16x32_bf16 a[192:195], a[152:155], v[8:11], a[192:195]\n"
"s_add_u32 m0, m0, 0x380\n"
"ds_read_b64_tr_b16 a[140:141], v235 offset:17408\n"
"v_mfma_f32_16x16x32_bf16 a[196:199], a[152:155], v[12:15], a[196:199]\n"
"s_lshl_b32 s89, s89, 10\n"
"ds_read_b128 a[76:79], v245 offset:20480\n"
"v_mfma_f32_16x16x32_bf16 a[200:203], a[152:155], v[120:123], a[200:203]\n"
"global_load_dwordx2 v[250:251], v247, s[94:95] offset:0 sc1\n"
"v_mfma_f32_16x16x32_bf16 a[204:207], a[152:155], v[124:127], a[204:207]\n"
"ds_read_b64_tr_b16 a[142:143], v235 offset:25600\n"
"v_xor_b32 v255, s90, v248\n"
"v_mfma_f32_16x16x32_bf16 a[220:223], a[156:159], v[124:127], a[220:223]\n"
"buffer_load_dwordx4 v240, s[72:75], s85 offen offset:128 lds\n"
"s_add_u32 m0, m0, 0x3c80\n"
"v_mfma_f32_16x16x32_bf16 a[216:219], a[156:159], v[120:123], a[216:219]\n"
"s_mov_b32 s85, 0x15000\n"
"ds_read_b128 a[108:111], v245 offset:21504\n"
"v_mfma_f32_16x16x32_bf16 a[212:215], a[156:159], v[12:15], a[212:215]\n"
"ds_read_b64_tr_b16 a[144:145], v234 offset:2048\n"
"v_lshl_add_u32 v255, v255, 5, v249\n"
"v_mfma_f32_16x16x32_bf16 a[208:211], a[156:159], v[8:11], a[208:211]\n"
"s_waitcnt lgkmcnt(8)\n"
"ds_read_b64_tr_b16 a[146:147], v234 offset:10240\n"
"v_mfma_f32_16x16x32_bf16 a[192:195], a[128:131], v[16:19], a[192:195]\n"
"ds_read_b64_tr_b16 a[148:149], v235 offset:2048\n"
"v_add_u32 v243, s89, v255\n"
"v_mfma_f32_16x16x32_bf16 a[196:199], a[128:131], v[20:23], a[196:199]\n"
"buffer_load_dwordx4 v240, s[72:75], s85 offen offset:0 lds\n"
"s_add_u32 m0, m0, 0x380\n"
"v_mfma_f32_16x16x32_bf16 a[200:203], a[128:131], v[128:131], a[200:203]\n"
"s_lshr_b32 s89, s88, 5\n"
"ds_read_b64_tr_b16 a[150:151], v235 offset:10240\n"
"v_mfma_f32_16x16x32_bf16 a[204:207], a[128:131], v[132:135], a[204:207]\n"
"s_and_b32 s90, s88, 1\n"
"ds_read_b64_tr_b16 a[152:153], v234 offset:18432\n"
"v_accvgpr_read_b32 v224, a160\n"
"v_mfma_f32_16x16x32_bf16 a[220:223], a[132:135], v[132:135], a[220:223]\n"
"s_lshl_b32 s90, s90, 3\n"
"ds_read_b64_tr_b16 a[154:155], v234 offset:26624\n"
"v_accvgpr_read_b32 v225, a161\n"
"v_mfma_f32_16x16x32_bf16 a[216:219], a[132:135], v[128:131], a[216:219]\n"
"s_add_u32 s89, s89, s90\n"
"ds_read_b64_tr_b16 a[156:157], v235 offset:18432\n"
"v_accvgpr_read_b32 v226, a162\n"
"v_mfma_f32_16x16x32_bf16 a[212:215], a[132:135], v[20:23], a[212:215]\n"
"buffer_load_dwordx4 v240, s[72:75], s85 offen offset:128 lds\n"
"s_add_u32 m0, m0, 0x3480\n"
"v_accvgpr_read_b32 v227, a163\n"
"v_mfma_f32_16x16x32_bf16 a[208:211], a[132:135], v[16:19], a[208:211]\n"
"s_waitcnt lgkmcnt(8)\n"
"ds_read_b64_tr_b16 a[158:159], v235 offset:26624\n"
"v_cvt_pk_bf16_f32 v228, v224, v225\n"
"v_mfma_f32_16x16x32_bf16 a[192:195], a[136:139], v[24:27], a[192:195]\n"
"s_mov_b32 s85, 0x1c000\n"
"ds_read_b64_tr_b16 a[128:129], v234 offset:3072\n"
"v_cvt_pk_bf16_f32 v229, v226, v227\n"
"v_mfma_f32_16x16x32_bf16 a[196:199], a[136:139], v[28:31], a[196:199]\n"
"ds_write_b64 v238, v[228:229] offset:0\n"
"v_mfma_f32_16x16x32_bf16 a[200:203], a[136:139], v[136:139], a[200:203]\n"
"s_lshl_b32 s90, s89, 2\n"
"ds_read_b64_tr_b16 a[130:131], v234 offset:11264\n"
"v_mfma_f32_16x16x32_bf16 a[204:207], a[136:139], v[140:143], a[204:207]\n"
"s_add_u32 s90, s90, s84\n"
"ds_read_b64_tr_b16 a[132:133], v235 offset:3072\n"
"v_mfma_f32_16x16x32_bf16 a[220:223], a[140:143], v[140:143], a[220:223]\n"
"s_mul_i32 s90, s90, 114688\n"
"ds_read_b64_tr_b16 a[134:135], v235 offset:11264\n"
"v_mfma_f32_16x16x32_bf16 a[216:219], a[140:143], v[136:139], a[216:219]\n"
"s_lshl_b32 s66, s89, 12\n"
"s_add_u32 s66, s66, 0x1000000\n"
"s_sub_u32 s90, s66, s90\n"
"v_mfma_f32_16x16x32_bf16 a[212:215], a[140:143], v[28:31], a[212:215]\n"
"s_sub_u32 s64, s94, s90\n"
"s_subb_u32 s65, s95, 0\n"
"s_and_b32 s65, s65, 0xffff\n"
"s_mov_b32 s66, 114688\n"
"v_accvgpr_read_b32 v224, a164\n"
"v_mfma_f32_16x16x32_bf16 a[208:211], a[140:143], v[24:27], a[208:211]\n"
"s_waitcnt lgkmcnt(9)\n"
"s_mov_b32 s67, 0x20000\n"
"s_and_b32 s89, s87, 3\n"
"v_accvgpr_read_b32 v225, a165\n"
"v_mfma_f32_16x16x32_bf16 a[192:195], a[144:147], v[32:35], a[192:195]\n"
"s_mul_i32 s89, s89, 28672\n"
"s_lshr_b32 s90, s87, 2\n"
"s_lshl_b32 s90, s90, 10\n"
"v_accvgpr_read_b32 v226, a166\n"
"v_mfma_f32_16x16x32_bf16 a[196:199], a[144:147], v[36:39], a[196:199]\n"
"s_add_u32 s69, s89, s90\n"
"v_accvgpr_read_b32 v227, a167\n"
"v_cvt_pk_bf16_f32 v228, v224, v225\n"
"v_mfma_f32_16x16x32_bf16 a[200:203], a[144:147], v[144:147], a[200:203]\n"
"v_cvt_pk_bf16_f32 v229, v226, v227\n"
"v_accvgpr_read_b32 v224, a168\n"
"v_mfma_f32_16x16x32_bf16 a[204:207], a[144:147], v[148:151], a[204:207]\n"
"ds_write_b64 v238, v[228:229] offset:1024\n"
"v_mfma_f32_16x16x32_bf16 a[220:223], a[148:151], v[148:151], a[220:223]\n"
"v_accvgpr_read_b32 v225, a169\n"
"v_accvgpr_read_b32 v226, a170\n"
"v_mfma_f32_16x16x32_bf16 a[216:219], a[148:151], v[144:147], a[216:219]\n"
"s_waitcnt vmcnt(9) lgkmcnt(14)\n"
"s_barrier\n"
"v_accvgpr_read_b32 v227, a171\n"
"v_mfma_f32_16x16x32_bf16 a[212:215], a[148:151], v[36:39], a[212:215]\n"
"s_waitcnt lgkmcnt(6)\n"
"v_cvt_pk_bf16_f32 v228, v224, v225\n"
"v_cvt_pk_bf16_f32 v229, v226, v227\n"
"v_mfma_f32_16x16x32_bf16 a[208:211], a[148:151], v[32:35], a[208:211]\n"
"ds_write_b64 v237, v[228:229] offset:0\n"
"v_mfma_f32_16x16x32_bf16 a[192:195], a[152:155], v[40:43], a[192:195]\n"
"v_accvgpr_read_b32 v224, a172\n"
"v_accvgpr_read_b32 v225, a173\n"
"v_mfma_f32_16x16x32_bf16 a[196:199], a[152:155], v[44:47], a[196:199]\n"
"buffer_load_dwordx4 v240, s[72:75], s85 offen offset:0 lds\n"
"s_add_u32 m0, m0, 0x380\n"
"v_mfma_f32_16x16x32_bf16 a[200:203], a[152:155], v[152:155], a[200:203]\n"
"v_add_u32 v234, 0x10000, v234\n"
"v_add_u32 v235, 0x10000, v235\n"
"v_mfma_f32_16x16x32_bf16 a[204:207], a[152:155], v[156:159], a[204:207]\n"
"ds_read_b64_tr_b16 a[136:137], v234 offset:0\n"
"v_accvgpr_read_b32 v226, a174\n"
"v_mfma_f32_16x16x32_bf16 a[220:223], a[156:159], v[156:159], a[220:223]\n"
"ds_read_b64_tr_b16 a[138:139], v234 offset:8192\n"
"v_accvgpr_read_b32 v227, a175\n"
"v_mfma_f32_16x16x32_bf16 a[216:219], a[156:159], v[152:155], a[216:219]\n"
"ds_read_b64_tr_b16 a[140:141], v235 offset:0\n"
"v_cvt_pk_bf16_f32 v228, v224, v225\n"
"v_mfma_f32_16x16x32_bf16 a[212:215], a[156:159], v[44:47], a[212:215]\n"
"ds_read_b64_tr_b16 a[142:143], v235 offset:8192\n"
"v_cvt_pk_bf16_f32 v229, v226, v227\n"
"v_mfma_f32_16x16x32_bf16 a[208:211], a[156:159], v[40:43], a[208:211]\n"
"s_waitcnt lgkmcnt(6)\n"
"buffer_load_dwordx4 v240, s[72:75], s85 offen offset:128 lds\n"
"v_mfma_f32_16x16x32_bf16 a[192:195], a[128:131], v[48:51], a[192:195]\n"
"ds_write_b64 v237, v[228:229] offset:1024\n"
"v_mfma_f32_16x16x32_bf16 a[196:199], a[128:131], v[52:55], a[196:199]\n"
"s_add_u32 m0, m0, 0x3c80\n"
"ds_read_b64_tr_b16 a[144:145], v234 offset:16384\n"
"v_mfma_f32_16x16x32_bf16 a[200:203], a[128:131], v[160:163], a[200:203]\n"
"s_mov_b32 s85, 0x23000\n"
"ds_read_b64_tr_b16 a[146:147], v234 offset:24576\n"
"v_mfma_f32_16x16x32_bf16 a[204:207], a[128:131], v[164:167], a[204:207]\n"
"ds_read_b64_tr_b16 a[148:149], v235 offset:16384\n"
"v_accvgpr_read_b32 v224, a176\n"
"v_mfma_f32_16x16x32_bf16 a[220:223], a[132:135], v[164:167], a[220:223]\n"
"buffer_load_dwordx4 v240, s[72:75], s85 offen offset:0 lds\n"
"s_add_u32 m0, m0, 0x380\n"
"v_mfma_f32_16x16x32_bf16 a[216:219], a[132:135], v[160:163], a[216:219]\n"
"ds_read_b64_tr_b16 a[150:151], v235 offset:24576\n"
"v_accvgpr_read_b32 v225, a177\n"
"v_mfma_f32_16x16x32_bf16 a[212:215], a[132:135], v[52:55], a[212:215]\n"
"ds_read_b64_tr_b16 a[152:153], v234 offset:1024\n"
"v_accvgpr_read_b32 v226, a178\n"
"v_mfma_f32_16x16x32_bf16 a[208:211], a[132:135], v[48:51], a[208:211]\n"
"s_waitcnt lgkmcnt(6)\n"
"ds_read_b64_tr_b16 a[154:155], v234 offset:9216\n"
"v_mfma_f32_16x16x32_bf16 a[192:195], a[136:139], v[56:59], a[192:195]\n"
"ds_read_b64_tr_b16 a[156:157], v235 offset:1024\n"
"v_accvgpr_read_b32 v227, a179\n"
"v_mfma_f32_16x16x32_bf16 a[196:199], a[136:139], v[60:63], a[196:199]\n"
"s_waitcnt vmcnt(6)\n"
"v_cmp_ne_u64 vcc, s[96:97], v[250:251]\n"
"s_cbranch_vccz .Lpok_3_%=\n"
"s_mov_b32 s90, 0x20000\n"
".Lpsp_3_%=:\n"
"global_load_dwordx2 v[250:251], v247, s[94:95] offset:0 sc1\n"
"s_waitcnt vmcnt(0)\n"
"v_cmp_ne_u64 vcc, s[96:97], v[250:251]\n"
"s_cbranch_vccz .Lpok_3_%=\n"
"s_sleep 1\n"
"s_sub_u32 s90, s90, 1\n"
"s_cmp_lg_u32 s90, 0\n"
"s_cbranch_scc1 .Lpsp_3_%=\n"
".Lpok_3_%=:\n"
"v_mfma_f32_16x16x32_bf16 a[200:203], a[136:139], v[168:171], a[200:203]\n"
"buffer_load_dwordx4 v240, s[72:75], s85 offen offset:128 lds\n"
"s_add_u32 m0, m0, 0xffffc480\n"
"v_mfma_f32_16x16x32_bf16 a[204:207], a[136:139], v[172:175], a[204:207]\n"
"s_mov_b32 s85, 0x2a000\n"
"ds_read_b64_tr_b16 a[158:159], v235 offset:9216\n"
"v_mfma_f32_16x16x32_bf16 a[220:223], a[140:143], v[172:175], a[220:223]\n"
"ds_read_b64_tr_b16 a[128:129], v234 offset:17408\n"
"v_cvt_pk_bf16_f32 v228, v224, v225\n"
"v_mfma_f32_16x16x32_bf16 a[216:219], a[140:143], v[168:171], a[216:219]\n"
"s_waitcnt lgkmcnt(5)\n"
"s_add_u32 s86, s68, 0\n"
"buffer_load_dwordx4 v[0:3], v239, s[56:59], s86 offen offset:0 sc1\n"
"v_mfma_f32_16x16x32_bf16 a[212:215], a[140:143], v[60:63], a[212:215]\n"
"ds_read_b64_tr_b16 a[130:131], v234 offset:25600\n"
"v_cvt_pk_bf16_f32 v229, v226, v227\n"
"v_mfma_f32_16x16x32_bf16 a[208:211], a[140:143], v[56:59], a[208:211]\n"
"buffer_load_dwordx4 v240, s[72:75], s85 offen offset:0 lds\n"
"s_add_u32 m0, m0, 0x380\n"
"v_mfma_f32_16x16x32_bf16 a[192:195], a[144:147], v[64:67], a[192:195]\n"
"ds_write_b64 v238, v[228:229] offset:512\n"
"v_mfma_f32_16x16x32_bf16 a[196:199], a[144:147], v[68:71], a[196:199]\n"
"s_add_u32 s86, s68, 0\n"
"buffer_load_dwordx4 v[4:7], v239, s[56:59], s86 offen offset:1024 sc1\n"
"v_mfma_f32_16x16x32_bf16 a[200:203], a[144:147], v[176:179], a[200:203]\n"
"ds_read_b64_tr_b16 a[132:133], v235 offset:17408\n"
"v_accvgpr_read_b32 v224, a180\n"
"v_mfma_f32_16x16x32_bf16 a[204:207], a[144:147], v[180:183], a[204:207]\n"
"ds_read_b64_tr_b16 a[134:135], v235 offset:25600\n"
"v_accvgpr_read_b32 v225, a181\n"
"v_mfma_f32_16x16x32_bf16 a[220:223], a[148:151], v[180:183], a[220:223]\n"
"buffer_load_dwordx4 v240, s[72:75], s85 offen offset:128 lds\n"
"s_add_u32 m0, m0, 0x3c80\n"
"v_mfma_f32_16x16x32_bf16 a[216:219], a[148:151], v[176:179], a[216:219]\n"
"s_mov_b32 s85, 0x31000\n"
"s_add_u32 s86, s68, 0\n"
"buffer_load_dwordx4 v[8:11], v239, s[56:59], s86 offen offset:2048 sc1\n"
"v_mfma_f32_16x16x32_bf16 a[212:215], a[148:151], v[68:71], a[212:215]\n"
"ds_read_b64_tr_b16 a[136:137], v234 offset:2048\n"
"v_accvgpr_read_b32 v226, a182\n"
"v_mfma_f32_16x16x32_bf16 a[208:211], a[148:151], v[64:67], a[208:211]\n"
"s_waitcnt lgkmcnt(6)\n"
"ds_read_b64_tr_b16 a[138:139], v234 offset:10240\n"
"v_mfma_f32_16x16x32_bf16 a[192:195], a[152:155], v[72:75], a[192:195]\n"
"ds_read_b64_tr_b16 a[140:141], v235 offset:2048\n"
"v_accvgpr_read_b32 v227, a183\n"
"v_mfma_f32_16x16x32_bf16 a[196:199], a[152:155], v[76:79], a[196:199]\n"
"buffer_load_dwordx4 v240, s[72:75], s85 offen offset:0 lds\n"
"s_add_u32 m0, m0, 0x380\n"
"v_mfma_f32_16x16x32_bf16 a[200:203], a[152:155], v[184:187], a[200:203]\n"
"s_add_u32 s86, s68, 0\n"
"buffer_load_dwordx4 v[12:15], v239, s[56:59], s86 offen offset:3072 sc1\n"
"v_mfma_f32_16x16x32_bf16 a[204:207], a[152:155], v[188:191], a[204:207]\n"
"ds_read_b64_tr_b16 a[142:143], v235 offset:10240\n"
"v_cvt_pk_bf16_f32 v228, v224, v225\n"
"v_mfma_f32_16x16x32_bf16 a[220:223], a[156:159], v[188:191], a[220:223]\n"
"ds_read_b64_tr_b16 a[144:145], v234 offset:18432\n"
"v_cvt_pk_bf16_f32 v229, v226, v227\n"
"v_mfma_f32_16x16x32_bf16 a[216:219], a[156:159], v[184:187], a[216:219]\n"
"ds_write_b64 v238, v[228:229] offset:1536\n"
"v_mfma_f32_16x16x32_bf16 a[212:215], a[156:159], v[76:79], a[212:215]\n"
"buffer_load_dwordx4 v240, s[72:75], s85 offen offset:128 lds\n"
"s_add_u32 m0, m0, 0x3480\n"
"v_mfma_f32_16x16x32_bf16 a[208:211], a[156:159], v[72:75], a[208:211]\n"
"s_waitcnt lgkmcnt(6)\n"
"s_add_u32 s86, s68, 4096\n"
"buffer_load_dwordx4 v[16:19], v239, s[56:59], s86 offen offset:0 sc1\n"
"v_mfma_f32_16x16x32_bf16 a[192:195], a[128:131], v[80:83], a[192:195]\n"
"s_mov_b32 s85, 0x38000\n"
"ds_read_b64_tr_b16 a[146:147], v234 offset:26624\n"
"v_mfma_f32_16x16x32_bf16 a[196:199], a[128:131], v[84:87], a[196:199]\n"
"ds_read_b64_tr_b16 a[148:149], v235 offset:18432\n"
"v_accvgpr_read_b32 v224, a184\n"
"v_mfma_f32_16x16x32_bf16 a[200:203], a[128:131], v[192:195], a[200:203]\n"
"ds_read_b64_tr_b16 a[150:151], v235 offset:26624\n"
"v_accvgpr_read_b32 v225, a185\n"
"v_mfma_f32_16x16x32_bf16 a[204:207], a[128:131], v[196:199], a[204:207]\n"
"s_add_u32 s86, s68, 4096\n"
"buffer_load_dwordx4 v[20:23], v239, s[56:59], s86 offen offset:1024 sc1\n"
"v_mfma_f32_16x16x32_bf16 a[220:223], a[132:135], v[196:199], a[220:223]\n"
"ds_read_b64_tr_b16 a[152:153], v234 offset:3072\n"
"v_accvgpr_read_b32 v226, a186\n"
"v_mfma_f32_16x16x32_bf16 a[216:219], a[132:135], v[192:195], a[216:219]\n"
"ds_read_b64_tr_b16 a[154:155], v234 offset:11264\n"
"v_accvgpr_read_b32 v227, a187\n"
"v_mfma_f32_16x16x32_bf16 a[212:215], a[132:135], v[84:87], a[212:215]\n"
"ds_read_b64_tr_b16 a[156:157], v235 offset:3072\n"
"v_cvt_pk_bf16_f32 v228, v224, v225\n"
"v_mfma_f32_16x16x32_bf16 a[208:211], a[132:135], v[80:83], a[208:211]\n"
"s_waitcnt lgkmcnt(8)\n"
"s_add_u32 s86, s68, 4096\n"
"buffer_load_dwordx4 v[24:27], v239, s[56:59], s86 offen offset:2048 sc1\n"
"v_mfma_f32_16x16x32_bf16 a[192:195], a[136:139], v[88:91], a[192:195]\n"
"ds_read_b64_tr_b16 a[158:159], v235 offset:11264\n"
"v_cvt_pk_bf16_f32 v229, v226, v227\n"
"v_mfma_f32_16x16x32_bf16 a[196:199], a[136:139], v[92:95], a[196:199]\n"
"ds_write_b64 v237, v[228:229] offset:512\n"
"v_mfma_f32_16x16x32_bf16 a[200:203], a[136:139], v[200:203], a[200:203]\n"
"v_accvgpr_read_b32 v224, a188\n"
"v_accvgpr_read_b32 v225, a189\n"
"v_mfma_f32_16x16x32_bf16 a[204:207], a[136:139], v[204:207], a[204:207]\n"
"s_add_u32 s86, s68, 4096\n"
"buffer_load_dwordx4 v[28:31], v239, s[56:59], s86 offen offset:3072 sc1\n"
"v_mfma_f32_16x16x32_bf16 a[220:223], a[140:143], v[204:207], a[220:223]\n"
"v_accvgpr_read_b32 v226, a190\n"
"v_accvgpr_read_b32 v227, a191\n"
"v_mfma_f32_16x16x32_bf16 a[216:219], a[140:143], v[200:203], a[216:219]\n"
"s_waitcnt lgkmcnt(5)\n"
"v_cvt_pk_bf16_f32 v228, v224, v225\n"
"v_cvt_pk_bf16_f32 v229, v226, v227\n"
"v_mfma_f32_16x16x32_bf16 a[212:215], a[140:143], v[92:95], a[212:215]\n"
"ds_write_b64 v237, v[228:229] offset:1536\n"
"v_mfma_f32_16x16x32_bf16 a[208:211], a[140:143], v[88:91], a[208:211]\n"
"s_add_u32 s86, s68, 8192\n"
"buffer_load_dwordx4 v[32:35], v239, s[56:59], s86 offen offset:0 sc1\n"
"v_mfma_f32_16x16x32_bf16 a[192:195], a[144:147], v[96:99], a[192:195]\n"
"v_mfma_f32_16x16x32_bf16 a[196:199], a[144:147], v[100:103], a[196:199]\n"
"v_mfma_f32_16x16x32_bf16 a[200:203], a[144:147], v[208:211], a[200:203]\n"
"v_mfma_f32_16x16x32_bf16 a[204:207], a[144:147], v[212:215], a[204:207]\n"
"s_waitcnt vmcnt(17) lgkmcnt(0)\n"
"s_barrier\n"
"ds_read_b64_tr_b16 a[128:129], v230 offset:0\n"
"v_mfma_f32_16x16x32_bf16 a[220:223], a[148:151], v[212:215], a[220:223]\n"
"ds_read_b64_tr_b16 a[130:131], v230 offset:8192\n"
"v_mfma_f32_16x16x32_bf16 a[216:219], a[148:151], v[208:211], a[216:219]\n"
"s_waitcnt lgkmcnt(4)\n"
"ds_read_b64_tr_b16 a[132:133], v231 offset:0\n"
"v_mfma_f32_16x16x32_bf16 a[212:215], a[148:151], v[100:103], a[212:215]\n"
"ds_read_b64_tr_b16 a[134:135], v231 offset:8192\n"
"s_add_u32 s86, s68, 8192\n"
"buffer_load_dwordx4 v[36:39], v239, s[56:59], s86 offen offset:1024 sc1\n"
"v_mfma_f32_16x16x32_bf16 a[208:211], a[148:151], v[96:99], a[208:211]\n"
"ds_read_b64_tr_b16 a[136:137], v232 offset:0\n"
"buffer_load_dwordx4 v240, s[72:75], s85 offen offset:0 lds\n"
"v_mfma_f32_16x16x32_bf16 a[192:195], a[152:155], v[104:107], a[192:195]\n"
"ds_read_b64_tr_b16 a[138:139], v232 offset:8192\n"
"ds_read_b128 a[16:19], v244 offset:0\n"
"v_mfma_f32_16x16x32_bf16 a[196:199], a[152:155], v[108:111], a[196:199]\n"
"s_add_u32 m0, m0, 0x380\n"
"ds_read_b64_tr_b16 a[140:141], v233 offset:0\n"
"v_mfma_f32_16x16x32_bf16 a[200:203], a[152:155], v[216:219], a[200:203]\n"
"s_waitcnt vmcnt(12)\n"
"ds_read_b64_tr_b16 a[142:143], v233 offset:8192\n"
"v_mfma_f32_16x16x32_bf16 a[204:207], a[152:155], v[220:223], a[204:207]\n"
"ds_read_b64_tr_b16 a[144:145], v241 offset:0\n"
"v_swap_b32 v2, v4\n"
"v_mfma_f32_16x16x32_bf16 a[220:223], a[156:159], v[220:223], a[220:223]\n"
"ds_read_b64_tr_b16 a[146:147], v241 offset:8192\n"
"ds_read_b128 a[48:51], v244 offset:1024\n"
"v_mfma_f32_16x16x32_bf16 a[216:219], a[156:159], v[216:219], a[216:219]\n"
"ds_read_b64_tr_b16 a[148:149], v242 offset:0\n"
"s_add_u32 s86, s68, 8192\n"
"buffer_load_dwordx4 v[40:43], v239, s[56:59], s86 offen offset:2048 sc1\n"
"v_swap_b32 v3, v5\n"
"v_mfma_f32_16x16x32_bf16 a[212:215], a[156:159], v[108:111], a[212:215]\n"
"s_waitcnt lgkmcnt(9)\n"
"ds_read_b64_tr_b16 a[150:151], v242 offset:8192\n"
"v_mfma_f32_16x16x32_bf16 a[208:211], a[156:159], v[104:107], a[208:211]\n"
"ds_read_b64_tr_b16 a[152:153], v243 offset:0\n"
"buffer_load_dwordx4 v240, s[72:75], s85 offen offset:128 lds\n"
"v_mfma_f32_16x16x32_bf16 v[56:59], a[128:131], v[0:3], 0\n"
"ds_read_b64_tr_b16 a[154:155], v243 offset:8192\n"
"ds_read_b128 a[80:83], v245 offset:0\n"
"v_mfma_f32_16x16x32_bf16 v[60:63], a[128:131], v[4:7], 0\n"
"s_add_u32 m0, m0, 0x3c80\n"
"ds_read_b64_tr_b16 a[156:157], v230 offset:16384\n"
"v_mfma_f32_16x16x32_bf16 v[68:71], a[132:135], v[4:7], 0\n"
"s_mov_b32 s85, 0x3f000\n"
"ds_read_b64_tr_b16 a[158:159], v230 offset:24576\n"
"v_mfma_f32_16x16x32_bf16 v[64:67], a[132:135], v[0:3], 0\n"
"s_waitcnt lgkmcnt(10)\n"
"ds_read_b64_tr_b16 a[128:129], v231 offset:16384\n"
"v_mfma_f32_16x16x32_bf16 v[72:75], a[136:139], v[0:3], 0\n"
"ds_read_b64_tr_b16 a[130:131], v231 offset:24576\n"
"s_add_u32 s86, s68, 8192\n"
"buffer_load_dwordx4 v[44:47], v239, s[56:59], s86 offen offset:3072 sc1\n"
"v_mfma_f32_16x16x32_bf16 v[76:79], a[136:139], v[4:7], 0\n"
"ds_read_b64_tr_b16 a[132:133], v232 offset:16384\n"
"ds_read_b128 a[112:115], v245 offset:1024\n"
"v_mfma_f32_16x16x32_bf16 v[84:87], a[140:143], v[4:7], 0\n"
"ds_read_b64_tr_b16 a[134:135], v232 offset:24576\n"
"v_accvgpr_read_b32 v224, a192\n"
"v_mfma_f32_16x16x32_bf16 v[80:83], a[140:143], v[0:3], 0\n"
"s_waitcnt lgkmcnt(10)\n"
"s_waitcnt vmcnt(11)\n"
"ds_read_b64_tr_b16 a[136:137], v233 offset:16384\n"
"buffer_load_dwordx4 v240, s[72:75], s85 offen offset:0 lds\n"
"v_mfma_f32_16x16x32_bf16 v[88:91], a[144:147], v[0:3], 0\n"
"s_add_u32 m0, m0, 0x380\n"
"ds_read_b64_tr_b16 a[138:139], v233 offset:24576\n"
"v_mfma_f32_16x16x32_bf16 v[92:95], a[144:147], v[4:7], 0\n"
"ds_read_b64_tr_b16 a[140:141], v241 offset:16384\n"
"ds_read_b128 a[20:23], v244 offset:4096\n"
"v_mfma_f32_16x16x32_bf16 v[100:103], a[148:151], v[4:7], 0\n"
"ds_read_b64_tr_b16 a[142:143], v241 offset:24576\n"
"v_accvgpr_read_b32 v225, a193\n"
"v_mfma_f32_16x16x32_bf16 v[96:99], a[148:151], v[0:3], 0\n"
"s_waitcnt lgkmcnt(10)\n"
"ds_read_b64_tr_b16 a[144:145], v242 offset:16384\n"
"s_add_u32 s86, s68, 12288\n"
"buffer_load_dwordx4 v[48:51], v239, s[56:59], s86 offen offset:0 sc1\n"
"v_swap_b32 v10, v12\n"
"v_swap_b32 v11, v13\n"
"v_mfma_f32_16x16x32_bf16 v[104:107], a[152:155], v[0:3], 0\n"
"ds_read_b64_tr_b16 a[146:147], v242 offset:24576\n"
"v_accvgpr_read_b32 v226, a194\n"
"v_mfma_f32_16x16x32_bf16 v[108:111], a[152:155], v[4:7], 0\n"
"ds_read_b64_tr_b16 a[148:149], v243 offset:16384\n"
"ds_read_b128 a[52:55], v244 offset:5120\n"
"v_mfma_f32_16x16x32_bf16 v[56:59], a[156:159], v[8:11], v[56:59]\n"
"ds_read_b64_tr_b16 a[150:151], v243 offset:24576\n"
"v_accvgpr_read_b32 v227, a195\n"
"v_mfma_f32_16x16x32_bf16 v[60:63], a[156:159], v[12:15], v[60:63]\n"
"s_waitcnt lgkmcnt(10)\n"
"ds_read_b64_tr_b16 a[152:153], v230 offset:2048\n"
"buffer_load_dwordx4 v240, s[72:75], s85 offen offset:128 lds\n"
"v_mfma_f32_16x16x32_bf16 v[68:71], a[128:131], v[12:15], v[68:71]\n"
"s_add_u32 m0, m0, 0xffffc480\n"
"ds_read_b64_tr_b16 a[154:155], v230 offset:10240\n"
"v_mfma_f32_16x16x32_bf16 v[64:67], a[128:131], v[8:11], v[64:67]\n"
"s_mov_b32 s85, 0x46000\n"
"ds_read_b64_tr_b16 a[156:157], v231 offset:2048\n"
"ds_read_b128 a[84:87], v245 offset:4096\n"
"v_mfma_f32_16x16x32_bf16 v[72:75], a[132:135], v[8:11], v[72:75]\n"
"ds_read_b64_tr_b16 a[158:159], v231 offset:10240\n"
"s_add_u32 s86, s68, 12288\n"
"buffer_load_dwordx4 v[52:55], v239, s[56:59], s86 offen offset:1024 sc1\n"
"v_mfma_f32_16x16x32_bf16 v[76:79], a[132:135], v[12:15], v[76:79]\n"
"s_waitcnt lgkmcnt(10)\n"
"ds_read_b64_tr_b16 a[128:129], v232 offset:2048\n"
"v_mfma_f32_16x16x32_bf16 v[84:87], a[136:139], v[12:15], v[84:87]\n"
"ds_read_b64_tr_b16 a[130:131], v232 offset:10240\n"
"v_cvt_pk_bf16_f32 v228, v224, v225\n"
"v_mfma_f32_16x16x32_bf16 v[80:83], a[136:139], v[8:11], v[80:83]\n"
"s_waitcnt vmcnt(12)\n"
"ds_read_b64_tr_b16 a[132:133], v233 offset:2048\n"
"ds_read_b128 a[116:119], v245 offset:5120\n"
"v_mfma_f32_16x16x32_bf16 v[88:91], a[140:143], v[8:11], v[88:91]\n"
"ds_read_b64_tr_b16 a[134:135], v233 offset:10240\n"
"v_cvt_pk_bf16_f32 v229, v226, v227\n"
"v_mfma_f32_16x16x32_bf16 v[92:95], a[140:143], v[12:15], v[92:95]\n"
"s_waitcnt lgkmcnt(10)\n"
"ds_read_b64_tr_b16 a[136:137], v241 offset:2048\n"
"buffer_load_dwordx4 v240, s[72:75], s85 offen offset:0 lds\n"
"v_mfma_f32_16x16x32_bf16 v[100:103], a[144:147], v[12:15], v[100:103]\n"
"s_add_u32 m0, m0, 0x380\n"
"ds_read_b64_tr_b16 a[138:139], v241 offset:10240\n"
"v_mfma_f32_16x16x32_bf16 v[96:99], a[144:147], v[8:11], v[96:99]\n"
"ds_read_b64_tr_b16 a[140:141], v242 offset:2048\n"
"s_add_u32 s86, s68, 12288\n"
"buffer_load_dwordx4 v[112:115], v239, s[56:59], s86 offen offset:2048 sc1\n"
"v_swap_b32 v18, v20\n"
"v_swap_b32 v19, v21\n"
"v_mfma_f32_16x16x32_bf16 v[104:107], a[148:151], v[8:11], v[104:107]\n"
"ds_read_b64_tr_b16 a[142:143], v242 offset:10240\n"
"ds_write_b64 v238, v[228:229] offset:16384\n"
"v_mfma_f32_16x16x32_bf16 v[108:111], a[148:151], v[12:15], v[108:111]\n"
"s_waitcnt lgkmcnt(10)\n"
"ds_read_b64_tr_b16 a[144:145], v243 offset:2048\n"
"v_mfma_f32_16x16x32_bf16 v[56:59], a[152:155], v[16:19], v[56:59]\n"
"ds_read_b64_tr_b16 a[146:147], v243 offset:10240\n"
"v_accvgpr_read_b32 v224, a196\n"
"v_mfma_f32_16x16x32_bf16 v[60:63], a[152:155], v[20:23], v[60:63]\n"
"ds_read_b64_tr_b16 a[148:149], v230 offset:18432\n"
"v_accvgpr_read_b32 v225, a197\n"
"v_mfma_f32_16x16x32_bf16 v[68:71], a[156:159], v[20:23], v[68:71]\n"
"ds_read_b64_tr_b16 a[150:151], v230 offset:26624\n"
"v_accvgpr_read_b32 v226, a198\n"
"v_mfma_f32_16x16x32_bf16 v[64:67], a[156:159], v[16:19], v[64:67]\n"
"s_waitcnt lgkmcnt(9)\n"
"ds_read_b64_tr_b16 a[152:153], v231 offset:18432\n"
"buffer_load_dwordx4 v240, s[72:75], s85 offen offset:128 lds\n"
"v_accvgpr_read_b32 v227, a199\n"
"v_mfma_f32_16x16x32_bf16 v[72:75], a[128:131], v[16:19], v[72:75]\n"
"ds_read_b64_tr_b16 a[154:155], v231 offset:26624\n"
"s_add_u32 s86, s68, 12288\n"
"buffer_load_dwordx4 v[116:119], v239, s[56:59], s86 offen offset:3072 sc1\n"
"v_cvt_pk_bf16_f32 v228, v224, v225\n"
"v_mfma_f32_16x16x32_bf16 v[76:79], a[128:131], v[20:23], v[76:79]\n"
"s_add_u32 m0, m0, 0x3c80\n"
"ds_read_b64_tr_b16 a[156:157], v232 offset:18432\n"
"v_cvt_pk_bf16_f32 v229, v226, v227\n"
"v_mfma_f32_16x16x32_bf16 v[84:87], a[132:135], v[20:23], v[84:87]\n"
"s_mov_b32 s85, 0x4d000\n"
"ds_read_b64_tr_b16 a[158:159], v232 offset:26624\n"
"v_mfma_f32_16x16x32_bf16 v[80:83], a[132:135], v[16:19], v[80:83]\n"
"s_waitcnt lgkmcnt(9)\n"
"s_waitcnt vmcnt(14)\n"
"ds_read_b64_tr_b16 a[128:129], v233 offset:18432\n"
"v_mfma_f32_16x16x32_bf16 v[88:91], a[136:139], v[16:19], v[88:91]\n"
"ds_read_b64_tr_b16 a[130:131], v233 offset:26624\n"
"v_accvgpr_read_b32 v224, a200\n"
"v_mfma_f32_16x16x32_bf16 v[92:95], a[136:139], v[20:23], v[92:95]\n"
"ds_read_b64_tr_b16 a[132:133], v241 offset:18432\n"
"ds_write_b64 v238, v[228:229] offset:17408\n"
"v_mfma_f32_16x16x32_bf16 v[100:103], a[140:143], v[20:23], v[100:103]\n"
"ds_read_b64_tr_b16 a[134:135], v241 offset:26624\n"
"v_accvgpr_read_b32 v225, a201\n"
"v_mfma_f32_16x16x32_bf16 v[96:99], a[140:143], v[16:19], v[96:99]\n"
"s_waitcnt lgkmcnt(9)\n"
"ds_read_b64_tr_b16 a[136:137], v242 offset:18432\n"
"s_add_u32 s86, s68, 16384\n"
"buffer_load_dwordx4 v[120:123], v239, s[56:59], s86 offen offset:0 sc1\n"
"v_swap_b32 v26, v28\n"
"v_swap_b32 v27, v29\n"
"v_mfma_f32_16x16x32_bf16 v[104:107], a[144:147], v[16:19], v[104:107]\n"
"ds_read_b64_tr_b16 a[138:139], v242 offset:26624\n"
"buffer_load_dwordx4 v240, s[72:75], s85 offen offset:0 lds\n"
"v_mfma_f32_16x16x32_bf16 v[108:111], a[144:147], v[20:23], v[108:111]\n"
"s_add_u32 m0, m0, 0x380\n"
"ds_read_b64_tr_b16 a[140:141], v243 offset:18432\n"
"v_mfma_f32_16x16x32_bf16 v[56:59], a[148:151], v[24:27], v[56:59]\n"
"ds_read_b64_tr_b16 a[142:143], v243 offset:26624\n"
"v_accvgpr_read_b32 v226, a202\n"
"v_accvgpr_read_b32 v227, a203\n"
"v_mfma_f32_16x16x32_bf16 v[60:63], a[148:151], v[28:31], v[60:63]\n"
"s_waitcnt lgkmcnt(9)\n"
"s_waitcnt vmcnt(20)\n"
"s_barrier\n"
"ds_read_b64_tr_b16 a[144:145], v230 offset:32768\n"
"v_cvt_pk_bf16_f32 v228, v224, v225\n"
"v_mfma_f32_16x16x32_bf16 v[68:71], a[152:155], v[28:31], v[68:71]\n"
"ds_read_b64_tr_b16 a[146:147], v230 offset:40960\n"
"v_cvt_pk_bf16_f32 v229, v226, v227\n"
"v_mfma_f32_16x16x32_bf16 v[64:67], a[152:155], v[24:27], v[64:67]\n"
"ds_read_b64_tr_b16 a[148:149], v231 offset:32768\n"
"v_accvgpr_read_b32 v224, a204\n"
"v_mfma_f32_16x16x32_bf16 v[72:75], a[156:159], v[24:27], v[72:75]\n"
"ds_read_b64_tr_b16 a[150:151], v231 offset:40960\n"
"s_add_u32 s86, s68, 16384\n"
"buffer_load_dwordx4 v[124:127], v239, s[56:59], s86 offen offset:1024 sc1\n"
"v_mfma_f32_16x16x32_bf16 v[76:79], a[156:159], v[28:31], v[76:79]\n"
"s_waitcnt lgkmcnt(8)\n"
"ds_read_b64_tr_b16 a[152:153], v232 offset:32768\n"
"v_mfma_f32_16x16x32_bf16 v[84:87], a[128:131], v[28:31], v[84:87]\n"
"ds_read_b64_tr_b16 a[154:155], v232 offset:40960\n"
"buffer_load_dwordx4 v240, s[72:75], s85 offen offset:128 lds\n"
"v_mfma_f32_16x16x32_bf16 v[80:83], a[128:131], v[24:27], v[80:83]\n"
"s_waitcnt vmcnt(16)\n"
"ds_read_b64_tr_b16 a[156:157], v233 offset:32768\n"
"ds_write_b64 v237, v[228:229] offset:16384\n"
"v_mfma_f32_16x16x32_bf16 v[88:91], a[132:135], v[24:27], v[88:91]\n"
"s_add_u32 m0, m0, 0xb480\n"
"ds_read_b64_tr_b16 a[158:159], v233 offset:40960\n"
"v_mfma_f32_16x16x32_bf16 v[92:95], a[132:135], v[28:31], v[92:95]\n"
"s_waitcnt lgkmcnt(9)\n"
"s_mov_b32 s85, 0x54000\n"
"ds_read_b64_tr_b16 a[128:129], v241 offset:32768\n"
"v_mfma_f32_16x16x32_bf16 v[100:103], a[136:139], v[28:31], v[100:103]\n"
"ds_read_b64_tr_b16 a[130:131], v241 offset:40960\n"
"v_accvgpr_read_b32 v225, a205\n"
"v_mfma_f32_16x16x32_bf16 v[96:99], a[136:139], v[24:27], v[96:99]\n"
"ds_read_b64_tr_b16 a[132:133], v242 offset:32768\n"
"s_add_u32 s86, s68, 16384\n"
"buffer_load_dwordx4 v[128:131], v239, s[56:59], s86 offen offset:2048 sc1\n"
"v_swap_b32 v34, v36\n"
"v_swap_b32 v35, v37\n"
"v_accvgpr_read_b32 v226, a206\n"
"v_mfma_f32_16x16x32_bf16 v[104:107], a[140:143], v[24:27], v[104:107]\n"
"ds_read_b64_tr_b16 a[134:135], v242 offset:40960\n"
"v_accvgpr_read_b32 v227, a207\n"
"v_cvt_pk_bf16_f32 v228, v224, v225\n"
"v_mfma_f32_16x16x32_bf16 v[108:111], a[140:143], v[28:31], v[108:111]\n"
"s_waitcnt lgkmcnt(9)\n"
"ds_read_b64_tr_b16 a[136:137], v243 offset:32768\n"
"v_cvt_pk_bf16_f32 v229, v226, v227\n"
"v_mfma_f32_16x16x32_bf16 v[56:59], a[144:147], v[32:35], v[56:59]\n"
"ds_read_b64_tr_b16 a[138:139], v243 offset:40960\n"
"buffer_load_dwordx4 v240, s[72:75], s85 offen offset:0 lds\n"
"v_mfma_f32_16x16x32_bf16 v[60:63], a[144:147], v[36:39], v[60:63]\n"
"s_add_u32 m0, m0, 0x380\n"
"ds_read_b64_tr_b16 a[140:141], v230 offset:49152\n"
"v_mfma_f32_16x16x32_bf16 v[68:71], a[148:151], v[36:39], v[68:71]\n"
"ds_read_b64_tr_b16 a[142:143], v230 offset:57344\n"
"v_accvgpr_read_b32 v224, a208\n"
"v_mfma_f32_16x16x32_bf16 v[64:67], a[148:151], v[32:35], v[64:67]\n"
"s_waitcnt lgkmcnt(8)\n"
"ds_read_b64_tr_b16 a[144:145], v231 offset:49152\n"
"ds_write_b64 v237, v[228:229] offset:17408\n"
"v_mfma_f32_16x16x32_bf16 v[72:75], a[152:155], v[32:35], v[72:75]\n"
"ds_read_b64_tr_b16 a[146:147], v231 offset:57344\n"
"s_add_u32 s86, s68, 16384\n"
"buffer_load_dwordx4 v[132:135], v239, s[56:59], s86 offen offset:3072 sc1\n"
"v_mfma_f32_16x16x32_bf16 v[76:79], a[152:155], v[36:39], v[76:79]\n"
"ds_read_b64_tr_b16 a[148:149], v232 offset:49152\n"
"v_accvgpr_read_b32 v225, a209\n"
"v_mfma_f32_16x16x32_bf16 v[84:87], a[156:159], v[36:39], v[84:87]\n"
"ds_read_b64_tr_b16 a[150:151], v232 offset:57344\n"
"v_accvgpr_read_b32 v226, a210\n"
"v_mfma_f32_16x16x32_bf16 v[80:83], a[156:159], v[32:35], v[80:83]\n"
"s_waitcnt lgkmcnt(9)\n"
"s_waitcnt vmcnt(15)\n"
"ds_read_b64_tr_b16 a[152:153], v233 offset:49152\n"
"v_mfma_f32_16x16x32_bf16 v[88:91], a[128:131], v[32:35], v[88:91]\n"
"ds_read_b64_tr_b16 a[154:155], v233 offset:57344\n"
"buffer_load_dwordx4 v240, s[72:75], s85 offen offset:128 lds\n"
"v_mfma_f32_16x16x32_bf16 v[92:95], a[128:131], v[36:39], v[92:95]\n"
"s_add_u32 m0, m0, 0x3c80\n"
"ds_read_b64_tr_b16 a[156:157], v241 offset:49152\n"
"v_accvgpr_read_b32 v227, a211\n"
"v_mfma_f32_16x16x32_bf16 v[100:103], a[132:135], v[36:39], v[100:103]\n"
"s_mov_b32 s85, 0x5b000\n"
"ds_read_b64_tr_b16 a[158:159], v241 offset:57344\n"
"v_cvt_pk_bf16_f32 v228, v224, v225\n"
"v_mfma_f32_16x16x32_bf16 v[96:99], a[132:135], v[32:35], v[96:99]\n"
"s_waitcnt lgkmcnt(9)\n"
"ds_read_b64_tr_b16 a[128:129], v242 offset:49152\n"
"s_add_u32 s86, s68, 20480\n"
"buffer_load_dwordx4 v[136:139], v239, s[56:59], s86 offen offset:0 sc1\n"
"v_swap_b32 v42, v44\n"
"v_swap_b32 v43, v45\n"
"v_cvt_pk_bf16_f32 v229, v226, v227\n"
"v_mfma_f32_16x16x32_bf16 v[104:107], a[136:139], v[32:35], v[104:107]\n"
"ds_read_b64_tr_b16 a[130:131], v242 offset:57344\n"
"v_accvgpr_read_b32 v224, a212\n"
"v_mfma_f32_16x16x32_bf16 v[108:111], a[136:139], v[36:39], v[108:111]\n"
"ds_read_b64_tr_b16 a[132:133], v243 offset:49152\n"
"v_accvgpr_read_b32 v225, a213\n"
"v_mfma_f32_16x16x32_bf16 v[56:59], a[140:143], v[40:43], v[56:59]\n"
"ds_read_b64_tr_b16 a[134:135], v243 offset:57344\n"
"v_accvgpr_read_b32 v226, a214\n"
"v_mfma_f32_16x16x32_bf16 v[60:63], a[140:143], v[44:47], v[60:63]\n"
"s_waitcnt lgkmcnt(8)\n"
"ds_read_b64_tr_b16 a[136:137], v230 offset:34816\n"
"ds_write_b64 v238, v[228:229] offset:16896\n"
"v_mfma_f32_16x16x32_bf16 v[68:71], a[144:147], v[44:47], v[68:71]\n"
"ds_read_b64_tr_b16 a[138:139], v230 offset:43008\n"
"buffer_load_dwordx4 v240, s[72:75], s85 offen offset:0 lds\n"
"v_mfma_f32_16x16x32_bf16 v[64:67], a[144:147], v[40:43], v[64:67]\n"
"s_add_u32 m0, m0, 0x380\n"
"ds_read_b64_tr_b16 a[140:141], v231 offset:34816\n"
"v_mfma_f32_16x16x32_bf16 v[72:75], a[148:151], v[40:43], v[72:75]\n"
"ds_read_b64_tr_b16 a[142:143], v231 offset:43008\n"
"s_add_u32 s86, s68, 20480\n"
"buffer_load_dwordx4 v[140:143], v239, s[56:59], s86 offen offset:1024 sc1\n"
"v_mfma_f32_16x16x32_bf16 v[76:79], a[148:151], v[44:47], v[76:79]\n"
"s_waitcnt lgkmcnt(9)\n"
"ds_read_b64_tr_b16 a[144:145], v232 offset:34816\n"
"v_mfma_f32_16x16x32_bf16 v[84:87], a[152:155], v[44:47], v[84:87]\n"
"ds_read_b64_tr_b16 a[146:147], v232 offset:43008\n"
"v_accvgpr_read_b32 v227, a215\n"
"v_mfma_f32_16x16x32_bf16 v[80:83], a[152:155], v[40:43], v[80:83]\n"
"s_waitcnt vmcnt(15)\n"
"ds_read_b64_tr_b16 a[148:149], v233 offset:34816\n"
"v_cvt_pk_bf16_f32 v228, v224, v225\n"
"v_mfma_f32_16x16x32_bf16 v[88:91], a[156:159], v[40:43], v[88:91]\n"
"ds_read_b64_tr_b16 a[150:151], v233 offset:43008\n"
"v_cvt_pk_bf16_f32 v229, v226, v227\n"
"v_mfma_f32_16x16x32_bf16 v[92:95], a[156:159], v[44:47], v[92:95]\n"
"s_waitcnt lgkmcnt(9)\n"
"ds_read_b64_tr_b16 a[152:153], v241 offset:34816\n"
"v_mfma_f32_16x16x32_bf16 v[100:103], a[128:131], v[44:47], v[100:103]\n"
"ds_read_b64_tr_b16 a[154:155], v241 offset:43008\n"
"buffer_load_dwordx4 v240, s[72:75], s85 offen offset:128 lds\n"
"v_mfma_f32_16x16x32_bf16 v[96:99], a[128:131], v[40:43], v[96:99]\n"
"ds_read_b64_tr_b16 a[156:157], v242 offset:34816\n"
"s_add_u32 s86, s68, 20480\n"
"buffer_load_dwordx4 v[144:147], v239, s[56:59], s86 offen offset:2048 sc1\n"
"v_swap_b32 v50, v52\n"
"v_swap_b32 v51, v53\n"
"v_mfma_f32_16x16x32_bf16 v[104:107], a[132:135], v[40:43], v[104:107]\n"
"s_add_u32 m0, m0, 0xfffdbc80\n"
"ds_read_b64_tr_b16 a[158:159], v242 offset:43008\n"
"ds_write_b64 v238, v[228:229] offset:17920\n"
"v_mfma_f32_16x16x32_bf16 v[108:111], a[132:135], v[44:47], v[108:111]\n"
"s_waitcnt lgkmcnt(9)\n"
"ds_read_b64_tr_b16 a[128:129], v243 offset:34816\n"
"v_mfma_f32_16x16x32_bf16 v[56:59], a[136:139], v[48:51], v[56:59]\n"
"ds_read_b64_tr_b16 a[130:131], v243 offset:43008\n"
"v_accvgpr_read_b32 v224, a216\n"
"v_mfma_f32_16x16x32_bf16 v[60:63], a[136:139], v[52:55], v[60:63]\n"
"ds_read_b64_tr_b16 a[132:133], v230 offset:51200\n"
"v_accvgpr_read_b32 v225, a217\n"
"v_mfma_f32_16x16x32_bf16 v[68:71], a[140:143], v[52:55], v[68:71]\n"
"ds_read_b64_tr_b16 a[134:135], v230 offset:59392\n"
"v_accvgpr_read_b32 v226, a218\n"
"v_mfma_f32_16x16x32_bf16 v[64:67], a[140:143], v[48:51], v[64:67]\n"
"s_waitcnt lgkmcnt(9)\n"
"ds_read_b64_tr_b16 a[136:137], v231 offset:51200\n"
"v_accvgpr_read_b32 v227, a219\n"
"v_mfma_f32_16x16x32_bf16 v[72:75], a[144:147], v[48:51], v[72:75]\n"
"ds_read_b64_tr_b16 a[138:139], v231 offset:59392\n"
"s_add_u32 s86, s68, 20480\n"
"buffer_load_dwordx4 v[148:151], v239, s[56:59], s86 offen offset:3072 sc1\n"
"v_cvt_pk_bf16_f32 v228, v224, v225\n"
"v_mfma_f32_16x16x32_bf16 v[76:79], a[144:147], v[52:55], v[76:79]\n"
"ds_read_b64_tr_b16 a[140:141], v232 offset:51200\n"
"v_cvt_pk_bf16_f32 v229, v226, v227\n"
"v_mfma_f32_16x16x32_bf16 v[84:87], a[148:151], v[52:55], v[84:87]\n"
"ds_read_b64_tr_b16 a[142:143], v232 offset:59392\n"
"v_accvgpr_read_b32 v224, a220\n"
"v_mfma_f32_16x16x32_bf16 v[80:83], a[148:151], v[48:51], v[80:83]\n"
"s_waitcnt lgkmcnt(9)\n"
"s_waitcnt vmcnt(14)\n"
"ds_read_b64_tr_b16 a[144:145], v233 offset:51200\n"
"v_mfma_f32_16x16x32_bf16 v[88:91], a[152:155], v[48:51], v[88:91]\n"
"ds_read_b64_tr_b16 a[146:147], v233 offset:59392\n"
"v_accvgpr_read_b32 v225, a221\n"
"v_mfma_f32_16x16x32_bf16 v[92:95], a[152:155], v[52:55], v[92:95]\n"
"ds_read_b64_tr_b16 a[148:149], v241 offset:51200\n"
"ds_write_b64 v237, v[228:229] offset:16896\n"
"v_mfma_f32_16x16x32_bf16 v[100:103], a[156:159], v[52:55], v[100:103]\n"
"ds_read_b64_tr_b16 a[150:151], v241 offset:59392\n"
"v_accvgpr_read_b32 v226, a222\n"
"v_accvgpr_read_b32 v227, a223\n"
"v_mfma_f32_16x16x32_bf16 v[96:99], a[156:159], v[48:51], v[96:99]\n"
"s_waitcnt lgkmcnt(9)\n"
"ds_read_b64_tr_b16 a[152:153], v242 offset:51200\n"
"s_add_u32 s86, s68, 24576\n"
"buffer_load_dwordx4 v[152:155], v239, s[56:59], s86 offen offset:0 sc1\n"
"v_cvt_pk_bf16_f32 v228, v224, v225\n"
"v_cvt_pk_bf16_f32 v229, v226, v227\n"
"v_swap_b32 v114, v116\n"
"v_swap_b32 v115, v117\n"
"v_mfma_f32_16x16x32_bf16 v[104:107], a[128:131], v[48:51], v[104:107]\n"
"ds_read_b64_tr_b16 a[154:155], v242 offset:59392\n"
"ds_write_b64 v237, v[228:229] offset:17920\n"
"v_mfma_f32_16x16x32_bf16 v[108:111], a[128:131], v[52:55], v[108:111]\n"
"ds_read_b64_tr_b16 a[156:157], v243 offset:51200\n"
"v_mbcnt_lo_u32_b32 v172, -1, 0\n"
"v_mfma_f32_16x16x32_bf16 v[56:59], a[132:135], v[112:115], v[56:59]\n"
"ds_read_b64_tr_b16 a[158:159], v243 offset:59392\n"
"v_add_u32 v230, 0x10000, v230\n"
"v_mfma_f32_16x16x32_bf16 v[60:63], a[132:135], v[116:119], v[60:63]\n"
"s_waitcnt lgkmcnt(10)\n"
"s_waitcnt vmcnt(11) lgkmcnt(2)\n"
"s_barrier\n"
"ds_read_b64_tr_b16 a[128:129], v230 offset:0\n"
"v_mfma_f32_16x16x32_bf16 v[68:71], a[136:139], v[116:119], v[68:71]\n"
"ds_read_b64_tr_b16 a[130:131], v230 offset:8192\n"
"v_add_u32 v231, 0x10000, v231\n"
"v_mfma_f32_16x16x32_bf16 v[64:67], a[136:139], v[112:115], v[64:67]\n"
"ds_read_b64_tr_b16 a[132:133], v231 offset:0\n"
"v_mbcnt_hi_u32_b32 v172, -1, v172\n"
"v_mfma_f32_16x16x32_bf16 v[72:75], a[140:143], v[112:115], v[72:75]\n"
"ds_read_b64_tr_b16 a[134:135], v231 offset:8192\n"
"s_add_u32 s86, s68, 24576\n"
"buffer_load_dwordx4 v[156:159], v239, s[56:59], s86 offen offset:1024 sc1\n"
"v_add_u32 v232, 0x10000, v232\n"
"v_mfma_f32_16x16x32_bf16 v[76:79], a[140:143], v[116:119], v[76:79]\n"
"s_waitcnt lgkmcnt(9)\n"
"ds_read_b64_tr_b16 a[136:137], v232 offset:0\n"
"buffer_load_dwordx4 v252, s[36:39], s44 offen offset:0 nt lds\n"
"v_mfma_f32_16x16x32_bf16 v[84:87], a[144:147], v[116:119], v[84:87]\n"
"ds_read_b64_tr_b16 a[138:139], v232 offset:8192\n"
"ds_read_b128 a[24:27], v244 offset:16384\n"
"v_add_u32 v233, 0x10000, v233\n"
"v_mfma_f32_16x16x32_bf16 v[80:83], a[144:147], v[112:115], v[80:83]\n"
"s_add_u32 m0, m0, 0x380\n"
"ds_read_b64_tr_b16 a[140:141], v233 offset:0\n"
"v_mfma_f32_16x16x32_bf16 v[88:91], a[148:151], v[112:115], v[88:91]\n"
"s_waitcnt vmcnt(14)\n"
"ds_read_b64_tr_b16 a[142:143], v233 offset:8192\n"
"v_add_u32 v241, 0x10000, v241\n"
"v_mfma_f32_16x16x32_bf16 v[92:95], a[148:151], v[116:119], v[92:95]\n"
"s_waitcnt lgkmcnt(9)\n"
"ds_read_b64_tr_b16 a[144:145], v241 offset:0\n"
"v_mfma_f32_16x16x32_bf16 v[100:103], a[152:155], v[116:119], v[100:103]\n"
"ds_read_b64_tr_b16 a[146:147], v241 offset:8192\n"
"ds_read_b128 a[56:59], v244 offset:17408\n"
"v_add_u32 v242, 0x10000, v242\n"
"v_mfma_f32_16x16x32_bf16 v[96:99], a[152:155], v[112:115], v[96:99]\n"
"ds_read_b64_tr_b16 a[148:149], v242 offset:0\n"
"s_add_u32 s86, s68, 24576\n"
"buffer_load_dwordx4 v[160:163], v239, s[56:59], s86 offen offset:2048 sc1\n"
"v_swap_b32 v122, v124\n"
"v_swap_b32 v123, v125\n"
"v_mfma_f32_16x16x32_bf16 v[104:107], a[156:159], v[112:115], v[104:107]\n"
"ds_read_b64_tr_b16 a[150:151], v242 offset:8192\n"
"v_add_u32 v243, 0x10000, v243\n"
"v_mfma_f32_16x16x32_bf16 v[108:111], a[156:159], v[116:119], v[108:111]\n"
"s_waitcnt lgkmcnt(10)\n"
"ds_read_b64_tr_b16 a[152:153], v243 offset:0\n"
"buffer_load_dwordx4 v252, s[36:39], s44 offen offset:128 nt lds\n"
"v_mfma_f32_16x16x32_bf16 v[56:59], a[128:131], v[120:123], v[56:59]\n"
"ds_read_b64_tr_b16 a[154:155], v243 offset:8192\n"
"ds_read_b128 a[88:91], v245 offset:16384\n"
"v_mfma_f32_16x16x32_bf16 v[60:63], a[128:131], v[124:127], v[60:63]\n"
"s_add_u32 m0, m0, 0x380\n"
"ds_read_b64_tr_b16 a[156:157], v230 offset:16384\n"
"v_mfma_f32_16x16x32_bf16 v[68:71], a[132:135], v[124:127], v[68:71]\n"
"s_and_b32 s91, s84, 1\n"
"ds_read_b64_tr_b16 a[158:159], v230 offset:24576\n"
"v_mfma_f32_16x16x32_bf16 v[64:67], a[132:135], v[120:123], v[64:67]\n"
"s_waitcnt lgkmcnt(10)\n"
"ds_read_b64_tr_b16 a[128:129], v231 offset:16384\n"
"v_mfma_f32_16x16x32_bf16 v[72:75], a[136:139], v[120:123], v[72:75]\n"
"ds_read_b64_tr_b16 a[130:131], v231 offset:24576\n"
"s_add_u32 s86, s68, 24576\n"
"buffer_load_dwordx4 v[164:167], v239, s[56:59], s86 offen offset:3072 sc1\n"
"v_mfma_f32_16x16x32_bf16 v[76:79], a[136:139], v[124:127], v[76:79]\n"
"ds_read_b64_tr_b16 a[132:133], v232 offset:16384\n"
"ds_read_b128 a[120:123], v245 offset:17408\n"
"v_mfma_f32_16x16x32_bf16 v[84:87], a[140:143], v[124:127], v[84:87]\n"
"s_lshl_b32 s91, s91, 14\n"
"ds_read_b64_tr_b16 a[134:135], v232 offset:24576\n"
"v_mfma_f32_16x16x32_bf16 v[80:83], a[140:143], v[120:123], v[80:83]\n"
"s_waitcnt lgkmcnt(10)\n"
"s_waitcnt vmcnt(13)\n"
"ds_read_b64_tr_b16 a[136:137], v233 offset:16384\n"
"buffer_load_dwordx4 v252, s[36:39], s44 offen offset:256 nt lds\n"
"v_mfma_f32_16x16x32_bf16 v[88:91], a[144:147], v[120:123], v[88:91]\n"
"s_add_u32 m0, m0, 0x380\n"
"ds_read_b64_tr_b16 a[138:139], v233 offset:24576\n"
"v_mfma_f32_16x16x32_bf16 v[92:95], a[144:147], v[124:127], v[92:95]\n"
"ds_read_b64_tr_b16 a[140:141], v241 offset:16384\n"
"ds_read_b128 a[28:31], v244 offset:20480\n"
"v_mfma_f32_16x16x32_bf16 v[100:103], a[148:151], v[124:127], v[100:103]\n"
"ds_read_b64_tr_b16 a[142:143], v241 offset:24576\n"
"v_and_b32 v168, 15, v172\n"
"v_mfma_f32_16x16x32_bf16 v[96:99], a[148:151], v[120:123], v[96:99]\n"
"s_waitcnt lgkmcnt(10)\n"
"ds_read_b64_tr_b16 a[144:145], v242 offset:16384\n"
"v_swap_b32 v130, v132\n"
"v_swap_b32 v131, v133\n"
"v_mfma_f32_16x16x32_bf16 v[104:107], a[152:155], v[120:123], v[104:107]\n"
"ds_read_b64_tr_b16 a[146:147], v242 offset:24576\n"
"v_lshrrev_b32 v169, 4, v172\n"
"v_mfma_f32_16x16x32_bf16 v[108:111], a[152:155], v[124:127], v[108:111]\n"
"ds_read_b64_tr_b16 a[148:149], v243 offset:16384\n"
"ds_read_b128 a[60:63], v244 offset:21504\n"
"v_mfma_f32_16x16x32_bf16 v[56:59], a[156:159], v[128:131], v[56:59]\n"
"ds_read_b64_tr_b16 a[150:151], v243 offset:24576\n"
"v_lshrrev_b32 v170, 3, v168\n"
"v_mfma_f32_16x16x32_bf16 v[60:63], a[156:159], v[132:135], v[60:63]\n"
"s_waitcnt lgkmcnt(10)\n"
"ds_read_b64_tr_b16 a[152:153], v230 offset:2048\n"
"buffer_load_dwordx4 v252, s[36:39], s44 offen offset:384 nt lds\n"
"v_mfma_f32_16x16x32_bf16 v[68:71], a[128:131], v[132:135], v[68:71]\n"
"s_add_u32 m0, m0, 0x3580\n"
"ds_read_b64_tr_b16 a[154:155], v230 offset:10240\n"
"v_mfma_f32_16x16x32_bf16 v[64:67], a[128:131], v[128:131], v[64:67]\n"
"ds_read_b64_tr_b16 a[156:157], v231 offset:2048\n"
"ds_read_b128 a[92:95], v245 offset:20480\n"
"v_mfma_f32_16x16x32_bf16 v[72:75], a[132:135], v[128:131], v[72:75]\n"
"ds_read_b64_tr_b16 a[158:159], v231 offset:10240\n"
"v_lshlrev_b32 v170, 12, v170\n"
"v_mfma_f32_16x16x32_bf16 v[76:79], a[132:135], v[132:135], v[76:79]\n"
"s_waitcnt lgkmcnt(10)\n"
"ds_read_b64_tr_b16 a[128:129], v232 offset:2048\n"
"v_mfma_f32_16x16x32_bf16 v[84:87], a[136:139], v[132:135], v[84:87]\n"
"ds_read_b64_tr_b16 a[130:131], v232 offset:10240\n"
"v_and_b32 v171, 7, v168\n"
"v_mfma_f32_16x16x32_bf16 v[80:83], a[136:139], v[128:131], v[80:83]\n"
"s_waitcnt vmcnt(11)\n"
"ds_read_b64_tr_b16 a[132:133], v233 offset:2048\n"
"ds_read_b128 a[124:127], v245 offset:21504\n"
"v_mfma_f32_16x16x32_bf16 v[88:91], a[140:143], v[128:131], v[88:91]\n"
"ds_read_b64_tr_b16 a[134:135], v233 offset:10240\n"
"v_lshl_add_u32 v170, v171, 7, v170\n"
"v_mfma_f32_16x16x32_bf16 v[92:95], a[140:143], v[132:135], v[92:95]\n"
"s_waitcnt lgkmcnt(10)\n"
"ds_read_b64_tr_b16 a[136:137], v241 offset:2048\n"
"buffer_load_dwordx4 v252, s[36:39], s45 offen offset:0 nt lds\n"
"v_mfma_f32_16x16x32_bf16 v[100:103], a[144:147], v[132:135], v[100:103]\n"
"s_add_u32 m0, m0, 0x380\n"
"ds_read_b64_tr_b16 a[138:139], v241 offset:10240\n"
"v_mfma_f32_16x16x32_bf16 v[96:99], a[144:147], v[128:131], v[96:99]\n"
"ds_read_b64_tr_b16 a[140:141], v242 offset:2048\n"
"v_swap_b32 v138, v140\n"
"v_swap_b32 v139, v141\n"
"v_mfma_f32_16x16x32_bf16 v[104:107], a[148:151], v[128:131], v[104:107]\n"
"ds_read_b64_tr_b16 a[142:143], v242 offset:10240\n"
"v_add_u32 v170, s91, v170\n"
"v_mfma_f32_16x16x32_bf16 v[108:111], a[148:151], v[132:135], v[108:111]\n"
"s_waitcnt lgkmcnt(9)\n"
"ds_read_b64_tr_b16 a[144:145], v243 offset:2048\n"
"v_mfma_f32_16x16x32_bf16 v[56:59], a[152:155], v[136:139], v[56:59]\n"
"ds_read_b64_tr_b16 a[146:147], v243 offset:10240\n"
"v_lshrrev_b32 v171, 1, v168\n"
"v_mfma_f32_16x16x32_bf16 v[60:63], a[152:155], v[140:143], v[60:63]\n"
"ds_read_b64_tr_b16 a[148:149], v230 offset:18432\n"
"v_xor_b32 v171, v171, v169\n"
"v_mfma_f32_16x16x32_bf16 v[68:71], a[156:159], v[140:143], v[68:71]\n"
"ds_read_b64_tr_b16 a[150:151], v230 offset:26624\n"
"v_lshl_add_u32 v246, v171, 4, v170\n"
"v_mfma_f32_16x16x32_bf16 v[64:67], a[156:159], v[136:139], v[64:67]\n"
"s_waitcnt lgkmcnt(8)\n"
"ds_read_b64_tr_b16 a[152:153], v231 offset:18432\n"
"buffer_load_dwordx4 v252, s[36:39], s45 offen offset:128 nt lds\n"
"v_mfma_f32_16x16x32_bf16 v[72:75], a[128:131], v[136:139], v[72:75]\n"
"s_add_u32 m0, m0, 0x380\n"
"ds_read_b64_tr_b16 a[154:155], v231 offset:26624\n"
"v_mfma_f32_16x16x32_bf16 v[76:79], a[128:131], v[140:143], v[76:79]\n"
"ds_read_b64_tr_b16 a[156:157], v232 offset:18432\n"
"v_xor_b32 v247, 64, v246\n"
"v_mfma_f32_16x16x32_bf16 v[84:87], a[132:135], v[140:143], v[84:87]\n"
"s_add_u32 s91, s91, 0x10000\n"
"ds_read_b64_tr_b16 a[158:159], v232 offset:26624\n"
"v_mfma_f32_16x16x32_bf16 v[80:83], a[132:135], v[136:139], v[80:83]\n"
"s_waitcnt lgkmcnt(8)\n"
"s_waitcnt vmcnt(10)\n"
"ds_read_b64_tr_b16 a[128:129], v233 offset:18432\n"
"v_mfma_f32_16x16x32_bf16 v[88:91], a[136:139], v[136:139], v[88:91]\n"
"ds_read_b64_tr_b16 a[130:131], v233 offset:26624\n"
"v_bfe_u32 v168, v172, 2, 3\n"
"v_mfma_f32_16x16x32_bf16 v[92:95], a[136:139], v[140:143], v[92:95]\n"
"ds_read_b64_tr_b16 a[132:133], v241 offset:18432\n"
"v_lshrrev_b32 v169, 5, v172\n"
"v_mfma_f32_16x16x32_bf16 v[100:103], a[140:143], v[140:143], v[100:103]\n"
"ds_read_b64_tr_b16 a[134:135], v241 offset:26624\n"
"v_lshlrev_b32 v169, 12, v169\n"
"v_mfma_f32_16x16x32_bf16 v[96:99], a[140:143], v[136:139], v[96:99]\n"
"s_waitcnt lgkmcnt(8)\n"
"ds_read_b64_tr_b16 a[136:137], v242 offset:18432\n"
"buffer_load_dwordx4 v252, s[36:39], s45 offen offset:256 nt lds\n"
"v_swap_b32 v146, v148\n"
"v_swap_b32 v147, v149\n"
"v_mfma_f32_16x16x32_bf16 v[104:107], a[144:147], v[136:139], v[104:107]\n"
"s_add_u32 m0, m0, 0x380\n"
"ds_read_b64_tr_b16 a[138:139], v242 offset:26624\n"
"v_mfma_f32_16x16x32_bf16 v[108:111], a[144:147], v[140:143], v[108:111]\n"
"ds_read_b64_tr_b16 a[140:141], v243 offset:18432\n"
"v_lshl_add_u32 v169, v168, 7, v169\n"
"v_mfma_f32_16x16x32_bf16 v[56:59], a[148:151], v[144:147], v[56:59]\n"
"ds_read_b64_tr_b16 a[142:143], v243 offset:26624\n"
"v_add_u32 v230, 0x10000, v230\n"
"v_mfma_f32_16x16x32_bf16 v[60:63], a[148:151], v[148:151], v[60:63]\n"
"s_waitcnt lgkmcnt(8)\n"
"s_waitcnt vmcnt(13)\n"
"s_barrier\n"
"ds_read_b64_tr_b16 a[144:145], v230 offset:0\n"
"v_mfma_f32_16x16x32_bf16 v[68:71], a[152:155], v[148:151], v[68:71]\n"
"ds_read_b64_tr_b16 a[146:147], v230 offset:8192\n"
"v_add_u32 v231, 0x10000, v231\n"
"v_mfma_f32_16x16x32_bf16 v[64:67], a[152:155], v[144:147], v[64:67]\n"
"ds_read_b64_tr_b16 a[148:149], v231 offset:0\n"
"v_and_b32 v170, 3, v172\n"
"v_mfma_f32_16x16x32_bf16 v[72:75], a[156:159], v[144:147], v[72:75]\n"
"ds_read_b64_tr_b16 a[150:151], v231 offset:8192\n"
"v_add_u32 v232, 0x10000, v232\n"
"v_mfma_f32_16x16x32_bf16 v[76:79], a[156:159], v[148:151], v[76:79]\n"
"s_waitcnt lgkmcnt(8)\n"
"ds_read_b64_tr_b16 a[152:153], v232 offset:0\n"
"buffer_load_dwordx4 v252, s[36:39], s45 offen offset:384 nt lds\n"
"v_mfma_f32_16x16x32_bf16 v[84:87], a[128:131], v[148:151], v[84:87]\n"
"ds_read_b64_tr_b16 a[154:155], v232 offset:8192\n"
"v_add_u32 v233, 0x10000, v233\n"
"v_mfma_f32_16x16x32_bf16 v[80:83], a[128:131], v[144:147], v[80:83]\n"
"s_add_u32 m0, m0, 0x3580\n"
"s_add_u32 s44, s44, 0x8000\n"
"s_add_u32 s45, s45, 0x8000\n"
"ds_read_b64_tr_b16 a[156:157], v233 offset:0\n"
"v_mfma_f32_16x16x32_bf16 v[88:91], a[132:135], v[144:147], v[88:91]\n"
"s_waitcnt vmcnt(10)\n"
"ds_read_b64_tr_b16 a[158:159], v233 offset:8192\n"
"v_add_u32 v241, 0x10000, v241\n"
"v_mfma_f32_16x16x32_bf16 v[92:95], a[132:135], v[148:151], v[92:95]\n"
"s_waitcnt lgkmcnt(8)\n"
"ds_read_b64_tr_b16 a[128:129], v241 offset:0\n"
"v_mfma_f32_16x16x32_bf16 v[100:103], a[136:139], v[148:151], v[100:103]\n"
"ds_read_b64_tr_b16 a[130:131], v241 offset:8192\n"
"v_add_u32 v242, 0x10000, v242\n"
"v_mfma_f32_16x16x32_bf16 v[96:99], a[136:139], v[144:147], v[96:99]\n"
"ds_read_b64_tr_b16 a[132:133], v242 offset:0\n"
"v_swap_b32 v154, v156\n"
"v_swap_b32 v155, v157\n"
"v_mfma_f32_16x16x32_bf16 v[104:107], a[140:143], v[144:147], v[104:107]\n"
"ds_read_b64_tr_b16 a[134:135], v242 offset:8192\n"
"v_add_u32 v243, 0x10000, v243\n"
"v_mfma_f32_16x16x32_bf16 v[108:111], a[140:143], v[148:151], v[108:111]\n"
"s_waitcnt lgkmcnt(8)\n"
"ds_read_b64_tr_b16 a[136:137], v243 offset:0\n"
"buffer_load_dwordx4 v252, s[36:39], s44 offen offset:0 nt lds\n"
"v_mfma_f32_16x16x32_bf16 v[56:59], a[144:147], v[152:155], v[56:59]\n"
"ds_read_b64_tr_b16 a[138:139], v243 offset:8192\n"
"v_lshl_add_u32 v169, v170, 3, v169\n"
"v_mfma_f32_16x16x32_bf16 v[60:63], a[144:147], v[156:159], v[60:63]\n"
"s_add_u32 m0, m0, 0x380\n"
"ds_read_b64_tr_b16 a[140:141], v230 offset:16384\n"
"v_lshrrev_b32 v168, 1, v168\n"
"v_mfma_f32_16x16x32_bf16 v[68:71], a[148:151], v[156:159], v[68:71]\n"
"ds_read_b64_tr_b16 a[142:143], v230 offset:24576\n"
"v_lshl_add_u32 v248, v168, 5, v169\n"
"v_mfma_f32_16x16x32_bf16 v[64:67], a[148:151], v[152:155], v[64:67]\n"
"s_waitcnt lgkmcnt(8)\n"
"ds_read_b64_tr_b16 a[144:145], v231 offset:16384\n"
"v_add_u32 v248, s91, v248\n"
"v_mfma_f32_16x16x32_bf16 v[72:75], a[152:155], v[152:155], v[72:75]\n"
"ds_read_b64_tr_b16 a[146:147], v231 offset:24576\n"
"v_xor_b32 v249, 32, v248\n"
"v_mfma_f32_16x16x32_bf16 v[76:79], a[152:155], v[156:159], v[76:79]\n"
"ds_read_b64_tr_b16 a[148:149], v232 offset:16384\n"
"v_xor_b32 v250, 64, v248\n"
"v_mfma_f32_16x16x32_bf16 v[84:87], a[156:159], v[156:159], v[84:87]\n"
"ds_read_b64_tr_b16 a[150:151], v232 offset:24576\n"
"v_xor_b32 v251, 96, v248\n"
"v_mfma_f32_16x16x32_bf16 v[80:83], a[156:159], v[152:155], v[80:83]\n"
"s_waitcnt lgkmcnt(8)\n"
"s_waitcnt vmcnt(7)\n"
"ds_read_b64_tr_b16 a[152:153], v233 offset:16384\n"
"buffer_load_dwordx4 v252, s[36:39], s44 offen offset:128 nt lds\n"
"v_mov_b32 v192, 0\n"
"v_mfma_f32_16x16x32_bf16 v[88:91], a[128:131], v[152:155], v[88:91]\n"
"ds_read_b64_tr_b16 a[154:155], v233 offset:24576\n"
"v_mov_b32 v193, 0\n"
"v_mfma_f32_16x16x32_bf16 v[92:95], a[128:131], v[156:159], v[92:95]\n"
"s_add_u32 m0, m0, 0x380\n"
"ds_read_b64_tr_b16 a[156:157], v241 offset:16384\n"
"v_mov_b32 v194, 0\n"
"v_mfma_f32_16x16x32_bf16 v[100:103], a[132:135], v[156:159], v[100:103]\n"
"ds_read_b64_tr_b16 a[158:159], v241 offset:24576\n"
"v_mov_b32 v195, 0\n"
"v_mfma_f32_16x16x32_bf16 v[96:99], a[132:135], v[152:155], v[96:99]\n"
"s_waitcnt lgkmcnt(8)\n"
"ds_read_b64_tr_b16 a[128:129], v242 offset:16384\n"
"v_swap_b32 v162, v164\n"
"v_swap_b32 v163, v165\n"
"v_mov_b32 v196, 0\n"
"v_mfma_f32_16x16x32_bf16 v[104:107], a[136:139], v[152:155], v[104:107]\n"
"ds_read_b64_tr_b16 a[130:131], v242 offset:24576\n"
"v_mov_b32 v197, 0\n"
"v_mfma_f32_16x16x32_bf16 v[108:111], a[136:139], v[156:159], v[108:111]\n"
"ds_read_b64_tr_b16 a[132:133], v243 offset:16384\n"
"v_mov_b32 v198, 0\n"
"v_mfma_f32_16x16x32_bf16 v[56:59], a[140:143], v[160:163], v[56:59]\n"
"ds_read_b64_tr_b16 a[134:135], v243 offset:24576\n"
"buffer_load_dwordx4 v252, s[36:39], s44 offen offset:256 nt lds\n"
"v_mov_b32 v199, 0\n"
"v_mfma_f32_16x16x32_bf16 v[60:63], a[140:143], v[164:167], v[60:63]\n"
"s_waitcnt lgkmcnt(8)\n"
"s_add_u32 m0, m0, 0x380\n"
"v_mov_b32 v200, 0\n"
"v_mfma_f32_16x16x32_bf16 v[68:71], a[144:147], v[164:167], v[68:71]\n"
"v_mov_b32 v201, 0\n"
"v_mfma_f32_16x16x32_bf16 v[64:67], a[144:147], v[160:163], v[64:67]\n"
"s_waitcnt lgkmcnt(4)\n"
"v_mov_b32 v202, 0\n"
"v_mfma_f32_16x16x32_bf16 v[72:75], a[148:151], v[160:163], v[72:75]\n"
"v_mov_b32 v203, 0\n"
"v_mfma_f32_16x16x32_bf16 v[76:79], a[148:151], v[164:167], v[76:79]\n"
"buffer_load_dwordx4 v252, s[36:39], s44 offen offset:384 nt lds\n"
"v_mov_b32 v204, 0\n"
"v_mfma_f32_16x16x32_bf16 v[84:87], a[152:155], v[164:167], v[84:87]\n"
"s_add_u32 m0, m0, 0x3580\n"
"v_mov_b32 v205, 0\n"
"v_cvt_pk_bf16_f32 v56, v56, v57\n"
"v_mfma_f32_16x16x32_bf16 v[80:83], a[152:155], v[160:163], v[80:83]\n"
"s_waitcnt lgkmcnt(0)\n"
"v_mov_b32 v206, 0\n"
"v_cvt_pk_bf16_f32 v57, v58, v59\n"
"v_mfma_f32_16x16x32_bf16 v[88:91], a[156:159], v[160:163], v[88:91]\n"
"v_mov_b32 v207, 0\n"
"v_cvt_pk_bf16_f32 v58, v60, v61\n"
"v_mfma_f32_16x16x32_bf16 v[92:95], a[156:159], v[164:167], v[92:95]\n"
"v_mov_b32 v176, 0\n"
"v_cvt_pk_bf16_f32 v59, v62, v63\n"
"v_mfma_f32_16x16x32_bf16 v[100:103], a[128:131], v[164:167], v[100:103]\n"
"buffer_load_dwordx4 v252, s[36:39], s45 offen offset:0 nt lds\n"
"v_mov_b32 v177, 0\n"
"v_mfma_f32_16x16x32_bf16 v[96:99], a[128:131], v[160:163], v[96:99]\n"
"s_add_u32 m0, m0, 0x380\n"
"v_mov_b32 v178, 0\n"
"v_cvt_pk_bf16_f32 v64, v64, v65\n"
"v_mfma_f32_16x16x32_bf16 v[104:107], a[132:135], v[160:163], v[104:107]\n"
"v_mov_b32 v179, 0\n"
"v_cvt_pk_bf16_f32 v65, v66, v67\n"
"v_mfma_f32_16x16x32_bf16 v[108:111], a[132:135], v[164:167], v[108:111]\n"
"v_cvt_pk_bf16_f32 v66, v68, v69\n"
"v_cvt_pk_bf16_f32 v67, v70, v71\n"
"s_nop 0\n"
"s_nop 7\n"
"v_cvt_pk_bf16_f32 v72, v72, v73\n"
"v_cvt_pk_bf16_f32 v73, v74, v75\n"
"v_cvt_pk_bf16_f32 v74, v76, v77\n"
"v_cvt_pk_bf16_f32 v75, v78, v79\n"
"v_cvt_pk_bf16_f32 v80, v80, v81\n"
"s_nop 0\n"
"buffer_load_dwordx4 v252, s[36:39], s45 offen offset:128 nt lds\n"
"s_add_u32 m0, m0, 0x380\n"
"v_cvt_pk_bf16_f32 v81, v82, v83\n"
"v_cvt_pk_bf16_f32 v82, v84, v85\n"
"v_cvt_pk_bf16_f32 v83, v86, v87\n"
"s_nop 0\n"
"v_cvt_pk_bf16_f32 v88, v88, v89\n"
"v_cvt_pk_bf16_f32 v89, v90, v91\n"
"v_cvt_pk_bf16_f32 v90, v92, v93\n"
"v_cvt_pk_bf16_f32 v91, v94, v95\n"
"v_cvt_pk_bf16_f32 v96, v96, v97\n"
"v_cvt_pk_bf16_f32 v97, v98, v99\n"
"s_nop 0\n"
"v_cvt_pk_bf16_f32 v98, v100, v101\n"
"v_cvt_pk_bf16_f32 v99, v102, v103\n"
"v_cvt_pk_bf16_f32 v104, v104, v105\n"
"v_cvt_pk_bf16_f32 v105, v106, v107\n"
"v_cvt_pk_bf16_f32 v106, v108, v109\n"
"v_cvt_pk_bf16_f32 v107, v110, v111\n"
"s_nop 0\n"
"buffer_load_dwordx4 v252, s[36:39], s45 offen offset:256 nt lds\n"
"s_add_u32 m0, m0, 0x380\n"
"s_nop 0\n"
"buffer_load_dwordx4 v252, s[36:39], s45 offen offset:384 nt lds\n"
"s_add_u32 s44, s44, 0x8000\n"
"s_add_u32 s45, s45, 0x8000\n"
"v_mov_b32 v255, v239\n"
"v_mov_b32 v240, 0\n"
"v_mov_b32 v241, 0\n"
"v_mov_b32 v242, 0\n"
"v_mov_b32 v243, 0\n"
"s_waitcnt vmcnt(8) lgkmcnt(0)\n"
"s_barrier\n"
"ds_read_b128 v[208:211], v246 offset:0\n"
"s_add_u32 m0, m0, 0x3580\n"
"s_nop 0\n"
"buffer_load_dwordx4 v253, s[40:43], s46 offen offset:0 nt lds\n"
"s_add_u32 s86, s69, 0\n"
"buffer_store_dwordx4 v[56:59], v255, s[64:67], s86 offen sc1\n"
"ds_read_b128 v[212:215], v247 offset:0\n"
"ds_read_b128 v[216:219], v246 offset:1024\n"
"ds_read_b128 v[220:223], v247 offset:1024\n"
"ds_read_b128 v[224:227], v246 offset:2048\n"
"s_add_u32 s86, s69, 4096\n"
"buffer_store_dwordx4 v[64:67], v255, s[64:67], s86 offen sc1\n"
"s_add_u32 m0, m0, 0x380\n"
"s_nop 0\n"
"buffer_load_dwordx4 v253, s[40:43], s46 offen offset:128 nt lds\n"
"ds_read_b128 v[228:231], v247 offset:2048\n"
"s_add_u32 s86, s69, 8192\n"
"buffer_store_dwordx4 v[72:75], v255, s[64:67], s86 offen sc1\n"
"ds_read_b128 v[232:235], v246 offset:3072\n"
"s_add_u32 m0, m0, 0x380\n"
"s_nop 0\n"
"buffer_load_dwordx4 v253, s[40:43], s46 offen offset:256 nt lds\n"
"s_waitcnt lgkmcnt(6)\n"
"ds_read_b128 v[236:239], v247 offset:3072\n"
"s_add_u32 s86, s69, 12288\n"
"buffer_store_dwordx4 v[80:83], v255, s[64:67], s86 offen sc1\n"
"v_mfma_f32_16x16x32_bf16 v[128:131], v[208:211], a[0:3], v[192:195]\n"
"v_mfma_f32_16x16x32_bf16 v[132:135], v[208:211], a[32:35], v[196:199]\n"
"v_mfma_f32_16x16x32_bf16 v[136:139], v[208:211], a[64:67], v[200:203]\n"
"s_add_u32 m0, m0, 0x380\n"
"s_nop 0\n"
"buffer_load_dwordx4 v253, s[40:43], s46 offen offset:384 nt lds\n"
"v_mfma_f32_16x16x32_bf16 v[140:143], v[208:211], a[96:99], v[204:207]\n"
"s_waitcnt lgkmcnt(6)\n"
"ds_read_b128 v[208:211], v246 offset:8192\n"
"v_mfma_f32_16x16x32_bf16 v[128:131], v[212:215], a[4:7], v[128:131]\n"
"v_mfma_f32_16x16x32_bf16 v[132:135], v[212:215], a[36:39], v[132:135]\n"
"s_add_u32 s86, s69, 16384\n"
"buffer_store_dwordx4 v[88:91], v255, s[64:67], s86 offen sc1\n"
"v_mfma_f32_16x16x32_bf16 v[136:139], v[212:215], a[68:71], v[136:139]\n"
"v_mfma_f32_16x16x32_bf16 v[140:143], v[212:215], a[100:103], v[140:143]\n"
"s_waitcnt lgkmcnt(6)\n"
"ds_read_b128 v[212:215], v247 offset:8192\n"
"v_mfma_f32_16x16x32_bf16 v[128:131], v[216:219], a[8:11], v[128:131]\n"
"v_mfma_f32_16x16x32_bf16 v[132:135], v[216:219], a[40:43], v[132:135]\n"
"s_add_u32 m0, m0, 0x3580\n"
"s_nop 0\n"
"buffer_load_dwordx4 v253, s[40:43], s47 offen offset:0 nt lds\n"
"v_mfma_f32_16x16x32_bf16 v[136:139], v[216:219], a[72:75], v[136:139]\n"
"v_mfma_f32_16x16x32_bf16 v[140:143], v[216:219], a[104:107], v[140:143]\n"
"s_waitcnt lgkmcnt(6)\n"
"ds_read_b128 v[216:219], v246 offset:9216\n"
"s_add_u32 s86, s69, 20480\n"
"buffer_store_dwordx4 v[96:99], v255, s[64:67], s86 offen sc1\n"
"v_mfma_f32_16x16x32_bf16 v[128:131], v[220:223], a[12:15], v[128:131]\n"
"v_mfma_f32_16x16x32_bf16 v[132:135], v[220:223], a[44:47], v[132:135]\n"
"v_mfma_f32_16x16x32_bf16 v[136:139], v[220:223], a[76:79], v[136:139]\n"
"v_mfma_f32_16x16x32_bf16 v[140:143], v[220:223], a[108:111], v[140:143]\n"
"s_waitcnt lgkmcnt(6)\n"
"ds_read_b128 v[220:223], v247 offset:9216\n"
"v_mfma_f32_16x16x32_bf16 v[128:131], v[224:227], a[16:19], v[128:131]\n"
"s_add_u32 m0, m0, 0x380\n"
"s_nop 0\n"
"buffer_load_dwordx4 v253, s[40:43], s47 offen offset:128 nt lds\n"
"v_mfma_f32_16x16x32_bf16 v[132:135], v[224:227], a[48:51], v[132:135]\n"
"s_add_u32 s86, s69, 24576\n"
"buffer_store_dwordx4 v[104:107], v255, s[64:67], s86 offen sc1\n"
"v_mfma_f32_16x16x32_bf16 v[136:139], v[224:227], a[80:83], v[136:139]\n"
"v_mfma_f32_16x16x32_bf16 v[140:143], v[224:227], a[112:115], v[140:143]\n"
"s_waitcnt lgkmcnt(6)\n"
"ds_read_b128 v[224:227], v246 offset:10240\n"
"v_mfma_f32_16x16x32_bf16 v[128:131], v[228:231], a[20:23], v[128:131]\n"
"v_mfma_f32_16x16x32_bf16 v[132:135], v[228:231], a[52:55], v[132:135]\n"
"v_mfma_f32_16x16x32_bf16 v[136:139], v[228:231], a[84:87], v[136:139]\n"
"v_mfma_f32_16x16x32_bf16 v[140:143], v[228:231], a[116:119], v[140:143]\n"
"s_waitcnt lgkmcnt(6)\n"
"ds_read_b128 v[228:231], v247 offset:10240\n"
"v_mfma_f32_16x16x32_bf16 v[128:131], v[232:235], a[24:27], v[128:131]\n"
"s_add_u32 m0, m0, 0x380\n"
"s_nop 0\n"
"buffer_load_dwordx4 v253, s[40:43], s47 offen offset:256 nt lds\n"
"v_mfma_f32_16x16x32_bf16 v[132:135], v[232:235], a[56:59], v[132:135]\n"
"v_mfma_f32_16x16x32_bf16 v[136:139], v[232:235], a[88:91], v[136:139]\n"
"v_mfma_f32_16x16x32_bf16 v[140:143], v[232:235], a[120:123], v[140:143]\n"
"s_waitcnt lgkmcnt(6)\n"
"ds_read_b128 v[232:235], v246 offset:11264\n"
"v_mfma_f32_16x16x32_bf16 v[128:131], v[236:239], a[28:31], v[128:131]\n"
"v_mfma_f32_16x16x32_bf16 v[132:135], v[236:239], a[60:63], v[132:135]\n"
"v_mfma_f32_16x16x32_bf16 v[136:139], v[236:239], a[92:95], v[136:139]\n"
"s_add_u32 m0, m0, 0x380\n"
"s_nop 0\n"
"buffer_load_dwordx4 v253, s[40:43], s47 offen offset:384 nt lds\n"
"v_mfma_f32_16x16x32_bf16 v[140:143], v[236:239], a[124:127], v[140:143]\n"
"s_waitcnt lgkmcnt(6)\n"
"ds_read_b128 v[236:239], v247 offset:11264\n"
"v_mfma_f32_16x16x32_bf16 v[144:147], v[208:211], a[0:3], v[192:195]\n"
"v_mfma_f32_16x16x32_bf16 v[148:151], v[208:211], a[32:35], v[196:199]\n"
"v_mfma_f32_16x16x32_bf16 v[152:155], v[208:211], a[64:67], v[200:203]\n"
"v_mfma_f32_16x16x32_bf16 v[156:159], v[208:211], a[96:99], v[204:207]\n"
"s_waitcnt lgkmcnt(6)\n"
"v_mfma_f32_16x16x32_bf16 v[144:147], v[212:215], a[4:7], v[144:147]\n"
"s_add_u32 m0, m0, 0xfffeb580\n"
"s_add_u32 s46, s46, 0x8000\n"
"s_add_u32 s47, s47, 0x8000\n"
"s_lshl_b32 s89, s84, 10\n"
"s_add_u32 s89, s89, 98304\n"
"v_add_u32 v244, s89, v255\n"
"v_xor_b32 v245, 0x400, v244\n"
"v_max3_f32 v188, v128, v129, v130\n"
"v_max3_f32 v189, v132, v133, v134\n"
"v_max3_f32 v190, v136, v137, v138\n"
"v_max3_f32 v191, v140, v141, v142\n"
"v_mfma_f32_16x16x32_bf16 v[148:151], v[212:215], a[36:39], v[148:151]\n"
"v_max_f32 v188, v131, v188\n"
"v_max_f32 v189, v135, v189\n"
"v_max_f32 v190, v139, v190\n"
"v_max_f32 v191, v143, v191\n"
"v_mfma_f32_16x16x32_bf16 v[152:155], v[212:215], a[68:71], v[152:155]\n"
"ds_write_b128 v244, v[188:191]\n"
"v_mfma_f32_16x16x32_bf16 v[156:159], v[212:215], a[100:103], v[156:159]\n"
"s_waitcnt lgkmcnt(6)\n"
"s_lshr_b32 s89, s88, 5\n"
"s_and_b32 s91, s88, 1\n"
"v_mfma_f32_16x16x32_bf16 v[144:147], v[216:219], a[8:11], v[144:147]\n"
"s_lshl_b32 s91, s91, 3\n"
"s_add_u32 s89, s89, s91\n"
"v_mfma_f32_16x16x32_bf16 v[148:151], v[216:219], a[40:43], v[148:151]\n"
"s_lshl_b32 s90, s89, 2\n"
"s_lshr_b32 s91, s87, 2\n"
"v_mfma_f32_16x16x32_bf16 v[152:155], v[216:219], a[72:75], v[152:155]\n"
"s_waitcnt lgkmcnt(0)\n"
"s_barrier\n"
"ds_read_b128 v[180:183], v245\n"
"v_mfma_f32_16x16x32_bf16 v[156:159], v[216:219], a[104:107], v[156:159]\n"
"s_add_u32 s90, s90, s91\n"
"s_mul_i32 s91, s90, 28672\n"
"v_mfma_f32_16x16x32_bf16 v[144:147], v[220:223], a[12:15], v[144:147]\n"
"s_add_u32 s60, s32, s91\n"
"s_addc_u32 s61, s33, 0\n"
"s_and_b32 s61, s61, 0xffff\n"
"v_mfma_f32_16x16x32_bf16 v[148:151], v[220:223], a[44:47], v[148:151]\n"
"s_mov_b32 s62, 28672\n"
"s_mov_b32 s63, 0x20000\n"
"v_mfma_f32_16x16x32_bf16 v[152:155], v[220:223], a[76:79], v[152:155]\n"
"buffer_load_dwordx4 v252, s[36:39], s44 offen offset:0 nt lds\n"
"s_add_u32 s64, s34, s91\n"
"s_addc_u32 s65, s35, 0\n"
"s_and_b32 s65, s65, 0xffff\n"
"v_mfma_f32_16x16x32_bf16 v[156:159], v[220:223], a[108:111], v[156:159]\n"
"s_add_u32 m0, m0, 0x380\n"
"s_mov_b32 s66, 28672\n"
"s_mov_b32 s67, 0x20000\n"
"v_mfma_f32_16x16x32_bf16 v[144:147], v[224:227], a[16:19], v[144:147]\n"
"s_mul_i32 s90, s90, 28\n"
"s_and_b32 s91, s87, 3\n"
"v_mfma_f32_16x16x32_bf16 v[148:151], v[224:227], a[48:51], v[148:151]\n"
"s_add_u32 s90, s90, s91\n"
"s_lshl_b32 s91, s90, 14\n"
"v_mfma_f32_16x16x32_bf16 v[152:155], v[224:227], a[80:83], v[152:155]\n"
"s_waitcnt lgkmcnt(0)\n"
"v_max_f32 v188, v188, v182\n"
"v_max_f32 v189, v189, v183\n"
"v_max_f32 v190, v190, v180\n"
"v_max_f32 v191, v191, v181\n"
"v_mfma_f32_16x16x32_bf16 v[156:159], v[224:227], a[112:115], v[156:159]\n"
"v_mov_b32 v180, v188\n"
"v_mov_b32 v181, v189\n"
"v_mov_b32 v182, v190\n"
"v_mov_b32 v183, v191\n"
"v_permlane32_swap_b32 v188, v180\n"
"v_permlane32_swap_b32 v189, v181\n"
"v_permlane32_swap_b32 v190, v182\n"
"v_permlane32_swap_b32 v191, v183\n"
"v_mfma_f32_16x16x32_bf16 v[144:147], v[228:231], a[20:23], v[144:147]\n"
"v_max_f32 v188, v188, v180\n"
"v_max_f32 v189, v189, v181\n"
"v_max_f32 v190, v190, v182\n"
"v_max_f32 v191, v191, v183\n"
"v_mfma_f32_16x16x32_bf16 v[148:151], v[228:231], a[52:55], v[148:151]\n"
"v_mov_b32 v180, v188\n"
"v_mov_b32 v181, v189\n"
"v_mov_b32 v182, v190\n"
"v_mov_b32 v183, v191\n"
"v_permlane16_swap_b32 v188, v180\n"
"v_permlane16_swap_b32 v189, v181\n"
"v_permlane16_swap_b32 v190, v182\n"
"v_permlane16_swap_b32 v191, v183\n"
"v_mfma_f32_16x16x32_bf16 v[152:155], v[228:231], a[84:87], v[152:155]\n"
"buffer_load_dwordx4 v252, s[36:39], s44 offen offset:128 nt lds\n"
"v_max_f32 v188, v188, v180\n"
"v_max_f32 v189, v189, v181\n"
"v_max_f32 v190, v190, v182\n"
"v_max_f32 v191, v191, v183\n"
"v_mfma_f32_16x16x32_bf16 v[156:159], v[228:231], a[116:119], v[156:159]\n"
"s_add_u32 m0, m0, 0x380\n"
"v_sub_f32 v128, v128, v188\n"
"v_sub_f32 v129, v129, v188\n"
"v_sub_f32 v130, v130, v188\n"
"v_sub_f32 v131, v131, v188\n"
"v_mfma_f32_16x16x32_bf16 v[144:147], v[232:235], a[24:27], v[144:147]\n"
"v_sub_f32 v132, v132, v189\n"
"v_sub_f32 v133, v133, v189\n"
"v_sub_f32 v134, v134, v189\n"
"v_sub_f32 v135, v135, v189\n"
"v_mfma_f32_16x16x32_bf16 v[148:151], v[232:235], a[56:59], v[148:151]\n"
"v_sub_f32 v136, v136, v190\n"
"v_sub_f32 v137, v137, v190\n"
"v_sub_f32 v138, v138, v190\n"
"v_sub_f32 v139, v139, v190\n"
"v_mfma_f32_16x16x32_bf16 v[152:155], v[232:235], a[88:91], v[152:155]\n"
"v_sub_f32 v140, v140, v191\n"
"v_sub_f32 v141, v141, v191\n"
"v_sub_f32 v142, v142, v191\n"
"v_sub_f32 v143, v143, v191\n"
"v_mfma_f32_16x16x32_bf16 v[156:159], v[232:235], a[120:123], v[156:159]\n"
"v_sub_f32 v192, 0, v188\n"
"v_sub_f32 v193, 0, v188\n"
"v_sub_f32 v194, 0, v188\n"
"v_sub_f32 v195, 0, v188\n"
"v_mfma_f32_16x16x32_bf16 v[144:147], v[236:239], a[28:31], v[144:147]\n"
"v_sub_f32 v196, 0, v189\n"
"v_sub_f32 v197, 0, v189\n"
"v_sub_f32 v198, 0, v189\n"
"v_sub_f32 v199, 0, v189\n"
"v_mfma_f32_16x16x32_bf16 v[148:151], v[236:239], a[60:63], v[148:151]\n"
"v_exp_f32 v128, v128\n"
"v_sub_f32 v200, 0, v190\n"
"v_sub_f32 v201, 0, v190\n"
"v_sub_f32 v202, 0, v190\n"
"v_sub_f32 v203, 0, v190\n"
"v_mfma_f32_16x16x32_bf16 v[152:155], v[236:239], a[92:95], v[152:155]\n"
"buffer_load_dwordx4 v252, s[36:39], s44 offen offset:256 nt lds\n"
"v_sub_f32 v204, 0, v191\n"
"v_sub_f32 v205, 0, v191\n"
"v_sub_f32 v206, 0, v191\n"
"v_sub_f32 v207, 0, v191\n"
"v_mfma_f32_16x16x32_bf16 v[156:159], v[236:239], a[124:127], v[156:159]\n"
"v_mfma_f32_32x32x16_bf16 a[128:143], v[176:179], v[176:179], 0\n"
"s_add_u32 m0, m0, 0x380\n"
"s_nop 0\n"
"v_exp_f32 v129, v129\n"
"s_nop 0\n"
"v_exp_f32 v130, v130\n"
"v_mfma_f32_32x32x16_bf16 a[144:159], v[176:179], v[176:179], 0\n"
"s_nop 0\n"
"v_add_f32 v240, v240, v128\n"
"s_nop 0\n"
"v_exp_f32 v131, v131\n"
"v_mfma_f32_32x32x16_bf16 a[160:175], v[176:179], v[176:179], 0\n"
"buffer_load_dwordx4 v252, s[36:39], s44 offen offset:384 nt lds\n"
"s_nop 0\n"
"v_sub_f32 v144, v144, v188\n"
"v_sub_f32 v145, v145, v188\n"
"v_sub_f32 v146, v146, v188\n"
"v_sub_f32 v147, v147, v188\n"
"s_nop 0\n"
"s_add_u32 m0, m0, 0x3580\n"
"v_add_f32 v240, v240, v129\n"
"v_cvt_pk_bf16_f32 v160, v128, v129\n"
"v_sub_f32 v148, v148, v189\n"
"v_sub_f32 v149, v149, v189\n"
"v_sub_f32 v150, v150, v189\n"
"v_sub_f32 v151, v151, v189\n"
"v_mfma_f32_32x32x16_bf16 a[176:191], v[176:179], v[176:179], 0\n"
"v_add_f32 v240, v240, v130\n"
"v_sub_f32 v152, v152, v190\n"
"v_sub_f32 v153, v153, v190\n"
"v_sub_f32 v154, v154, v190\n"
"v_sub_f32 v155, v155, v190\n"
"s_nop 0\n"
"v_exp_f32 v132, v132\n"
"v_sub_f32 v156, v156, v191\n"
"v_sub_f32 v157, v157, v191\n"
"v_sub_f32 v158, v158, v191\n"
"v_sub_f32 v159, v159, v191\n"
"v_mfma_f32_32x32x16_bf16 a[192:207], v[176:179], v[176:179], 0\n"
"v_add_f32 v240, v240, v131\n"
"s_nop 0\n"
"v_exp_f32 v133, v133\n"
"s_nop 0\n"
"v_cvt_pk_bf16_f32 v161, v130, v131\n"
"v_add_f32 v241, v241, v132\n"
"v_mfma_f32_32x32x16_bf16 a[208:223], v[176:179], v[176:179], 0\n"
"buffer_load_dwordx4 v252, s[36:39], s45 offen offset:0 nt lds\n"
"s_nop 0\n"
"s_add_u32 m0, m0, 0x380\n"
"v_exp_f32 v134, v134\n"
"s_nop 0\n"
"v_add_f32 v241, v241, v133\n"
"v_mfma_f32_32x32x16_bf16 a[224:239], v[176:179], v[176:179], 0\n"
"v_cvt_pk_bf16_f32 v164, v132, v133\n"
"s_nop 0\n"
"v_exp_f32 v135, v135\n"
"s_nop 0\n"
"v_add_f32 v241, v241, v134\n"
"v_mfma_f32_32x32x16_bf16 a[240:255], v[176:179], v[176:179], 0\n"
"v_add_f32 v241, v241, v135\n"
"s_nop 0\n"
"v_exp_f32 v136, v136\n"
"s_nop 0\n"
"buffer_load_dwordx4 v252, s[36:39], s45 offen offset:128 nt lds\n"
"v_mfma_f32_32x32x16_bf16 v[0:15], v[176:179], v[176:179], 0\n"
"v_cvt_pk_bf16_f32 v165, v134, v135\n"
"s_nop 0\n"
"s_add_u32 m0, m0, 0x380\n"
"s_nop 0\n"
"v_exp_f32 v137, v137\n"
"v_mfma_f32_32x32x16_bf16 v[16:31], v[176:179], v[176:179], 0\n"
"v_exp_f32 v138, v138\n"
"s_nop 0\n"
"v_add_f32 v242, v242, v136\n"
"s_nop 0\n"
"v_add_f32 v242, v242, v137\n"
"v_mfma_f32_32x32x16_bf16 v[32:47], v[176:179], v[176:179], 0\n"
"v_exp_f32 v139, v139\n"
"s_nop 0\n"
"buffer_load_dwordx4 v252, s[36:39], s45 offen offset:256 nt lds\n"
"s_nop 0\n"
"v_cvt_pk_bf16_f32 v168, v136, v137\n"
"v_mfma_f32_32x32x16_bf16 v[48:63], v[176:179], v[176:179], 0\n"
"v_add_f32 v242, v242, v138\n"
"s_nop 0\n"
"s_add_u32 m0, m0, 0x380\n"
"v_exp_f32 v140, v140\n"
"s_nop 0\n"
"v_exp_f32 v141, v141\n"
"v_mfma_f32_32x32x16_bf16 v[64:79], v[176:179], v[176:179], 0\n"
"v_add_f32 v242, v242, v139\n"
"v_cvt_pk_bf16_f32 v169, v138, v139\n"
"s_nop 0\n"
"buffer_load_dwordx4 v252, s[36:39], s45 offen offset:384 nt lds\n"
"s_nop 0\n"
"s_add_u32 m0, m0, 0x13580\n"
"v_exp_f32 v142, v142\n"
"v_mfma_f32_32x32x16_bf16 v[80:95], v[176:179], v[176:179], 0\n"
"v_add_f32 v243, v243, v140\n"
"v_add_f32 v243, v243, v141\n"
"s_nop 0\n"
"v_exp_f32 v143, v143\n"
"v_mfma_f32_32x32x16_bf16 v[96:111], v[176:179], v[176:179], 0\n"
"v_cvt_pk_bf16_f32 v172, v140, v141\n"
"v_add_f32 v243, v243, v142\n"
"s_nop 0\n"
"s_waitcnt vmcnt(23) lgkmcnt(0)\n"
"s_barrier\n"
"v_add_f32 v243, v243, v143\n"
"s_nop 0\n"
"ds_read_b128 v[208:211], v246 offset:32768\n"
"v_cvt_pk_bf16_f32 v173, v142, v143\n"
"v_mfma_f32_32x32x16_bf16 v[112:127], v[176:179], v[176:179], 0\n"
"s_add_u32 s44, s44, 0x8000\n"
"buffer_load_dwordx4 v253, s[40:43], s46 offen offset:0 nt lds\n"
"s_nop 0\n"
"s_add_u32 m0, m0, 0x380\n"
"ds_read_b128 v[212:215], v247 offset:32768\n"
"s_nop 0\n"
"s_add_u32 s45, s45, 0x8000\n"
"ds_read_b128 v[216:219], v246 offset:33792\n"
"s_nop 0\n"
"ds_read_b128 v[220:223], v247 offset:33792\n"
"s_nop 0\n"
"ds_read_b128 v[224:227], v246 offset:34816\n"
"s_nop 0\n"
"buffer_load_dwordx4 v253, s[40:43], s46 offen offset:128 nt lds\n"
"s_nop 0\n"
"s_add_u32 m0, m0, 0x380\n"
"s_nop 0\n"
"ds_read_b128 v[228:231], v247 offset:34816\n"
"s_nop 0\n"
"v_exp_f32 v144, v144\n"
"s_nop 0\n"
"v_add_f32 v240, v240, v144\n"
"s_nop 0\n"
"ds_read_b128 v[232:235], v246 offset:35840\n"
"s_nop 0\n"
"buffer_load_dwordx4 v253, s[40:43], s46 offen offset:256 nt lds\n"
"s_nop 0\n"
"s_add_u32 m0, m0, 0x380\n"
"v_exp_f32 v145, v145\n"
"s_nop 0\n"
"v_exp_f32 v146, v146\n"
"s_nop 0\n"
"s_waitcnt lgkmcnt(6)\n"
"ds_read_b128 v[236:239], v247 offset:35840\n"
"v_mfma_f32_16x16x32_bf16 v[128:131], v[208:211], a[0:3], v[192:195]\n"
"v_exp_f32 v147, v147\n"
"s_lshl_b32 s58, s89, 12\n"
"v_mfma_f32_16x16x32_bf16 v[132:135], v[208:211], a[32:35], v[196:199]\n"
"v_exp_f32 v148, v148\n"
"s_add_u32 s58, s58, 0x4000000\n"
"v_mfma_f32_16x16x32_bf16 v[136:139], v[208:211], a[64:67], v[200:203]\n"
"buffer_load_dwordx4 v253, s[40:43], s46 offen offset:384 nt lds\n"
"s_sub_u32 s91, s58, s91\n"
"v_mfma_f32_16x16x32_bf16 v[140:143], v[208:211], a[96:99], v[204:207]\n"
"s_waitcnt lgkmcnt(6)\n"
"ds_read_b128 v[208:211], v246 offset:40960\n"
"s_sub_u32 s56, s94, s91\n"
"s_subb_u32 s57, s95, 0\n"
"v_mfma_f32_16x16x32_bf16 v[128:131], v[212:215], a[4:7], v[128:131]\n"
"s_add_u32 m0, m0, 0x3580\n"
"v_exp_f32 v149, v149\n"
"s_and_b32 s57, s57, 0xffff\n"
"v_mfma_f32_16x16x32_bf16 v[132:135], v[212:215], a[36:39], v[132:135]\n"
"v_add_f32 v240, v240, v145\n"
"v_cvt_pk_bf16_f32 v162, v144, v145\n"
"s_mov_b32 s58, 409600\n"
"v_mfma_f32_16x16x32_bf16 v[136:139], v[212:215], a[68:71], v[136:139]\n"
"v_exp_f32 v150, v150\n"
"s_mov_b32 s59, 0x20000\n"
"v_mfma_f32_16x16x32_bf16 v[140:143], v[212:215], a[100:103], v[140:143]\n"
"s_waitcnt lgkmcnt(6)\n"
"v_add_f32 v240, v240, v146\n"
"v_add_f32 v240, v240, v147\n"
"s_lshl_b32 s91, s90, 12\n"
"v_mfma_f32_16x16x32_bf16 v[128:131], v[216:219], a[8:11], v[128:131]\n"
"ds_read_b128 v[212:215], v247 offset:40960\n"
"v_cvt_pk_bf16_f32 v163, v146, v147\n"
"s_lshl_b32 s70, s89, 12\n"
"v_mfma_f32_16x16x32_bf16 v[132:135], v[216:219], a[40:43], v[132:135]\n"
"v_exp_f32 v151, v151\n"
"s_add_u32 s70, s70, 0x1000000\n"
"v_mfma_f32_16x16x32_bf16 v[136:139], v[216:219], a[72:75], v[136:139]\n"
"buffer_load_dwordx4 v253, s[40:43], s47 offen offset:0 nt lds\n"
"s_sub_u32 s91, s70, s91\n"
"v_mfma_f32_16x16x32_bf16 v[140:143], v[216:219], a[104:107], v[140:143]\n"
"s_waitcnt lgkmcnt(6)\n"
"v_add_f32 v241, v241, v148\n"
"v_add_f32 v241, v241, v149\n"
"s_sub_u32 s68, s94, s91\n"
"s_subb_u32 s69, s95, 0\n"
"v_mfma_f32_16x16x32_bf16 v[128:131], v[220:223], a[12:15], v[128:131]\n"
"s_add_u32 m0, m0, 0x380\n"
"v_exp_f32 v152, v152\n"
"s_and_b32 s69, s69, 0xffff\n"
"v_mfma_f32_16x16x32_bf16 v[132:135], v[220:223], a[44:47], v[132:135]\n"
"ds_read_b128 v[216:219], v246 offset:41984\n"
"v_cvt_pk_bf16_f32 v166, v148, v149\n"
"s_mov_b32 s70, 102400\n"
"v_mfma_f32_16x16x32_bf16 v[136:139], v[220:223], a[76:79], v[136:139]\n"
"v_exp_f32 v153, v153\n"
"s_mov_b32 s71, 0x20000\n"
"v_mfma_f32_16x16x32_bf16 v[140:143], v[220:223], a[108:111], v[140:143]\n"
"s_waitcnt lgkmcnt(6)\n"
"v_add_f32 v241, v241, v150\n"
"v_add_f32 v241, v241, v151\n"
"v_mfma_f32_16x16x32_bf16 v[128:131], v[224:227], a[16:19], v[128:131]\n"
"v_exp_f32 v154, v154\n"
"v_mfma_f32_16x16x32_bf16 v[132:135], v[224:227], a[48:51], v[132:135]\n"
"ds_read_b128 v[220:223], v247 offset:41984\n"
"v_cvt_pk_bf16_f32 v167, v150, v151\n"
"v_mfma_f32_16x16x32_bf16 v[136:139], v[224:227], a[80:83], v[136:139]\n"
"v_exp_f32 v155, v155\n"
"v_mfma_f32_16x16x32_bf16 v[140:143], v[224:227], a[112:115], v[140:143]\n"
"s_waitcnt lgkmcnt(6)\n"
"buffer_load_dwordx4 v253, s[40:43], s47 offen offset:128 nt lds\n"
"v_add_f32 v242, v242, v152\n"
"v_mfma_f32_16x16x32_bf16 v[128:131], v[228:231], a[20:23], v[128:131]\n"
"s_add_u32 m0, m0, 0x380\n"
"v_exp_f32 v156, v156\n"
"v_mfma_f32_16x16x32_bf16 v[132:135], v[228:231], a[52:55], v[132:135]\n"
"v_add_f32 v242, v242, v153\n"
"v_cvt_pk_bf16_f32 v170, v152, v153\n"
"v_mfma_f32_16x16x32_bf16 v[136:139], v[228:231], a[84:87], v[136:139]\n"
"ds_read_b128 v[224:227], v246 offset:43008\n"
"v_add_f32 v242, v242, v154\n"
"v_mfma_f32_16x16x32_bf16 v[140:143], v[228:231], a[116:119], v[140:143]\n"
"s_waitcnt lgkmcnt(6)\n"
"v_exp_f32 v157, v157\n"
"v_mfma_f32_16x16x32_bf16 v[128:131], v[232:235], a[24:27], v[128:131]\n"
"v_exp_f32 v158, v158\n"
"v_mfma_f32_16x16x32_bf16 v[132:135], v[232:235], a[56:59], v[132:135]\n"
"v_add_f32 v242, v242, v155\n"
"v_cvt_pk_bf16_f32 v171, v154, v155\n"
"v_mfma_f32_16x16x32_bf16 v[136:139], v[232:235], a[88:91], v[136:139]\n"
"ds_read_b128 v[228:231], v247 offset:43008\n"
"buffer_load_dwordx4 v253, s[40:43], s47 offen offset:256 nt lds\n"
"v_mfma_f32_16x16x32_bf16 v[140:143], v[232:235], a[120:123], v[140:143]\n"
"s_waitcnt lgkmcnt(6)\n"
"v_exp_f32 v159, v159\n"
"v_add_f32 v243, v243, v156\n"
"v_mfma_f32_16x16x32_bf16 v[128:131], v[236:239], a[28:31], v[128:131]\n"
"ds_read_b128 v[232:235], v246 offset:44032\n"
"v_add_f32 v243, v243, v157\n"
"v_mfma_f32_16x16x32_bf16 v[132:135], v[236:239], a[60:63], v[132:135]\n"
"s_add_u32 m0, m0, 0x380\n"
"v_cvt_pk_bf16_f32 v174, v156, v157\n"
"v_add_f32 v243, v243, v158\n"
"v_mfma_f32_16x16x32_bf16 v[136:139], v[236:239], a[92:95], v[136:139]\n"
"buffer_load_dwordx4 v253, s[40:43], s47 offen offset:384 nt lds\n"
"v_mfma_f32_16x16x32_bf16 v[140:143], v[236:239], a[124:127], v[140:143]\n"
"s_waitcnt lgkmcnt(6)\n"
"ds_read_b128 v[236:239], v247 offset:44032\n"
"v_mfma_f32_16x16x32_bf16 v[144:147], v[208:211], a[0:3], v[192:195]\n"
"v_add_f32 v243, v243, v159\n"
"v_cvt_pk_bf16_f32 v175, v158, v159\n"
"v_mfma_f32_16x16x32_bf16 v[148:151], v[208:211], a[32:35], v[196:199]\n"
"v_mfma_f32_16x16x32_bf16 v[152:155], v[208:211], a[64:67], v[200:203]\n"
"v_mfma_f32_16x16x32_bf16 v[156:159], v[208:211], a[96:99], v[204:207]\n"
"s_waitcnt lgkmcnt(6)\n"
"s_mov_b32 s48, 15\n"
".p2align 6\n"
".Lloopf_%=:\n"
"v_mfma_f32_16x16x32_bf16 v[144:147], v[212:215], a[4:7], v[144:147]\n"
"s_add_u32 m0, m0, 0xfffeb580\n"
"s_add_u32 s46, s46, 0x8000\n"
"s_add_u32 s47, s47, 0x8000\n"
"v_mfma_f32_16x16x32_bf16 v[148:151], v[212:215], a[36:39], v[148:151]\n"
"v_exp_f32 v128, v128\n"
"v_mfma_f32_16x16x32_bf16 v[152:155], v[212:215], a[68:71], v[152:155]\n"
"v_add_f32 v240, v240, v128\n"
"v_mfma_f32_16x16x32_bf16 v[156:159], v[212:215], a[100:103], v[156:159]\n"
"s_waitcnt lgkmcnt(5)\n"
"v_mfma_f32_16x16x32_bf16 v[144:147], v[216:219], a[8:11], v[144:147]\n"
"v_exp_f32 v129, v129\n"
"v_mfma_f32_16x16x32_bf16 v[148:151], v[216:219], a[40:43], v[148:151]\n"
"v_add_f32 v240, v240, v129\n"
"v_cvt_pk_bf16_f32 v176, v128, v129\n"
"v_mfma_f32_16x16x32_bf16 v[152:155], v[216:219], a[72:75], v[152:155]\n"
"s_waitcnt vmcnt(16) lgkmcnt(0)\n"
"s_barrier\n"
"v_mfma_f32_16x16x32_bf16 v[156:159], v[216:219], a[104:107], v[156:159]\n"
"ds_read_b64_tr_b16 v[208:209], v248 offset:0\n"
"v_mfma_f32_16x16x32_bf16 v[144:147], v[220:223], a[12:15], v[144:147]\n"
"ds_read_b64_tr_b16 v[210:211], v248 offset:8192\n"
"v_mfma_f32_16x16x32_bf16 v[148:151], v[220:223], a[44:47], v[148:151]\n"
"ds_read_b64_tr_b16 v[212:213], v249 offset:0\n"
"v_mfma_f32_16x16x32_bf16 v[152:155], v[220:223], a[76:79], v[152:155]\n"
"v_exp_f32 v130, v130\n"
"v_mfma_f32_16x16x32_bf16 v[156:159], v[220:223], a[108:111], v[156:159]\n"
"buffer_load_dwordx4 v252, s[36:39], s44 offen offset:0 nt lds\n"
"v_mfma_f32_16x16x32_bf16 v[144:147], v[224:227], a[16:19], v[144:147]\n"
"s_add_u32 m0, m0, 0x380\n"
"ds_read_b64_tr_b16 v[214:215], v249 offset:8192\n"
"v_mfma_f32_16x16x32_bf16 v[148:151], v[224:227], a[48:51], v[148:151]\n"
"ds_read_b64_tr_b16 v[216:217], v250 offset:0\n"
"v_add_f32 v240, v240, v130\n"
"v_mfma_f32_16x16x32_bf16 v[152:155], v[224:227], a[80:83], v[152:155]\n"
"ds_read_b64_tr_b16 v[218:219], v250 offset:8192\n"
"v_mfma_f32_16x16x32_bf16 v[156:159], v[224:227], a[112:115], v[156:159]\n"
"v_exp_f32 v131, v131\n"
"v_mfma_f32_16x16x32_bf16 v[144:147], v[228:231], a[20:23], v[144:147]\n"
"ds_read_b64_tr_b16 v[220:221], v251 offset:0\n"
"v_add_f32 v240, v240, v131\n"
"v_mfma_f32_16x16x32_bf16 v[148:151], v[228:231], a[52:55], v[148:151]\n"
"ds_read_b64_tr_b16 v[222:223], v251 offset:8192\n"
"v_cvt_pk_bf16_f32 v177, v130, v131\n"
"v_mfma_f32_16x16x32_bf16 v[152:155], v[228:231], a[84:87], v[152:155]\n"
"ds_read_b64_tr_b16 v[224:225], v248 offset:1024\n"
"v_mfma_f32_16x16x32_bf16 v[156:159], v[228:231], a[116:119], v[156:159]\n"
"ds_read_b64_tr_b16 v[226:227], v248 offset:9216\n"
"v_exp_f32 v132, v132\n"
"v_mfma_f32_16x16x32_bf16 v[144:147], v[232:235], a[24:27], v[144:147]\n"
"buffer_load_dwordx4 v252, s[36:39], s44 offen offset:128 nt lds\n"
"v_mfma_f32_16x16x32_bf16 v[148:151], v[232:235], a[56:59], v[148:151]\n"
"s_add_u32 m0, m0, 0x380\n"
"ds_read_b64_tr_b16 v[228:229], v249 offset:1024\n"
"v_mfma_f32_16x16x32_bf16 v[152:155], v[232:235], a[88:91], v[152:155]\n"
"ds_read_b64_tr_b16 v[230:231], v249 offset:9216\n"
"v_add_f32 v241, v241, v132\n"
"v_mfma_f32_16x16x32_bf16 v[156:159], v[232:235], a[120:123], v[156:159]\n"
"v_exp_f32 v133, v133\n"
"v_mfma_f32_16x16x32_bf16 v[144:147], v[236:239], a[28:31], v[144:147]\n"
"ds_read_b64_tr_b16 v[232:233], v250 offset:1024\n"
"v_add_f32 v241, v241, v133\n"
"v_mfma_f32_16x16x32_bf16 v[148:151], v[236:239], a[60:63], v[148:151]\n"
"ds_read_b64_tr_b16 v[234:235], v250 offset:9216\n"
"v_cvt_pk_bf16_f32 v180, v132, v133\n"
"v_mfma_f32_16x16x32_bf16 v[152:155], v[236:239], a[92:95], v[152:155]\n"
"v_exp_f32 v134, v134\n"
"v_mfma_f32_16x16x32_bf16 v[156:159], v[236:239], a[124:127], v[156:159]\n"
"s_waitcnt lgkmcnt(12)\n"
"buffer_load_dwordx4 v252, s[36:39], s44 offen offset:256 nt lds\n"
"v_mfma_f32_16x16x32_bf16 v[0:3], v[208:211], v[160:163], v[0:3]\n"
"ds_read_b64_tr_b16 v[236:237], v251 offset:1024\n"
"v_add_f32 v241, v241, v134\n"
"v_mfma_f32_16x16x32_bf16 v[4:7], v[208:211], v[164:167], v[4:7]\n"
"s_add_u32 m0, m0, 0x380\n"
"ds_read_b64_tr_b16 v[238:239], v251 offset:9216\n"
"v_mfma_f32_16x16x32_bf16 a[128:131], v[208:211], v[168:171], a[128:131]\n"
"v_exp_f32 v135, v135\n"
"v_mfma_f32_16x16x32_bf16 a[132:135], v[208:211], v[172:175], a[132:135]\n"
"s_waitcnt lgkmcnt(12)\n"
"ds_read_b64_tr_b16 v[208:209], v248 offset:2048\n"
"v_mfma_f32_16x16x32_bf16 a[140:143], v[212:215], v[172:175], a[140:143]\n"
"ds_read_b64_tr_b16 v[210:211], v248 offset:10240\n"
"v_add_f32 v241, v241, v135\n"
"v_mfma_f32_16x16x32_bf16 a[136:139], v[212:215], v[168:171], a[136:139]\n"
"v_exp_f32 v136, v136\n"
"v_mfma_f32_16x16x32_bf16 v[12:15], v[212:215], v[164:167], v[12:15]\n"
"buffer_load_dwordx4 v252, s[36:39], s44 offen offset:384 nt lds\n"
"v_mfma_f32_16x16x32_bf16 v[8:11], v[212:215], v[160:163], v[8:11]\n"
"s_waitcnt lgkmcnt(12)\n"
"v_exp_f32 v137, v137\n"
"v_mfma_f32_16x16x32_bf16 v[16:19], v[216:219], v[160:163], v[16:19]\n"
"ds_read_b64_tr_b16 v[212:213], v249 offset:2048\n"
"v_cvt_pk_bf16_f32 v181, v134, v135\n"
"v_mfma_f32_16x16x32_bf16 v[20:23], v[216:219], v[164:167], v[20:23]\n"
"ds_read_b64_tr_b16 v[214:215], v249 offset:10240\n"
"v_add_f32 v242, v242, v136\n"
"v_mfma_f32_16x16x32_bf16 a[144:147], v[216:219], v[168:171], a[144:147]\n"
"s_add_u32 m0, m0, 0x3580\n"
"v_exp_f32 v138, v138\n"
"v_mfma_f32_16x16x32_bf16 a[148:151], v[216:219], v[172:175], a[148:151]\n"
"s_waitcnt lgkmcnt(12)\n"
"ds_read_b64_tr_b16 v[216:217], v250 offset:2048\n"
"v_mfma_f32_16x16x32_bf16 a[156:159], v[220:223], v[172:175], a[156:159]\n"
"ds_read_b64_tr_b16 v[218:219], v250 offset:10240\n"
"v_add_f32 v242, v242, v137\n"
"v_mfma_f32_16x16x32_bf16 a[152:155], v[220:223], v[168:171], a[152:155]\n"
"v_exp_f32 v139, v139\n"
"v_mfma_f32_16x16x32_bf16 v[28:31], v[220:223], v[164:167], v[28:31]\n"
"buffer_load_dwordx4 v252, s[36:39], s45 offen offset:0 nt lds\n"
"v_mfma_f32_16x16x32_bf16 v[24:27], v[220:223], v[160:163], v[24:27]\n"
"s_waitcnt lgkmcnt(12)\n"
"v_cvt_pk_bf16_f32 v184, v136, v137\n"
"v_add_f32 v242, v242, v138\n"
"v_mfma_f32_16x16x32_bf16 v[32:35], v[224:227], v[160:163], v[32:35]\n"
"s_add_u32 m0, m0, 0x380\n"
"ds_read_b64_tr_b16 v[220:221], v251 offset:2048\n"
"v_mfma_f32_16x16x32_bf16 v[36:39], v[224:227], v[164:167], v[36:39]\n"
"ds_read_b64_tr_b16 v[222:223], v251 offset:10240\n"
"v_add_f32 v242, v242, v139\n"
"v_mfma_f32_16x16x32_bf16 a[160:163], v[224:227], v[168:171], a[160:163]\n"
"v_exp_f32 v140, v140\n"
"v_mfma_f32_16x16x32_bf16 a[164:167], v[224:227], v[172:175], a[164:167]\n"
"s_waitcnt lgkmcnt(12)\n"
"ds_read_b64_tr_b16 v[224:225], v248 offset:3072\n"
"v_mfma_f32_16x16x32_bf16 a[172:175], v[228:231], v[172:175], a[172:175]\n"
"ds_read_b64_tr_b16 v[226:227], v248 offset:11264\n"
"v_cvt_pk_bf16_f32 v185, v138, v139\n"
"v_mfma_f32_16x16x32_bf16 a[168:171], v[228:231], v[168:171], a[168:171]\n"
"v_exp_f32 v141, v141\n"
"v_mfma_f32_16x16x32_bf16 v[44:47], v[228:231], v[164:167], v[44:47]\n"
"buffer_load_dwordx4 v252, s[36:39], s45 offen offset:128 nt lds\n"
"v_mfma_f32_16x16x32_bf16 v[40:43], v[228:231], v[160:163], v[40:43]\n"
"s_waitcnt lgkmcnt(12)\n"
"ds_read_b64_tr_b16 v[228:229], v249 offset:3072\n"
"v_mfma_f32_16x16x32_bf16 v[48:51], v[232:235], v[160:163], v[48:51]\n"
"ds_read_b64_tr_b16 v[230:231], v249 offset:11264\n"
"v_add_f32 v243, v243, v140\n"
"v_mfma_f32_16x16x32_bf16 v[52:55], v[232:235], v[164:167], v[52:55]\n"
"s_add_u32 m0, m0, 0x380\n"
"v_exp_f32 v142, v142\n"
"v_mfma_f32_16x16x32_bf16 a[176:179], v[232:235], v[168:171], a[176:179]\n"
"v_add_f32 v243, v243, v141\n"
"v_cvt_pk_bf16_f32 v188, v140, v141\n"
"v_mfma_f32_16x16x32_bf16 a[180:183], v[232:235], v[172:175], a[180:183]\n"
"s_waitcnt lgkmcnt(12)\n"
"ds_read_b64_tr_b16 v[232:233], v250 offset:3072\n"
"v_mfma_f32_16x16x32_bf16 a[188:191], v[236:239], v[172:175], a[188:191]\n"
"ds_read_b64_tr_b16 v[234:235], v250 offset:11264\n"
"v_add_f32 v243, v243, v142\n"
"v_mfma_f32_16x16x32_bf16 a[184:187], v[236:239], v[168:171], a[184:187]\n"
"v_exp_f32 v143, v143\n"
"v_mfma_f32_16x16x32_bf16 v[60:63], v[236:239], v[164:167], v[60:63]\n"
"buffer_load_dwordx4 v252, s[36:39], s45 offen offset:256 nt lds\n"
"v_mfma_f32_16x16x32_bf16 v[56:59], v[236:239], v[160:163], v[56:59]\n"
"s_waitcnt lgkmcnt(12)\n"
"ds_read_b64_tr_b16 v[236:237], v251 offset:3072\n"
"v_mfma_f32_16x16x32_bf16 v[64:67], v[208:211], v[160:163], v[64:67]\n"
"s_add_u32 m0, m0, 0x380\n"
"ds_read_b64_tr_b16 v[238:239], v251 offset:11264\n"
"v_mfma_f32_16x16x32_bf16 v[68:71], v[208:211], v[164:167], v[68:71]\n"
"v_exp_f32 v144, v144\n"
"v_mfma_f32_16x16x32_bf16 a[192:195], v[208:211], v[168:171], a[192:195]\n"
"v_exp_f32 v145, v145\n"
"v_mfma_f32_16x16x32_bf16 a[196:199], v[208:211], v[172:175], a[196:199]\n"
"s_waitcnt lgkmcnt(12)\n"
"v_add_f32 v243, v243, v143\n"
"v_cvt_pk_bf16_f32 v189, v142, v143\n"
"v_mfma_f32_16x16x32_bf16 a[204:207], v[212:215], v[172:175], a[204:207]\n"
"v_add_f32 v240, v240, v144\n"
"v_add_f32 v240, v240, v145\n"
"v_mfma_f32_16x16x32_bf16 a[200:203], v[212:215], v[168:171], a[200:203]\n"
"buffer_load_dwordx4 v252, s[36:39], s45 offen offset:384 nt lds\n"
"v_mfma_f32_16x16x32_bf16 v[76:79], v[212:215], v[164:167], v[76:79]\n"
"s_add_u32 m0, m0, 0x3580\n"
"v_exp_f32 v146, v146\n"
"v_mfma_f32_16x16x32_bf16 v[72:75], v[212:215], v[160:163], v[72:75]\n"
"s_waitcnt lgkmcnt(10)\n"
"v_cvt_pk_bf16_f32 v178, v144, v145\n"
"v_add_f32 v240, v240, v146\n"
"v_mfma_f32_16x16x32_bf16 v[80:83], v[216:219], v[160:163], v[80:83]\n"
"v_mfma_f32_16x16x32_bf16 v[84:87], v[216:219], v[164:167], v[84:87]\n"
"v_mfma_f32_16x16x32_bf16 a[208:211], v[216:219], v[168:171], a[208:211]\n"
"v_exp_f32 v147, v147\n"
"v_mfma_f32_16x16x32_bf16 a[212:215], v[216:219], v[172:175], a[212:215]\n"
"s_waitcnt vmcnt(16) lgkmcnt(0)\n"
"s_barrier\n"
"v_add_f32 v240, v240, v147\n"
"v_mfma_f32_16x16x32_bf16 a[220:223], v[220:223], v[172:175], a[220:223]\n"
"ds_read_b128 v[208:211], v246 offset:0\n"
"v_cvt_pk_bf16_f32 v179, v146, v147\n"
"v_mfma_f32_16x16x32_bf16 a[216:219], v[220:223], v[168:171], a[216:219]\n"
"s_add_u32 s44, s44, 0x8000\n"
"buffer_load_dwordx4 v253, s[40:43], s46 offen offset:0 nt lds\n"
"v_mfma_f32_16x16x32_bf16 v[92:95], v[220:223], v[164:167], v[92:95]\n"
"s_add_u32 m0, m0, 0x380\n"
"ds_read_b128 v[212:215], v247 offset:0\n"
"v_mfma_f32_16x16x32_bf16 v[88:91], v[220:223], v[160:163], v[88:91]\n"
"s_add_u32 s45, s45, 0x8000\n"
"v_exp_f32 v148, v148\n"
"v_mfma_f32_16x16x32_bf16 v[96:99], v[224:227], v[160:163], v[96:99]\n"
"ds_read_b128 v[216:219], v246 offset:1024\n"
"v_add_f32 v241, v241, v148\n"
"v_mfma_f32_16x16x32_bf16 v[100:103], v[224:227], v[164:167], v[100:103]\n"
"ds_read_b128 v[220:223], v247 offset:1024\n"
"v_mfma_f32_16x16x32_bf16 a[224:227], v[224:227], v[168:171], a[224:227]\n"
"v_exp_f32 v149, v149\n"
"v_mfma_f32_16x16x32_bf16 a[228:231], v[224:227], v[172:175], a[228:231]\n"
"ds_read_b128 v[224:227], v246 offset:2048\n"
"v_add_f32 v241, v241, v149\n"
"v_mfma_f32_16x16x32_bf16 a[236:239], v[228:231], v[172:175], a[236:239]\n"
"buffer_load_dwordx4 v253, s[40:43], s46 offen offset:128 nt lds\n"
"v_mfma_f32_16x16x32_bf16 a[232:235], v[228:231], v[168:171], a[232:235]\n"
"s_add_u32 m0, m0, 0x380\n"
"v_exp_f32 v150, v150\n"
"v_mfma_f32_16x16x32_bf16 v[108:111], v[228:231], v[164:167], v[108:111]\n"
"v_cvt_pk_bf16_f32 v182, v148, v149\n"
"v_add_f32 v241, v241, v150\n"
"v_mfma_f32_16x16x32_bf16 v[104:107], v[228:231], v[160:163], v[104:107]\n"
"ds_read_b128 v[228:231], v247 offset:2048\n"
"v_mfma_f32_16x16x32_bf16 v[112:115], v[232:235], v[160:163], v[112:115]\n"
"v_exp_f32 v151, v151\n"
"v_mfma_f32_16x16x32_bf16 v[116:119], v[232:235], v[164:167], v[116:119]\n"
"v_add_f32 v241, v241, v151\n"
"v_cvt_pk_bf16_f32 v183, v150, v151\n"
"v_mfma_f32_16x16x32_bf16 a[240:243], v[232:235], v[168:171], a[240:243]\n"
"v_mfma_f32_16x16x32_bf16 a[244:247], v[232:235], v[172:175], a[244:247]\n"
"ds_read_b128 v[232:235], v246 offset:3072\n"
"v_mfma_f32_16x16x32_bf16 a[252:255], v[236:239], v[172:175], a[252:255]\n"
"buffer_load_dwordx4 v253, s[40:43], s46 offen offset:256 nt lds\n"
"v_mfma_f32_16x16x32_bf16 a[248:251], v[236:239], v[168:171], a[248:251]\n"
"s_add_u32 m0, m0, 0x380\n"
"v_exp_f32 v152, v152\n"
"v_mfma_f32_16x16x32_bf16 v[124:127], v[236:239], v[164:167], v[124:127]\n"
"v_add_f32 v242, v242, v152\n"
"v_mfma_f32_16x16x32_bf16 v[120:123], v[236:239], v[160:163], v[120:123]\n"
"s_waitcnt lgkmcnt(6)\n"
"ds_read_b128 v[236:239], v247 offset:3072\n"
"v_mfma_f32_16x16x32_bf16 v[128:131], v[208:211], a[0:3], v[192:195]\n"
"v_exp_f32 v153, v153\n"
"v_mfma_f32_16x16x32_bf16 v[132:135], v[208:211], a[32:35], v[196:199]\n"
"v_add_f32 v242, v242, v153\n"
"v_cvt_pk_bf16_f32 v186, v152, v153\n"
"v_mfma_f32_16x16x32_bf16 v[136:139], v[208:211], a[64:67], v[200:203]\n"
"buffer_load_dwordx4 v253, s[40:43], s46 offen offset:384 nt lds\n"
"v_mfma_f32_16x16x32_bf16 v[140:143], v[208:211], a[96:99], v[204:207]\n"
"s_waitcnt lgkmcnt(6)\n"
"ds_read_b128 v[208:211], v246 offset:8192\n"
"v_mfma_f32_16x16x32_bf16 v[128:131], v[212:215], a[4:7], v[128:131]\n"
"s_add_u32 m0, m0, 0x3580\n"
"v_exp_f32 v154, v154\n"
"v_mfma_f32_16x16x32_bf16 v[132:135], v[212:215], a[36:39], v[132:135]\n"
"v_add_f32 v242, v242, v154\n"
"v_mfma_f32_16x16x32_bf16 v[136:139], v[212:215], a[68:71], v[136:139]\n"
"v_exp_f32 v155, v155\n"
"v_mfma_f32_16x16x32_bf16 v[140:143], v[212:215], a[100:103], v[140:143]\n"
"s_waitcnt lgkmcnt(6)\n"
"ds_read_b128 v[212:215], v247 offset:8192\n"
"v_mfma_f32_16x16x32_bf16 v[128:131], v[216:219], a[8:11], v[128:131]\n"
"v_add_f32 v242, v242, v155\n"
"v_cvt_pk_bf16_f32 v187, v154, v155\n"
"v_mfma_f32_16x16x32_bf16 v[132:135], v[216:219], a[40:43], v[132:135]\n"
"buffer_load_dwordx4 v253, s[40:43], s47 offen offset:0 nt lds\n"
"v_mfma_f32_16x16x32_bf16 v[136:139], v[216:219], a[72:75], v[136:139]\n"
"s_add_u32 m0, m0, 0x380\n"
"v_exp_f32 v156, v156\n"
"v_mfma_f32_16x16x32_bf16 v[140:143], v[216:219], a[104:107], v[140:143]\n"
"s_waitcnt lgkmcnt(6)\n"
"ds_read_b128 v[216:219], v246 offset:9216\n"
"v_mfma_f32_16x16x32_bf16 v[128:131], v[220:223], a[12:15], v[128:131]\n"
"v_add_f32 v243, v243, v156\n"
"v_mfma_f32_16x16x32_bf16 v[132:135], v[220:223], a[44:47], v[132:135]\n"
"v_mfma_f32_16x16x32_bf16 v[136:139], v[220:223], a[76:79], v[136:139]\n"
"v_exp_f32 v157, v157\n"
"v_mfma_f32_16x16x32_bf16 v[140:143], v[220:223], a[108:111], v[140:143]\n"
"s_waitcnt lgkmcnt(6)\n"
"ds_read_b128 v[220:223], v247 offset:9216\n"
"v_mfma_f32_16x16x32_bf16 v[128:131], v[224:227], a[16:19], v[128:131]\n"
"buffer_load_dwordx4 v253, s[40:43], s47 offen offset:128 nt lds\n"
"v_mfma_f32_16x16x32_bf16 v[132:135], v[224:227], a[48:51], v[132:135]\n"
"s_add_u32 m0, m0, 0x380\n"
"v_exp_f32 v158, v158\n"
"v_mfma_f32_16x16x32_bf16 v[136:139], v[224:227], a[80:83], v[136:139]\n"
"v_add_f32 v243, v243, v157\n"
"v_cvt_pk_bf16_f32 v190, v156, v157\n"
"v_mfma_f32_16x16x32_bf16 v[140:143], v[224:227], a[112:115], v[140:143]\n"
"s_waitcnt lgkmcnt(6)\n"
"ds_read_b128 v[224:227], v246 offset:10240\n"
"v_mfma_f32_16x16x32_bf16 v[128:131], v[228:231], a[20:23], v[128:131]\n"
"v_add_f32 v243, v243, v158\n"
"v_mfma_f32_16x16x32_bf16 v[132:135], v[228:231], a[52:55], v[132:135]\n"
"v_exp_f32 v159, v159\n"
"v_mfma_f32_16x16x32_bf16 v[136:139], v[228:231], a[84:87], v[136:139]\n"
"v_add_f32 v243, v243, v159\n"
"v_cvt_pk_bf16_f32 v191, v158, v159\n"
"v_mfma_f32_16x16x32_bf16 v[140:143], v[228:231], a[116:119], v[140:143]\n"
"s_waitcnt lgkmcnt(6)\n"
"ds_read_b128 v[228:231], v247 offset:10240\n"
"v_mfma_f32_16x16x32_bf16 v[128:131], v[232:235], a[24:27], v[128:131]\n"
"buffer_load_dwordx4 v253, s[40:43], s47 offen offset:256 nt lds\n"
"v_mfma_f32_16x16x32_bf16 v[132:135], v[232:235], a[56:59], v[132:135]\n"
"s_add_u32 m0, m0, 0x380\n"
"v_mfma_f32_16x16x32_bf16 v[136:139], v[232:235], a[88:91], v[136:139]\n"
"v_mfma_f32_16x16x32_bf16 v[140:143], v[232:235], a[120:123], v[140:143]\n"
"s_waitcnt lgkmcnt(6)\n"
"ds_read_b128 v[232:235], v246 offset:11264\n"
"v_mfma_f32_16x16x32_bf16 v[128:131], v[236:239], a[28:31], v[128:131]\n"
"v_mfma_f32_16x16x32_bf16 v[132:135], v[236:239], a[60:63], v[132:135]\n"
"v_mfma_f32_16x16x32_bf16 v[136:139], v[236:239], a[92:95], v[136:139]\n"
"buffer_load_dwordx4 v253, s[40:43], s47 offen offset:384 nt lds\n"
"v_mfma_f32_16x16x32_bf16 v[140:143], v[236:239], a[124:127], v[140:143]\n"
"s_waitcnt lgkmcnt(6)\n"
"ds_read_b128 v[236:239], v247 offset:11264\n"
"v_mfma_f32_16x16x32_bf16 v[144:147], v[208:211], a[0:3], v[192:195]\n"
"v_mfma_f32_16x16x32_bf16 v[148:151], v[208:211], a[32:35], v[196:199]\n"
"v_mfma_f32_16x16x32_bf16 v[152:155], v[208:211], a[64:67], v[200:203]\n"
"v_mfma_f32_16x16x32_bf16 v[156:159], v[208:211], a[96:99], v[204:207]\n"
"s_waitcnt lgkmcnt(6)\n"
"v_mfma_f32_16x16x32_bf16 v[144:147], v[212:215], a[4:7], v[144:147]\n"
"s_add_u32 m0, m0, 0xfffeb580\n"
"s_add_u32 s46, s46, 0x8000\n"
"s_add_u32 s47, s47, 0x8000\n"
"v_mfma_f32_16x16x32_bf16 v[148:151], v[212:215], a[36:39], v[148:151]\n"
"v_exp_f32 v128, v128\n"
"v_mfma_f32_16x16x32_bf16 v[152:155], v[212:215], a[68:71], v[152:155]\n"
"v_add_f32 v240, v240, v128\n"
"v_mfma_f32_16x16x32_bf16 v[156:159], v[212:215], a[100:103], v[156:159]\n"
"s_waitcnt lgkmcnt(5)\n"
"v_mfma_f32_16x16x32_bf16 v[144:147], v[216:219], a[8:11], v[144:147]\n"
"v_exp_f32 v129, v129\n"
"v_mfma_f32_16x16x32_bf16 v[148:151], v[216:219], a[40:43], v[148:151]\n"
"v_add_f32 v240, v240, v129\n"
"v_cvt_pk_bf16_f32 v160, v128, v129\n"
"v_mfma_f32_16x16x32_bf16 v[152:155], v[216:219], a[72:75], v[152:155]\n"
"s_waitcnt vmcnt(16) lgkmcnt(0)\n"
"s_barrier\n"
"v_mfma_f32_16x16x32_bf16 v[156:159], v[216:219], a[104:107], v[156:159]\n"
"ds_read_b64_tr_b16 v[208:209], v248 offset:32768\n"
"v_mfma_f32_16x16x32_bf16 v[144:147], v[220:223], a[12:15], v[144:147]\n"
"ds_read_b64_tr_b16 v[210:211], v248 offset:40960\n"
"v_mfma_f32_16x16x32_bf16 v[148:151], v[220:223], a[44:47], v[148:151]\n"
"ds_read_b64_tr_b16 v[212:213], v249 offset:32768\n"
"v_mfma_f32_16x16x32_bf16 v[152:155], v[220:223], a[76:79], v[152:155]\n"
"v_exp_f32 v130, v130\n"
"v_mfma_f32_16x16x32_bf16 v[156:159], v[220:223], a[108:111], v[156:159]\n"
"buffer_load_dwordx4 v252, s[36:39], s44 offen offset:0 nt lds\n"
"v_mfma_f32_16x16x32_bf16 v[144:147], v[224:227], a[16:19], v[144:147]\n"
"s_add_u32 m0, m0, 0x380\n"
"ds_read_b64_tr_b16 v[214:215], v249 offset:40960\n"
"v_mfma_f32_16x16x32_bf16 v[148:151], v[224:227], a[48:51], v[148:151]\n"
"ds_read_b64_tr_b16 v[216:217], v250 offset:32768\n"
"v_add_f32 v240, v240, v130\n"
"v_mfma_f32_16x16x32_bf16 v[152:155], v[224:227], a[80:83], v[152:155]\n"
"ds_read_b64_tr_b16 v[218:219], v250 offset:40960\n"
"v_mfma_f32_16x16x32_bf16 v[156:159], v[224:227], a[112:115], v[156:159]\n"
"v_exp_f32 v131, v131\n"
"v_mfma_f32_16x16x32_bf16 v[144:147], v[228:231], a[20:23], v[144:147]\n"
"ds_read_b64_tr_b16 v[220:221], v251 offset:32768\n"
"v_add_f32 v240, v240, v131\n"
"v_mfma_f32_16x16x32_bf16 v[148:151], v[228:231], a[52:55], v[148:151]\n"
"ds_read_b64_tr_b16 v[222:223], v251 offset:40960\n"
"v_cvt_pk_bf16_f32 v161, v130, v131\n"
"v_mfma_f32_16x16x32_bf16 v[152:155], v[228:231], a[84:87], v[152:155]\n"
"ds_read_b64_tr_b16 v[224:225], v248 offset:33792\n"
"v_mfma_f32_16x16x32_bf16 v[156:159], v[228:231], a[116:119], v[156:159]\n"
"ds_read_b64_tr_b16 v[226:227], v248 offset:41984\n"
"v_exp_f32 v132, v132\n"
"v_mfma_f32_16x16x32_bf16 v[144:147], v[232:235], a[24:27], v[144:147]\n"
"buffer_load_dwordx4 v252, s[36:39], s44 offen offset:128 nt lds\n"
"v_mfma_f32_16x16x32_bf16 v[148:151], v[232:235], a[56:59], v[148:151]\n"
"s_add_u32 m0, m0, 0x380\n"
"ds_read_b64_tr_b16 v[228:229], v249 offset:33792\n"
"v_mfma_f32_16x16x32_bf16 v[152:155], v[232:235], a[88:91], v[152:155]\n"
"ds_read_b64_tr_b16 v[230:231], v249 offset:41984\n"
"v_add_f32 v241, v241, v132\n"
"v_mfma_f32_16x16x32_bf16 v[156:159], v[232:235], a[120:123], v[156:159]\n"
"v_exp_f32 v133, v133\n"
"v_mfma_f32_16x16x32_bf16 v[144:147], v[236:239], a[28:31], v[144:147]\n"
"ds_read_b64_tr_b16 v[232:233], v250 offset:33792\n"
"v_add_f32 v241, v241, v133\n"
"v_mfma_f32_16x16x32_bf16 v[148:151], v[236:239], a[60:63], v[148:151]\n"
"ds_read_b64_tr_b16 v[234:235], v250 offset:41984\n"
"v_cvt_pk_bf16_f32 v164, v132, v133\n"
"v_mfma_f32_16x16x32_bf16 v[152:155], v[236:239], a[92:95], v[152:155]\n"
"v_exp_f32 v134, v134\n"
"v_mfma_f32_16x16x32_bf16 v[156:159], v[236:239], a[124:127], v[156:159]\n"
"s_waitcnt lgkmcnt(12)\n"
"buffer_load_dwordx4 v252, s[36:39], s44 offen offset:256 nt lds\n"
"v_mfma_f32_16x16x32_bf16 v[0:3], v[208:211], v[176:179], v[0:3]\n"
"ds_read_b64_tr_b16 v[236:237], v251 offset:33792\n"
"v_add_f32 v241, v241, v134\n"
"v_mfma_f32_16x16x32_bf16 v[4:7], v[208:211], v[180:183], v[4:7]\n"
"s_add_u32 m0, m0, 0x380\n"
"ds_read_b64_tr_b16 v[238:239], v251 offset:41984\n"
"v_mfma_f32_16x16x32_bf16 a[128:131], v[208:211], v[184:187], a[128:131]\n"
"v_exp_f32 v135, v135\n"
"v_mfma_f32_16x16x32_bf16 a[132:135], v[208:211], v[188:191], a[132:135]\n"
"s_waitcnt lgkmcnt(12)\n"
"ds_read_b64_tr_b16 v[208:209], v248 offset:34816\n"
"v_mfma_f32_16x16x32_bf16 a[140:143], v[212:215], v[188:191], a[140:143]\n"
"ds_read_b64_tr_b16 v[210:211], v248 offset:43008\n"
"v_add_f32 v241, v241, v135\n"
"v_mfma_f32_16x16x32_bf16 a[136:139], v[212:215], v[184:187], a[136:139]\n"
"v_exp_f32 v136, v136\n"
"v_mfma_f32_16x16x32_bf16 v[12:15], v[212:215], v[180:183], v[12:15]\n"
"buffer_load_dwordx4 v252, s[36:39], s44 offen offset:384 nt lds\n"
"v_mfma_f32_16x16x32_bf16 v[8:11], v[212:215], v[176:179], v[8:11]\n"
"s_waitcnt lgkmcnt(12)\n"
"v_exp_f32 v137, v137\n"
"v_mfma_f32_16x16x32_bf16 v[16:19], v[216:219], v[176:179], v[16:19]\n"
"ds_read_b64_tr_b16 v[212:213], v249 offset:34816\n"
"v_cvt_pk_bf16_f32 v165, v134, v135\n"
"v_mfma_f32_16x16x32_bf16 v[20:23], v[216:219], v[180:183], v[20:23]\n"
"ds_read_b64_tr_b16 v[214:215], v249 offset:43008\n"
"v_add_f32 v242, v242, v136\n"
"v_mfma_f32_16x16x32_bf16 a[144:147], v[216:219], v[184:187], a[144:147]\n"
"s_add_u32 m0, m0, 0x3580\n"
"v_exp_f32 v138, v138\n"
"v_mfma_f32_16x16x32_bf16 a[148:151], v[216:219], v[188:191], a[148:151]\n"
"s_waitcnt lgkmcnt(12)\n"
"ds_read_b64_tr_b16 v[216:217], v250 offset:34816\n"
"v_mfma_f32_16x16x32_bf16 a[156:159], v[220:223], v[188:191], a[156:159]\n"
"ds_read_b64_tr_b16 v[218:219], v250 offset:43008\n"
"v_add_f32 v242, v242, v137\n"
"v_mfma_f32_16x16x32_bf16 a[152:155], v[220:223], v[184:187], a[152:155]\n"
"v_exp_f32 v139, v139\n"
"v_mfma_f32_16x16x32_bf16 v[28:31], v[220:223], v[180:183], v[28:31]\n"
"buffer_load_dwordx4 v252, s[36:39], s45 offen offset:0 nt lds\n"
"v_mfma_f32_16x16x32_bf16 v[24:27], v[220:223], v[176:179], v[24:27]\n"
"s_waitcnt lgkmcnt(12)\n"
"v_cvt_pk_bf16_f32 v168, v136, v137\n"
"v_add_f32 v242, v242, v138\n"
"v_mfma_f32_16x16x32_bf16 v[32:35], v[224:227], v[176:179], v[32:35]\n"
"s_add_u32 m0, m0, 0x380\n"
"ds_read_b64_tr_b16 v[220:221], v251 offset:34816\n"
"v_mfma_f32_16x16x32_bf16 v[36:39], v[224:227], v[180:183], v[36:39]\n"
"ds_read_b64_tr_b16 v[222:223], v251 offset:43008\n"
"v_add_f32 v242, v242, v139\n"
"v_mfma_f32_16x16x32_bf16 a[160:163], v[224:227], v[184:187], a[160:163]\n"
"v_exp_f32 v140, v140\n"
"v_mfma_f32_16x16x32_bf16 a[164:167], v[224:227], v[188:191], a[164:167]\n"
"s_waitcnt lgkmcnt(12)\n"
"ds_read_b64_tr_b16 v[224:225], v248 offset:35840\n"
"v_mfma_f32_16x16x32_bf16 a[172:175], v[228:231], v[188:191], a[172:175]\n"
"ds_read_b64_tr_b16 v[226:227], v248 offset:44032\n"
"v_cvt_pk_bf16_f32 v169, v138, v139\n"
"v_mfma_f32_16x16x32_bf16 a[168:171], v[228:231], v[184:187], a[168:171]\n"
"v_exp_f32 v141, v141\n"
"v_mfma_f32_16x16x32_bf16 v[44:47], v[228:231], v[180:183], v[44:47]\n"
"buffer_load_dwordx4 v252, s[36:39], s45 offen offset:128 nt lds\n"
"v_mfma_f32_16x16x32_bf16 v[40:43], v[228:231], v[176:179], v[40:43]\n"
"s_waitcnt lgkmcnt(12)\n"
"ds_read_b64_tr_b16 v[228:229], v249 offset:35840\n"
"v_mfma_f32_16x16x32_bf16 v[48:51], v[232:235], v[176:179], v[48:51]\n"
"ds_read_b64_tr_b16 v[230:231], v249 offset:44032\n"
"v_add_f32 v243, v243, v140\n"
"v_mfma_f32_16x16x32_bf16 v[52:55], v[232:235], v[180:183], v[52:55]\n"
"s_add_u32 m0, m0, 0x380\n"
"v_exp_f32 v142, v142\n"
"v_mfma_f32_16x16x32_bf16 a[176:179], v[232:235], v[184:187], a[176:179]\n"
"v_add_f32 v243, v243, v141\n"
"v_cvt_pk_bf16_f32 v172, v140, v141\n"
"v_mfma_f32_16x16x32_bf16 a[180:183], v[232:235], v[188:191], a[180:183]\n"
"s_waitcnt lgkmcnt(12)\n"
"ds_read_b64_tr_b16 v[232:233], v250 offset:35840\n"
"v_mfma_f32_16x16x32_bf16 a[188:191], v[236:239], v[188:191], a[188:191]\n"
"ds_read_b64_tr_b16 v[234:235], v250 offset:44032\n"
"v_add_f32 v243, v243, v142\n"
"v_mfma_f32_16x16x32_bf16 a[184:187], v[236:239], v[184:187], a[184:187]\n"
"v_exp_f32 v143, v143\n"
"v_mfma_f32_16x16x32_bf16 v[60:63], v[236:239], v[180:183], v[60:63]\n"
"buffer_load_dwordx4 v252, s[36:39], s45 offen offset:256 nt lds\n"
"v_mfma_f32_16x16x32_bf16 v[56:59], v[236:239], v[176:179], v[56:59]\n"
"s_waitcnt lgkmcnt(12)\n"
"ds_read_b64_tr_b16 v[236:237], v251 offset:35840\n"
"v_mfma_f32_16x16x32_bf16 v[64:67], v[208:211], v[176:179], v[64:67]\n"
"s_add_u32 m0, m0, 0x380\n"
"ds_read_b64_tr_b16 v[238:239], v251 offset:44032\n"
"v_mfma_f32_16x16x32_bf16 v[68:71], v[208:211], v[180:183], v[68:71]\n"
"v_exp_f32 v144, v144\n"
"v_mfma_f32_16x16x32_bf16 a[192:195], v[208:211], v[184:187], a[192:195]\n"
"v_exp_f32 v145, v145\n"
"v_mfma_f32_16x16x32_bf16 a[196:199], v[208:211], v[188:191], a[196:199]\n"
"s_waitcnt lgkmcnt(12)\n"
"v_add_f32 v243, v243, v143\n"
"v_cvt_pk_bf16_f32 v173, v142, v143\n"
"v_mfma_f32_16x16x32_bf16 a[204:207], v[212:215], v[188:191], a[204:207]\n"
"v_add_f32 v240, v240, v144\n"
"v_add_f32 v240, v240, v145\n"
"v_mfma_f32_16x16x32_bf16 a[200:203], v[212:215], v[184:187], a[200:203]\n"
"buffer_load_dwordx4 v252, s[36:39], s45 offen offset:384 nt lds\n"
"v_mfma_f32_16x16x32_bf16 v[76:79], v[212:215], v[180:183], v[76:79]\n"
"s_add_u32 m0, m0, 0x13580\n"
"v_exp_f32 v146, v146\n"
"v_mfma_f32_16x16x32_bf16 v[72:75], v[212:215], v[176:179], v[72:75]\n"
"s_waitcnt lgkmcnt(10)\n"
"v_cvt_pk_bf16_f32 v162, v144, v145\n"
"v_add_f32 v240, v240, v146\n"
"v_mfma_f32_16x16x32_bf16 v[80:83], v[216:219], v[176:179], v[80:83]\n"
"v_mfma_f32_16x16x32_bf16 v[84:87], v[216:219], v[180:183], v[84:87]\n"
"v_mfma_f32_16x16x32_bf16 a[208:211], v[216:219], v[184:187], a[208:211]\n"
"v_exp_f32 v147, v147\n"
"v_mfma_f32_16x16x32_bf16 a[212:215], v[216:219], v[188:191], a[212:215]\n"
"s_waitcnt vmcnt(16) lgkmcnt(0)\n"
"s_barrier\n"
"v_add_f32 v240, v240, v147\n"
"v_mfma_f32_16x16x32_bf16 a[220:223], v[220:223], v[188:191], a[220:223]\n"
"ds_read_b128 v[208:211], v246 offset:32768\n"
"v_cvt_pk_bf16_f32 v163, v146, v147\n"
"v_mfma_f32_16x16x32_bf16 a[216:219], v[220:223], v[184:187], a[216:219]\n"
"s_add_u32 s44, s44, 0x8000\n"
"buffer_load_dwordx4 v253, s[40:43], s46 offen offset:0 nt lds\n"
"v_mfma_f32_16x16x32_bf16 v[92:95], v[220:223], v[180:183], v[92:95]\n"
"s_add_u32 m0, m0, 0x380\n"
"ds_read_b128 v[212:215], v247 offset:32768\n"
"v_mfma_f32_16x16x32_bf16 v[88:91], v[220:223], v[176:179], v[88:91]\n"
"s_add_u32 s45, s45, 0x8000\n"
"v_exp_f32 v148, v148\n"
"v_mfma_f32_16x16x32_bf16 v[96:99], v[224:227], v[176:179], v[96:99]\n"
"ds_read_b128 v[216:219], v246 offset:33792\n"
"v_add_f32 v241, v241, v148\n"
"v_mfma_f32_16x16x32_bf16 v[100:103], v[224:227], v[180:183], v[100:103]\n"
"ds_read_b128 v[220:223], v247 offset:33792\n"
"v_mfma_f32_16x16x32_bf16 a[224:227], v[224:227], v[184:187], a[224:227]\n"
"v_exp_f32 v149, v149\n"
"v_mfma_f32_16x16x32_bf16 a[228:231], v[224:227], v[188:191], a[228:231]\n"
"ds_read_b128 v[224:227], v246 offset:34816\n"
"v_add_f32 v241, v241, v149\n"
"v_mfma_f32_16x16x32_bf16 a[236:239], v[228:231], v[188:191], a[236:239]\n"
"buffer_load_dwordx4 v253, s[40:43], s46 offen offset:128 nt lds\n"
"v_mfma_f32_16x16x32_bf16 a[232:235], v[228:231], v[184:187], a[232:235]\n"
"s_add_u32 m0, m0, 0x380\n"
"v_exp_f32 v150, v150\n"
"v_mfma_f32_16x16x32_bf16 v[108:111], v[228:231], v[180:183], v[108:111]\n"
"v_cvt_pk_bf16_f32 v166, v148, v149\n"
"v_add_f32 v241, v241, v150\n"
"v_mfma_f32_16x16x32_bf16 v[104:107], v[228:231], v[176:179], v[104:107]\n"
"ds_read_b128 v[228:231], v247 offset:34816\n"
"v_mfma_f32_16x16x32_bf16 v[112:115], v[232:235], v[176:179], v[112:115]\n"
"v_exp_f32 v151, v151\n"
"v_mfma_f32_16x16x32_bf16 v[116:119], v[232:235], v[180:183], v[116:119]\n"
"v_add_f32 v241, v241, v151\n"
"v_cvt_pk_bf16_f32 v167, v150, v151\n"
"v_mfma_f32_16x16x32_bf16 a[240:243], v[232:235], v[184:187], a[240:243]\n"
"v_mfma_f32_16x16x32_bf16 a[244:247], v[232:235], v[188:191], a[244:247]\n"
"ds_read_b128 v[232:235], v246 offset:35840\n"
"v_mfma_f32_16x16x32_bf16 a[252:255], v[236:239], v[188:191], a[252:255]\n"
"buffer_load_dwordx4 v253, s[40:43], s46 offen offset:256 nt lds\n"
"v_mfma_f32_16x16x32_bf16 a[248:251], v[236:239], v[184:187], a[248:251]\n"
"s_add_u32 m0, m0, 0x380\n"
"v_exp_f32 v152, v152\n"
"v_mfma_f32_16x16x32_bf16 v[124:127], v[236:239], v[180:183], v[124:127]\n"
"v_add_f32 v242, v242, v152\n"
"v_mfma_f32_16x16x32_bf16 v[120:123], v[236:239], v[176:179], v[120:123]\n"
"s_waitcnt lgkmcnt(6)\n"
"ds_read_b128 v[236:239], v247 offset:35840\n"
"v_mfma_f32_16x16x32_bf16 v[128:131], v[208:211], a[0:3], v[192:195]\n"
"v_exp_f32 v153, v153\n"
"v_mfma_f32_16x16x32_bf16 v[132:135], v[208:211], a[32:35], v[196:199]\n"
"v_add_f32 v242, v242, v153\n"
"v_cvt_pk_bf16_f32 v170, v152, v153\n"
"v_mfma_f32_16x16x32_bf16 v[136:139], v[208:211], a[64:67], v[200:203]\n"
"buffer_load_dwordx4 v253, s[40:43], s46 offen offset:384 nt lds\n"
"v_mfma_f32_16x16x32_bf16 v[140:143], v[208:211], a[96:99], v[204:207]\n"
"s_waitcnt lgkmcnt(6)\n"
"ds_read_b128 v[208:211], v246 offset:40960\n"
"v_mfma_f32_16x16x32_bf16 v[128:131], v[212:215], a[4:7], v[128:131]\n"
"s_add_u32 m0, m0, 0x3580\n"
"v_exp_f32 v154, v154\n"
"v_mfma_f32_16x16x32_bf16 v[132:135], v[212:215], a[36:39], v[132:135]\n"
"v_add_f32 v242, v242, v154\n"
"v_mfma_f32_16x16x32_bf16 v[136:139], v[212:215], a[68:71], v[136:139]\n"
"v_exp_f32 v155, v155\n"
"v_mfma_f32_16x16x32_bf16 v[140:143], v[212:215], a[100:103], v[140:143]\n"
"s_waitcnt lgkmcnt(6)\n"
"ds_read_b128 v[212:215], v247 offset:40960\n"
"v_mfma_f32_16x16x32_bf16 v[128:131], v[216:219], a[8:11], v[128:131]\n"
"v_add_f32 v242, v242, v155\n"
"v_cvt_pk_bf16_f32 v171, v154, v155\n"
"v_mfma_f32_16x16x32_bf16 v[132:135], v[216:219], a[40:43], v[132:135]\n"
"buffer_load_dwordx4 v253, s[40:43], s47 offen offset:0 nt lds\n"
"v_mfma_f32_16x16x32_bf16 v[136:139], v[216:219], a[72:75], v[136:139]\n"
"s_add_u32 m0, m0, 0x380\n"
"v_exp_f32 v156, v156\n"
"v_mfma_f32_16x16x32_bf16 v[140:143], v[216:219], a[104:107], v[140:143]\n"
"s_waitcnt lgkmcnt(6)\n"
"ds_read_b128 v[216:219], v246 offset:41984\n"
"v_mfma_f32_16x16x32_bf16 v[128:131], v[220:223], a[12:15], v[128:131]\n"
"v_add_f32 v243, v243, v156\n"
"v_mfma_f32_16x16x32_bf16 v[132:135], v[220:223], a[44:47], v[132:135]\n"
"v_mfma_f32_16x16x32_bf16 v[136:139], v[220:223], a[76:79], v[136:139]\n"
"v_exp_f32 v157, v157\n"
"v_mfma_f32_16x16x32_bf16 v[140:143], v[220:223], a[108:111], v[140:143]\n"
"s_waitcnt lgkmcnt(6)\n"
"ds_read_b128 v[220:223], v247 offset:41984\n"
"v_mfma_f32_16x16x32_bf16 v[128:131], v[224:227], a[16:19], v[128:131]\n"
"buffer_load_dwordx4 v253, s[40:43], s47 offen offset:128 nt lds\n"
"v_mfma_f32_16x16x32_bf16 v[132:135], v[224:227], a[48:51], v[132:135]\n"
"s_add_u32 m0, m0, 0x380\n"
"v_exp_f32 v158, v158\n"
"v_mfma_f32_16x16x32_bf16 v[136:139], v[224:227], a[80:83], v[136:139]\n"
"v_add_f32 v243, v243, v157\n"
"v_cvt_pk_bf16_f32 v174, v156, v157\n"
"v_mfma_f32_16x16x32_bf16 v[140:143], v[224:227], a[112:115], v[140:143]\n"
"s_waitcnt lgkmcnt(6)\n"
"ds_read_b128 v[224:227], v246 offset:43008\n"
"v_mfma_f32_16x16x32_bf16 v[128:131], v[228:231], a[20:23], v[128:131]\n"
"v_add_f32 v243, v243, v158\n"
"v_mfma_f32_16x16x32_bf16 v[132:135], v[228:231], a[52:55], v[132:135]\n"
"v_exp_f32 v159, v159\n"
"v_mfma_f32_16x16x32_bf16 v[136:139], v[228:231], a[84:87], v[136:139]\n"
"v_add_f32 v243, v243, v159\n"
"v_cvt_pk_bf16_f32 v175, v158, v159\n"
"v_mfma_f32_16x16x32_bf16 v[140:143], v[228:231], a[116:119], v[140:143]\n"
"s_waitcnt lgkmcnt(6)\n"
"ds_read_b128 v[228:231], v247 offset:43008\n"
"v_mfma_f32_16x16x32_bf16 v[128:131], v[232:235], a[24:27], v[128:131]\n"
"buffer_load_dwordx4 v253, s[40:43], s47 offen offset:256 nt lds\n"
"v_mfma_f32_16x16x32_bf16 v[132:135], v[232:235], a[56:59], v[132:135]\n"
"s_add_u32 m0, m0, 0x380\n"
"v_mfma_f32_16x16x32_bf16 v[136:139], v[232:235], a[88:91], v[136:139]\n"
"v_mfma_f32_16x16x32_bf16 v[140:143], v[232:235], a[120:123], v[140:143]\n"
"s_waitcnt lgkmcnt(6)\n"
"ds_read_b128 v[232:235], v246 offset:44032\n"
"v_mfma_f32_16x16x32_bf16 v[128:131], v[236:239], a[28:31], v[128:131]\n"
"v_mfma_f32_16x16x32_bf16 v[132:135], v[236:239], a[60:63], v[132:135]\n"
"v_mfma_f32_16x16x32_bf16 v[136:139], v[236:239], a[92:95], v[136:139]\n"
"buffer_load_dwordx4 v253, s[40:43], s47 offen offset:384 nt lds\n"
"v_mfma_f32_16x16x32_bf16 v[140:143], v[236:239], a[124:127], v[140:143]\n"
"s_waitcnt lgkmcnt(6)\n"
"ds_read_b128 v[236:239], v247 offset:44032\n"
"v_mfma_f32_16x16x32_bf16 v[144:147], v[208:211], a[0:3], v[192:195]\n"
"v_mfma_f32_16x16x32_bf16 v[148:151], v[208:211], a[32:35], v[196:199]\n"
"s_bitcmp1_b32 0x8018, s48\n"
"s_cbranch_scc1 .Lonce_%=\n"
".Loncer_%=:\n"
"v_mfma_f32_16x16x32_bf16 v[152:155], v[208:211], a[64:67], v[200:203]\n"
"s_sub_u32 s48, s48, 1\n"
"v_mfma_f32_16x16x32_bf16 v[156:159], v[208:211], a[96:99], v[204:207]\n"
"s_waitcnt lgkmcnt(6)\n"
"s_cmp_lg_u32 s48, 0\n"
"s_cbranch_scc1 .Lloopf_%=\n"
".Ltail_%=:\n"
"s_mov_b32 s89, 384\n"
"v_lshrrev_b32 v246, 9, v253\n"
"v_mad_u32_u24 v246, v246, s89, v253\n"
"v_mbcnt_lo_u32_b32 v252, -1, 0\n"
"v_mbcnt_hi_u32_b32 v252, -1, v252\n"
"v_lshlrev_b32 v252, 4, v252\n"
"s_lshl_b32 s89, s84, 14\n"
"s_add_u32 s89, s89, 32768\n"
"v_add_u32 v254, s89, v252\n"
"v_xor_b32 v255, 0x4000, v254\n"
"s_lshl_b32 s89, s84, 12\n"
"s_add_u32 s89, s89, 98304\n"
"v_add_u32 v247, s89, v252\n"
"v_mfma_f32_16x16x32_bf16 v[144:147], v[212:215], a[4:7], v[144:147]\n"
"s_add_u32 m0, m0, 0xfffe3580\n"
"s_mov_b32 s85, 0x0\n"
"v_mfma_f32_16x16x32_bf16 v[148:151], v[212:215], a[36:39], v[148:151]\n"
"v_exp_f32 v128, v128\n"
"v_mfma_f32_16x16x32_bf16 v[152:155], v[212:215], a[68:71], v[152:155]\n"
"v_add_f32 v240, v240, v128\n"
"v_mfma_f32_16x16x32_bf16 v[156:159], v[212:215], a[100:103], v[156:159]\n"
"s_waitcnt lgkmcnt(5)\n"
"buffer_load_dwordx4 v246, s[76:79], s85 offen offset:0 lds\n"
"v_mfma_f32_16x16x32_bf16 v[144:147], v[216:219], a[8:11], v[144:147]\n"
"s_add_u32 m0, m0, 0x4000\n"
"s_mov_b32 s85, 0x7000\n"
"v_exp_f32 v129, v129\n"
"v_mfma_f32_16x16x32_bf16 v[148:151], v[216:219], a[40:43], v[148:151]\n"
"v_add_f32 v240, v240, v129\n"
"v_cvt_pk_bf16_f32 v176, v128, v129\n"
"v_mfma_f32_16x16x32_bf16 v[152:155], v[216:219], a[72:75], v[152:155]\n"
"s_waitcnt vmcnt(17) lgkmcnt(0)\n"
"s_barrier\n"
"v_mfma_f32_16x16x32_bf16 v[156:159], v[216:219], a[104:107], v[156:159]\n"
"ds_read_b64_tr_b16 v[208:209], v248 offset:0\n"
"v_mfma_f32_16x16x32_bf16 v[144:147], v[220:223], a[12:15], v[144:147]\n"
"v_exp_f32 v130, v130\n"
"v_mfma_f32_16x16x32_bf16 v[148:151], v[220:223], a[44:47], v[148:151]\n"
"ds_read_b64_tr_b16 v[210:211], v248 offset:8192\n"
"v_add_f32 v240, v240, v130\n"
"v_mfma_f32_16x16x32_bf16 v[152:155], v[220:223], a[76:79], v[152:155]\n"
"ds_read_b64_tr_b16 v[212:213], v249 offset:0\n"
"v_mfma_f32_16x16x32_bf16 v[156:159], v[220:223], a[108:111], v[156:159]\n"
"v_exp_f32 v131, v131\n"
"v_mfma_f32_16x16x32_bf16 v[144:147], v[224:227], a[16:19], v[144:147]\n"
"buffer_load_dwordx4 v246, s[76:79], s85 offen offset:0 lds\n"
"v_mfma_f32_16x16x32_bf16 v[148:151], v[224:227], a[48:51], v[148:151]\n"
"s_add_u32 m0, m0, 0xffffc400\n"
"s_mov_b32 s85, 0xe000\n"
"ds_read_b64_tr_b16 v[214:215], v249 offset:8192\n"
"v_mfma_f32_16x16x32_bf16 v[152:155], v[224:227], a[80:83], v[152:155]\n"
"ds_read_b64_tr_b16 v[216:217], v250 offset:0\n"
"v_add_f32 v240, v240, v131\n"
"v_mfma_f32_16x16x32_bf16 v[156:159], v[224:227], a[112:115], v[156:159]\n"
"ds_read_b64_tr_b16 v[218:219], v250 offset:8192\n"
"v_exp_f32 v132, v132\n"
"v_mfma_f32_16x16x32_bf16 v[144:147], v[228:231], a[20:23], v[144:147]\n"
"ds_read_b64_tr_b16 v[220:221], v251 offset:0\n"
"v_cvt_pk_bf16_f32 v177, v130, v131\n"
"v_mfma_f32_16x16x32_bf16 v[148:151], v[228:231], a[52:55], v[148:151]\n"
"ds_read_b64_tr_b16 v[222:223], v251 offset:8192\n"
"buffer_load_dwordx4 v246, s[76:79], s85 offen offset:0 lds\n"
"v_exp_f32 v133, v133\n"
"v_mfma_f32_16x16x32_bf16 v[152:155], v[228:231], a[84:87], v[152:155]\n"
"s_add_u32 m0, m0, 0x4000\n"
"s_mov_b32 s85, 0x15000\n"
"ds_read_b64_tr_b16 v[224:225], v248 offset:1024\n"
"v_mfma_f32_16x16x32_bf16 v[156:159], v[228:231], a[116:119], v[156:159]\n"
"ds_read_b64_tr_b16 v[226:227], v248 offset:9216\n"
"v_add_f32 v241, v241, v132\n"
"v_mfma_f32_16x16x32_bf16 v[144:147], v[232:235], a[24:27], v[144:147]\n"
"v_exp_f32 v134, v134\n"
"v_mfma_f32_16x16x32_bf16 v[148:151], v[232:235], a[56:59], v[148:151]\n"
"ds_read_b64_tr_b16 v[228:229], v249 offset:1024\n"
"v_add_f32 v241, v241, v133\n"
"v_mfma_f32_16x16x32_bf16 v[152:155], v[232:235], a[88:91], v[152:155]\n"
"ds_read_b64_tr_b16 v[230:231], v249 offset:9216\n"
"buffer_load_dwordx4 v246, s[76:79], s85 offen offset:0 lds\n"
"v_exp_f32 v135, v135\n"
"v_mfma_f32_16x16x32_bf16 v[156:159], v[232:235], a[120:123], v[156:159]\n"
"ds_read_b64_tr_b16 v[232:233], v250 offset:1024\n"
"v_cvt_pk_bf16_f32 v180, v132, v133\n"
"v_mfma_f32_16x16x32_bf16 v[144:147], v[236:239], a[28:31], v[144:147]\n"
"s_add_u32 m0, m0, 0xffffc400\n"
"s_mov_b32 s85, 0x1c000\n"
"v_exp_f32 v136, v136\n"
"v_mfma_f32_16x16x32_bf16 v[148:151], v[236:239], a[60:63], v[148:151]\n"
"ds_read_b64_tr_b16 v[234:235], v250 offset:9216\n"
"v_add_f32 v241, v241, v134\n"
"v_mfma_f32_16x16x32_bf16 v[152:155], v[236:239], a[92:95], v[152:155]\n"
"v_exp_f32 v137, v137\n"
"v_mfma_f32_16x16x32_bf16 v[156:159], v[236:239], a[124:127], v[156:159]\n"
"s_waitcnt lgkmcnt(12)\n"
"buffer_load_dwordx4 v246, s[76:79], s85 offen offset:0 lds\n"
"v_mfma_f32_16x16x32_bf16 v[0:3], v[208:211], v[160:163], v[0:3]\n"
"s_add_u32 m0, m0, 0x4000\n"
"s_mov_b32 s85, 0x23000\n"
"v_add_f32 v241, v241, v135\n"
"v_cvt_pk_bf16_f32 v181, v134, v135\n"
"v_mfma_f32_16x16x32_bf16 v[4:7], v[208:211], v[164:167], v[4:7]\n"
"ds_read_b64_tr_b16 v[236:237], v251 offset:1024\n"
"v_add_f32 v242, v242, v136\n"
"v_mfma_f32_16x16x32_bf16 a[128:131], v[208:211], v[168:171], a[128:131]\n"
"ds_read_b64_tr_b16 v[238:239], v251 offset:9216\n"
"v_exp_f32 v138, v138\n"
"v_mfma_f32_16x16x32_bf16 a[132:135], v[208:211], v[172:175], a[132:135]\n"
"s_waitcnt lgkmcnt(12)\n"
"v_exp_f32 v139, v139\n"
"v_mfma_f32_16x16x32_bf16 a[140:143], v[212:215], v[172:175], a[140:143]\n"
"buffer_load_dwordx4 v246, s[76:79], s85 offen offset:0 lds\n"
"v_mfma_f32_16x16x32_bf16 a[136:139], v[212:215], v[168:171], a[136:139]\n"
"ds_read_b64_tr_b16 v[208:209], v248 offset:2048\n"
"v_add_f32 v242, v242, v137\n"
"v_mfma_f32_16x16x32_bf16 v[12:15], v[212:215], v[164:167], v[12:15]\n"
"s_add_u32 m0, m0, 0xffffc400\n"
"s_mov_b32 s85, 0x2a000\n"
"ds_read_b64_tr_b16 v[210:211], v248 offset:10240\n"
"v_cvt_pk_bf16_f32 v184, v136, v137\n"
"v_mfma_f32_16x16x32_bf16 v[8:11], v[212:215], v[160:163], v[8:11]\n"
"s_waitcnt lgkmcnt(12)\n"
"v_exp_f32 v140, v140\n"
"v_mfma_f32_16x16x32_bf16 v[16:19], v[216:219], v[160:163], v[16:19]\n"
"ds_read_b64_tr_b16 v[212:213], v249 offset:2048\n"
"v_add_f32 v242, v242, v138\n"
"v_mfma_f32_16x16x32_bf16 v[20:23], v[216:219], v[164:167], v[20:23]\n"
"ds_read_b64_tr_b16 v[214:215], v249 offset:10240\n"
"v_add_f32 v242, v242, v139\n"
"v_mfma_f32_16x16x32_bf16 a[144:147], v[216:219], v[168:171], a[144:147]\n"
"buffer_load_dwordx4 v246, s[76:79], s85 offen offset:0 lds\n"
"v_exp_f32 v141, v141\n"
"v_mfma_f32_16x16x32_bf16 a[148:151], v[216:219], v[172:175], a[148:151]\n"
"s_waitcnt lgkmcnt(12)\n"
"s_add_u32 m0, m0, 0x4000\n"
"s_mov_b32 s85, 0x31000\n"
"v_cvt_pk_bf16_f32 v185, v138, v139\n"
"v_mfma_f32_16x16x32_bf16 a[156:159], v[220:223], v[172:175], a[156:159]\n"
"v_exp_f32 v142, v142\n"
"v_mfma_f32_16x16x32_bf16 a[152:155], v[220:223], v[168:171], a[152:155]\n"
"ds_read_b64_tr_b16 v[216:217], v250 offset:2048\n"
"v_add_f32 v243, v243, v140\n"
"v_mfma_f32_16x16x32_bf16 v[28:31], v[220:223], v[164:167], v[28:31]\n"
"ds_read_b64_tr_b16 v[218:219], v250 offset:10240\n"
"v_add_f32 v243, v243, v141\n"
"v_mfma_f32_16x16x32_bf16 v[24:27], v[220:223], v[160:163], v[24:27]\n"
"s_waitcnt lgkmcnt(12)\n"
"v_exp_f32 v143, v143\n"
"v_mfma_f32_16x16x32_bf16 v[32:35], v[224:227], v[160:163], v[32:35]\n"
"buffer_load_dwordx4 v246, s[76:79], s85 offen offset:0 lds\n"
"v_cvt_pk_bf16_f32 v188, v140, v141\n"
"v_mfma_f32_16x16x32_bf16 v[36:39], v[224:227], v[164:167], v[36:39]\n"
"s_add_u32 m0, m0, 0x1b400\n"
"s_mov_b32 s85, 0x80\n"
"v_add_f32 v243, v243, v142\n"
"v_mfma_f32_16x16x32_bf16 a[160:163], v[224:227], v[168:171], a[160:163]\n"
"ds_read_b64_tr_b16 v[220:221], v251 offset:2048\n"
"v_exp_f32 v144, v144\n"
"v_mfma_f32_16x16x32_bf16 a[164:167], v[224:227], v[172:175], a[164:167]\n"
"s_waitcnt lgkmcnt(11)\n"
"ds_read_b64_tr_b16 v[222:223], v251 offset:10240\n"
"v_mfma_f32_16x16x32_bf16 a[172:175], v[228:231], v[172:175], a[172:175]\n"
"v_exp_f32 v145, v145\n"
"v_mfma_f32_16x16x32_bf16 a[168:171], v[228:231], v[168:171], a[168:171]\n"
"buffer_load_dwordx4 v246, s[76:79], s85 offen offset:0 lds\n"
"v_add_f32 v243, v243, v143\n"
"v_cvt_pk_bf16_f32 v189, v142, v143\n"
"v_mfma_f32_16x16x32_bf16 v[44:47], v[228:231], v[164:167], v[44:47]\n"
"s_add_u32 m0, m0, 0x4000\n"
"s_mov_b32 s85, 0x7080\n"
"ds_read_b64_tr_b16 v[224:225], v248 offset:3072\n"
"v_mfma_f32_16x16x32_bf16 v[40:43], v[228:231], v[160:163], v[40:43]\n"
"s_waitcnt lgkmcnt(11)\n"
"ds_read_b64_tr_b16 v[226:227], v248 offset:11264\n"
"v_exp_f32 v146, v146\n"
"v_mfma_f32_16x16x32_bf16 v[48:51], v[232:235], v[160:163], v[48:51]\n"
"ds_read_b64_tr_b16 v[228:229], v249 offset:3072\n"
"v_add_f32 v240, v240, v144\n"
"v_mfma_f32_16x16x32_bf16 v[52:55], v[232:235], v[164:167], v[52:55]\n"
"v_exp_f32 v147, v147\n"
"v_mfma_f32_16x16x32_bf16 a[176:179], v[232:235], v[168:171], a[176:179]\n"
"ds_read_b64_tr_b16 v[230:231], v249 offset:11264\n"
"buffer_load_dwordx4 v246, s[76:79], s85 offen offset:0 lds\n"
"v_add_f32 v240, v240, v145\n"
"v_cvt_pk_bf16_f32 v178, v144, v145\n"
"v_mfma_f32_16x16x32_bf16 a[180:183], v[232:235], v[172:175], a[180:183]\n"
"s_waitcnt lgkmcnt(12)\n"
"v_exp_f32 v148, v148\n"
"v_mfma_f32_16x16x32_bf16 a[188:191], v[236:239], v[172:175], a[188:191]\n"
"s_add_u32 m0, m0, 0xffffc400\n"
"s_mov_b32 s85, 0xe080\n"
"ds_read_b64_tr_b16 v[232:233], v250 offset:3072\n"
"v_mfma_f32_16x16x32_bf16 a[184:187], v[236:239], v[168:171], a[184:187]\n"
"ds_read_b64_tr_b16 v[234:235], v250 offset:11264\n"
"v_add_f32 v240, v240, v146\n"
"v_mfma_f32_16x16x32_bf16 v[60:63], v[236:239], v[164:167], v[60:63]\n"
"v_exp_f32 v149, v149\n"
"v_mfma_f32_16x16x32_bf16 v[56:59], v[236:239], v[160:163], v[56:59]\n"
"s_waitcnt lgkmcnt(12)\n"
"v_add_f32 v240, v240, v147\n"
"v_cvt_pk_bf16_f32 v179, v146, v147\n"
"v_mfma_f32_16x16x32_bf16 v[64:67], v[208:211], v[160:163], v[64:67]\n"
"buffer_load_dwordx4 v246, s[76:79], s85 offen offset:0 lds\n"
"v_mfma_f32_16x16x32_bf16 v[68:71], v[208:211], v[164:167], v[68:71]\n"
"s_add_u32 m0, m0, 0x4000\n"
"s_mov_b32 s85, 0x15080\n"
"ds_read_b64_tr_b16 v[236:237], v251 offset:3072\n"
"v_exp_f32 v150, v150\n"
"v_mfma_f32_16x16x32_bf16 a[192:195], v[208:211], v[168:171], a[192:195]\n"
"ds_read_b64_tr_b16 v[238:239], v251 offset:11264\n"
"v_add_f32 v241, v241, v148\n"
"v_mfma_f32_16x16x32_bf16 a[196:199], v[208:211], v[172:175], a[196:199]\n"
"s_waitcnt lgkmcnt(12)\n"
"v_exp_f32 v151, v151\n"
"v_mfma_f32_16x16x32_bf16 a[204:207], v[212:215], v[172:175], a[204:207]\n"
"v_add_f32 v241, v241, v149\n"
"v_cvt_pk_bf16_f32 v182, v148, v149\n"
"v_mfma_f32_16x16x32_bf16 a[200:203], v[212:215], v[168:171], a[200:203]\n"
"buffer_load_dwordx4 v246, s[76:79], s85 offen offset:0 lds\n"
"v_mfma_f32_16x16x32_bf16 v[76:79], v[212:215], v[164:167], v[76:79]\n"
"s_add_u32 m0, m0, 0xffffc400\n"
"s_mov_b32 s85, 0x1c080\n"
"v_exp_f32 v152, v152\n"
"v_mfma_f32_16x16x32_bf16 v[72:75], v[212:215], v[160:163], v[72:75]\n"
"s_waitcnt lgkmcnt(10)\n"
"v_add_f32 v241, v241, v150\n"
"v_add_f32 v241, v241, v151\n"
"v_mfma_f32_16x16x32_bf16 v[80:83], v[216:219], v[160:163], v[80:83]\n"
"v_exp_f32 v153, v153\n"
"v_mfma_f32_16x16x32_bf16 v[84:87], v[216:219], v[164:167], v[84:87]\n"
"v_cvt_pk_bf16_f32 v183, v150, v151\n"
"v_mfma_f32_16x16x32_bf16 a[208:211], v[216:219], v[168:171], a[208:211]\n"
"buffer_load_dwordx4 v246, s[76:79], s85 offen offset:0 lds\n"
"v_mfma_f32_16x16x32_bf16 a[212:215], v[216:219], v[172:175], a[212:215]\n"
"s_waitcnt vmcnt(13) lgkmcnt(0)\n"
"s_barrier\n"
"v_exp_f32 v154, v154\n"
"v_mfma_f32_16x16x32_bf16 a[220:223], v[220:223], v[172:175], a[220:223]\n"
"s_add_u32 m0, m0, 0x4000\n"
"s_mov_b32 s85, 0x23080\n"
"v_add_f32 v242, v242, v152\n"
"v_add_f32 v242, v242, v153\n"
"v_mfma_f32_16x16x32_bf16 a[216:219], v[220:223], v[168:171], a[216:219]\n"
"v_exp_f32 v155, v155\n"
"v_mfma_f32_16x16x32_bf16 v[92:95], v[220:223], v[164:167], v[92:95]\n"
"v_exp_f32 v156, v156\n"
"v_mfma_f32_16x16x32_bf16 v[88:91], v[220:223], v[160:163], v[88:91]\n"
"ds_read_b64_tr_b16 v[208:209], v248 offset:32768\n"
"v_cvt_pk_bf16_f32 v186, v152, v153\n"
"v_mfma_f32_16x16x32_bf16 v[96:99], v[224:227], v[160:163], v[96:99]\n"
"buffer_load_dwordx4 v246, s[76:79], s85 offen offset:0 lds\n"
"v_mfma_f32_16x16x32_bf16 v[100:103], v[224:227], v[164:167], v[100:103]\n"
"s_add_u32 m0, m0, 0xffffc400\n"
"s_mov_b32 s85, 0x2a080\n"
"v_add_f32 v242, v242, v154\n"
"v_add_f32 v242, v242, v155\n"
"v_mfma_f32_16x16x32_bf16 a[224:227], v[224:227], v[168:171], a[224:227]\n"
"ds_read_b64_tr_b16 v[210:211], v248 offset:40960\n"
"v_cvt_pk_bf16_f32 v187, v154, v155\n"
"v_mfma_f32_16x16x32_bf16 a[228:231], v[224:227], v[172:175], a[228:231]\n"
"ds_read_b64_tr_b16 v[212:213], v249 offset:32768\n"
"v_exp_f32 v157, v157\n"
"v_mfma_f32_16x16x32_bf16 a[236:239], v[228:231], v[172:175], a[236:239]\n"
"ds_read_b64_tr_b16 v[214:215], v249 offset:40960\n"
"v_add_f32 v243, v243, v156\n"
"v_mfma_f32_16x16x32_bf16 a[232:235], v[228:231], v[168:171], a[232:235]\n"
"ds_read_b64_tr_b16 v[216:217], v250 offset:32768\n"
"buffer_load_dwordx4 v246, s[76:79], s85 offen offset:0 lds\n"
"v_exp_f32 v158, v158\n"
"v_mfma_f32_16x16x32_bf16 v[108:111], v[228:231], v[164:167], v[108:111]\n"
"s_add_u32 m0, m0, 0x4000\n"
"s_mov_b32 s85, 0x31080\n"
"ds_read_b64_tr_b16 v[218:219], v250 offset:40960\n"
"v_mfma_f32_16x16x32_bf16 v[104:107], v[228:231], v[160:163], v[104:107]\n"
"ds_read_b64_tr_b16 v[220:221], v251 offset:32768\n"
"v_mfma_f32_16x16x32_bf16 v[112:115], v[232:235], v[160:163], v[112:115]\n"
"ds_read_b64_tr_b16 v[222:223], v251 offset:40960\n"
"v_exp_f32 v159, v159\n"
"v_mfma_f32_16x16x32_bf16 v[116:119], v[232:235], v[164:167], v[116:119]\n"
"ds_read_b64_tr_b16 v[224:225], v248 offset:33792\n"
"v_add_f32 v243, v243, v157\n"
"v_cvt_pk_bf16_f32 v190, v156, v157\n"
"v_mfma_f32_16x16x32_bf16 a[240:243], v[232:235], v[168:171], a[240:243]\n"
"ds_read_b64_tr_b16 v[226:227], v248 offset:41984\n"
"v_add_f32 v243, v243, v158\n"
"buffer_load_dwordx2 v[200:201], v245, s[60:63], 0 offen offset:0\n"
"v_mfma_f32_16x16x32_bf16 a[244:247], v[232:235], v[172:175], a[244:247]\n"
"buffer_load_dwordx4 v246, s[76:79], s85 offen offset:0 lds\n"
"v_mfma_f32_16x16x32_bf16 a[252:255], v[236:239], v[172:175], a[252:255]\n"
"ds_read_b64_tr_b16 v[228:229], v249 offset:33792\n"
"v_add_f32 v243, v243, v159\n"
"buffer_load_dwordx2 v[202:203], v245, s[60:63], 0 offen offset:256\n"
"v_mfma_f32_16x16x32_bf16 a[248:251], v[236:239], v[168:171], a[248:251]\n"
"ds_read_b64_tr_b16 v[230:231], v249 offset:41984\n"
"v_cvt_pk_bf16_f32 v191, v158, v159\n"
"buffer_load_dwordx2 v[204:205], v245, s[60:63], 0 offen offset:512\n"
"v_mfma_f32_16x16x32_bf16 v[124:127], v[236:239], v[164:167], v[124:127]\n"
"buffer_load_dwordx2 v[206:207], v245, s[60:63], 0 offen offset:768\n"
"v_mfma_f32_16x16x32_bf16 v[120:123], v[236:239], v[160:163], v[120:123]\n"
"ds_read_b64_tr_b16 v[232:233], v250 offset:33792\n"
"ds_read_b64_tr_b16 v[234:235], v250 offset:41984\n"
"s_waitcnt lgkmcnt(12)\n"
"v_mfma_f32_16x16x32_bf16 v[0:3], v[208:211], v[176:179], v[0:3]\n"
"v_mfma_f32_16x16x32_bf16 v[4:7], v[208:211], v[180:183], v[4:7]\n"
"ds_read_b64_tr_b16 v[236:237], v251 offset:33792\n"
"v_mbcnt_lo_u32_b32 v192, -1, 0\n"
"v_mbcnt_hi_u32_b32 v192, -1, v192\n"
"v_and_b32 v192, 1, v192\n"
"v_lshlrev_b32 v192, 4, v192\n"
"s_mov_b32 s89, 0x3f80\n"
"v_lshlrev_b32_e64 v192, v192, s89\n"
"s_mov_b32 s90, 0x300003\n"
"s_mov_b32 s91, 0x30000300\n"
"v_cndmask_b32 v128, 0, v192, s[90:91]\n"
"s_lshl_b64 s[90:91], s[90:91], 2\n"
"v_cndmask_b32 v129, 0, v192, s[90:91]\n"
"v_mov_b32 v130, 0\n"
"v_mov_b32 v131, 0\n"
"v_mov_b32 v132, 0\n"
"v_mov_b32 v133, 0\n"
"v_mov_b32 v134, v128\n"
"v_mov_b32 v135, v129\n"
"v_mfma_f32_16x16x32_bf16 a[128:131], v[208:211], v[184:187], a[128:131]\n"
"ds_read_b64_tr_b16 v[238:239], v251 offset:41984\n"
"v_mfma_f32_16x16x32_bf16 a[132:135], v[208:211], v[188:191], a[132:135]\n"
"s_waitcnt lgkmcnt(12)\n"
"v_mfma_f32_16x16x32_bf16 a[140:143], v[212:215], v[188:191], a[140:143]\n"
"v_mfma_f32_16x16x32_bf16 a[136:139], v[212:215], v[184:187], a[136:139]\n"
"ds_read_b64_tr_b16 v[208:209], v248 offset:34816\n"
"v_mfma_f32_16x16x32_bf16 v[12:15], v[212:215], v[180:183], v[12:15]\n"
"ds_read_b64_tr_b16 v[210:211], v248 offset:43008\n"
"v_accvgpr_read_b32 v136, a128\n"
"v_accvgpr_read_b32 v137, a129\n"
"v_accvgpr_read_b32 v138, a130\n"
"v_mfma_f32_16x16x32_bf16 v[8:11], v[212:215], v[176:179], v[8:11]\n"
"s_waitcnt lgkmcnt(12)\n"
"v_accvgpr_read_b32 v139, a131\n"
"v_mfma_f32_16x16x32_bf16 v[16:19], v[216:219], v[176:179], v[16:19]\n"
"ds_read_b64_tr_b16 v[212:213], v249 offset:34816\n"
"v_mfma_f32_16x16x32_bf16 v[20:23], v[216:219], v[180:183], v[20:23]\n"
"ds_read_b64_tr_b16 v[214:215], v249 offset:43008\n"
"v_mfma_f32_16x16x32_bf16 a[144:147], v[216:219], v[184:187], a[144:147]\n"
"v_accvgpr_read_b32 v140, a136\n"
"v_accvgpr_read_b32 v141, a137\n"
"v_accvgpr_read_b32 v142, a138\n"
"v_mfma_f32_16x16x32_bf16 a[148:151], v[216:219], v[188:191], a[148:151]\n"
"s_waitcnt lgkmcnt(12)\n"
"v_accvgpr_read_b32 v143, a139\n"
"v_cvt_pk_bf16_f32 v136, v136, v137\n"
"v_cvt_pk_bf16_f32 v137, v138, v139\n"
"v_mfma_f32_16x16x32_bf16 a[156:159], v[220:223], v[188:191], a[156:159]\n"
"v_cvt_pk_bf16_f32 v138, v140, v141\n"
"v_cvt_pk_bf16_f32 v139, v142, v143\n"
"ds_write_b128 v254, v[136:139] offset:0\n"
"v_mfma_f32_16x16x32_bf16 a[152:155], v[220:223], v[184:187], a[152:155]\n"
"ds_read_b64_tr_b16 v[216:217], v250 offset:34816\n"
"v_accvgpr_read_b32 v144, a132\n"
"v_accvgpr_read_b32 v145, a133\n"
"v_accvgpr_read_b32 v146, a134\n"
"v_mfma_f32_16x16x32_bf16 v[28:31], v[220:223], v[180:183], v[28:31]\n"
"ds_read_b64_tr_b16 v[218:219], v250 offset:43008\n"
"v_accvgpr_read_b32 v147, a135\n"
"v_accvgpr_read_b32 v148, a140\n"
"v_accvgpr_read_b32 v149, a141\n"
"v_mfma_f32_16x16x32_bf16 v[24:27], v[220:223], v[176:179], v[24:27]\n"
"s_waitcnt lgkmcnt(13)\n"
"v_accvgpr_read_b32 v150, a142\n"
"v_accvgpr_read_b32 v151, a143\n"
"v_cvt_pk_bf16_f32 v144, v144, v145\n"
"v_mfma_f32_16x16x32_bf16 v[32:35], v[224:227], v[176:179], v[32:35]\n"
"v_cvt_pk_bf16_f32 v145, v146, v147\n"
"v_cvt_pk_bf16_f32 v146, v148, v149\n"
"v_cvt_pk_bf16_f32 v147, v150, v151\n"
"v_mfma_f32_16x16x32_bf16 v[36:39], v[224:227], v[180:183], v[36:39]\n"
"ds_write_b128 v254, v[144:147] offset:1024\n"
"v_mfma_f32_16x16x32_bf16 a[160:163], v[224:227], v[184:187], a[160:163]\n"
"ds_read_b64_tr_b16 v[220:221], v251 offset:34816\n"
"v_accvgpr_read_b32 v152, a144\n"
"v_accvgpr_read_b32 v153, a145\n"
"v_accvgpr_read_b32 v154, a146\n"
"v_mfma_f32_16x16x32_bf16 a[164:167], v[224:227], v[188:191], a[164:167]\n"
"s_waitcnt lgkmcnt(13)\n"
"ds_read_b64_tr_b16 v[222:223], v251 offset:43008\n"
"v_accvgpr_read_b32 v155, a147\n"
"v_accvgpr_read_b32 v156, a152\n"
"v_accvgpr_read_b32 v157, a153\n"
"v_mfma_f32_16x16x32_bf16 a[172:175], v[228:231], v[188:191], a[172:175]\n"
"v_accvgpr_read_b32 v158, a154\n"
"v_accvgpr_read_b32 v159, a155\n"
"v_cvt_pk_bf16_f32 v152, v152, v153\n"
"v_mfma_f32_16x16x32_bf16 a[168:171], v[228:231], v[184:187], a[168:171]\n"
"v_cvt_pk_bf16_f32 v153, v154, v155\n"
"v_cvt_pk_bf16_f32 v154, v156, v157\n"
"v_cvt_pk_bf16_f32 v155, v158, v159\n"
"v_mfma_f32_16x16x32_bf16 v[44:47], v[228:231], v[180:183], v[44:47]\n"
"ds_read_b64_tr_b16 v[224:225], v248 offset:35840\n"
"ds_write_b128 v254, v[152:155] offset:2048\n"
"v_mfma_f32_16x16x32_bf16 v[40:43], v[228:231], v[176:179], v[40:43]\n"
"s_waitcnt lgkmcnt(14)\n"
"ds_read_b64_tr_b16 v[226:227], v248 offset:44032\n"
"v_accvgpr_read_b32 v160, a148\n"
"v_accvgpr_read_b32 v161, a149\n"
"v_accvgpr_read_b32 v162, a150\n"
"v_mfma_f32_16x16x32_bf16 v[48:51], v[232:235], v[176:179], v[48:51]\n"
"ds_read_b64_tr_b16 v[228:229], v249 offset:35840\n"
"v_accvgpr_read_b32 v163, a151\n"
"v_accvgpr_read_b32 v164, a156\n"
"v_accvgpr_read_b32 v165, a157\n"
"v_mfma_f32_16x16x32_bf16 v[52:55], v[232:235], v[180:183], v[52:55]\n"
"v_accvgpr_read_b32 v166, a158\n"
"v_accvgpr_read_b32 v167, a159\n"
"v_cvt_pk_bf16_f32 v160, v160, v161\n"
"v_mfma_f32_16x16x32_bf16 a[176:179], v[232:235], v[184:187], a[176:179]\n"
"ds_read_b64_tr_b16 v[230:231], v249 offset:44032\n"
"v_cvt_pk_bf16_f32 v161, v162, v163\n"
"v_cvt_pk_bf16_f32 v162, v164, v165\n"
"v_cvt_pk_bf16_f32 v163, v166, v167\n"
"v_mfma_f32_16x16x32_bf16 a[180:183], v[232:235], v[188:191], a[180:183]\n"
"s_waitcnt lgkmcnt(15)\n"
"ds_write_b128 v254, v[160:163] offset:3072\n"
"v_mfma_f32_16x16x32_bf16 a[188:191], v[236:239], v[188:191], a[188:191]\n"
"ds_read_b64_tr_b16 v[232:233], v250 offset:35840\n"
"v_accvgpr_read_b32 v168, a160\n"
"v_accvgpr_read_b32 v169, a161\n"
"v_accvgpr_read_b32 v170, a162\n"
"v_mfma_f32_16x16x32_bf16 a[184:187], v[236:239], v[184:187], a[184:187]\n"
"ds_read_b64_tr_b16 v[234:235], v250 offset:44032\n"
"v_accvgpr_read_b32 v171, a163\n"
"v_accvgpr_read_b32 v172, a168\n"
"v_accvgpr_read_b32 v173, a169\n"
"v_mfma_f32_16x16x32_bf16 v[60:63], v[236:239], v[180:183], v[60:63]\n"
"v_accvgpr_read_b32 v174, a170\n"
"v_accvgpr_read_b32 v175, a171\n"
"v_cvt_pk_bf16_f32 v168, v168, v169\n"
"v_mfma_f32_16x16x32_bf16 v[56:59], v[236:239], v[176:179], v[56:59]\n"
"s_waitcnt lgkmcnt(15)\n"
"v_cvt_pk_bf16_f32 v169, v170, v171\n"
"v_cvt_pk_bf16_f32 v170, v172, v173\n"
"v_cvt_pk_bf16_f32 v171, v174, v175\n"
"v_mfma_f32_16x16x32_bf16 v[64:67], v[208:211], v[176:179], v[64:67]\n"
"ds_write_b128 v254, v[168:171] offset:4096\n"
"v_mfma_f32_16x16x32_bf16 v[68:71], v[208:211], v[180:183], v[68:71]\n"
"ds_read_b64_tr_b16 v[236:237], v251 offset:35840\n"
"v_accvgpr_read_b32 v136, a164\n"
"v_accvgpr_read_b32 v137, a165\n"
"v_accvgpr_read_b32 v138, a166\n"
"v_mfma_f32_16x16x32_bf16 a[192:195], v[208:211], v[184:187], a[192:195]\n"
"ds_read_b64_tr_b16 v[238:239], v251 offset:44032\n"
"v_accvgpr_read_b32 v139, a167\n"
"v_accvgpr_read_b32 v140, a172\n"
"v_accvgpr_read_b32 v141, a173\n"
"v_mfma_f32_16x16x32_bf16 a[196:199], v[208:211], v[188:191], a[196:199]\n"
"s_waitcnt lgkmcnt(15)\n"
"v_accvgpr_read_b32 v142, a174\n"
"v_accvgpr_read_b32 v143, a175\n"
"v_cvt_pk_bf16_f32 v136, v136, v137\n"
"v_mfma_f32_16x16x32_bf16 a[204:207], v[212:215], v[188:191], a[204:207]\n"
"v_cvt_pk_bf16_f32 v137, v138, v139\n"
"v_cvt_pk_bf16_f32 v138, v140, v141\n"
"v_cvt_pk_bf16_f32 v139, v142, v143\n"
"v_mfma_f32_16x16x32_bf16 a[200:203], v[212:215], v[184:187], a[200:203]\n"
"ds_write_b128 v254, v[136:139] offset:5120\n"
"v_mfma_f32_16x16x32_bf16 v[76:79], v[212:215], v[180:183], v[76:79]\n"
"v_accvgpr_read_b32 v144, a176\n"
"v_accvgpr_read_b32 v145, a177\n"
"v_accvgpr_read_b32 v146, a178\n"
"v_mfma_f32_16x16x32_bf16 v[72:75], v[212:215], v[176:179], v[72:75]\n"
"s_waitcnt lgkmcnt(15)\n"
"v_accvgpr_read_b32 v147, a179\n"
"v_accvgpr_read_b32 v148, a184\n"
"v_accvgpr_read_b32 v149, a185\n"
"v_mfma_f32_16x16x32_bf16 v[80:83], v[216:219], v[176:179], v[80:83]\n"
"v_accvgpr_read_b32 v150, a186\n"
"v_accvgpr_read_b32 v151, a187\n"
"v_cvt_pk_bf16_f32 v144, v144, v145\n"
"v_mfma_f32_16x16x32_bf16 v[84:87], v[216:219], v[180:183], v[84:87]\n"
"v_cvt_pk_bf16_f32 v145, v146, v147\n"
"v_cvt_pk_bf16_f32 v146, v148, v149\n"
"v_cvt_pk_bf16_f32 v147, v150, v151\n"
"v_mfma_f32_16x16x32_bf16 a[208:211], v[216:219], v[184:187], a[208:211]\n"
"ds_write_b128 v254, v[144:147] offset:6144\n"
"v_mfma_f32_16x16x32_bf16 a[212:215], v[216:219], v[188:191], a[212:215]\n"
"s_waitcnt lgkmcnt(0)\n"
"s_barrier\n"
"v_accvgpr_read_b32 v152, a180\n"
"v_accvgpr_read_b32 v153, a181\n"
"v_accvgpr_read_b32 v154, a182\n"
"v_mfma_f32_16x16x32_bf16 a[220:223], v[220:223], v[188:191], a[220:223]\n"
"v_accvgpr_read_b32 v155, a183\n"
"v_accvgpr_read_b32 v156, a188\n"
"v_accvgpr_read_b32 v157, a189\n"
"s_add_u32 m0, m0, 0xffff3400\n"
"s_mov_b32 s85, 0x100\n"
"buffer_load_dwordx4 v246, s[76:79], s85 offen offset:0 lds\n"
"ds_write_b128 v247, v[240:243] offset:19456\n"
"v_mfma_f32_16x16x32_bf16 a[216:219], v[220:223], v[184:187], a[216:219]\n"
"v_accvgpr_read_b32 v158, a190\n"
"v_accvgpr_read_b32 v159, a191\n"
"v_cvt_pk_bf16_f32 v152, v152, v153\n"
"v_mfma_f32_16x16x32_bf16 v[92:95], v[220:223], v[180:183], v[92:95]\n"
"v_cvt_pk_bf16_f32 v153, v154, v155\n"
"v_cvt_pk_bf16_f32 v154, v156, v157\n"
"v_cvt_pk_bf16_f32 v155, v158, v159\n"
"v_mfma_f32_16x16x32_bf16 v[88:91], v[220:223], v[176:179], v[88:91]\n"
"ds_write_b128 v254, v[152:155] offset:7168\n"
"s_add_u32 m0, m0, 0x4000\n"
"s_mov_b32 s85, 0x7100\n"
"buffer_load_dwordx4 v246, s[76:79], s85 offen offset:0 lds\n"
"v_mfma_f32_16x16x32_bf16 v[96:99], v[224:227], v[176:179], v[96:99]\n"
"v_accvgpr_read_b32 v160, a192\n"
"v_accvgpr_read_b32 v161, a193\n"
"v_accvgpr_read_b32 v162, a194\n"
"v_mfma_f32_16x16x32_bf16 v[100:103], v[224:227], v[180:183], v[100:103]\n"
"v_accvgpr_read_b32 v163, a195\n"
"v_accvgpr_read_b32 v164, a200\n"
"v_accvgpr_read_b32 v165, a201\n"
"v_mfma_f32_16x16x32_bf16 a[224:227], v[224:227], v[184:187], a[224:227]\n"
"v_accvgpr_read_b32 v166, a202\n"
"v_accvgpr_read_b32 v167, a203\n"
"v_cvt_pk_bf16_f32 v160, v160, v161\n"
"s_add_u32 m0, m0, 0xffffc400\n"
"s_mov_b32 s85, 0xe100\n"
"buffer_load_dwordx4 v246, s[76:79], s85 offen offset:0 lds\n"
"v_mfma_f32_16x16x32_bf16 a[228:231], v[224:227], v[188:191], a[228:231]\n"
"v_cvt_pk_bf16_f32 v161, v162, v163\n"
"v_cvt_pk_bf16_f32 v162, v164, v165\n"
"v_cvt_pk_bf16_f32 v163, v166, v167\n"
"v_mfma_f32_16x16x32_bf16 a[236:239], v[228:231], v[188:191], a[236:239]\n"
"ds_write_b128 v254, v[160:163] offset:8192\n"
"v_mfma_f32_16x16x32_bf16 a[232:235], v[228:231], v[184:187], a[232:235]\n"
"v_accvgpr_read_b32 v168, a196\n"
"v_accvgpr_read_b32 v169, a197\n"
"v_accvgpr_read_b32 v170, a198\n"
"s_add_u32 m0, m0, 0x4000\n"
"s_mov_b32 s85, 0x15100\n"
"buffer_load_dwordx4 v246, s[76:79], s85 offen offset:0 lds\n"
"v_mfma_f32_16x16x32_bf16 v[108:111], v[228:231], v[180:183], v[108:111]\n"
"v_accvgpr_read_b32 v171, a199\n"
"v_accvgpr_read_b32 v172, a204\n"
"v_accvgpr_read_b32 v173, a205\n"
"v_mfma_f32_16x16x32_bf16 v[104:107], v[228:231], v[176:179], v[104:107]\n"
"v_accvgpr_read_b32 v174, a206\n"
"v_accvgpr_read_b32 v175, a207\n"
"v_cvt_pk_bf16_f32 v168, v168, v169\n"
"v_mfma_f32_16x16x32_bf16 v[112:115], v[232:235], v[176:179], v[112:115]\n"
"v_cvt_pk_bf16_f32 v169, v170, v171\n"
"v_cvt_pk_bf16_f32 v170, v172, v173\n"
"v_cvt_pk_bf16_f32 v171, v174, v175\n"
"s_add_u32 m0, m0, 0xffffc400\n"
"s_mov_b32 s85, 0x1c100\n"
"buffer_load_dwordx4 v246, s[76:79], s85 offen offset:0 lds\n"
"v_mfma_f32_16x16x32_bf16 v[116:119], v[232:235], v[180:183], v[116:119]\n"
"ds_write_b128 v254, v[168:171] offset:9216\n"
"v_mfma_f32_16x16x32_bf16 a[240:243], v[232:235], v[184:187], a[240:243]\n"
"v_accvgpr_read_b32 v136, a208\n"
"v_accvgpr_read_b32 v137, a209\n"
"v_accvgpr_read_b32 v138, a210\n"
"v_mfma_f32_16x16x32_bf16 a[244:247], v[232:235], v[188:191], a[244:247]\n"
"v_accvgpr_read_b32 v139, a211\n"
"v_accvgpr_read_b32 v140, a216\n"
"v_accvgpr_read_b32 v141, a217\n"
"s_add_u32 m0, m0, 0x4000\n"
"s_mov_b32 s85, 0x23100\n"
"buffer_load_dwordx4 v246, s[76:79], s85 offen offset:0 lds\n"
"v_mfma_f32_16x16x32_bf16 a[252:255], v[236:239], v[188:191], a[252:255]\n"
"v_accvgpr_read_b32 v142, a218\n"
"v_accvgpr_read_b32 v143, a219\n"
"v_cvt_pk_bf16_f32 v136, v136, v137\n"
"v_mfma_f32_16x16x32_bf16 a[248:251], v[236:239], v[184:187], a[248:251]\n"
"v_cvt_pk_bf16_f32 v137, v138, v139\n"
"v_cvt_pk_bf16_f32 v138, v140, v141\n"
"v_cvt_pk_bf16_f32 v139, v142, v143\n"
"v_mfma_f32_16x16x32_bf16 v[124:127], v[236:239], v[180:183], v[124:127]\n"
"ds_write_b128 v254, v[136:139] offset:10240\n"
"s_add_u32 m0, m0, 0xffffc400\n"
"s_mov_b32 s85, 0x2a100\n"
"buffer_load_dwordx4 v246, s[76:79], s85 offen offset:0 lds\n"
"v_mfma_f32_16x16x32_bf16 v[120:123], v[236:239], v[176:179], v[120:123]\n"
"v_accvgpr_read_b32 v144, a212\n"
"v_accvgpr_read_b32 v145, a213\n"
"v_accvgpr_read_b32 v146, a214\n"
"s_nop 7\n"
"v_accvgpr_read_b32 v147, a215\n"
"v_accvgpr_read_b32 v148, a220\n"
"v_accvgpr_read_b32 v149, a221\n"
"v_accvgpr_read_b32 v150, a222\n"
"v_accvgpr_read_b32 v151, a223\n"
"v_cvt_pk_bf16_f32 v144, v144, v145\n"
"v_cvt_pk_bf16_f32 v145, v146, v147\n"
"v_cvt_pk_bf16_f32 v146, v148, v149\n"
"v_cvt_pk_bf16_f32 v147, v150, v151\n"
"ds_write_b128 v254, v[144:147] offset:11264\n"
"v_accvgpr_read_b32 v152, a224\n"
"v_accvgpr_read_b32 v153, a225\n"
"v_accvgpr_read_b32 v154, a226\n"
"v_accvgpr_read_b32 v155, a227\n"
"v_accvgpr_read_b32 v156, a232\n"
"v_accvgpr_read_b32 v157, a233\n"
"v_accvgpr_read_b32 v158, a234\n"
"v_accvgpr_read_b32 v159, a235\n"
"v_cvt_pk_bf16_f32 v152, v152, v153\n"
"v_cvt_pk_bf16_f32 v153, v154, v155\n"
"v_cvt_pk_bf16_f32 v154, v156, v157\n"
"v_cvt_pk_bf16_f32 v155, v158, v159\n"
"ds_write_b128 v254, v[152:155] offset:12288\n"
"v_accvgpr_read_b32 v160, a228\n"
"v_accvgpr_read_b32 v161, a229\n"
"v_accvgpr_read_b32 v162, a230\n"
"v_accvgpr_read_b32 v163, a231\n"
"v_accvgpr_read_b32 v164, a236\n"
"v_accvgpr_read_b32 v165, a237\n"
"v_accvgpr_read_b32 v166, a238\n"
"v_accvgpr_read_b32 v167, a239\n"
"v_cvt_pk_bf16_f32 v160, v160, v161\n"
"v_cvt_pk_bf16_f32 v161, v162, v163\n"
"v_cvt_pk_bf16_f32 v162, v164, v165\n"
"v_cvt_pk_bf16_f32 v163, v166, v167\n"
"ds_write_b128 v254, v[160:163] offset:13312\n"
"v_accvgpr_read_b32 v168, a240\n"
"v_accvgpr_read_b32 v169, a241\n"
"v_accvgpr_read_b32 v170, a242\n"
"v_accvgpr_read_b32 v171, a243\n"
"v_accvgpr_read_b32 v172, a248\n"
"v_accvgpr_read_b32 v173, a249\n"
"v_accvgpr_read_b32 v174, a250\n"
"v_accvgpr_read_b32 v175, a251\n"
"v_cvt_pk_bf16_f32 v168, v168, v169\n"
"v_cvt_pk_bf16_f32 v169, v170, v171\n"
"v_cvt_pk_bf16_f32 v170, v172, v173\n"
"v_cvt_pk_bf16_f32 v171, v174, v175\n"
"ds_write_b128 v254, v[168:171] offset:14336\n"
"v_accvgpr_read_b32 v136, a244\n"
"v_accvgpr_read_b32 v137, a245\n"
"v_accvgpr_read_b32 v138, a246\n"
"v_accvgpr_read_b32 v139, a247\n"
"v_accvgpr_read_b32 v140, a252\n"
"v_accvgpr_read_b32 v141, a253\n"
"v_accvgpr_read_b32 v142, a254\n"
"v_accvgpr_read_b32 v143, a255\n"
"v_cvt_pk_bf16_f32 v136, v136, v137\n"
"v_cvt_pk_bf16_f32 v137, v138, v139\n"
"v_cvt_pk_bf16_f32 v138, v140, v141\n"
"v_cvt_pk_bf16_f32 v139, v142, v143\n"
"ds_write_b128 v254, v[136:139] offset:15360\n"
"s_waitcnt lgkmcnt(0)\n"
"s_barrier\n"
".Ljoin_%=:\n"
"v_xor_b32 v212, 0x1000, v247\n"
"ds_read_b128 v[136:139], v212 offset:19456\n"
"v_xor_b32 v212, 0x2000, v247\n"
"ds_read_b128 v[140:143], v212 offset:19456\n"
"v_xor_b32 v212, 0x3000, v247\n"
"ds_read_b128 v[144:147], v212 offset:19456\n"
"ds_read_b128 v[148:151], v255 offset:0\n"
"ds_read_b128 v[152:155], v255 offset:1024\n"
"ds_read_b128 v[156:159], v255 offset:2048\n"
"ds_read_b128 v[160:163], v255 offset:3072\n"
"ds_read_b128 v[164:167], v255 offset:4096\n"
"ds_read_b128 v[168:171], v255 offset:5120\n"
"ds_read_b128 v[172:175], v255 offset:6144\n"
"ds_read_b128 v[176:179], v255 offset:7168\n"
"ds_read_b128 v[180:183], v255 offset:8192\n"
"ds_read_b128 v[184:187], v255 offset:9216\n"
"ds_read_b128 v[188:191], v255 offset:10240\n"
"ds_read_b128 v[192:195], v255 offset:11264\n"
"ds_read_b128 v[196:199], v255 offset:12288\n"
"ds_read_b128 v[216:219], v255 offset:13312\n"
"ds_read_b128 v[220:223], v255 offset:14336\n"
"ds_read_b128 v[224:227], v255 offset:15360\n"
"s_waitcnt lgkmcnt(15)\n"
"v_mfma_f32_16x16x32_bf16 v[0:3], v[128:131], v[148:151], v[0:3]\n"
"v_mfma_f32_16x16x32_bf16 v[8:11], v[132:135], v[148:151], v[8:11]\n"
"s_waitcnt lgkmcnt(15)\n"
"v_max3_f32 v212, v240, v241, v242\n"
"v_max3_f32 v213, v243, v136, v137\n"
"v_max3_f32 v212, v212, v138, v139\n"
"v_max3_f32 v213, v213, v140, v141\n"
"v_max3_f32 v212, v212, v142, v143\n"
"v_max3_f32 v213, v213, v144, v145\n"
"v_max3_f32 v212, v212, v146, v147\n"
"v_max_f32 v212, v212, v213\n"
"v_cmp_lt_f32 vcc, s54, v212\n"
"s_cbranch_vccnz .Lredo_%=\n"
"v_add_f32 v240, v240, v138\n"
"v_add_f32 v241, v241, v139\n"
"s_waitcnt lgkmcnt(14)\n"
"v_mfma_f32_16x16x32_bf16 v[4:7], v[128:131], v[152:155], v[4:7]\n"
"v_mfma_f32_16x16x32_bf16 v[12:15], v[132:135], v[152:155], v[12:15]\n"
"v_mov_b32 v242, v240\n"
"v_mov_b32 v243, v241\n"
"s_nop 0\n"
"v_permlane32_swap_b32 v240, v242\n"
"v_permlane32_swap_b32 v241, v243\n"
"s_nop 0\n"
"v_add_f32 v240, v240, v242\n"
"v_add_f32 v241, v241, v243\n"
"s_waitcnt lgkmcnt(13)\n"
"v_mfma_f32_16x16x32_bf16 v[16:19], v[128:131], v[156:159], v[16:19]\n"
"v_mfma_f32_16x16x32_bf16 v[24:27], v[132:135], v[156:159], v[24:27]\n"
"v_mov_b32 v242, v240\n"
"v_mov_b32 v243, v241\n"
"s_nop 0\n"
"v_permlane16_swap_b32 v240, v242\n"
"v_permlane16_swap_b32 v241, v243\n"
"s_nop 0\n"
"v_add_f32 v240, v240, v242\n"
"v_add_f32 v241, v241, v243\n"
"s_waitcnt lgkmcnt(12)\n"
"v_mfma_f32_16x16x32_bf16 v[20:23], v[128:131], v[160:163], v[20:23]\n"
"v_mfma_f32_16x16x32_bf16 v[28:31], v[132:135], v[160:163], v[28:31]\n"
"v_rcp_f32 v242, v240\n"
"v_rcp_f32 v243, v241\n"
"s_and_b32 s91, s84, 1\n"
"s_lshl_b32 s91, s91, 14\n"
"s_add_u32 s91, s91, 0x10000\n"
"v_subrev_u32 v232, s91, v248\n"
"v_subrev_u32 v233, s91, v249\n"
"v_subrev_u32 v234, s91, v250\n"
"v_subrev_u32 v235, s91, v251\n"
"v_mov_b32 v239, v252\n"
"s_waitcnt lgkmcnt(11)\n"
"v_mfma_f32_16x16x32_bf16 v[32:35], v[128:131], v[164:167], v[32:35]\n"
"v_mfma_f32_16x16x32_bf16 v[40:43], v[132:135], v[164:167], v[40:43]\n"
"v_mul_f32 v0, v0, v242\n"
"v_mul_f32 v1, v1, v242\n"
"v_mul_f32 v2, v2, v242\n"
"v_mul_f32 v3, v3, v242\n"
"v_mul_f32 v8, v8, v242\n"
"v_mul_f32 v9, v9, v242\n"
"v_mul_f32 v10, v10, v242\n"
"v_mul_f32 v11, v11, v242\n"
"v_cvt_pk_bf16_f32 v0, v0, v1\n"
"v_cvt_pk_bf16_f32 v1, v2, v3\n"
"v_cvt_pk_bf16_f32 v2, v8, v9\n"
"v_cvt_pk_bf16_f32 v3, v10, v11\n"
"s_mov_b32 s86, 0\n"
"buffer_load_dwordx4 a[192:195], v244, s[68:71], s86 offen sc1\n"
"s_waitcnt lgkmcnt(10)\n"
"v_mfma_f32_16x16x32_bf16 v[36:39], v[128:131], v[168:171], v[36:39]\n"
"v_mfma_f32_16x16x32_bf16 v[44:47], v[132:135], v[168:171], v[44:47]\n"
"v_mul_f32 v4, v4, v243\n"
"v_mul_f32 v5, v5, v243\n"
"v_mul_f32 v6, v6, v243\n"
"v_mul_f32 v7, v7, v243\n"
"v_mul_f32 v12, v12, v243\n"
"v_mul_f32 v13, v13, v243\n"
"v_mul_f32 v14, v14, v243\n"
"v_mul_f32 v15, v15, v243\n"
"v_cvt_pk_bf16_f32 v4, v4, v5\n"
"v_cvt_pk_bf16_f32 v5, v6, v7\n"
"v_cvt_pk_bf16_f32 v6, v12, v13\n"
"v_cvt_pk_bf16_f32 v7, v14, v15\n"
"s_mov_b32 s86, 16384\n"
"buffer_load_dwordx4 a[196:199], v244, s[68:71], s86 offen sc1\n"
"s_waitcnt lgkmcnt(9)\n"
"v_mfma_f32_16x16x32_bf16 v[48:51], v[128:131], v[172:175], v[48:51]\n"
"v_mfma_f32_16x16x32_bf16 v[56:59], v[132:135], v[172:175], v[56:59]\n"
"v_mul_f32 v16, v16, v242\n"
"v_mul_f32 v17, v17, v242\n"
"v_mul_f32 v18, v18, v242\n"
"v_mul_f32 v19, v19, v242\n"
"v_mul_f32 v24, v24, v242\n"
"v_mul_f32 v25, v25, v242\n"
"v_mul_f32 v26, v26, v242\n"
"v_mul_f32 v27, v27, v242\n"
"v_cvt_pk_bf16_f32 v16, v16, v17\n"
"v_cvt_pk_bf16_f32 v17, v18, v19\n"
"v_cvt_pk_bf16_f32 v18, v24, v25\n"
"v_cvt_pk_bf16_f32 v19, v26, v27\n"
"s_mov_b32 s86, 32768\n"
"buffer_load_dwordx4 a[200:203], v244, s[68:71], s86 offen sc1\n"
"s_waitcnt lgkmcnt(8)\n"
"v_mfma_f32_16x16x32_bf16 v[52:55], v[128:131], v[176:179], v[52:55]\n"
"v_mfma_f32_16x16x32_bf16 v[60:63], v[132:135], v[176:179], v[60:63]\n"
"v_mul_f32 v20, v20, v243\n"
"v_mul_f32 v21, v21, v243\n"
"v_mul_f32 v22, v22, v243\n"
"v_mul_f32 v23, v23, v243\n"
"v_mul_f32 v28, v28, v243\n"
"v_mul_f32 v29, v29, v243\n"
"v_mul_f32 v30, v30, v243\n"
"v_mul_f32 v31, v31, v243\n"
"v_cvt_pk_bf16_f32 v20, v20, v21\n"
"v_cvt_pk_bf16_f32 v21, v22, v23\n"
"v_cvt_pk_bf16_f32 v22, v28, v29\n"
"v_cvt_pk_bf16_f32 v23, v30, v31\n"
"s_mov_b32 s86, 49152\n"
"buffer_load_dwordx4 a[204:207], v244, s[68:71], s86 offen sc1\n"
"s_waitcnt lgkmcnt(7)\n"
"v_mfma_f32_16x16x32_bf16 v[64:67], v[128:131], v[180:183], v[64:67]\n"
"v_mfma_f32_16x16x32_bf16 v[72:75], v[132:135], v[180:183], v[72:75]\n"
"v_mul_f32 v32, v32, v242\n"
"v_mul_f32 v33, v33, v242\n"
"v_mul_f32 v34, v34, v242\n"
"v_mul_f32 v35, v35, v242\n"
"v_mul_f32 v40, v40, v242\n"
"v_mul_f32 v41, v41, v242\n"
"v_mul_f32 v42, v42, v242\n"
"v_mul_f32 v43, v43, v242\n"
"v_cvt_pk_bf16_f32 v32, v32, v33\n"
"v_cvt_pk_bf16_f32 v33, v34, v35\n"
"v_cvt_pk_bf16_f32 v34, v40, v41\n"
"v_cvt_pk_bf16_f32 v35, v42, v43\n"
"s_mov_b32 s86, 65536\n"
"buffer_load_dwordx4 a[208:211], v244, s[68:71], s86 offen sc1\n"
"s_waitcnt lgkmcnt(6)\n"
"v_mfma_f32_16x16x32_bf16 v[68:71], v[128:131], v[184:187], v[68:71]\n"
"v_mfma_f32_16x16x32_bf16 v[76:79], v[132:135], v[184:187], v[76:79]\n"
"v_mul_f32 v36, v36, v243\n"
"v_mul_f32 v37, v37, v243\n"
"v_mul_f32 v38, v38, v243\n"
"v_mul_f32 v39, v39, v243\n"
"v_mul_f32 v44, v44, v243\n"
"v_mul_f32 v45, v45, v243\n"
"v_mul_f32 v46, v46, v243\n"
"v_mul_f32 v47, v47, v243\n"
"v_cvt_pk_bf16_f32 v36, v36, v37\n"
"v_cvt_pk_bf16_f32 v37, v38, v39\n"
"v_cvt_pk_bf16_f32 v38, v44, v45\n"
"v_cvt_pk_bf16_f32 v39, v46, v47\n"
"s_mov_b32 s86, 81920\n"
"buffer_load_dwordx4 a[212:215], v244, s[68:71], s86 offen sc1\n"
"s_waitcnt lgkmcnt(5)\n"
"v_mfma_f32_16x16x32_bf16 v[80:83], v[128:131], v[188:191], v[80:83]\n"
"v_mfma_f32_16x16x32_bf16 v[88:91], v[132:135], v[188:191], v[88:91]\n"
"v_mul_f32 v48, v48, v242\n"
"v_mul_f32 v49, v49, v242\n"
"v_mul_f32 v50, v50, v242\n"
"v_mul_f32 v51, v51, v242\n"
"v_mul_f32 v56, v56, v242\n"
"v_mul_f32 v57, v57, v242\n"
"v_mul_f32 v58, v58, v242\n"
"v_mul_f32 v59, v59, v242\n"
"v_cvt_pk_bf16_f32 v48, v48, v49\n"
"v_cvt_pk_bf16_f32 v49, v50, v51\n"
"v_cvt_pk_bf16_f32 v50, v56, v57\n"
"v_cvt_pk_bf16_f32 v51, v58, v59\n"
"s_mov_b32 s86, 98304\n"
"buffer_load_dwordx4 a[216:219], v244, s[68:71], s86 offen sc1\n"
"s_waitcnt lgkmcnt(4)\n"
"v_mfma_f32_16x16x32_bf16 v[84:87], v[128:131], v[192:195], v[84:87]\n"
"v_mfma_f32_16x16x32_bf16 v[92:95], v[132:135], v[192:195], v[92:95]\n"
"v_mul_f32 v52, v52, v243\n"
"v_mul_f32 v53, v53, v243\n"
"v_mul_f32 v54, v54, v243\n"
"v_mul_f32 v55, v55, v243\n"
"v_mul_f32 v60, v60, v243\n"
"v_mul_f32 v61, v61, v243\n"
"v_mul_f32 v62, v62, v243\n"
"v_mul_f32 v63, v63, v243\n"
"v_cvt_pk_bf16_f32 v52, v52, v53\n"
"v_cvt_pk_bf16_f32 v53, v54, v55\n"
"v_cvt_pk_bf16_f32 v54, v60, v61\n"
"v_cvt_pk_bf16_f32 v55, v62, v63\n"
"s_waitcnt lgkmcnt(3)\n"
"v_mfma_f32_16x16x32_bf16 v[96:99], v[128:131], v[196:199], v[96:99]\n"
"v_mfma_f32_16x16x32_bf16 v[104:107], v[132:135], v[196:199], v[104:107]\n"
"v_mul_f32 v64, v64, v242\n"
"v_mul_f32 v65, v65, v242\n"
"v_mul_f32 v66, v66, v242\n"
"v_mul_f32 v67, v67, v242\n"
"v_mul_f32 v72, v72, v242\n"
"v_mul_f32 v73, v73, v242\n"
"v_mul_f32 v74, v74, v242\n"
"v_mul_f32 v75, v75, v242\n"
"v_cvt_pk_bf16_f32 v64, v64, v65\n"
"v_cvt_pk_bf16_f32 v65, v66, v67\n"
"v_cvt_pk_bf16_f32 v66, v72, v73\n"
"v_cvt_pk_bf16_f32 v67, v74, v75\n"
"s_waitcnt lgkmcnt(2)\n"
"v_mfma_f32_16x16x32_bf16 v[100:103], v[128:131], v[216:219], v[100:103]\n"
"v_mfma_f32_16x16x32_bf16 v[108:111], v[132:135], v[216:219], v[108:111]\n"
"v_mul_f32 v68, v68, v243\n"
"v_mul_f32 v69, v69, v243\n"
"v_mul_f32 v70, v70, v243\n"
"v_mul_f32 v71, v71, v243\n"
"v_mul_f32 v76, v76, v243\n"
"v_mul_f32 v77, v77, v243\n"
"v_mul_f32 v78, v78, v243\n"
"v_mul_f32 v79, v79, v243\n"
"v_cvt_pk_bf16_f32 v68, v68, v69\n"
"v_cvt_pk_bf16_f32 v69, v70, v71\n"
"v_cvt_pk_bf16_f32 v70, v76, v77\n"
"v_cvt_pk_bf16_f32 v71, v78, v79\n"
"s_waitcnt lgkmcnt(1)\n"
"v_mfma_f32_16x16x32_bf16 v[112:115], v[128:131], v[220:223], v[112:115]\n"
"v_mfma_f32_16x16x32_bf16 v[120:123], v[132:135], v[220:223], v[120:123]\n"
"v_mul_f32 v80, v80, v242\n"
"v_mul_f32 v81, v81, v242\n"
"v_mul_f32 v82, v82, v242\n"
"v_mul_f32 v83, v83, v242\n"
"v_mul_f32 v88, v88, v242\n"
"v_mul_f32 v89, v89, v242\n"
"v_mul_f32 v90, v90, v242\n"
"v_mul_f32 v91, v91, v242\n"
"v_cvt_pk_bf16_f32 v80, v80, v81\n"
"v_cvt_pk_bf16_f32 v81, v82, v83\n"
"v_cvt_pk_bf16_f32 v82, v88, v89\n"
"v_cvt_pk_bf16_f32 v83, v90, v91\n"
"s_waitcnt lgkmcnt(0)\n"
"v_mfma_f32_16x16x32_bf16 v[116:119], v[128:131], v[224:227], v[116:119]\n"
"v_mfma_f32_16x16x32_bf16 v[124:127], v[132:135], v[224:227], v[124:127]\n"
"s_nop 4\n"
"s_nop 0\n"
"s_add_u32 m0, m0, 0x4000\n"
"s_mov_b32 s85, 0x31100\n"
"s_nop 0\n"
"s_waitcnt vmcnt(26)\n"
"s_barrier\n"
"ds_read_b64_tr_b16 a[128:129], v232 offset:0\n"
"v_mul_f32 v84, v84, v243\n"
"v_mul_f32 v85, v85, v243\n"
"s_nop 0\n"
"buffer_load_dwordx4 v246, s[76:79], s85 offen offset:0 lds\n"
"s_add_u32 m0, m0, 0xfffeb400\n"
"s_mov_b32 s85, 0x180\n"
"v_mul_f32 v86, v86, v243\n"
"v_mul_f32 v87, v87, v243\n"
"s_nop 0\n"
"ds_read_b64_tr_b16 a[130:131], v232 offset:8192\n"
"v_mul_f32 v92, v92, v243\n"
"v_mul_f32 v93, v93, v243\n"
"v_mul_f32 v94, v94, v243\n"
"v_mul_f32 v95, v95, v243\n"
"s_nop 0\n"
"ds_read_b64_tr_b16 a[132:133], v233 offset:0\n"
"v_cvt_pk_bf16_f32 v84, v84, v85\n"
"v_cvt_pk_bf16_f32 v85, v86, v87\n"
"v_cvt_pk_bf16_f32 v86, v92, v93\n"
"v_cvt_pk_bf16_f32 v87, v94, v95\n"
"s_nop 0\n"
"ds_read_b64_tr_b16 a[134:135], v233 offset:8192\n"
"v_mul_f32 v96, v96, v242\n"
"v_mul_f32 v97, v97, v242\n"
"v_mul_f32 v98, v98, v242\n"
"v_mul_f32 v99, v99, v242\n"
"s_nop 0\n"
"ds_read_b64_tr_b16 a[136:137], v234 offset:0\n"
"v_mul_f32 v104, v104, v242\n"
"v_mul_f32 v105, v105, v242\n"
"v_mul_f32 v106, v106, v242\n"
"v_mul_f32 v107, v107, v242\n"
"s_nop 0\n"
"ds_read_b64_tr_b16 a[138:139], v234 offset:8192\n"
"buffer_load_dwordx4 v246, s[76:79], s85 offen offset:0 lds\n"
"s_add_u32 m0, m0, 0x4000\n"
"s_mov_b32 s85, 0x7180\n"
"s_nop 0\n"
"ds_read_b64_tr_b16 a[140:141], v235 offset:0\n"
"v_cvt_pk_bf16_f32 v96, v96, v97\n"
"v_cvt_pk_bf16_f32 v97, v98, v99\n"
"v_cvt_pk_bf16_f32 v98, v104, v105\n"
"v_cvt_pk_bf16_f32 v99, v106, v107\n"
"s_nop 0\n"
"ds_read_b64_tr_b16 a[142:143], v235 offset:8192\n"
"v_mul_f32 v100, v100, v243\n"
"v_mul_f32 v101, v101, v243\n"
"v_mul_f32 v102, v102, v243\n"
"v_mul_f32 v103, v103, v243\n"
"s_nop 0\n"
"ds_read_b64_tr_b16 a[144:145], v232 offset:16384\n"
"v_mul_f32 v108, v108, v243\n"
"v_mul_f32 v109, v109, v243\n"
"v_mul_f32 v110, v110, v243\n"
"v_mul_f32 v111, v111, v243\n"
"s_nop 0\n"
"ds_read_b64_tr_b16 a[146:147], v232 offset:24576\n"
"v_cvt_pk_bf16_f32 v100, v100, v101\n"
"v_cvt_pk_bf16_f32 v101, v102, v103\n"
"v_cvt_pk_bf16_f32 v102, v108, v109\n"
"v_cvt_pk_bf16_f32 v103, v110, v111\n"
"s_nop 0\n"
"ds_read_b64_tr_b16 a[148:149], v233 offset:16384\n"
"buffer_load_dwordx4 v246, s[76:79], s85 offen offset:0 lds\n"
"s_add_u32 m0, m0, 0xffffc400\n"
"s_mov_b32 s85, 0xe180\n"
"s_nop 0\n"
"s_waitcnt lgkmcnt(7)\n"
"ds_read_b64_tr_b16 a[150:151], v233 offset:24576\n"
"v_mul_f32 v112, v112, v242\n"
"v_mul_f32 v113, v113, v242\n"
"v_mul_f32 v114, v114, v242\n"
"s_nop 0\n"
"ds_read_b64_tr_b16 a[152:153], v234 offset:16384\n"
"v_mul_f32 v115, v115, v242\n"
"v_mul_f32 v120, v120, v242\n"
"v_mul_f32 v121, v121, v242\n"
"v_mul_f32 v122, v122, v242\n"
"s_nop 0\n"
"ds_read_b64_tr_b16 a[154:155], v234 offset:24576\n"
"v_mul_f32 v123, v123, v242\n"
"v_cvt_pk_bf16_f32 v112, v112, v113\n"
"v_cvt_pk_bf16_f32 v113, v114, v115\n"
"v_cvt_pk_bf16_f32 v114, v120, v121\n"
"v_mfma_f32_16x16x32_bf16 v[8:11], a[128:131], v[0:3], 0\n"
"ds_read_b64_tr_b16 a[156:157], v235 offset:16384\n"
"v_mfma_f32_16x16x32_bf16 v[12:15], a[128:131], v[4:7], 0\n"
"ds_read_b64_tr_b16 a[158:159], v235 offset:24576\n"
"buffer_load_dwordx4 v246, s[76:79], s85 offen offset:0 lds\n"
"v_mfma_f32_16x16x32_bf16 v[28:31], a[132:135], v[4:7], 0\n"
"s_add_u32 m0, m0, 0x4000\n"
"ds_read_b64_tr_b16 a[160:161], v232 offset:1024\n"
"v_mfma_f32_16x16x32_bf16 v[24:27], a[132:135], v[0:3], 0\n"
"s_waitcnt lgkmcnt(9)\n"
"s_mov_b32 s85, 0x15180\n"
"ds_read_b64_tr_b16 a[162:163], v232 offset:9216\n"
"v_mfma_f32_16x16x32_bf16 v[40:43], a[136:139], v[0:3], 0\n"
"ds_read_b64_tr_b16 a[164:165], v233 offset:1024\n"
"v_cvt_pk_bf16_f32 v115, v122, v123\n"
"v_mfma_f32_16x16x32_bf16 v[44:47], a[136:139], v[4:7], 0\n"
"s_waitcnt lgkmcnt(7)\n"
"ds_read_b64_tr_b16 a[166:167], v233 offset:9216\n"
"v_mfma_f32_16x16x32_bf16 v[60:63], a[140:143], v[4:7], 0\n"
"ds_read_b64_tr_b16 a[168:169], v234 offset:1024\n"
"buffer_load_dwordx4 v246, s[76:79], s85 offen offset:0 lds\n"
"v_mfma_f32_16x16x32_bf16 v[56:59], a[140:143], v[0:3], 0\n"
"s_add_u32 m0, m0, 0xffffc400\n"
"ds_read_b64_tr_b16 a[170:171], v234 offset:9216\n"
"v_mfma_f32_16x16x32_bf16 v[8:11], a[144:147], v[16:19], v[8:11]\n"
"s_mov_b32 s85, 0x1c180\n"
"ds_read_b64_tr_b16 a[172:173], v235 offset:1024\n"
"v_mfma_f32_16x16x32_bf16 v[12:15], a[144:147], v[20:23], v[12:15]\n"
"s_waitcnt lgkmcnt(7)\n"
"ds_read_b64_tr_b16 a[174:175], v235 offset:9216\n"
"v_mfma_f32_16x16x32_bf16 v[28:31], a[148:151], v[20:23], v[28:31]\n"
"ds_read_b64_tr_b16 a[176:177], v232 offset:17408\n"
"v_mul_f32 v116, v116, v243\n"
"v_mfma_f32_16x16x32_bf16 v[24:27], a[148:151], v[16:19], v[24:27]\n"
"ds_read_b64_tr_b16 a[178:179], v232 offset:25600\n"
"buffer_load_dwordx4 v246, s[76:79], s85 offen offset:0 lds\n"
"v_mfma_f32_16x16x32_bf16 v[40:43], a[152:155], v[16:19], v[40:43]\n"
"s_add_u32 m0, m0, 0x4000\n"
"ds_read_b64_tr_b16 a[180:181], v233 offset:17408\n"
"v_mfma_f32_16x16x32_bf16 v[44:47], a[152:155], v[20:23], v[44:47]\n"
"s_mov_b32 s85, 0x23180\n"
"ds_read_b64_tr_b16 a[182:183], v233 offset:25600\n"
"v_mfma_f32_16x16x32_bf16 v[60:63], a[156:159], v[20:23], v[60:63]\n"
"s_waitcnt lgkmcnt(8)\n"
"ds_read_b64_tr_b16 a[184:185], v234 offset:17408\n"
"v_mfma_f32_16x16x32_bf16 v[56:59], a[156:159], v[16:19], v[56:59]\n"
"ds_read_b64_tr_b16 a[186:187], v234 offset:25600\n"
"v_mul_f32 v117, v117, v243\n"
"v_mfma_f32_16x16x32_bf16 v[8:11], a[160:163], v[32:35], v[8:11]\n"
"ds_read_b64_tr_b16 a[188:189], v235 offset:17408\n"
"buffer_load_dwordx4 v246, s[76:79], s85 offen offset:0 lds\n"
"v_mfma_f32_16x16x32_bf16 v[12:15], a[160:163], v[36:39], v[12:15]\n"
"s_add_u32 m0, m0, 0xffffc400\n"
"ds_read_b64_tr_b16 a[190:191], v235 offset:25600\n"
"v_mfma_f32_16x16x32_bf16 v[28:31], a[164:167], v[36:39], v[28:31]\n"
"s_mov_b32 s85, 0x2a180\n"
"ds_read_b64_tr_b16 a[128:129], v232 offset:2048\n"
"v_mfma_f32_16x16x32_bf16 v[24:27], a[164:167], v[32:35], v[24:27]\n"
"s_waitcnt lgkmcnt(9)\n"
"ds_read_b64_tr_b16 a[130:131], v232 offset:10240\n"
"v_mfma_f32_16x16x32_bf16 v[40:43], a[168:171], v[32:35], v[40:43]\n"
"ds_read_b64_tr_b16 a[132:133], v233 offset:2048\n"
"v_mul_f32 v118, v118, v243\n"
"v_mfma_f32_16x16x32_bf16 v[44:47], a[168:171], v[36:39], v[44:47]\n"
"ds_read_b64_tr_b16 a[134:135], v233 offset:10240\n"
"buffer_load_dwordx4 v246, s[76:79], s85 offen offset:0 lds\n"
"v_mfma_f32_16x16x32_bf16 v[60:63], a[172:175], v[36:39], v[60:63]\n"
"s_add_u32 m0, m0, 0x4000\n"
"ds_read_b64_tr_b16 a[136:137], v234 offset:2048\n"
"v_mfma_f32_16x16x32_bf16 v[56:59], a[172:175], v[32:35], v[56:59]\n"
"s_waitcnt lgkmcnt(9)\n"
"s_mov_b32 s85, 0x31180\n"
"ds_read_b64_tr_b16 a[138:139], v234 offset:10240\n"
"v_mfma_f32_16x16x32_bf16 v[8:11], a[176:179], v[48:51], v[8:11]\n"
"ds_read_b64_tr_b16 a[140:141], v235 offset:2048\n"
"v_mul_f32 v119, v119, v243\n"
"v_mfma_f32_16x16x32_bf16 v[12:15], a[176:179], v[52:55], v[12:15]\n"
"ds_read_b64_tr_b16 a[142:143], v235 offset:10240\n"
"v_mul_f32 v124, v124, v243\n"
"v_mfma_f32_16x16x32_bf16 v[28:31], a[180:183], v[52:55], v[28:31]\n"
"ds_read_b64_tr_b16 a[144:145], v232 offset:18432\n"
"buffer_load_dwordx4 v246, s[76:79], s85 offen offset:0 lds\n"
"v_mfma_f32_16x16x32_bf16 v[24:27], a[180:183], v[48:51], v[24:27]\n"
"s_waitcnt lgkmcnt(9)\n"
"ds_read_b64_tr_b16 a[146:147], v232 offset:26624\n"
"v_mfma_f32_16x16x32_bf16 v[40:43], a[184:187], v[48:51], v[40:43]\n"
"s_add_u32 m0, m0, 0x3400\n"
"ds_read_b64_tr_b16 a[148:149], v233 offset:18432\n"
"v_mfma_f32_16x16x32_bf16 v[44:47], a[184:187], v[52:55], v[44:47]\n"
"s_mov_b32 s85, 0x200\n"
"ds_read_b64_tr_b16 a[150:151], v233 offset:26624\n"
"v_mul_f32 v125, v125, v243\n"
"v_mfma_f32_16x16x32_bf16 v[60:63], a[188:191], v[52:55], v[60:63]\n"
"ds_read_b64_tr_b16 a[152:153], v234 offset:18432\n"
"v_mul_f32 v126, v126, v243\n"
"v_mul_f32 v127, v127, v243\n"
"v_mfma_f32_16x16x32_bf16 v[56:59], a[188:191], v[48:51], v[56:59]\n"
"s_waitcnt lgkmcnt(9)\n"
"ds_read_b64_tr_b16 a[154:155], v234 offset:26624\n"
"buffer_load_dwordx4 v246, s[76:79], s85 offen offset:0 lds\n"
"v_mfma_f32_16x16x32_bf16 v[8:11], a[128:131], v[64:67], v[8:11]\n"
"s_add_u32 m0, m0, 0x4000\n"
"ds_read_b64_tr_b16 a[156:157], v235 offset:18432\n"
"v_mfma_f32_16x16x32_bf16 v[12:15], a[128:131], v[68:71], v[12:15]\n"
"s_mov_b32 s85, 0x7200\n"
"ds_read_b64_tr_b16 a[158:159], v235 offset:26624\n"
"v_cvt_pk_bf16_f32 v116, v116, v117\n"
"v_mfma_f32_16x16x32_bf16 v[28:31], a[132:135], v[68:71], v[28:31]\n"
"ds_read_b64_tr_b16 a[160:161], v232 offset:3072\n"
"v_cvt_pk_bf16_f32 v117, v118, v119\n"
"v_mfma_f32_16x16x32_bf16 v[24:27], a[132:135], v[64:67], v[24:27]\n"
"s_waitcnt lgkmcnt(9)\n"
"ds_read_b64_tr_b16 a[162:163], v232 offset:11264\n"
"v_cvt_pk_bf16_f32 v118, v124, v125\n"
"v_mfma_f32_16x16x32_bf16 v[40:43], a[136:139], v[64:67], v[40:43]\n"
"ds_read_b64_tr_b16 a[164:165], v233 offset:3072\n"
"buffer_load_dwordx4 v246, s[76:79], s85 offen offset:0 lds\n"
"v_cvt_pk_bf16_f32 v119, v126, v127\n"
"v_mfma_f32_16x16x32_bf16 v[44:47], a[136:139], v[68:71], v[44:47]\n"
"s_add_u32 m0, m0, 0xffffc400\n"
"ds_read_b64_tr_b16 a[166:167], v233 offset:11264\n"
"v_mfma_f32_16x16x32_bf16 v[60:63], a[140:143], v[68:71], v[60:63]\n"
"s_mov_b32 s85, 0xe200\n"
"ds_read_b64_tr_b16 a[168:169], v234 offset:3072\n"
"v_mfma_f32_16x16x32_bf16 v[56:59], a[140:143], v[64:67], v[56:59]\n"
"s_waitcnt lgkmcnt(9)\n"
"ds_read_b64_tr_b16 a[170:171], v234 offset:11264\n"
"v_mfma_f32_16x16x32_bf16 v[8:11], a[144:147], v[80:83], v[8:11]\n"
"ds_read_b64_tr_b16 a[172:173], v235 offset:3072\n"
"v_mfma_f32_16x16x32_bf16 v[12:15], a[144:147], v[84:87], v[12:15]\n"
"ds_read_b64_tr_b16 a[174:175], v235 offset:11264\n"
"buffer_load_dwordx4 v246, s[76:79], s85 offen offset:0 lds\n"
"v_mfma_f32_16x16x32_bf16 v[28:31], a[148:151], v[84:87], v[28:31]\n"
"s_add_u32 m0, m0, 0x4000\n"
"ds_read_b64_tr_b16 a[176:177], v232 offset:19456\n"
"v_mfma_f32_16x16x32_bf16 v[24:27], a[148:151], v[80:83], v[24:27]\n"
"s_waitcnt lgkmcnt(9)\n"
"s_mov_b32 s85, 0x15200\n"
"ds_read_b64_tr_b16 a[178:179], v232 offset:27648\n"
"v_mfma_f32_16x16x32_bf16 v[40:43], a[152:155], v[80:83], v[40:43]\n"
"ds_read_b64_tr_b16 a[180:181], v233 offset:19456\n"
"v_add_u32 v232, 0x20000, v232\n"
"v_mfma_f32_16x16x32_bf16 v[44:47], a[152:155], v[84:87], v[44:47]\n"
"s_waitcnt lgkmcnt(7)\n"
"ds_read_b64_tr_b16 a[182:183], v233 offset:27648\n"
"v_mfma_f32_16x16x32_bf16 v[60:63], a[156:159], v[84:87], v[60:63]\n"
"ds_read_b64_tr_b16 a[184:185], v234 offset:19456\n"
"buffer_load_dwordx4 v246, s[76:79], s85 offen offset:0 lds\n"
"v_mfma_f32_16x16x32_bf16 v[56:59], a[156:159], v[80:83], v[56:59]\n"
"s_add_u32 m0, m0, 0xffffc400\n"
"ds_read_b64_tr_b16 a[186:187], v234 offset:27648\n"
"v_mfma_f32_16x16x32_bf16 v[8:11], a[160:163], v[96:99], v[8:11]\n"
"s_mov_b32 s85, 0x1c200\n"
"ds_read_b64_tr_b16 a[188:189], v235 offset:19456\n"
"v_mfma_f32_16x16x32_bf16 v[12:15], a[160:163], v[100:103], v[12:15]\n"
"ds_read_b64_tr_b16 a[190:191], v235 offset:27648\n"
"s_waitcnt vmcnt(30)\n"
"s_barrier\n"
"v_mfma_f32_16x16x32_bf16 v[28:31], a[164:167], v[100:103], v[28:31]\n"
"s_waitcnt lgkmcnt(8)\n"
"ds_read_b64_tr_b16 a[128:129], v232 offset:0\n"
"v_mfma_f32_16x16x32_bf16 v[24:27], a[164:167], v[96:99], v[24:27]\n"
"ds_read_b64_tr_b16 a[130:131], v232 offset:8192\n"
"buffer_load_dwordx4 v246, s[76:79], s85 offen offset:0 lds\n"
"v_add_u32 v233, 0x20000, v233\n"
"v_mfma_f32_16x16x32_bf16 v[40:43], a[168:171], v[96:99], v[40:43]\n"
"s_add_u32 m0, m0, 0x4000\n"
"ds_read_b64_tr_b16 a[132:133], v233 offset:0\n"
"v_mfma_f32_16x16x32_bf16 v[44:47], a[168:171], v[100:103], v[44:47]\n"
"s_mov_b32 s85, 0x23200\n"
"ds_read_b64_tr_b16 a[134:135], v233 offset:8192\n"
"v_add_u32 v234, 0x20000, v234\n"
"v_mfma_f32_16x16x32_bf16 v[60:63], a[172:175], v[100:103], v[60:63]\n"
"ds_read_b64_tr_b16 a[136:137], v234 offset:0\n"
"v_add_u32 v235, 0x20000, v235\n"
"v_mfma_f32_16x16x32_bf16 v[56:59], a[172:175], v[96:99], v[56:59]\n"
"s_waitcnt lgkmcnt(9)\n"
"ds_read_b64_tr_b16 a[138:139], v234 offset:8192\n"
"v_mfma_f32_16x16x32_bf16 v[8:11], a[176:179], v[112:115], v[8:11]\n"
"ds_read_b64_tr_b16 a[140:141], v235 offset:0\n"
"buffer_load_dwordx4 v246, s[76:79], s85 offen offset:0 lds\n"
"v_mfma_f32_16x16x32_bf16 v[12:15], a[176:179], v[116:119], v[12:15]\n"
"s_add_u32 m0, m0, 0xffffc400\n"
"ds_read_b64_tr_b16 a[142:143], v235 offset:8192\n"
"v_mfma_f32_16x16x32_bf16 v[28:31], a[180:183], v[116:119], v[28:31]\n"
"s_mov_b32 s85, 0x2a200\n"
"ds_read_b64_tr_b16 a[144:145], v232 offset:16384\n"
"v_mfma_f32_16x16x32_bf16 v[24:27], a[180:183], v[112:115], v[24:27]\n"
"s_waitcnt lgkmcnt(9)\n"
"ds_read_b64_tr_b16 a[146:147], v232 offset:24576\n"
"v_mfma_f32_16x16x32_bf16 v[40:43], a[184:187], v[112:115], v[40:43]\n"
"ds_read_b64_tr_b16 a[148:149], v233 offset:16384\n"
"v_mfma_f32_16x16x32_bf16 v[44:47], a[184:187], v[116:119], v[44:47]\n"
"ds_read_b64_tr_b16 a[150:151], v233 offset:24576\n"
"buffer_load_dwordx4 v246, s[76:79], s85 offen offset:0 lds\n"
"v_mfma_f32_16x16x32_bf16 v[60:63], a[188:191], v[116:119], v[60:63]\n"
"s_add_u32 m0, m0, 0x4000\n"
"ds_read_b64_tr_b16 a[152:153], v234 offset:16384\n"
"v_mfma_f32_16x16x32_bf16 v[56:59], a[188:191], v[112:115], v[56:59]\n"
"s_waitcnt lgkmcnt(9)\n"
"s_mov_b32 s85, 0x31200\n"
"ds_read_b64_tr_b16 a[154:155], v234 offset:24576\n"
"v_mfma_f32_16x16x32_bf16 v[72:75], a[128:131], v[0:3], 0\n"
"ds_read_b64_tr_b16 a[156:157], v235 offset:16384\n"
"v_mfma_f32_16x16x32_bf16 v[76:79], a[128:131], v[4:7], 0\n"
"s_waitcnt lgkmcnt(7)\n"
"ds_read_b64_tr_b16 a[158:159], v235 offset:24576\n"
"v_mfma_f32_16x16x32_bf16 v[92:95], a[132:135], v[4:7], 0\n"
"ds_read_b64_tr_b16 a[160:161], v232 offset:1024\n"
"buffer_load_dwordx4 v246, s[76:79], s85 offen offset:0 lds\n"
"v_mfma_f32_16x16x32_bf16 v[88:91], a[132:135], v[0:3], 0\n"
"s_add_u32 m0, m0, 0xfffeb400\n"
"ds_read_b64_tr_b16 a[162:163], v232 offset:9216\n"
"v_mfma_f32_16x16x32_bf16 v[104:107], a[136:139], v[0:3], 0\n"
"s_mov_b32 s85, 0x280\n"
"ds_read_b64_tr_b16 a[164:165], v233 offset:1024\n"
"v_mfma_f32_16x16x32_bf16 v[108:111], a[136:139], v[4:7], 0\n"
"ds_read_b64_tr_b16 a[166:167], v233 offset:9216\n"
"v_cvt_pk_bf16_f32 v8, v8, v9\n"
"v_mfma_f32_16x16x32_bf16 v[124:127], a[140:143], v[4:7], 0\n"
"s_waitcnt lgkmcnt(8)\n"
"ds_read_b64_tr_b16 a[168:169], v234 offset:1024\n"
"v_mfma_f32_16x16x32_bf16 v[120:123], a[140:143], v[0:3], 0\n"
"ds_read_b64_tr_b16 a[170:171], v234 offset:9216\n"
"v_cvt_pk_bf16_f32 v9, v10, v11\n"
"v_mfma_f32_16x16x32_bf16 v[72:75], a[144:147], v[16:19], v[72:75]\n"
"ds_read_b64_tr_b16 a[172:173], v235 offset:1024\n"
"v_cvt_pk_bf16_f32 v10, v12, v13\n"
"v_mfma_f32_16x16x32_bf16 v[76:79], a[144:147], v[20:23], v[76:79]\n"
"ds_read_b64_tr_b16 a[174:175], v235 offset:9216\n"
"v_cvt_pk_bf16_f32 v11, v14, v15\n"
"v_mfma_f32_16x16x32_bf16 v[92:95], a[148:151], v[20:23], v[92:95]\n"
"ds_read_b64_tr_b16 a[176:177], v232 offset:17408\n"
"s_mov_b32 s86, 0x0\n"
"buffer_store_dwordx4 v[8:11], v239, s[80:83], s86 offen sc1\n"
"v_mfma_f32_16x16x32_bf16 v[88:91], a[148:151], v[16:19], v[88:91]\n"
"s_waitcnt lgkmcnt(9)\n"
"ds_read_b64_tr_b16 a[178:179], v232 offset:25600\n"
"v_mfma_f32_16x16x32_bf16 v[104:107], a[152:155], v[16:19], v[104:107]\n"
"ds_read_b64_tr_b16 a[180:181], v233 offset:17408\n"
"v_cvt_pk_bf16_f32 v24, v24, v25\n"
"v_mfma_f32_16x16x32_bf16 v[108:111], a[152:155], v[20:23], v[108:111]\n"
"s_waitcnt lgkmcnt(7)\n"
"ds_read_b64_tr_b16 a[182:183], v233 offset:25600\n"
"v_mfma_f32_16x16x32_bf16 v[124:127], a[156:159], v[20:23], v[124:127]\n"
"ds_read_b64_tr_b16 a[184:185], v234 offset:17408\n"
"v_cvt_pk_bf16_f32 v25, v26, v27\n"
"v_mfma_f32_16x16x32_bf16 v[120:123], a[156:159], v[16:19], v[120:123]\n"
"ds_read_b64_tr_b16 a[186:187], v234 offset:25600\n"
"v_cvt_pk_bf16_f32 v26, v28, v29\n"
"v_mfma_f32_16x16x32_bf16 v[72:75], a[160:163], v[32:35], v[72:75]\n"
"ds_read_b64_tr_b16 a[188:189], v235 offset:17408\n"
"v_cvt_pk_bf16_f32 v27, v30, v31\n"
"v_mfma_f32_16x16x32_bf16 v[76:79], a[160:163], v[36:39], v[76:79]\n"
"s_waitcnt lgkmcnt(7)\n"
"ds_read_b64_tr_b16 a[190:191], v235 offset:25600\n"
"v_mfma_f32_16x16x32_bf16 v[92:95], a[164:167], v[36:39], v[92:95]\n"
"ds_read_b64_tr_b16 a[128:129], v232 offset:2048\n"
"v_cvt_pk_bf16_f32 v40, v40, v41\n"
"v_mfma_f32_16x16x32_bf16 v[88:91], a[164:167], v[32:35], v[88:91]\n"
"ds_read_b64_tr_b16 a[130:131], v232 offset:10240\n"
"v_cvt_pk_bf16_f32 v41, v42, v43\n"
"v_mfma_f32_16x16x32_bf16 v[104:107], a[168:171], v[32:35], v[104:107]\n"
"ds_read_b64_tr_b16 a[132:133], v233 offset:2048\n"
"s_mov_b32 s86, 0x4000\n"
"buffer_store_dwordx4 v[24:27], v239, s[80:83], s86 offen sc1\n"
"v_mfma_f32_16x16x32_bf16 v[108:111], a[168:171], v[36:39], v[108:111]\n"
"s_waitcnt lgkmcnt(7)\n"
"ds_read_b64_tr_b16 a[134:135], v233 offset:10240\n"
"v_mfma_f32_16x16x32_bf16 v[124:127], a[172:175], v[36:39], v[124:127]\n"
"ds_read_b64_tr_b16 a[136:137], v234 offset:2048\n"
"v_cvt_pk_bf16_f32 v42, v44, v45\n"
"v_mfma_f32_16x16x32_bf16 v[120:123], a[172:175], v[32:35], v[120:123]\n"
"ds_read_b64_tr_b16 a[138:139], v234 offset:10240\n"
"v_cvt_pk_bf16_f32 v43, v46, v47\n"
"v_mfma_f32_16x16x32_bf16 v[72:75], a[176:179], v[48:51], v[72:75]\n"
"ds_read_b64_tr_b16 a[140:141], v235 offset:2048\n"
"v_cvt_pk_bf16_f32 v56, v56, v57\n"
"v_mfma_f32_16x16x32_bf16 v[76:79], a[176:179], v[52:55], v[76:79]\n"
"s_waitcnt lgkmcnt(7)\n"
"ds_read_b64_tr_b16 a[142:143], v235 offset:10240\n"
"v_mfma_f32_16x16x32_bf16 v[92:95], a[180:183], v[52:55], v[92:95]\n"
"ds_read_b64_tr_b16 a[144:145], v232 offset:18432\n"
"v_cvt_pk_bf16_f32 v57, v58, v59\n"
"v_mfma_f32_16x16x32_bf16 v[88:91], a[180:183], v[48:51], v[88:91]\n"
"ds_read_b64_tr_b16 a[146:147], v232 offset:26624\n"
"v_cvt_pk_bf16_f32 v58, v60, v61\n"
"v_mfma_f32_16x16x32_bf16 v[104:107], a[184:187], v[48:51], v[104:107]\n"
"ds_read_b64_tr_b16 a[148:149], v233 offset:18432\n"
"v_cvt_pk_bf16_f32 v59, v62, v63\n"
"v_mfma_f32_16x16x32_bf16 v[108:111], a[184:187], v[52:55], v[108:111]\n"
"s_waitcnt lgkmcnt(7)\n"
"ds_read_b64_tr_b16 a[150:151], v233 offset:26624\n"
"v_mfma_f32_16x16x32_bf16 v[124:127], a[188:191], v[52:55], v[124:127]\n"
"ds_read_b64_tr_b16 a[152:153], v234 offset:18432\n"
"v_mfma_f32_16x16x32_bf16 v[120:123], a[188:191], v[48:51], v[120:123]\n"
"ds_read_b64_tr_b16 a[154:155], v234 offset:26624\n"
"v_mfma_f32_16x16x32_bf16 v[72:75], a[128:131], v[64:67], v[72:75]\n"
"ds_read_b64_tr_b16 a[156:157], v235 offset:18432\n"
"s_mov_b32 s86, 0x8000\n"
"buffer_store_dwordx4 v[40:43], v239, s[80:83], s86 offen sc1\n"
"v_mfma_f32_16x16x32_bf16 v[76:79], a[128:131], v[68:71], v[76:79]\n"
"s_waitcnt lgkmcnt(7)\n"
"ds_read_b64_tr_b16 a[158:159], v235 offset:26624\n"
"v_mfma_f32_16x16x32_bf16 v[92:95], a[132:135], v[68:71], v[92:95]\n"
"ds_read_b64_tr_b16 a[160:161], v232 offset:3072\n"
"v_mfma_f32_16x16x32_bf16 v[88:91], a[132:135], v[64:67], v[88:91]\n"
"ds_read_b64_tr_b16 a[162:163], v232 offset:11264\n"
"v_mfma_f32_16x16x32_bf16 v[104:107], a[136:139], v[64:67], v[104:107]\n"
"ds_read_b64_tr_b16 a[164:165], v233 offset:3072\n"
"v_mfma_f32_16x16x32_bf16 v[108:111], a[136:139], v[68:71], v[108:111]\n"
"s_waitcnt lgkmcnt(7)\n"
"ds_read_b64_tr_b16 a[166:167], v233 offset:11264\n"
"v_mfma_f32_16x16x32_bf16 v[124:127], a[140:143], v[68:71], v[124:127]\n"
"ds_read_b64_tr_b16 a[168:169], v234 offset:3072\n"
"v_mfma_f32_16x16x32_bf16 v[120:123], a[140:143], v[64:67], v[120:123]\n"
"ds_read_b64_tr_b16 a[170:171], v234 offset:11264\n"
"v_mfma_f32_16x16x32_bf16 v[72:75], a[144:147], v[80:83], v[72:75]\n"
"ds_read_b64_tr_b16 a[172:173], v235 offset:3072\n"
"v_mfma_f32_16x16x32_bf16 v[76:79], a[144:147], v[84:87], v[76:79]\n"
"s_waitcnt lgkmcnt(7)\n"
"ds_read_b64_tr_b16 a[174:175], v235 offset:11264\n"
"v_mfma_f32_16x16x32_bf16 v[92:95], a[148:151], v[84:87], v[92:95]\n"
"ds_read_b64_tr_b16 a[176:177], v232 offset:19456\n"
"v_mfma_f32_16x16x32_bf16 v[88:91], a[148:151], v[80:83], v[88:91]\n"
"ds_read_b64_tr_b16 a[178:179], v232 offset:27648\n"
"v_add_u32 v232, 0xffff0000, v232\n"
"v_mfma_f32_16x16x32_bf16 v[104:107], a[152:155], v[80:83], v[104:107]\n"
"ds_read_b64_tr_b16 a[180:181], v233 offset:19456\n"
"s_mov_b32 s86, 0xc000\n"
"buffer_store_dwordx4 v[56:59], v239, s[80:83], s86 offen sc1\n"
"v_mfma_f32_16x16x32_bf16 v[108:111], a[152:155], v[84:87], v[108:111]\n"
"s_waitcnt lgkmcnt(7)\n"
"ds_read_b64_tr_b16 a[182:183], v233 offset:27648\n"
"v_mfma_f32_16x16x32_bf16 v[124:127], a[156:159], v[84:87], v[124:127]\n"
"ds_read_b64_tr_b16 a[184:185], v234 offset:19456\n"
"v_add_u32 v233, 0xffff0000, v233\n"
"v_mfma_f32_16x16x32_bf16 v[120:123], a[156:159], v[80:83], v[120:123]\n"
"ds_read_b64_tr_b16 a[186:187], v234 offset:27648\n"
"v_add_u32 v234, 0xffff0000, v234\n"
"v_mfma_f32_16x16x32_bf16 v[72:75], a[160:163], v[96:99], v[72:75]\n"
"ds_read_b64_tr_b16 a[188:189], v235 offset:19456\n"
"v_mfma_f32_16x16x32_bf16 v[76:79], a[160:163], v[100:103], v[76:79]\n"
"ds_read_b64_tr_b16 a[190:191], v235 offset:27648\n"
"s_waitcnt vmcnt(20)\n"
"s_barrier\n"
"v_mfma_f32_16x16x32_bf16 v[92:95], a[164:167], v[100:103], v[92:95]\n"
"ds_read_b64_tr_b16 a[128:129], v232 offset:32768\n"
"buffer_load_dwordx4 v246, s[76:79], s85 offen offset:0 lds\n"
"v_mfma_f32_16x16x32_bf16 v[88:91], a[164:167], v[96:99], v[88:91]\n"
"s_waitcnt lgkmcnt(9)\n"
"ds_read_b64_tr_b16 a[130:131], v232 offset:40960\n"
"v_mfma_f32_16x16x32_bf16 v[104:107], a[168:171], v[96:99], v[104:107]\n"
"s_add_u32 m0, m0, 0x4000\n"
"ds_read_b64_tr_b16 a[132:133], v233 offset:32768\n"
"v_mfma_f32_16x16x32_bf16 v[108:111], a[168:171], v[100:103], v[108:111]\n"
"s_mov_b32 s85, 0x7280\n"
"ds_read_b64_tr_b16 a[134:135], v233 offset:40960\n"
"v_mfma_f32_16x16x32_bf16 v[124:127], a[172:175], v[100:103], v[124:127]\n"
"ds_read_b64_tr_b16 a[136:137], v234 offset:32768\n"
"v_add_u32 v235, 0xffff0000, v235\n"
"v_mfma_f32_16x16x32_bf16 v[120:123], a[172:175], v[96:99], v[120:123]\n"
"s_waitcnt lgkmcnt(9)\n"
"ds_read_b64_tr_b16 a[138:139], v234 offset:40960\n"
"buffer_load_dwordx4 v246, s[76:79], s85 offen offset:0 lds\n"
"v_mfma_f32_16x16x32_bf16 v[72:75], a[176:179], v[112:115], v[72:75]\n"
"s_add_u32 m0, m0, 0xffffc400\n"
"ds_read_b64_tr_b16 a[140:141], v235 offset:32768\n"
"v_mfma_f32_16x16x32_bf16 v[76:79], a[176:179], v[116:119], v[76:79]\n"
"s_mov_b32 s85, 0xe280\n"
"ds_read_b64_tr_b16 a[142:143], v235 offset:40960\n"
"v_mfma_f32_16x16x32_bf16 v[92:95], a[180:183], v[116:119], v[92:95]\n"
"s_waitcnt lgkmcnt(8)\n"
"ds_read_b64_tr_b16 a[144:145], v232 offset:49152\n"
"v_mfma_f32_16x16x32_bf16 v[88:91], a[180:183], v[112:115], v[88:91]\n"
"ds_read_b64_tr_b16 a[146:147], v232 offset:57344\n"
"v_mfma_f32_16x16x32_bf16 v[104:107], a[184:187], v[112:115], v[104:107]\n"
"ds_read_b64_tr_b16 a[148:149], v233 offset:49152\n"
"buffer_load_dwordx4 v246, s[76:79], s85 offen offset:0 lds\n"
"v_mfma_f32_16x16x32_bf16 v[108:111], a[184:187], v[116:119], v[108:111]\n"
"s_add_u32 m0, m0, 0x4000\n"
"ds_read_b64_tr_b16 a[150:151], v233 offset:57344\n"
"v_mfma_f32_16x16x32_bf16 v[124:127], a[188:191], v[116:119], v[124:127]\n"
"s_mov_b32 s85, 0x15280\n"
"ds_read_b64_tr_b16 a[152:153], v234 offset:49152\n"
"v_mfma_f32_16x16x32_bf16 v[120:123], a[188:191], v[112:115], v[120:123]\n"
"s_waitcnt lgkmcnt(9)\n"
"ds_read_b64_tr_b16 a[154:155], v234 offset:57344\n"
"v_mfma_f32_16x16x32_bf16 v[8:11], a[128:131], v[0:3], 0\n"
"ds_read_b64_tr_b16 a[156:157], v235 offset:49152\n"
"v_mfma_f32_16x16x32_bf16 v[12:15], a[128:131], v[4:7], 0\n"
"ds_read_b64_tr_b16 a[158:159], v235 offset:57344\n"
"buffer_load_dwordx4 v246, s[76:79], s85 offen offset:0 lds\n"
"v_mfma_f32_16x16x32_bf16 v[28:31], a[132:135], v[4:7], 0\n"
"s_add_u32 m0, m0, 0xffffc400\n"
"ds_read_b64_tr_b16 a[160:161], v232 offset:33792\n"
"v_mfma_f32_16x16x32_bf16 v[24:27], a[132:135], v[0:3], 0\n"
"s_waitcnt lgkmcnt(9)\n"
"s_mov_b32 s85, 0x1c280\n"
"ds_read_b64_tr_b16 a[162:163], v232 offset:41984\n"
"v_mfma_f32_16x16x32_bf16 v[40:43], a[136:139], v[0:3], 0\n"
"ds_read_b64_tr_b16 a[164:165], v233 offset:33792\n"
"v_cvt_pk_bf16_f32 v72, v72, v73\n"
"v_mfma_f32_16x16x32_bf16 v[44:47], a[136:139], v[4:7], 0\n"
"s_waitcnt lgkmcnt(7)\n"
"ds_read_b64_tr_b16 a[166:167], v233 offset:41984\n"
"v_mfma_f32_16x16x32_bf16 v[60:63], a[140:143], v[4:7], 0\n"
"ds_read_b64_tr_b16 a[168:169], v234 offset:33792\n"
"buffer_load_dwordx4 v246, s[76:79], s85 offen offset:0 lds\n"
"v_mfma_f32_16x16x32_bf16 v[56:59], a[140:143], v[0:3], 0\n"
"s_add_u32 m0, m0, 0x4000\n"
"ds_read_b64_tr_b16 a[170:171], v234 offset:41984\n"
"v_mfma_f32_16x16x32_bf16 v[8:11], a[144:147], v[16:19], v[8:11]\n"
"s_mov_b32 s85, 0x23280\n"
"ds_read_b64_tr_b16 a[172:173], v235 offset:33792\n"
"v_mfma_f32_16x16x32_bf16 v[12:15], a[144:147], v[20:23], v[12:15]\n"
"ds_read_b64_tr_b16 a[174:175], v235 offset:41984\n"
"v_cvt_pk_bf16_f32 v73, v74, v75\n"
"v_mfma_f32_16x16x32_bf16 v[28:31], a[148:151], v[20:23], v[28:31]\n"
"ds_read_b64_tr_b16 a[176:177], v232 offset:50176\n"
"v_cvt_pk_bf16_f32 v74, v76, v77\n"
"v_mfma_f32_16x16x32_bf16 v[24:27], a[148:151], v[16:19], v[24:27]\n"
"s_waitcnt lgkmcnt(9)\n"
"ds_read_b64_tr_b16 a[178:179], v232 offset:58368\n"
"buffer_load_dwordx4 v246, s[76:79], s85 offen offset:0 lds\n"
"v_mfma_f32_16x16x32_bf16 v[40:43], a[152:155], v[16:19], v[40:43]\n"
"s_add_u32 m0, m0, 0xffffc400\n"
"ds_read_b64_tr_b16 a[180:181], v233 offset:50176\n"
"v_mfma_f32_16x16x32_bf16 v[44:47], a[152:155], v[20:23], v[44:47]\n"
"s_mov_b32 s85, 0x2a280\n"
"ds_read_b64_tr_b16 a[182:183], v233 offset:58368\n"
"v_mfma_f32_16x16x32_bf16 v[60:63], a[156:159], v[20:23], v[60:63]\n"
"s_lshl_b32 s89, s84, 7\n"
"ds_read_b64_tr_b16 a[184:185], v234 offset:50176\n"
"v_mfma_f32_16x16x32_bf16 v[56:59], a[156:159], v[16:19], v[56:59]\n"
"s_waitcnt lgkmcnt(9)\n"
"ds_read_b64_tr_b16 a[186:187], v234 offset:58368\n"
"v_mfma_f32_16x16x32_bf16 v[8:11], a[160:163], v[32:35], v[8:11]\n"
"ds_read_b64_tr_b16 a[188:189], v235 offset:50176\n"
"buffer_load_dwordx4 v246, s[76:79], s85 offen offset:0 lds\n"
"v_mfma_f32_16x16x32_bf16 v[12:15], a[160:163], v[36:39], v[12:15]\n"
"s_add_u32 m0, m0, 0x4000\n"
"ds_read_b64_tr_b16 a[190:191], v235 offset:58368\n"
"v_mfma_f32_16x16x32_bf16 v[28:31], a[164:167], v[36:39], v[28:31]\n"
"s_mov_b32 s85, 0x31280\n"
"ds_read_b64_tr_b16 a[128:129], v232 offset:34816\n"
"v_mfma_f32_16x16x32_bf16 v[24:27], a[164:167], v[32:35], v[24:27]\n"
"s_waitcnt lgkmcnt(9)\n"
"ds_read_b64_tr_b16 a[130:131], v232 offset:43008\n"
"v_mfma_f32_16x16x32_bf16 v[40:43], a[168:171], v[32:35], v[40:43]\n"
"s_lshl_b32 s90, s87, 3\n"
"ds_read_b64_tr_b16 a[132:133], v233 offset:34816\n"
"v_mfma_f32_16x16x32_bf16 v[44:47], a[168:171], v[36:39], v[44:47]\n"
"ds_read_b64_tr_b16 a[134:135], v233 offset:43008\n"
"buffer_load_dwordx4 v246, s[76:79], s85 offen offset:0 lds\n"
"v_mfma_f32_16x16x32_bf16 v[60:63], a[172:175], v[36:39], v[60:63]\n"
"s_add_u32 m0, m0, 0x1b400\n"
"ds_read_b64_tr_b16 a[136:137], v234 offset:34816\n"
"v_mfma_f32_16x16x32_bf16 v[56:59], a[172:175], v[32:35], v[56:59]\n"
"s_waitcnt lgkmcnt(9)\n"
"s_mov_b32 s85, 0x300\n"
"ds_read_b64_tr_b16 a[138:139], v234 offset:43008\n"
"v_cvt_pk_bf16_f32 v75, v78, v79\n"
"v_mfma_f32_16x16x32_bf16 v[8:11], a[176:179], v[48:51], v[8:11]\n"
"ds_read_b64_tr_b16 a[140:141], v235 offset:34816\n"
"s_mov_b32 s86, 0x10000\n"
"buffer_store_dwordx4 v[72:75], v239, s[80:83], s86 offen sc1\n"
"v_mfma_f32_16x16x32_bf16 v[12:15], a[176:179], v[52:55], v[12:15]\n"
"s_add_u32 s89, s89, s90\n"
"ds_read_b64_tr_b16 a[142:143], v235 offset:43008\n"
"v_mfma_f32_16x16x32_bf16 v[28:31], a[180:183], v[52:55], v[28:31]\n"
"ds_read_b64_tr_b16 a[144:145], v232 offset:51200\n"
"v_mov_b32 v196, s89\n"
"v_mfma_f32_16x16x32_bf16 v[24:27], a[180:183], v[48:51], v[24:27]\n"
"s_waitcnt lgkmcnt(9)\n"
"ds_read_b64_tr_b16 a[146:147], v232 offset:59392\n"
"v_mfma_f32_16x16x32_bf16 v[40:43], a[184:187], v[48:51], v[40:43]\n"
"ds_read_b64_tr_b16 a[148:149], v233 offset:51200\n"
"v_mov_b32 v198, s96\n"
"v_mfma_f32_16x16x32_bf16 v[44:47], a[184:187], v[52:55], v[44:47]\n"
"ds_read_b64_tr_b16 a[150:151], v233 offset:59392\n"
"v_mov_b32 v199, s97\n"
"v_mfma_f32_16x16x32_bf16 v[60:63], a[188:191], v[52:55], v[60:63]\n"
"ds_read_b64_tr_b16 a[152:153], v234 offset:51200\n"
"v_mbcnt_lo_u32_b32 v197, -1, 0\n"
"v_mfma_f32_16x16x32_bf16 v[56:59], a[188:191], v[48:51], v[56:59]\n"
"s_waitcnt lgkmcnt(9)\n"
"ds_read_b64_tr_b16 a[154:155], v234 offset:59392\n"
"v_mfma_f32_16x16x32_bf16 v[8:11], a[128:131], v[64:67], v[8:11]\n"
"ds_read_b64_tr_b16 a[156:157], v235 offset:51200\n"
"v_mbcnt_hi_u32_b32 v197, -1, v197\n"
"v_cvt_pk_bf16_f32 v88, v88, v89\n"
"v_mfma_f32_16x16x32_bf16 v[12:15], a[128:131], v[68:71], v[12:15]\n"
"ds_read_b64_tr_b16 a[158:159], v235 offset:59392\n"
"v_and_b32 v197, 15, v197\n"
"v_cvt_pk_bf16_f32 v89, v90, v91\n"
"v_mfma_f32_16x16x32_bf16 v[28:31], a[132:135], v[68:71], v[28:31]\n"
"s_lshr_b32 s89, s87, 2\n"
"ds_read_b64_tr_b16 a[160:161], v232 offset:35840\n"
"v_cvt_pk_bf16_f32 v90, v92, v93\n"
"v_mfma_f32_16x16x32_bf16 v[24:27], a[132:135], v[64:67], v[24:27]\n"
"s_waitcnt lgkmcnt(9)\n"
"ds_read_b64_tr_b16 a[162:163], v232 offset:44032\n"
"v_cvt_pk_bf16_f32 v91, v94, v95\n"
"v_mfma_f32_16x16x32_bf16 v[40:43], a[136:139], v[64:67], v[40:43]\n"
"ds_read_b64_tr_b16 a[164:165], v233 offset:35840\n"
"s_mov_b32 s86, 0x14000\n"
"buffer_store_dwordx4 v[88:91], v239, s[80:83], s86 offen sc1\n"
"v_mfma_f32_16x16x32_bf16 v[44:47], a[136:139], v[68:71], v[44:47]\n"
"s_lshl_b32 s89, s89, 7\n"
"ds_read_b64_tr_b16 a[166:167], v233 offset:44032\n"
"v_mfma_f32_16x16x32_bf16 v[60:63], a[140:143], v[68:71], v[60:63]\n"
"ds_read_b64_tr_b16 a[168:169], v234 offset:35840\n"
"v_lshl_add_u32 v197, v197, 3, s89\n"
"v_mfma_f32_16x16x32_bf16 v[56:59], a[140:143], v[64:67], v[56:59]\n"
"s_waitcnt lgkmcnt(9)\n"
"ds_read_b64_tr_b16 a[170:171], v234 offset:44032\n"
"v_mfma_f32_16x16x32_bf16 v[8:11], a[144:147], v[80:83], v[8:11]\n"
"ds_read_b64_tr_b16 a[172:173], v235 offset:35840\n"
"v_cvt_pk_bf16_f32 v104, v104, v105\n"
"v_mfma_f32_16x16x32_bf16 v[12:15], a[144:147], v[84:87], v[12:15]\n"
"ds_read_b64_tr_b16 a[174:175], v235 offset:44032\n"
"v_cvt_pk_bf16_f32 v105, v106, v107\n"
"v_mfma_f32_16x16x32_bf16 v[28:31], a[148:151], v[84:87], v[28:31]\n"
"ds_read_b64_tr_b16 a[176:177], v232 offset:52224\n"
"v_cvt_pk_bf16_f32 v106, v108, v109\n"
"v_mfma_f32_16x16x32_bf16 v[24:27], a[148:151], v[80:83], v[24:27]\n"
"s_waitcnt lgkmcnt(9)\n"
"ds_read_b64_tr_b16 a[178:179], v232 offset:60416\n"
"v_mfma_f32_16x16x32_bf16 v[40:43], a[152:155], v[80:83], v[40:43]\n"
"ds_read_b64_tr_b16 a[180:181], v233 offset:52224\n"
"v_cvt_pk_bf16_f32 v107, v110, v111\n"
"v_mfma_f32_16x16x32_bf16 v[44:47], a[152:155], v[84:87], v[44:47]\n"
"ds_read_b64_tr_b16 a[182:183], v233 offset:60416\n"
"s_mov_b32 s86, 0x18000\n"
"buffer_store_dwordx4 v[104:107], v239, s[80:83], s86 offen sc1\n"
"v_mfma_f32_16x16x32_bf16 v[60:63], a[156:159], v[84:87], v[60:63]\n"
"ds_read_b64_tr_b16 a[184:185], v234 offset:52224\n"
"v_cvt_pk_bf16_f32 v120, v120, v121\n"
"v_mfma_f32_16x16x32_bf16 v[56:59], a[156:159], v[80:83], v[56:59]\n"
"s_waitcnt lgkmcnt(9)\n"
"ds_read_b64_tr_b16 a[186:187], v234 offset:60416\n"
"v_mfma_f32_16x16x32_bf16 v[8:11], a[160:163], v[96:99], v[8:11]\n"
"ds_read_b64_tr_b16 a[188:189], v235 offset:52224\n"
"v_cvt_pk_bf16_f32 v121, v122, v123\n"
"v_mfma_f32_16x16x32_bf16 v[12:15], a[160:163], v[100:103], v[12:15]\n"
"ds_read_b64_tr_b16 a[190:191], v235 offset:60416\n"
"s_waitcnt vmcnt(23)\n"
"s_barrier\n"
"v_add_u32 v232, 0xffff0000, v232\n"
"v_mfma_f32_16x16x32_bf16 v[28:31], a[164:167], v[100:103], v[28:31]\n"
"ds_read_b64_tr_b16 a[128:129], v232 offset:32768\n"
"buffer_load_dwordx4 v246, s[76:79], s85 offen offset:0 lds\n"
"v_mfma_f32_16x16x32_bf16 v[24:27], a[164:167], v[96:99], v[24:27]\n"
"s_waitcnt lgkmcnt(9)\n"
"ds_read_b64_tr_b16 a[130:131], v232 offset:40960\n"
"v_add_u32 v233, 0xffff0000, v233\n"
"v_mfma_f32_16x16x32_bf16 v[40:43], a[168:171], v[96:99], v[40:43]\n"
"s_add_u32 m0, m0, 0x4000\n"
"ds_read_b64_tr_b16 a[132:133], v233 offset:32768\n"
"v_mfma_f32_16x16x32_bf16 v[44:47], a[168:171], v[100:103], v[44:47]\n"
"s_mov_b32 s85, 0x7300\n"
"ds_read_b64_tr_b16 a[134:135], v233 offset:40960\n"
"v_add_u32 v234, 0xffff0000, v234\n"
"v_mfma_f32_16x16x32_bf16 v[60:63], a[172:175], v[100:103], v[60:63]\n"
"ds_read_b64_tr_b16 a[136:137], v234 offset:32768\n"
"v_cvt_pk_bf16_f32 v122, v124, v125\n"
"v_mfma_f32_16x16x32_bf16 v[56:59], a[172:175], v[96:99], v[56:59]\n"
"s_waitcnt lgkmcnt(9)\n"
"ds_read_b64_tr_b16 a[138:139], v234 offset:40960\n"
"buffer_load_dwordx4 v246, s[76:79], s85 offen offset:0 lds\n"
"v_add_u32 v235, 0xffff0000, v235\n"
"v_mfma_f32_16x16x32_bf16 v[8:11], a[176:179], v[112:115], v[8:11]\n"
"s_add_u32 m0, m0, 0xffffc400\n"
"ds_read_b64_tr_b16 a[140:141], v235 offset:32768\n"
"v_mfma_f32_16x16x32_bf16 v[12:15], a[176:179], v[116:119], v[12:15]\n"
"s_mov_b32 s85, 0xe300\n"
"ds_read_b64_tr_b16 a[142:143], v235 offset:40960\n"
"v_mfma_f32_16x16x32_bf16 v[28:31], a[180:183], v[116:119], v[28:31]\n"
"ds_read_b64_tr_b16 a[144:145], v232 offset:49152\n"
"v_cvt_pk_bf16_f32 v123, v126, v127\n"
"v_mfma_f32_16x16x32_bf16 v[24:27], a[180:183], v[112:115], v[24:27]\n"
"s_waitcnt lgkmcnt(9)\n"
"ds_read_b64_tr_b16 a[146:147], v232 offset:57344\n"
"s_mov_b32 s86, 0x1c000\n"
"buffer_store_dwordx4 v[120:123], v239, s[80:83], s86 offen sc1\n"
"v_mfma_f32_16x16x32_bf16 v[40:43], a[184:187], v[112:115], v[40:43]\n"
"ds_read_b64_tr_b16 a[148:149], v233 offset:49152\n"
"buffer_load_dwordx4 v246, s[76:79], s85 offen offset:0 lds\n"
"v_mfma_f32_16x16x32_bf16 v[44:47], a[184:187], v[116:119], v[44:47]\n"
"s_add_u32 m0, m0, 0x4000\n"
"ds_read_b64_tr_b16 a[150:151], v233 offset:57344\n"
"v_mfma_f32_16x16x32_bf16 v[60:63], a[188:191], v[116:119], v[60:63]\n"
"s_mov_b32 s85, 0x15300\n"
"ds_read_b64_tr_b16 a[152:153], v234 offset:49152\n"
"v_mfma_f32_16x16x32_bf16 v[56:59], a[188:191], v[112:115], v[56:59]\n"
"s_waitcnt lgkmcnt(9)\n"
"ds_read_b64_tr_b16 a[154:155], v234 offset:57344\n"
"v_mfma_f32_16x16x32_bf16 v[72:75], a[128:131], v[0:3], 0\n"
"ds_read_b64_tr_b16 a[156:157], v235 offset:49152\n"
"v_mbcnt_lo_u32_b32 v146, -1, 0\n"
"v_mfma_f32_16x16x32_bf16 v[76:79], a[128:131], v[4:7], 0\n"
"ds_read_b64_tr_b16 a[158:159], v235 offset:57344\n"
"buffer_load_dwordx4 v246, s[76:79], s85 offen offset:0 lds\n"
"v_mfma_f32_16x16x32_bf16 v[92:95], a[132:135], v[4:7], 0\n"
"s_add_u32 m0, m0, 0xffffc400\n"
"ds_read_b64_tr_b16 a[160:161], v232 offset:33792\n"
"v_mfma_f32_16x16x32_bf16 v[88:91], a[132:135], v[0:3], 0\n"
"s_waitcnt lgkmcnt(9)\n"
"s_mov_b32 s85, 0x1c300\n"
"ds_read_b64_tr_b16 a[162:163], v232 offset:41984\n"
"v_mfma_f32_16x16x32_bf16 v[104:107], a[136:139], v[0:3], 0\n"
"ds_read_b64_tr_b16 a[164:165], v233 offset:33792\n"
"v_cvt_pk_bf16_f32 v8, v8, v9\n"
"v_mfma_f32_16x16x32_bf16 v[108:111], a[136:139], v[4:7], 0\n"
"s_waitcnt lgkmcnt(7)\n"
"ds_read_b64_tr_b16 a[166:167], v233 offset:41984\n"
"v_mfma_f32_16x16x32_bf16 v[124:127], a[140:143], v[4:7], 0\n"
"ds_read_b64_tr_b16 a[168:169], v234 offset:33792\n"
"buffer_load_dwordx4 v246, s[76:79], s85 offen offset:0 lds\n"
"v_mfma_f32_16x16x32_bf16 v[120:123], a[140:143], v[0:3], 0\n"
"s_add_u32 m0, m0, 0x4000\n"
"ds_read_b64_tr_b16 a[170:171], v234 offset:41984\n"
"v_mfma_f32_16x16x32_bf16 v[72:75], a[144:147], v[16:19], v[72:75]\n"
"s_mov_b32 s85, 0x23300\n"
"ds_read_b64_tr_b16 a[172:173], v235 offset:33792\n"
"v_mfma_f32_16x16x32_bf16 v[76:79], a[144:147], v[20:23], v[76:79]\n"
"ds_read_b64_tr_b16 a[174:175], v235 offset:41984\n"
"v_cvt_pk_bf16_f32 v9, v10, v11\n"
"v_mfma_f32_16x16x32_bf16 v[92:95], a[148:151], v[20:23], v[92:95]\n"
"ds_read_b64_tr_b16 a[176:177], v232 offset:50176\n"
"v_cvt_pk_bf16_f32 v10, v12, v13\n"
"v_mfma_f32_16x16x32_bf16 v[88:91], a[148:151], v[16:19], v[88:91]\n"
"s_waitcnt lgkmcnt(9)\n"
"ds_read_b64_tr_b16 a[178:179], v232 offset:58368\n"
"buffer_load_dwordx4 v246, s[76:79], s85 offen offset:0 lds\n"
"v_mfma_f32_16x16x32_bf16 v[104:107], a[152:155], v[16:19], v[104:107]\n"
"s_add_u32 m0, m0, 0xffffc400\n"
"ds_read_b64_tr_b16 a[180:181], v233 offset:50176\n"
"v_mfma_f32_16x16x32_bf16 v[108:111], a[152:155], v[20:23], v[108:111]\n"
"s_mov_b32 s85, 0x2a300\n"
"ds_read_b64_tr_b16 a[182:183], v233 offset:58368\n"
"v_mfma_f32_16x16x32_bf16 v[124:127], a[156:159], v[20:23], v[124:127]\n"
"ds_read_b64_tr_b16 a[184:185], v234 offset:50176\n"
"v_cvt_pk_bf16_f32 v11, v14, v15\n"
"v_mfma_f32_16x16x32_bf16 v[120:123], a[156:159], v[16:19], v[120:123]\n"
"s_waitcnt lgkmcnt(9)\n"
"ds_read_b64_tr_b16 a[186:187], v234 offset:58368\n"
"s_mov_b32 s86, 0x20000\n"
"buffer_store_dwordx4 v[8:11], v239, s[80:83], s86 offen sc1\n"
"v_mfma_f32_16x16x32_bf16 v[72:75], a[160:163], v[32:35], v[72:75]\n"
"ds_read_b64_tr_b16 a[188:189], v235 offset:50176\n"
"buffer_load_dwordx4 v246, s[76:79], s85 offen offset:0 lds\n"
"v_mfma_f32_16x16x32_bf16 v[76:79], a[160:163], v[36:39], v[76:79]\n"
"s_add_u32 m0, m0, 0x4000\n"
"ds_read_b64_tr_b16 a[190:191], v235 offset:58368\n"
"v_mfma_f32_16x16x32_bf16 v[92:95], a[164:167], v[36:39], v[92:95]\n"
"s_mov_b32 s85, 0x31300\n"
"ds_read_b64_tr_b16 a[128:129], v232 offset:34816\n"
"v_mfma_f32_16x16x32_bf16 v[88:91], a[164:167], v[32:35], v[88:91]\n"
"s_waitcnt lgkmcnt(9)\n"
"ds_read_b64_tr_b16 a[130:131], v232 offset:43008\n"
"v_mfma_f32_16x16x32_bf16 v[104:107], a[168:171], v[32:35], v[104:107]\n"
"ds_read_b64_tr_b16 a[132:133], v233 offset:34816\n"
"v_cvt_pk_bf16_f32 v24, v24, v25\n"
"v_mfma_f32_16x16x32_bf16 v[108:111], a[168:171], v[36:39], v[108:111]\n"
"ds_read_b64_tr_b16 a[134:135], v233 offset:43008\n"
"buffer_load_dwordx4 v246, s[76:79], s85 offen offset:0 lds\n"
"v_mfma_f32_16x16x32_bf16 v[124:127], a[172:175], v[36:39], v[124:127]\n"
"ds_read_b64_tr_b16 a[136:137], v234 offset:34816\n"
"v_cvt_pk_bf16_f32 v25, v26, v27\n"
"v_mfma_f32_16x16x32_bf16 v[120:123], a[172:175], v[32:35], v[120:123]\n"
"s_waitcnt lgkmcnt(9)\n"
"ds_read_b64_tr_b16 a[138:139], v234 offset:43008\n"
"v_mfma_f32_16x16x32_bf16 v[72:75], a[176:179], v[48:51], v[72:75]\n"
"ds_read_b64_tr_b16 a[140:141], v235 offset:34816\n"
"v_cvt_pk_bf16_f32 v26, v28, v29\n"
"v_mfma_f32_16x16x32_bf16 v[76:79], a[176:179], v[52:55], v[76:79]\n"
"ds_read_b64_tr_b16 a[142:143], v235 offset:43008\n"
"v_cvt_pk_bf16_f32 v27, v30, v31\n"
"v_mfma_f32_16x16x32_bf16 v[92:95], a[180:183], v[52:55], v[92:95]\n"
"ds_read_b64_tr_b16 a[144:145], v232 offset:51200\n"
"s_mov_b32 s86, 0x24000\n"
"buffer_store_dwordx4 v[24:27], v239, s[80:83], s86 offen sc1\n"
"v_mfma_f32_16x16x32_bf16 v[88:91], a[180:183], v[48:51], v[88:91]\n"
"s_waitcnt lgkmcnt(9)\n"
"ds_read_b64_tr_b16 a[146:147], v232 offset:59392\n"
"v_mfma_f32_16x16x32_bf16 v[104:107], a[184:187], v[48:51], v[104:107]\n"
"ds_read_b64_tr_b16 a[148:149], v233 offset:51200\n"
"v_cvt_pk_bf16_f32 v40, v40, v41\n"
"v_mfma_f32_16x16x32_bf16 v[108:111], a[184:187], v[52:55], v[108:111]\n"
"s_waitcnt lgkmcnt(7)\n"
"ds_read_b64_tr_b16 a[150:151], v233 offset:59392\n"
"v_mfma_f32_16x16x32_bf16 v[124:127], a[188:191], v[52:55], v[124:127]\n"
"ds_read_b64_tr_b16 a[152:153], v234 offset:51200\n"
"v_cvt_pk_bf16_f32 v41, v42, v43\n"
"v_mfma_f32_16x16x32_bf16 v[120:123], a[188:191], v[48:51], v[120:123]\n"
"ds_read_b64_tr_b16 a[154:155], v234 offset:59392\n"
"v_cvt_pk_bf16_f32 v42, v44, v45\n"
"v_mfma_f32_16x16x32_bf16 v[72:75], a[128:131], v[64:67], v[72:75]\n"
"ds_read_b64_tr_b16 a[156:157], v235 offset:51200\n"
"v_cvt_pk_bf16_f32 v43, v46, v47\n"
"v_mfma_f32_16x16x32_bf16 v[76:79], a[128:131], v[68:71], v[76:79]\n"
"ds_read_b64_tr_b16 a[158:159], v235 offset:59392\n"
"s_mov_b32 s86, 0x28000\n"
"buffer_store_dwordx4 v[40:43], v239, s[80:83], s86 offen sc1\n"
"v_mfma_f32_16x16x32_bf16 v[92:95], a[132:135], v[68:71], v[92:95]\n"
"s_waitcnt lgkmcnt(8)\n"
"ds_read_b64_tr_b16 a[160:161], v232 offset:35840\n"
"v_mfma_f32_16x16x32_bf16 v[88:91], a[132:135], v[64:67], v[88:91]\n"
"ds_read_b64_tr_b16 a[162:163], v232 offset:44032\n"
"v_cvt_pk_bf16_f32 v56, v56, v57\n"
"v_mfma_f32_16x16x32_bf16 v[104:107], a[136:139], v[64:67], v[104:107]\n"
"ds_read_b64_tr_b16 a[164:165], v233 offset:35840\n"
"v_cvt_pk_bf16_f32 v57, v58, v59\n"
"v_mfma_f32_16x16x32_bf16 v[108:111], a[136:139], v[68:71], v[108:111]\n"
"s_waitcnt lgkmcnt(7)\n"
"ds_read_b64_tr_b16 a[166:167], v233 offset:44032\n"
"v_mfma_f32_16x16x32_bf16 v[124:127], a[140:143], v[68:71], v[124:127]\n"
"ds_read_b64_tr_b16 a[168:169], v234 offset:35840\n"
"v_cvt_pk_bf16_f32 v58, v60, v61\n"
"v_mfma_f32_16x16x32_bf16 v[120:123], a[140:143], v[64:67], v[120:123]\n"
"ds_read_b64_tr_b16 a[170:171], v234 offset:44032\n"
"v_cvt_pk_bf16_f32 v59, v62, v63\n"
"v_mfma_f32_16x16x32_bf16 v[72:75], a[144:147], v[80:83], v[72:75]\n"
"ds_read_b64_tr_b16 a[172:173], v235 offset:35840\n"
"v_mbcnt_hi_u32_b32 v146, -1, v146\n"
"v_mfma_f32_16x16x32_bf16 v[76:79], a[144:147], v[84:87], v[76:79]\n"
"s_waitcnt lgkmcnt(7)\n"
"ds_read_b64_tr_b16 a[174:175], v235 offset:44032\n"
"v_mfma_f32_16x16x32_bf16 v[92:95], a[148:151], v[84:87], v[92:95]\n"
"ds_read_b64_tr_b16 a[176:177], v232 offset:52224\n"
"v_and_b32 v146, 1, v146\n"
"v_mfma_f32_16x16x32_bf16 v[88:91], a[148:151], v[80:83], v[88:91]\n"
"ds_read_b64_tr_b16 a[178:179], v232 offset:60416\n"
"v_add_u32 v232, 0x10000, v232\n"
"v_mfma_f32_16x16x32_bf16 v[104:107], a[152:155], v[80:83], v[104:107]\n"
"ds_read_b64_tr_b16 a[180:181], v233 offset:52224\n"
"s_mov_b32 s86, 0x2c000\n"
"buffer_store_dwordx4 v[56:59], v239, s[80:83], s86 offen sc1\n"
"v_mfma_f32_16x16x32_bf16 v[108:111], a[152:155], v[84:87], v[108:111]\n"
"s_waitcnt lgkmcnt(7)\n"
"ds_read_b64_tr_b16 a[182:183], v233 offset:60416\n"
"v_mfma_f32_16x16x32_bf16 v[124:127], a[156:159], v[84:87], v[124:127]\n"
"ds_read_b64_tr_b16 a[184:185], v234 offset:52224\n"
"v_add_u32 v233, 0x10000, v233\n"
"v_mfma_f32_16x16x32_bf16 v[120:123], a[156:159], v[80:83], v[120:123]\n"
"ds_read_b64_tr_b16 a[186:187], v234 offset:60416\n"
"v_add_u32 v234, 0x10000, v234\n"
"v_mfma_f32_16x16x32_bf16 v[72:75], a[160:163], v[96:99], v[72:75]\n"
"ds_read_b64_tr_b16 a[188:189], v235 offset:52224\n"
"v_lshlrev_b32 v146, 4, v146\n"
"v_mfma_f32_16x16x32_bf16 v[76:79], a[160:163], v[100:103], v[76:79]\n"
"ds_read_b64_tr_b16 a[190:191], v235 offset:60416\n"
"s_waitcnt vmcnt(28)\n"
"s_barrier\n"
"v_mfma_f32_16x16x32_bf16 v[92:95], a[164:167], v[100:103], v[92:95]\n"
"s_waitcnt lgkmcnt(8)\n"
"ds_read_b64_tr_b16 a[128:129], v232 offset:0\n"
"v_mfma_f32_16x16x32_bf16 v[88:91], a[164:167], v[96:99], v[88:91]\n"
"ds_read_b64_tr_b16 a[130:131], v232 offset:8192\n"
"v_add_u32 v235, 0x10000, v235\n"
"v_mfma_f32_16x16x32_bf16 v[104:107], a[168:171], v[96:99], v[104:107]\n"
"s_mov_b32 s89, 0x3f80\n"
"ds_read_b64_tr_b16 a[132:133], v233 offset:0\n"
"v_mfma_f32_16x16x32_bf16 v[108:111], a[168:171], v[100:103], v[108:111]\n"
"s_waitcnt lgkmcnt(7)\n"
"ds_read_b64_tr_b16 a[134:135], v233 offset:8192\n"
"v_mfma_f32_16x16x32_bf16 v[124:127], a[172:175], v[100:103], v[124:127]\n"
"ds_read_b64_tr_b16 a[136:137], v234 offset:0\n"
"v_lshlrev_b32_e64 v146, v146, s89\n"
"v_mfma_f32_16x16x32_bf16 v[120:123], a[172:175], v[96:99], v[120:123]\n"
"s_mov_b32 s90, 0x00030003\n"
"ds_read_b64_tr_b16 a[138:139], v234 offset:8192\n"
"v_mfma_f32_16x16x32_bf16 v[72:75], a[176:179], v[112:115], v[72:75]\n"
"s_mov_b32 s91, 0x00030003\n"
"ds_read_b64_tr_b16 a[140:141], v235 offset:0\n"
"v_mfma_f32_16x16x32_bf16 v[76:79], a[176:179], v[116:119], v[76:79]\n"
"ds_read_b64_tr_b16 a[142:143], v235 offset:8192\n"
"v_cndmask_b32 v128, 0, v146, s[90:91]\n"
"v_mfma_f32_16x16x32_bf16 v[92:95], a[180:183], v[116:119], v[92:95]\n"
"s_lshl_b64 s[90:91], s[90:91], 2\n"
"ds_read_b64_tr_b16 a[144:145], v232 offset:16384\n"
"v_mfma_f32_16x16x32_bf16 v[88:91], a[180:183], v[112:115], v[88:91]\n"
"s_waitcnt lgkmcnt(9)\n"
"ds_read_b64_tr_b16 a[146:147], v232 offset:24576\n"
"v_mfma_f32_16x16x32_bf16 v[104:107], a[184:187], v[112:115], v[104:107]\n"
"ds_read_b64_tr_b16 a[148:149], v233 offset:16384\n"
"v_cndmask_b32 v129, 0, v146, s[90:91]\n"
"v_mfma_f32_16x16x32_bf16 v[108:111], a[184:187], v[116:119], v[108:111]\n"
"s_lshl_b64 s[90:91], s[90:91], 2\n"
"ds_read_b64_tr_b16 a[150:151], v233 offset:24576\n"
"v_mfma_f32_16x16x32_bf16 v[124:127], a[188:191], v[116:119], v[124:127]\n"
"ds_read_b64_tr_b16 a[152:153], v234 offset:16384\n"
"v_cndmask_b32 v130, 0, v146, s[90:91]\n"
"v_mfma_f32_16x16x32_bf16 v[120:123], a[188:191], v[112:115], v[120:123]\n"
"s_waitcnt lgkmcnt(9)\n"
"ds_read_b64_tr_b16 a[154:155], v234 offset:24576\n"
"v_mfma_f32_16x16x32_bf16 v[8:11], a[128:131], v[0:3], 0\n"
"s_lshl_b64 s[90:91], s[90:91], 2\n"
"ds_read_b64_tr_b16 a[156:157], v235 offset:16384\n"
"v_mfma_f32_16x16x32_bf16 v[12:15], a[128:131], v[4:7], 0\n"
"ds_read_b64_tr_b16 a[158:159], v235 offset:24576\n"
"v_cndmask_b32 v131, 0, v146, s[90:91]\n"
"v_mfma_f32_16x16x32_bf16 v[28:31], a[132:135], v[4:7], 0\n"
"s_lshl_b64 s[90:91], s[90:91], 2\n"
"ds_read_b64_tr_b16 a[160:161], v232 offset:1024\n"
"v_mfma_f32_16x16x32_bf16 v[24:27], a[132:135], v[0:3], 0\n"
"s_waitcnt lgkmcnt(9)\n"
"ds_read_b64_tr_b16 a[162:163], v232 offset:9216\n"
"v_mfma_f32_16x16x32_bf16 v[40:43], a[136:139], v[0:3], 0\n"
"ds_read_b64_tr_b16 a[164:165], v233 offset:1024\n"
"v_cndmask_b32 v132, 0, v146, s[90:91]\n"
"v_mfma_f32_16x16x32_bf16 v[44:47], a[136:139], v[4:7], 0\n"
"s_lshl_b64 s[90:91], s[90:91], 2\n"
"ds_read_b64_tr_b16 a[166:167], v233 offset:9216\n"
"v_mfma_f32_16x16x32_bf16 v[60:63], a[140:143], v[4:7], 0\n"
"ds_read_b64_tr_b16 a[168:169], v234 offset:1024\n"
"v_cndmask_b32 v133, 0, v146, s[90:91]\n"
"v_mfma_f32_16x16x32_bf16 v[56:59], a[140:143], v[0:3], 0\n"
"s_waitcnt lgkmcnt(9)\n"
"ds_read_b64_tr_b16 a[170:171], v234 offset:9216\n"
"v_mfma_f32_16x16x32_bf16 v[8:11], a[144:147], v[16:19], v[8:11]\n"
"s_lshl_b64 s[90:91], s[90:91], 2\n"
"ds_read_b64_tr_b16 a[172:173], v235 offset:1024\n"
"v_mfma_f32_16x16x32_bf16 v[12:15], a[144:147], v[20:23], v[12:15]\n"
"ds_read_b64_tr_b16 a[174:175], v235 offset:9216\n"
"v_cndmask_b32 v134, 0, v146, s[90:91]\n"
"v_mfma_f32_16x16x32_bf16 v[28:31], a[148:151], v[20:23], v[28:31]\n"
"s_lshl_b64 s[90:91], s[90:91], 2\n"
"ds_read_b64_tr_b16 a[176:177], v232 offset:17408\n"
"v_mfma_f32_16x16x32_bf16 v[24:27], a[148:151], v[16:19], v[24:27]\n"
"s_waitcnt lgkmcnt(9)\n"
"ds_read_b64_tr_b16 a[178:179], v232 offset:25600\n"
"v_mfma_f32_16x16x32_bf16 v[40:43], a[152:155], v[16:19], v[40:43]\n"
"ds_read_b64_tr_b16 a[180:181], v233 offset:17408\n"
"v_cndmask_b32 v135, 0, v146, s[90:91]\n"
"v_mfma_f32_16x16x32_bf16 v[44:47], a[152:155], v[20:23], v[44:47]\n"
"ds_read_b64_tr_b16 a[182:183], v233 offset:25600\n"
"v_cvt_pk_bf16_f32 v72, v72, v73\n"
"v_mfma_f32_16x16x32_bf16 v[60:63], a[156:159], v[20:23], v[60:63]\n"
"ds_read_b64_tr_b16 a[184:185], v234 offset:17408\n"
"v_cvt_pk_bf16_f32 v73, v74, v75\n"
"v_mfma_f32_16x16x32_bf16 v[56:59], a[156:159], v[16:19], v[56:59]\n"
"s_waitcnt lgkmcnt(9)\n"
"ds_read_b64_tr_b16 a[186:187], v234 offset:25600\n"
"v_mfma_f32_16x16x32_bf16 v[8:11], a[160:163], v[32:35], v[8:11]\n"
"ds_read_b64_tr_b16 a[188:189], v235 offset:17408\n"
"v_cvt_pk_bf16_f32 v74, v76, v77\n"
"v_mfma_f32_16x16x32_bf16 v[12:15], a[160:163], v[36:39], v[12:15]\n"
"ds_read_b64_tr_b16 a[190:191], v235 offset:25600\n"
"v_cvt_pk_bf16_f32 v75, v78, v79\n"
"v_mfma_f32_16x16x32_bf16 v[28:31], a[164:167], v[36:39], v[28:31]\n"
"ds_read_b64_tr_b16 a[128:129], v232 offset:2048\n"
"s_mov_b32 s86, 0x30000\n"
"buffer_store_dwordx4 v[72:75], v239, s[80:83], s86 offen sc1\n"
"v_mfma_f32_16x16x32_bf16 v[24:27], a[164:167], v[32:35], v[24:27]\n"
"s_waitcnt lgkmcnt(9)\n"
"ds_read_b64_tr_b16 a[130:131], v232 offset:10240\n"
"v_mfma_f32_16x16x32_bf16 v[40:43], a[168:171], v[32:35], v[40:43]\n"
"ds_read_b64_tr_b16 a[132:133], v233 offset:2048\n"
"v_cvt_pk_bf16_f32 v88, v88, v89\n"
"v_mfma_f32_16x16x32_bf16 v[44:47], a[168:171], v[36:39], v[44:47]\n"
"ds_read_b64_tr_b16 a[134:135], v233 offset:10240\n"
"v_cvt_pk_bf16_f32 v89, v90, v91\n"
"v_mfma_f32_16x16x32_bf16 v[60:63], a[172:175], v[36:39], v[60:63]\n"
"ds_read_b64_tr_b16 a[136:137], v234 offset:2048\n"
"v_cvt_pk_bf16_f32 v90, v92, v93\n"
"v_mfma_f32_16x16x32_bf16 v[56:59], a[172:175], v[32:35], v[56:59]\n"
"s_waitcnt lgkmcnt(9)\n"
"ds_read_b64_tr_b16 a[138:139], v234 offset:10240\n"
"v_mfma_f32_16x16x32_bf16 v[8:11], a[176:179], v[48:51], v[8:11]\n"
"ds_read_b64_tr_b16 a[140:141], v235 offset:2048\n"
"v_cvt_pk_bf16_f32 v91, v94, v95\n"
"v_mfma_f32_16x16x32_bf16 v[12:15], a[176:179], v[52:55], v[12:15]\n"
"ds_read_b64_tr_b16 a[142:143], v235 offset:10240\n"
"s_mov_b32 s86, 0x34000\n"
"buffer_store_dwordx4 v[88:91], v239, s[80:83], s86 offen sc1\n"
"v_mfma_f32_16x16x32_bf16 v[28:31], a[180:183], v[52:55], v[28:31]\n"
"s_waitcnt lgkmcnt(8)\n"
"ds_read_b64_tr_b16 a[144:145], v232 offset:18432\n"
"v_mfma_f32_16x16x32_bf16 v[24:27], a[180:183], v[48:51], v[24:27]\n"
"ds_read_b64_tr_b16 a[146:147], v232 offset:26624\n"
"v_cvt_pk_bf16_f32 v104, v104, v105\n"
"v_mfma_f32_16x16x32_bf16 v[40:43], a[184:187], v[48:51], v[40:43]\n"
"ds_read_b64_tr_b16 a[148:149], v233 offset:18432\n"
"v_cvt_pk_bf16_f32 v105, v106, v107\n"
"v_mfma_f32_16x16x32_bf16 v[44:47], a[184:187], v[52:55], v[44:47]\n"
"s_waitcnt lgkmcnt(7)\n"
"ds_read_b64_tr_b16 a[150:151], v233 offset:26624\n"
"v_mfma_f32_16x16x32_bf16 v[60:63], a[188:191], v[52:55], v[60:63]\n"
"ds_read_b64_tr_b16 a[152:153], v234 offset:18432\n"
"v_cvt_pk_bf16_f32 v106, v108, v109\n"
"v_mfma_f32_16x16x32_bf16 v[56:59], a[188:191], v[48:51], v[56:59]\n"
"ds_read_b64_tr_b16 a[154:155], v234 offset:26624\n"
"v_cvt_pk_bf16_f32 v107, v110, v111\n"
"v_mfma_f32_16x16x32_bf16 v[8:11], a[128:131], v[64:67], v[8:11]\n"
"ds_read_b64_tr_b16 a[156:157], v235 offset:18432\n"
"s_mov_b32 s86, 0x38000\n"
"buffer_store_dwordx4 v[104:107], v239, s[80:83], s86 offen sc1\n"
"v_mfma_f32_16x16x32_bf16 v[12:15], a[128:131], v[68:71], v[12:15]\n"
"s_waitcnt lgkmcnt(7)\n"
"ds_read_b64_tr_b16 a[158:159], v235 offset:26624\n"
"v_mfma_f32_16x16x32_bf16 v[28:31], a[132:135], v[68:71], v[28:31]\n"
"ds_read_b64_tr_b16 a[160:161], v232 offset:3072\n"
"v_cvt_pk_bf16_f32 v120, v120, v121\n"
"v_mfma_f32_16x16x32_bf16 v[24:27], a[132:135], v[64:67], v[24:27]\n"
"ds_read_b64_tr_b16 a[162:163], v232 offset:11264\n"
"v_cvt_pk_bf16_f32 v121, v122, v123\n"
"v_mfma_f32_16x16x32_bf16 v[40:43], a[136:139], v[64:67], v[40:43]\n"
"ds_read_b64_tr_b16 a[164:165], v233 offset:3072\n"
"v_cvt_pk_bf16_f32 v122, v124, v125\n"
"v_mfma_f32_16x16x32_bf16 v[44:47], a[136:139], v[68:71], v[44:47]\n"
"s_waitcnt lgkmcnt(7)\n"
"ds_read_b64_tr_b16 a[166:167], v233 offset:11264\n"
"v_mfma_f32_16x16x32_bf16 v[60:63], a[140:143], v[68:71], v[60:63]\n"
"ds_read_b64_tr_b16 a[168:169], v234 offset:3072\n"
"v_cvt_pk_bf16_f32 v123, v126, v127\n"
"v_mfma_f32_16x16x32_bf16 v[56:59], a[140:143], v[64:67], v[56:59]\n"
"ds_read_b64_tr_b16 a[170:171], v234 offset:11264\n"
"v_mfma_f32_16x16x32_bf16 v[8:11], a[144:147], v[80:83], v[8:11]\n"
"ds_read_b64_tr_b16 a[172:173], v235 offset:3072\n"
"v_mfma_f32_16x16x32_bf16 v[12:15], a[144:147], v[84:87], v[12:15]\n"
"s_waitcnt lgkmcnt(7)\n"
"ds_read_b64_tr_b16 a[174:175], v235 offset:11264\n"
"v_mfma_f32_16x16x32_bf16 v[28:31], a[148:151], v[84:87], v[28:31]\n"
"ds_read_b64_tr_b16 a[176:177], v232 offset:19456\n"
"v_mfma_f32_16x16x32_bf16 v[24:27], a[148:151], v[80:83], v[24:27]\n"
"ds_read_b64_tr_b16 a[178:179], v232 offset:27648\n"
"v_add_u32 v232, 0xffff0000, v232\n"
"v_mfma_f32_16x16x32_bf16 v[40:43], a[152:155], v[80:83], v[40:43]\n"
"ds_read_b64_tr_b16 a[180:181], v233 offset:19456\n"
"s_mov_b32 s86, 0x3c000\n"
"buffer_store_dwordx4 v[120:123], v239, s[80:83], s86 offen sc1\n"
"v_mfma_f32_16x16x32_bf16 v[44:47], a[152:155], v[84:87], v[44:47]\n"
"s_waitcnt lgkmcnt(7)\n"
"ds_read_b64_tr_b16 a[182:183], v233 offset:27648\n"
"v_mfma_f32_16x16x32_bf16 v[60:63], a[156:159], v[84:87], v[60:63]\n"
"ds_read_b64_tr_b16 a[184:185], v234 offset:19456\n"
"v_add_u32 v233, 0xffff0000, v233\n"
"v_mfma_f32_16x16x32_bf16 v[56:59], a[156:159], v[80:83], v[56:59]\n"
"ds_read_b64_tr_b16 a[186:187], v234 offset:27648\n"
"v_add_u32 v234, 0xffff0000, v234\n"
"v_mfma_f32_16x16x32_bf16 v[8:11], a[160:163], v[96:99], v[8:11]\n"
"ds_read_b64_tr_b16 a[188:189], v235 offset:19456\n"
"v_mfma_f32_16x16x32_bf16 v[12:15], a[160:163], v[100:103], v[12:15]\n"
"ds_read_b64_tr_b16 a[190:191], v235 offset:27648\n"
"s_waitcnt vmcnt(20)\n"
"s_barrier\n"
"v_mfma_f32_16x16x32_bf16 v[28:31], a[164:167], v[100:103], v[28:31]\n"
"s_waitcnt lgkmcnt(8)\n"
"ds_read_b64_tr_b16 a[128:129], v232 offset:0\n"
"v_mfma_f32_16x16x32_bf16 v[24:27], a[164:167], v[96:99], v[24:27]\n"
"ds_read_b64_tr_b16 a[130:131], v232 offset:8192\n"
"v_add_u32 v235, 0xffff0000, v235\n"
"v_mfma_f32_16x16x32_bf16 v[40:43], a[168:171], v[96:99], v[40:43]\n"
"ds_read_b64_tr_b16 a[132:133], v233 offset:0\n"
"v_mfma_f32_16x16x32_bf16 v[44:47], a[168:171], v[100:103], v[44:47]\n"
"s_waitcnt lgkmcnt(7)\n"
"ds_read_b64_tr_b16 a[134:135], v233 offset:8192\n"
"v_mfma_f32_16x16x32_bf16 v[60:63], a[172:175], v[100:103], v[60:63]\n"
"ds_read_b64_tr_b16 a[136:137], v234 offset:0\n"
"v_mfma_f32_16x16x32_bf16 v[56:59], a[172:175], v[96:99], v[56:59]\n"
"ds_read_b64_tr_b16 a[138:139], v234 offset:8192\n"
"v_mfma_f32_16x16x32_bf16 v[8:11], a[176:179], v[112:115], v[8:11]\n"
"ds_read_b64_tr_b16 a[140:141], v235 offset:0\n"
"v_mfma_f32_16x16x32_bf16 v[12:15], a[176:179], v[116:119], v[12:15]\n"
"s_waitcnt lgkmcnt(7)\n"
"ds_read_b64_tr_b16 a[142:143], v235 offset:8192\n"
"v_mfma_f32_16x16x32_bf16 v[28:31], a[180:183], v[116:119], v[28:31]\n"
"ds_read_b64_tr_b16 a[144:145], v232 offset:16384\n"
"v_mfma_f32_16x16x32_bf16 v[24:27], a[180:183], v[112:115], v[24:27]\n"
"ds_read_b64_tr_b16 a[146:147], v232 offset:24576\n"
"v_mfma_f32_16x16x32_bf16 v[40:43], a[184:187], v[112:115], v[40:43]\n"
"ds_read_b64_tr_b16 a[148:149], v233 offset:16384\n"
"v_mfma_f32_16x16x32_bf16 v[44:47], a[184:187], v[116:119], v[44:47]\n"
"s_waitcnt lgkmcnt(7)\n"
"ds_read_b64_tr_b16 a[150:151], v233 offset:24576\n"
"v_mfma_f32_16x16x32_bf16 v[60:63], a[188:191], v[116:119], v[60:63]\n"
"ds_read_b64_tr_b16 a[152:153], v234 offset:16384\n"
"v_mfma_f32_16x16x32_bf16 v[56:59], a[188:191], v[112:115], v[56:59]\n"
"ds_read_b64_tr_b16 a[154:155], v234 offset:24576\n"
"v_mfma_f32_16x16x32_bf16 v[72:75], a[128:131], v[0:3], 0\n"
"ds_read_b64_tr_b16 a[156:157], v235 offset:16384\n"
"v_mfma_f32_16x16x32_bf16 v[76:79], a[128:131], v[4:7], 0\n"
"s_waitcnt lgkmcnt(7)\n"
"ds_read_b64_tr_b16 a[158:159], v235 offset:24576\n"
"v_mfma_f32_16x16x32_bf16 v[92:95], a[132:135], v[4:7], 0\n"
"ds_read_b64_tr_b16 a[160:161], v232 offset:1024\n"
"v_mfma_f32_16x16x32_bf16 v[88:91], a[132:135], v[0:3], 0\n"
"ds_read_b64_tr_b16 a[162:163], v232 offset:9216\n"
"v_cvt_pk_bf16_f32 v8, v8, v9\n"
"v_mfma_f32_16x16x32_bf16 v[104:107], a[136:139], v[0:3], 0\n"
"ds_read_b64_tr_b16 a[164:165], v233 offset:1024\n"
"v_cvt_pk_bf16_f32 v9, v10, v11\n"
"v_mfma_f32_16x16x32_bf16 v[108:111], a[136:139], v[4:7], 0\n"
"s_waitcnt lgkmcnt(7)\n"
"ds_read_b64_tr_b16 a[166:167], v233 offset:9216\n"
"v_mfma_f32_16x16x32_bf16 v[124:127], a[140:143], v[4:7], 0\n"
"ds_read_b64_tr_b16 a[168:169], v234 offset:1024\n"
"v_cvt_pk_bf16_f32 v10, v12, v13\n"
"v_mfma_f32_16x16x32_bf16 v[120:123], a[140:143], v[0:3], 0\n"
"ds_read_b64_tr_b16 a[170:171], v234 offset:9216\n"
"v_cvt_pk_bf16_f32 v11, v14, v15\n"
"v_mfma_f32_16x16x32_bf16 v[72:75], a[144:147], v[16:19], v[72:75]\n"
"ds_read_b64_tr_b16 a[172:173], v235 offset:1024\n"
"s_mov_b32 s86, 0x40000\n"
"buffer_store_dwordx4 v[8:11], v239, s[80:83], s86 offen sc1\n"
"v_mfma_f32_16x16x32_bf16 v[76:79], a[144:147], v[20:23], v[76:79]\n"
"s_waitcnt lgkmcnt(7)\n"
"ds_read_b64_tr_b16 a[174:175], v235 offset:9216\n"
"v_mfma_f32_16x16x32_bf16 v[92:95], a[148:151], v[20:23], v[92:95]\n"
"ds_read_b64_tr_b16 a[176:177], v232 offset:17408\n"
"v_cvt_pk_bf16_f32 v24, v24, v25\n"
"v_mfma_f32_16x16x32_bf16 v[88:91], a[148:151], v[16:19], v[88:91]\n"
"ds_read_b64_tr_b16 a[178:179], v232 offset:25600\n"
"v_cvt_pk_bf16_f32 v25, v26, v27\n"
"v_mfma_f32_16x16x32_bf16 v[104:107], a[152:155], v[16:19], v[104:107]\n"
"ds_read_b64_tr_b16 a[180:181], v233 offset:17408\n"
"v_cvt_pk_bf16_f32 v26, v28, v29\n"
"v_mfma_f32_16x16x32_bf16 v[108:111], a[152:155], v[20:23], v[108:111]\n"
"s_waitcnt lgkmcnt(7)\n"
"ds_read_b64_tr_b16 a[182:183], v233 offset:25600\n"
"v_mfma_f32_16x16x32_bf16 v[124:127], a[156:159], v[20:23], v[124:127]\n"
"ds_read_b64_tr_b16 a[184:185], v234 offset:17408\n"
"v_cvt_pk_bf16_f32 v27, v30, v31\n"
"v_mfma_f32_16x16x32_bf16 v[120:123], a[156:159], v[16:19], v[120:123]\n"
"ds_read_b64_tr_b16 a[186:187], v234 offset:25600\n"
"v_cvt_pk_bf16_f32 v40, v40, v41\n"
"v_mfma_f32_16x16x32_bf16 v[72:75], a[160:163], v[32:35], v[72:75]\n"
"ds_read_b64_tr_b16 a[188:189], v235 offset:17408\n"
"v_cvt_pk_bf16_f32 v41, v42, v43\n"
"v_mfma_f32_16x16x32_bf16 v[76:79], a[160:163], v[36:39], v[76:79]\n"
"s_waitcnt lgkmcnt(7)\n"
"ds_read_b64_tr_b16 a[190:191], v235 offset:25600\n"
"v_mfma_f32_16x16x32_bf16 v[92:95], a[164:167], v[36:39], v[92:95]\n"
"ds_read_b64_tr_b16 a[128:129], v232 offset:2048\n"
"v_cvt_pk_bf16_f32 v42, v44, v45\n"
"v_mfma_f32_16x16x32_bf16 v[88:91], a[164:167], v[32:35], v[88:91]\n"
"ds_read_b64_tr_b16 a[130:131], v232 offset:10240\n"
"v_cvt_pk_bf16_f32 v43, v46, v47\n"
"v_mfma_f32_16x16x32_bf16 v[104:107], a[168:171], v[32:35], v[104:107]\n"
"ds_read_b64_tr_b16 a[132:133], v233 offset:2048\n"
"s_mov_b32 s86, 0x44000\n"
"buffer_store_dwordx4 v[24:27], v239, s[80:83], s86 offen sc1\n"
"v_mfma_f32_16x16x32_bf16 v[108:111], a[168:171], v[36:39], v[108:111]\n"
"s_waitcnt lgkmcnt(7)\n"
"ds_read_b64_tr_b16 a[134:135], v233 offset:10240\n"
"v_mfma_f32_16x16x32_bf16 v[124:127], a[172:175], v[36:39], v[124:127]\n"
"ds_read_b64_tr_b16 a[136:137], v234 offset:2048\n"
"v_cvt_pk_bf16_f32 v56, v56, v57\n"
"v_mfma_f32_16x16x32_bf16 v[120:123], a[172:175], v[32:35], v[120:123]\n"
"ds_read_b64_tr_b16 a[138:139], v234 offset:10240\n"
"v_cvt_pk_bf16_f32 v57, v58, v59\n"
"v_mfma_f32_16x16x32_bf16 v[72:75], a[176:179], v[48:51], v[72:75]\n"
"ds_read_b64_tr_b16 a[140:141], v235 offset:2048\n"
"v_cvt_pk_bf16_f32 v58, v60, v61\n"
"v_mfma_f32_16x16x32_bf16 v[76:79], a[176:179], v[52:55], v[76:79]\n"
"s_waitcnt lgkmcnt(7)\n"
"ds_read_b64_tr_b16 a[142:143], v235 offset:10240\n"
"v_mfma_f32_16x16x32_bf16 v[92:95], a[180:183], v[52:55], v[92:95]\n"
"ds_read_b64_tr_b16 a[144:145], v232 offset:18432\n"
"v_cvt_pk_bf16_f32 v59, v62, v63\n"
"v_mfma_f32_16x16x32_bf16 v[88:91], a[180:183], v[48:51], v[88:91]\n"
"ds_read_b64_tr_b16 a[146:147], v232 offset:26624\n"
"v_mfma_f32_16x16x32_bf16 v[104:107], a[184:187], v[48:51], v[104:107]\n"
"ds_read_b64_tr_b16 a[148:149], v233 offset:18432\n"
"v_mfma_f32_16x16x32_bf16 v[108:111], a[184:187], v[52:55], v[108:111]\n"
"s_waitcnt lgkmcnt(7)\n"
"ds_read_b64_tr_b16 a[150:151], v233 offset:26624\n"
"v_mfma_f32_16x16x32_bf16 v[124:127], a[188:191], v[52:55], v[124:127]\n"
"ds_read_b64_tr_b16 a[152:153], v234 offset:18432\n"
"s_waitcnt vmcnt(2)\n"
"s_mov_b64 exec, 1\n"
"global_store_dwordx2 v196, v[198:199], s[94:95] offset:1024 sc1\n"
"s_mov_b64 exec, -1\n"
"v_mfma_f32_16x16x32_bf16 v[120:123], a[188:191], v[48:51], v[120:123]\n"
"ds_read_b64_tr_b16 a[154:155], v234 offset:26624\n"
"v_mfma_f32_16x16x32_bf16 v[72:75], a[128:131], v[64:67], v[72:75]\n"
"ds_read_b64_tr_b16 a[156:157], v235 offset:18432\n"
"s_mov_b32 s86, 0x48000\n"
"buffer_store_dwordx4 v[40:43], v239, s[80:83], s86 offen sc1\n"
"v_mfma_f32_16x16x32_bf16 v[76:79], a[128:131], v[68:71], v[76:79]\n"
"s_waitcnt lgkmcnt(7)\n"
"ds_read_b64_tr_b16 a[158:159], v235 offset:26624\n"
"v_mfma_f32_16x16x32_bf16 v[92:95], a[132:135], v[68:71], v[92:95]\n"
"ds_read_b64_tr_b16 a[160:161], v232 offset:3072\n"
"v_mfma_f32_16x16x32_bf16 v[88:91], a[132:135], v[64:67], v[88:91]\n"
"ds_read_b64_tr_b16 a[162:163], v232 offset:11264\n"
"v_mfma_f32_16x16x32_bf16 v[104:107], a[136:139], v[64:67], v[104:107]\n"
"ds_read_b64_tr_b16 a[164:165], v233 offset:3072\n"
"v_mfma_f32_16x16x32_bf16 v[108:111], a[136:139], v[68:71], v[108:111]\n"
"s_waitcnt lgkmcnt(7)\n"
"ds_read_b64_tr_b16 a[166:167], v233 offset:11264\n"
"v_mfma_f32_16x16x32_bf16 v[124:127], a[140:143], v[68:71], v[124:127]\n"
"ds_read_b64_tr_b16 a[168:169], v234 offset:3072\n"
"v_mfma_f32_16x16x32_bf16 v[120:123], a[140:143], v[64:67], v[120:123]\n"
"ds_read_b64_tr_b16 a[170:171], v234 offset:11264\n"
"v_mfma_f32_16x16x32_bf16 v[72:75], a[144:147], v[80:83], v[72:75]\n"
"ds_read_b64_tr_b16 a[172:173], v235 offset:3072\n"
"v_mfma_f32_16x16x32_bf16 v[76:79], a[144:147], v[84:87], v[76:79]\n"
"s_waitcnt lgkmcnt(7)\n"
"ds_read_b64_tr_b16 a[174:175], v235 offset:11264\n"
"v_mfma_f32_16x16x32_bf16 v[92:95], a[148:151], v[84:87], v[92:95]\n"
"ds_read_b64_tr_b16 a[176:177], v232 offset:19456\n"
"v_mfma_f32_16x16x32_bf16 v[88:91], a[148:151], v[80:83], v[88:91]\n"
"ds_read_b64_tr_b16 a[178:179], v232 offset:27648\n"
"v_add_u32 v232, 0x20000, v232\n"
"v_mfma_f32_16x16x32_bf16 v[104:107], a[152:155], v[80:83], v[104:107]\n"
"ds_read_b64_tr_b16 a[180:181], v233 offset:19456\n"
"s_mov_b32 s86, 0x4c000\n"
"buffer_store_dwordx4 v[56:59], v239, s[80:83], s86 offen sc1\n"
"v_mfma_f32_16x16x32_bf16 v[108:111], a[152:155], v[84:87], v[108:111]\n"
"s_waitcnt lgkmcnt(7)\n"
"ds_read_b64_tr_b16 a[182:183], v233 offset:27648\n"
"v_mfma_f32_16x16x32_bf16 v[124:127], a[156:159], v[84:87], v[124:127]\n"
"ds_read_b64_tr_b16 a[184:185], v234 offset:19456\n"
"v_add_u32 v233, 0x20000, v233\n"
"v_mfma_f32_16x16x32_bf16 v[120:123], a[156:159], v[80:83], v[120:123]\n"
"ds_read_b64_tr_b16 a[186:187], v234 offset:27648\n"
"v_add_u32 v234, 0x20000, v234\n"
"v_mfma_f32_16x16x32_bf16 v[72:75], a[160:163], v[96:99], v[72:75]\n"
"ds_read_b64_tr_b16 a[188:189], v235 offset:19456\n"
"v_mfma_f32_16x16x32_bf16 v[76:79], a[160:163], v[100:103], v[76:79]\n"
"ds_read_b64_tr_b16 a[190:191], v235 offset:27648\n"
"s_waitcnt vmcnt(12)\n"
"s_barrier\n"
"v_mfma_f32_16x16x32_bf16 v[92:95], a[164:167], v[100:103], v[92:95]\n"
"s_waitcnt lgkmcnt(8)\n"
"ds_read_b64_tr_b16 a[128:129], v232 offset:0\n"
"v_mfma_f32_16x16x32_bf16 v[88:91], a[164:167], v[96:99], v[88:91]\n"
"ds_read_b64_tr_b16 a[130:131], v232 offset:8192\n"
"v_add_u32 v235, 0x20000, v235\n"
"v_mfma_f32_16x16x32_bf16 v[104:107], a[168:171], v[96:99], v[104:107]\n"
"ds_read_b64_tr_b16 a[132:133], v233 offset:0\n"
"v_mfma_f32_16x16x32_bf16 v[108:111], a[168:171], v[100:103], v[108:111]\n"
"s_waitcnt lgkmcnt(7)\n"
"ds_read_b64_tr_b16 a[134:135], v233 offset:8192\n"
"v_mfma_f32_16x16x32_bf16 v[124:127], a[172:175], v[100:103], v[124:127]\n"
"ds_read_b64_tr_b16 a[136:137], v234 offset:0\n"
"v_mfma_f32_16x16x32_bf16 v[120:123], a[172:175], v[96:99], v[120:123]\n"
"ds_read_b64_tr_b16 a[138:139], v234 offset:8192\n"
"v_mfma_f32_16x16x32_bf16 v[72:75], a[176:179], v[112:115], v[72:75]\n"
"ds_read_b64_tr_b16 a[140:141], v235 offset:0\n"
"v_mfma_f32_16x16x32_bf16 v[76:79], a[176:179], v[116:119], v[76:79]\n"
"s_waitcnt lgkmcnt(7)\n"
"ds_read_b64_tr_b16 a[142:143], v235 offset:8192\n"
"v_mfma_f32_16x16x32_bf16 v[92:95], a[180:183], v[116:119], v[92:95]\n"
"ds_read_b64_tr_b16 a[144:145], v232 offset:16384\n"
"v_mfma_f32_16x16x32_bf16 v[88:91], a[180:183], v[112:115], v[88:91]\n"
"ds_read_b64_tr_b16 a[146:147], v232 offset:24576\n"
"v_mfma_f32_16x16x32_bf16 v[104:107], a[184:187], v[112:115], v[104:107]\n"
"ds_read_b64_tr_b16 a[148:149], v233 offset:16384\n"
"v_mfma_f32_16x16x32_bf16 v[108:111], a[184:187], v[116:119], v[108:111]\n"
"s_waitcnt lgkmcnt(7)\n"
"ds_read_b64_tr_b16 a[150:151], v233 offset:24576\n"
"v_mfma_f32_16x16x32_bf16 v[124:127], a[188:191], v[116:119], v[124:127]\n"
"ds_read_b64_tr_b16 a[152:153], v234 offset:16384\n"
"v_mfma_f32_16x16x32_bf16 v[120:123], a[188:191], v[112:115], v[120:123]\n"
"ds_read_b64_tr_b16 a[154:155], v234 offset:24576\n"
"v_mfma_f32_16x16x32_bf16 v[8:11], a[128:131], v[0:3], 0\n"
"ds_read_b64_tr_b16 a[156:157], v235 offset:16384\n"
"v_mfma_f32_16x16x32_bf16 v[12:15], a[128:131], v[4:7], 0\n"
"s_waitcnt lgkmcnt(7)\n"
"ds_read_b64_tr_b16 a[158:159], v235 offset:24576\n"
"v_mfma_f32_16x16x32_bf16 v[28:31], a[132:135], v[4:7], 0\n"
"ds_read_b64_tr_b16 a[160:161], v232 offset:1024\n"
"v_mfma_f32_16x16x32_bf16 v[24:27], a[132:135], v[0:3], 0\n"
"ds_read_b64_tr_b16 a[162:163], v232 offset:9216\n"
"v_cvt_pk_bf16_f32 v72, v72, v73\n"
"v_mfma_f32_16x16x32_bf16 v[40:43], a[136:139], v[0:3], 0\n"
"ds_read_b64_tr_b16 a[164:165], v233 offset:1024\n"
"v_cvt_pk_bf16_f32 v73, v74, v75\n"
"v_mfma_f32_16x16x32_bf16 v[44:47], a[136:139], v[4:7], 0\n"
"s_waitcnt lgkmcnt(7)\n"
"ds_read_b64_tr_b16 a[166:167], v233 offset:9216\n"
"v_mfma_f32_16x16x32_bf16 v[60:63], a[140:143], v[4:7], 0\n"
"ds_read_b64_tr_b16 a[168:169], v234 offset:1024\n"
"v_cvt_pk_bf16_f32 v74, v76, v77\n"
"v_mfma_f32_16x16x32_bf16 v[56:59], a[140:143], v[0:3], 0\n"
"ds_read_b64_tr_b16 a[170:171], v234 offset:9216\n"
"v_cvt_pk_bf16_f32 v75, v78, v79\n"
"v_mfma_f32_16x16x32_bf16 v[8:11], a[144:147], v[16:19], v[8:11]\n"
"s_mov_b32 s86, 0x50000\n"
"buffer_store_dwordx4 v[72:75], v239, s[80:83], s86 offen sc1\n"
"v_mfma_f32_16x16x32_bf16 v[12:15], a[144:147], v[20:23], v[12:15]\n"
"s_waitcnt lgkmcnt(6)\n"
"ds_read_b64_tr_b16 a[172:173], v235 offset:1024\n"
"v_mfma_f32_16x16x32_bf16 v[28:31], a[148:151], v[20:23], v[28:31]\n"
"ds_read_b64_tr_b16 a[174:175], v235 offset:9216\n"
"v_cvt_pk_bf16_f32 v88, v88, v89\n"
"v_mfma_f32_16x16x32_bf16 v[24:27], a[148:151], v[16:19], v[24:27]\n"
"ds_read_b64_tr_b16 a[176:177], v232 offset:17408\n"
"v_cvt_pk_bf16_f32 v89, v90, v91\n"
"v_mfma_f32_16x16x32_bf16 v[40:43], a[152:155], v[16:19], v[40:43]\n"
"ds_read_b64_tr_b16 a[178:179], v232 offset:25600\n"
"v_cvt_pk_bf16_f32 v90, v92, v93\n"
"v_mfma_f32_16x16x32_bf16 v[44:47], a[152:155], v[20:23], v[44:47]\n"
"s_waitcnt lgkmcnt(6)\n"
"ds_read_b64_tr_b16 a[180:181], v233 offset:17408\n"
"v_mfma_f32_16x16x32_bf16 v[60:63], a[156:159], v[20:23], v[60:63]\n"
"ds_read_b64_tr_b16 a[182:183], v233 offset:25600\n"
"v_cvt_pk_bf16_f32 v91, v94, v95\n"
"v_mfma_f32_16x16x32_bf16 v[56:59], a[156:159], v[16:19], v[56:59]\n"
"ds_read_b64_tr_b16 a[184:185], v234 offset:17408\n"
"v_cvt_pk_bf16_f32 v104, v104, v105\n"
"v_mfma_f32_16x16x32_bf16 v[8:11], a[160:163], v[32:35], v[8:11]\n"
"ds_read_b64_tr_b16 a[186:187], v234 offset:25600\n"
"v_cvt_pk_bf16_f32 v105, v106, v107\n"
"v_mfma_f32_16x16x32_bf16 v[12:15], a[160:163], v[36:39], v[12:15]\n"
"s_waitcnt lgkmcnt(6)\n"
"ds_read_b64_tr_b16 a[188:189], v235 offset:17408\n"
"v_mfma_f32_16x16x32_bf16 v[28:31], a[164:167], v[36:39], v[28:31]\n"
"ds_read_b64_tr_b16 a[190:191], v235 offset:25600\n"
"v_cvt_pk_bf16_f32 v106, v108, v109\n"
"v_mfma_f32_16x16x32_bf16 v[24:27], a[164:167], v[32:35], v[24:27]\n"
"ds_read_b64_tr_b16 a[128:129], v232 offset:2048\n"
"v_cvt_pk_bf16_f32 v107, v110, v111\n"
"v_mfma_f32_16x16x32_bf16 v[40:43], a[168:171], v[32:35], v[40:43]\n"
"ds_read_b64_tr_b16 a[130:131], v232 offset:10240\n"
"s_mov_b32 s86, 0x54000\n"
"buffer_store_dwordx4 v[88:91], v239, s[80:83], s86 offen sc1\n"
"v_mfma_f32_16x16x32_bf16 v[44:47], a[168:171], v[36:39], v[44:47]\n"
"s_waitcnt lgkmcnt(6)\n"
"ds_read_b64_tr_b16 a[132:133], v233 offset:2048\n"
"v_mfma_f32_16x16x32_bf16 v[60:63], a[172:175], v[36:39], v[60:63]\n"
"ds_read_b64_tr_b16 a[134:135], v233 offset:10240\n"
"v_cvt_pk_bf16_f32 v120, v120, v121\n"
"v_mfma_f32_16x16x32_bf16 v[56:59], a[172:175], v[32:35], v[56:59]\n"
"ds_read_b64_tr_b16 a[136:137], v234 offset:2048\n"
"v_cvt_pk_bf16_f32 v121, v122, v123\n"
"v_mfma_f32_16x16x32_bf16 v[8:11], a[176:179], v[48:51], v[8:11]\n"
"ds_read_b64_tr_b16 a[138:139], v234 offset:10240\n"
"global_load_dwordx2 v[224:225], v197, s[94:95] offset:1024 sc1\n"
"v_mfma_f32_16x16x32_bf16 v[12:15], a[176:179], v[52:55], v[12:15]\n"
"s_waitcnt lgkmcnt(6)\n"
"ds_read_b64_tr_b16 a[140:141], v235 offset:2048\n"
"v_mfma_f32_16x16x32_bf16 v[28:31], a[180:183], v[52:55], v[28:31]\n"
"ds_read_b64_tr_b16 a[142:143], v235 offset:10240\n"
"v_cvt_pk_bf16_f32 v122, v124, v125\n"
"v_mfma_f32_16x16x32_bf16 v[24:27], a[180:183], v[48:51], v[24:27]\n"
"ds_read_b64_tr_b16 a[144:145], v232 offset:18432\n"
"v_cvt_pk_bf16_f32 v123, v126, v127\n"
"v_mfma_f32_16x16x32_bf16 v[40:43], a[184:187], v[48:51], v[40:43]\n"
"ds_read_b64_tr_b16 a[146:147], v232 offset:26624\n"
"v_mfma_f32_16x16x32_bf16 v[44:47], a[184:187], v[52:55], v[44:47]\n"
"s_waitcnt lgkmcnt(6)\n"
"ds_read_b64_tr_b16 a[148:149], v233 offset:18432\n"
"v_mfma_f32_16x16x32_bf16 v[60:63], a[188:191], v[52:55], v[60:63]\n"
"ds_read_b64_tr_b16 a[150:151], v233 offset:26624\n"
"v_mfma_f32_16x16x32_bf16 v[56:59], a[188:191], v[48:51], v[56:59]\n"
"ds_read_b64_tr_b16 a[152:153], v234 offset:18432\n"
"v_mfma_f32_16x16x32_bf16 v[8:11], a[128:131], v[64:67], v[8:11]\n"
"ds_read_b64_tr_b16 a[154:155], v234 offset:26624\n"
"s_mov_b32 s86, 0x58000\n"
"buffer_store_dwordx4 v[104:107], v239, s[80:83], s86 offen sc1\n"
"v_mfma_f32_16x16x32_bf16 v[12:15], a[128:131], v[68:71], v[12:15]\n"
"s_waitcnt lgkmcnt(6)\n"
"ds_read_b64_tr_b16 a[156:157], v235 offset:18432\n"
"v_mfma_f32_16x16x32_bf16 v[28:31], a[132:135], v[68:71], v[28:31]\n"
"ds_read_b64_tr_b16 a[158:159], v235 offset:26624\n"
"v_mfma_f32_16x16x32_bf16 v[24:27], a[132:135], v[64:67], v[24:27]\n"
"ds_read_b64_tr_b16 a[160:161], v232 offset:3072\n"
"v_mfma_f32_16x16x32_bf16 v[40:43], a[136:139], v[64:67], v[40:43]\n"
"ds_read_b64_tr_b16 a[162:163], v232 offset:11264\n"
"v_mfma_f32_16x16x32_bf16 v[44:47], a[136:139], v[68:71], v[44:47]\n"
"s_waitcnt lgkmcnt(6)\n"
"ds_read_b64_tr_b16 a[164:165], v233 offset:3072\n"
"v_mfma_f32_16x16x32_bf16 v[60:63], a[140:143], v[68:71], v[60:63]\n"
"ds_read_b64_tr_b16 a[166:167], v233 offset:11264\n"
"v_mfma_f32_16x16x32_bf16 v[56:59], a[140:143], v[64:67], v[56:59]\n"
"ds_read_b64_tr_b16 a[168:169], v234 offset:3072\n"
"v_mfma_f32_16x16x32_bf16 v[8:11], a[144:147], v[80:83], v[8:11]\n"
"ds_read_b64_tr_b16 a[170:171], v234 offset:11264\n"
"v_mfma_f32_16x16x32_bf16 v[12:15], a[144:147], v[84:87], v[12:15]\n"
"s_waitcnt lgkmcnt(6)\n"
"ds_read_b64_tr_b16 a[172:173], v235 offset:3072\n"
"v_mfma_f32_16x16x32_bf16 v[28:31], a[148:151], v[84:87], v[28:31]\n"
"ds_read_b64_tr_b16 a[174:175], v235 offset:11264\n"
"v_mfma_f32_16x16x32_bf16 v[24:27], a[148:151], v[80:83], v[24:27]\n"
"ds_read_b64_tr_b16 a[176:177], v232 offset:19456\n"
"v_mfma_f32_16x16x32_bf16 v[40:43], a[152:155], v[80:83], v[40:43]\n"
"ds_read_b64_tr_b16 a[178:179], v232 offset:27648\n"
"s_mov_b32 s86, 0x5c000\n"
"buffer_store_dwordx4 v[120:123], v239, s[80:83], s86 offen sc1\n"
"v_mfma_f32_16x16x32_bf16 v[44:47], a[152:155], v[84:87], v[44:47]\n"
"s_waitcnt lgkmcnt(6)\n"
"ds_read_b64_tr_b16 a[180:181], v233 offset:19456\n"
"v_mfma_f32_16x16x32_bf16 v[60:63], a[156:159], v[84:87], v[60:63]\n"
"ds_read_b64_tr_b16 a[182:183], v233 offset:27648\n"
"v_mfma_f32_16x16x32_bf16 v[56:59], a[156:159], v[80:83], v[56:59]\n"
"ds_read_b64_tr_b16 a[184:185], v234 offset:19456\n"
"v_mfma_f32_16x16x32_bf16 v[8:11], a[160:163], v[96:99], v[8:11]\n"
"ds_read_b64_tr_b16 a[186:187], v234 offset:27648\n"
"v_mfma_f32_16x16x32_bf16 v[12:15], a[160:163], v[100:103], v[12:15]\n"
"s_waitcnt lgkmcnt(6)\n"
"ds_read_b64_tr_b16 a[188:189], v235 offset:19456\n"
"v_mfma_f32_16x16x32_bf16 v[28:31], a[164:167], v[100:103], v[28:31]\n"
"ds_read_b64_tr_b16 a[190:191], v235 offset:27648\n"
"v_mfma_f32_16x16x32_bf16 v[24:27], a[164:167], v[96:99], v[24:27]\n"
"v_mfma_f32_16x16x32_bf16 v[40:43], a[168:171], v[96:99], v[40:43]\n"
"v_mfma_f32_16x16x32_bf16 v[44:47], a[168:171], v[100:103], v[44:47]\n"
"s_waitcnt lgkmcnt(4)\n"
"v_mfma_f32_16x16x32_bf16 v[60:63], a[172:175], v[100:103], v[60:63]\n"
"v_mfma_f32_16x16x32_bf16 v[56:59], a[172:175], v[96:99], v[56:59]\n"
"v_mfma_f32_16x16x32_bf16 v[8:11], a[176:179], v[112:115], v[8:11]\n"
"v_mfma_f32_16x16x32_bf16 v[12:15], a[176:179], v[116:119], v[12:15]\n"
"s_waitcnt lgkmcnt(0)\n"
"v_mfma_f32_16x16x32_bf16 v[28:31], a[180:183], v[116:119], v[28:31]\n"
"v_mfma_f32_16x16x32_bf16 v[24:27], a[180:183], v[112:115], v[24:27]\n"
"v_mfma_f32_16x16x32_bf16 v[40:43], a[184:187], v[112:115], v[40:43]\n"
"v_mfma_f32_16x16x32_bf16 v[44:47], a[184:187], v[116:119], v[44:47]\n"
"v_mfma_f32_16x16x32_bf16 v[60:63], a[188:191], v[116:119], v[60:63]\n"
"v_mfma_f32_16x16x32_bf16 v[56:59], a[188:191], v[112:115], v[56:59]\n"
"s_nop 0\n"
"s_nop 7\n"
"s_nop 0\n"
"v_cvt_pk_bf16_f32 v8, v8, v9\n"
"v_cvt_pk_bf16_f32 v9, v10, v11\n"
"v_cvt_pk_bf16_f32 v10, v12, v13\n"
"v_cvt_pk_bf16_f32 v24, v24, v25\n"
"v_cvt_pk_bf16_f32 v11, v14, v15\n"
"v_cvt_pk_bf16_f32 v25, v26, v27\n"
"s_nop 0\n"
"v_cvt_pk_bf16_f32 v26, v28, v29\n"
"v_cvt_pk_bf16_f32 v40, v40, v41\n"
"v_cvt_pk_bf16_f32 v27, v30, v31\n"
"v_cvt_pk_bf16_f32 v41, v42, v43\n"
"v_cvt_pk_bf16_f32 v42, v44, v45\n"
"v_cvt_pk_bf16_f32 v56, v56, v57\n"
"s_nop 0\n"
"v_cvt_pk_bf16_f32 v43, v46, v47\n"
"v_cvt_pk_bf16_f32 v57, v58, v59\n"
"v_cvt_pk_bf16_f32 v58, v60, v61\n"
"v_cvt_pk_bf16_f32 v59, v62, v63\n"
"s_nop 0\n"
"s_mov_b32 s86, 0x60000\n"
"buffer_store_dwordx4 v[8:11], v239, s[80:83], s86 offen sc1\n"
"s_nop 0\n"
"s_mov_b32 s86, 0x64000\n"
"buffer_store_dwordx4 v[24:27], v239, s[80:83], s86 offen sc1\n"
"s_nop 0\n"
"s_mov_b32 s86, 0x68000\n"
"buffer_store_dwordx4 v[40:43], v239, s[80:83], s86 offen sc1\n"
"s_nop 0\n"
"s_mov_b32 s86, 0x6c000\n"
"buffer_store_dwordx4 v[56:59], v239, s[80:83], s86 offen sc1\n"
"s_waitcnt vmcnt(63)\n"
"v_mfma_f32_16x16x32_bf16 v[148:151], v[128:131], a[192:195], 0\n"
"v_mfma_f32_16x16x32_bf16 v[148:151], v[132:135], a[196:199], v[148:151]\n"
"v_mfma_f32_16x16x32_bf16 v[152:155], v[128:131], a[200:203], 0\n"
"v_mfma_f32_16x16x32_bf16 v[152:155], v[132:135], a[204:207], v[152:155]\n"
"v_mfma_f32_16x16x32_bf16 v[156:159], v[128:131], a[208:211], 0\n"
"v_mfma_f32_16x16x32_bf16 v[156:159], v[132:135], a[212:215], v[156:159]\n"
"v_mfma_f32_16x16x32_bf16 v[160:163], v[128:131], a[216:219], 0\n"
"s_waitcnt vmcnt(6)\n"
"v_cmp_ne_u64 vcc, s[96:97], v[224:225]\n"
"s_cbranch_vccz .Lpok_4_%=\n"
"s_mov_b32 s90, 0x20000\n"
".Lpsp_4_%=:\n"
"global_load_dwordx2 v[224:225], v197, s[94:95] offset:1024 sc1\n"
"s_waitcnt vmcnt(0)\n"
"v_cmp_ne_u64 vcc, s[96:97], v[224:225]\n"
"s_cbranch_vccz .Lpok_4_%=\n"
"s_sleep 1\n"
"s_sub_u32 s90, s90, 1\n"
"s_cmp_lg_u32 s90, 0\n"
"s_cbranch_scc1 .Lpsp_4_%=\n"
".Lpok_4_%=:\n"
"s_mov_b32 s86, 0\n"
"buffer_load_dwordx4 a[0:3], v244, s[56:59], s86 offen sc1\n"
"s_mov_b32 s86, 4096\n"
"buffer_load_dwordx4 a[4:7], v244, s[56:59], s86 offen sc1\n"
"s_mov_b32 s86, 8192\n"
"buffer_load_dwordx4 a[8:11], v244, s[56:59], s86 offen sc1\n"
"s_mov_b32 s86, 12288\n"
"buffer_load_dwordx4 a[12:15], v244, s[56:59], s86 offen sc1\n"
"s_waitcnt vmcnt(4)\n"
"s_mov_b64 exec, 1\n"
"global_store_dwordx2 v196, v[198:199], s[94:95] offset:1536 sc1\n"
"s_mov_b64 exec, -1\n"
"s_mov_b32 s86, 65536\n"
"buffer_load_dwordx4 a[16:19], v244, s[56:59], s86 offen sc1\n"
"s_mov_b32 s86, 69632\n"
"buffer_load_dwordx4 a[20:23], v244, s[56:59], s86 offen sc1\n"
"s_mov_b32 s86, 73728\n"
"buffer_load_dwordx4 a[24:27], v244, s[56:59], s86 offen sc1\n"
"s_mov_b32 s86, 77824\n"
"buffer_load_dwordx4 a[28:31], v244, s[56:59], s86 offen sc1\n"
"s_mov_b32 s86, 131072\n"
"buffer_load_dwordx4 a[32:35], v244, s[56:59], s86 offen sc1\n"
"s_mov_b32 s86, 135168\n"
"buffer_load_dwordx4 a[36:39], v244, s[56:59], s86 offen sc1\n"
"s_mov_b32 s86, 139264\n"
"buffer_load_dwordx4 a[40:43], v244, s[56:59], s86 offen sc1\n"
"s_mov_b32 s86, 143360\n"
"buffer_load_dwordx4 a[44:47], v244, s[56:59], s86 offen sc1\n"
"s_mov_b32 s86, 196608\n"
"buffer_load_dwordx4 a[48:51], v244, s[56:59], s86 offen sc1\n"
"s_mov_b32 s86, 200704\n"
"buffer_load_dwordx4 a[52:55], v244, s[56:59], s86 offen sc1\n"
"s_mov_b32 s86, 204800\n"
"buffer_load_dwordx4 a[56:59], v244, s[56:59], s86 offen sc1\n"
"s_mov_b32 s86, 208896\n"
"buffer_load_dwordx4 a[60:63], v244, s[56:59], s86 offen sc1\n"
"v_lshlrev_b32 v180, 16, v200\n"
"v_and_b32 v181, 0xffff0000, v200\n"
"v_lshlrev_b32 v182, 16, v201\n"
"v_and_b32 v183, 0xffff0000, v201\n"
"v_lshlrev_b32 v184, 16, v202\n"
"v_and_b32 v185, 0xffff0000, v202\n"
"v_lshlrev_b32 v186, 16, v203\n"
"v_and_b32 v187, 0xffff0000, v203\n"
"v_lshlrev_b32 v188, 16, v204\n"
"v_and_b32 v189, 0xffff0000, v204\n"
"v_lshlrev_b32 v190, 16, v205\n"
"v_and_b32 v191, 0xffff0000, v205\n"
"v_lshlrev_b32 v192, 16, v206\n"
"v_and_b32 v193, 0xffff0000, v206\n"
"v_lshlrev_b32 v194, 16, v207\n"
"v_and_b32 v195, 0xffff0000, v207\n"
"v_mul_f32 v208, 0xbfb8aa3b, v148\n"
"v_mul_f32 v209, 0xbfb8aa3b, v149\n"
"v_mul_f32 v210, 0xbfb8aa3b, v150\n"
"v_mul_f32 v211, 0xbfb8aa3b, v151\n"
"v_mul_f32 v212, 0xbfb8aa3b, v152\n"
"v_mul_f32 v213, 0xbfb8aa3b, v153\n"
"v_mul_f32 v214, 0xbfb8aa3b, v154\n"
"v_mul_f32 v215, 0xbfb8aa3b, v155\n"
"v_mul_f32 v216, 0xbfb8aa3b, v156\n"
"v_mul_f32 v217, 0xbfb8aa3b, v157\n"
"v_mul_f32 v218, 0xbfb8aa3b, v158\n"
"v_mul_f32 v219, 0xbfb8aa3b, v159\n"
"v_mul_f32 v220, 0xbfb8aa3b, v160\n"
"v_mul_f32 v221, 0xbfb8aa3b, v161\n"
"v_mul_f32 v222, 0xbfb8aa3b, v162\n"
"v_mul_f32 v223, 0xbfb8aa3b, v163\n"
"v_exp_f32 v208, v208\n"
"v_exp_f32 v209, v209\n"
"v_exp_f32 v210, v210\n"
"v_exp_f32 v211, v211\n"
"v_exp_f32 v212, v212\n"
"v_exp_f32 v213, v213\n"
"v_exp_f32 v214, v214\n"
"v_exp_f32 v215, v215\n"
"v_exp_f32 v216, v216\n"
"v_exp_f32 v217, v217\n"
"v_exp_f32 v218, v218\n"
"v_exp_f32 v219, v219\n"
"v_exp_f32 v220, v220\n"
"v_exp_f32 v221, v221\n"
"v_exp_f32 v222, v222\n"
"v_exp_f32 v223, v223\n"
"v_add_f32 v208, 1.0, v208\n"
"v_add_f32 v209, 1.0, v209\n"
"v_add_f32 v210, 1.0, v210\n"
"v_add_f32 v211, 1.0, v211\n"
"v_add_f32 v212, 1.0, v212\n"
"v_add_f32 v213, 1.0, v213\n"
"v_add_f32 v214, 1.0, v214\n"
"v_add_f32 v215, 1.0, v215\n"
"v_add_f32 v216, 1.0, v216\n"
"v_add_f32 v217, 1.0, v217\n"
"v_add_f32 v218, 1.0, v218\n"
"v_add_f32 v219, 1.0, v219\n"
"v_add_f32 v220, 1.0, v220\n"
"v_add_f32 v221, 1.0, v221\n"
"v_add_f32 v222, 1.0, v222\n"
"v_add_f32 v223, 1.0, v223\n"
"v_rcp_f32 v208, v208\n"
"v_rcp_f32 v209, v209\n"
"v_rcp_f32 v210, v210\n"
"v_rcp_f32 v211, v211\n"
"v_rcp_f32 v212, v212\n"
"v_rcp_f32 v213, v213\n"
"v_rcp_f32 v214, v214\n"
"v_rcp_f32 v215, v215\n"
"v_rcp_f32 v216, v216\n"
"v_rcp_f32 v217, v217\n"
"v_rcp_f32 v218, v218\n"
"v_rcp_f32 v219, v219\n"
"v_rcp_f32 v220, v220\n"
"v_rcp_f32 v221, v221\n"
"v_rcp_f32 v222, v222\n"
"v_rcp_f32 v223, v223\n"
"v_mul_f32 v148, v148, v208\n"
"v_mul_f32 v149, v149, v209\n"
"v_mul_f32 v150, v150, v210\n"
"v_mul_f32 v151, v151, v211\n"
"v_mul_f32 v152, v152, v212\n"
"v_mul_f32 v153, v153, v213\n"
"v_mul_f32 v154, v154, v214\n"
"v_mul_f32 v155, v155, v215\n"
"v_mul_f32 v156, v156, v216\n"
"v_mul_f32 v157, v157, v217\n"
"v_mul_f32 v158, v158, v218\n"
"v_mul_f32 v159, v159, v219\n"
"v_mul_f32 v160, v160, v220\n"
"v_mul_f32 v161, v161, v221\n"
"v_mul_f32 v162, v162, v222\n"
"v_mul_f32 v163, v163, v223\n"
"global_load_dwordx2 v[226:227], v197, s[94:95] offset:1536 sc1\n"
"s_waitcnt vmcnt(17)\n"
"v_mfma_f32_16x16x32_bf16 v[164:167], v[128:131], a[0:3], 0\n"
"s_waitcnt vmcnt(16)\n"
"v_mfma_f32_16x16x32_bf16 v[164:167], v[128:131], a[4:7], v[164:167]\n"
"s_waitcnt vmcnt(15)\n"
"v_mfma_f32_16x16x32_bf16 v[164:167], v[128:131], a[8:11], v[164:167]\n"
"s_waitcnt vmcnt(14)\n"
"v_mfma_f32_16x16x32_bf16 v[164:167], v[128:131], a[12:15], v[164:167]\n"
"s_waitcnt vmcnt(12)\n"
"v_mfma_f32_16x16x32_bf16 v[164:167], v[132:135], a[16:19], v[164:167]\n"
"s_waitcnt vmcnt(11)\n"
"v_mfma_f32_16x16x32_bf16 v[164:167], v[132:135], a[20:23], v[164:167]\n"
"s_waitcnt vmcnt(10)\n"
"v_mfma_f32_16x16x32_bf16 v[164:167], v[132:135], a[24:27], v[164:167]\n"
"s_waitcnt vmcnt(9)\n"
"v_mfma_f32_16x16x32_bf16 v[164:167], v[132:135], a[28:31], v[164:167]\n"
"s_waitcnt vmcnt(8)\n"
"v_mfma_f32_16x16x32_bf16 v[168:171], v[128:131], a[32:35], 0\n"
"s_waitcnt vmcnt(7)\n"
"v_mfma_f32_16x16x32_bf16 v[168:171], v[128:131], a[36:39], v[168:171]\n"
"s_waitcnt vmcnt(6)\n"
"v_mfma_f32_16x16x32_bf16 v[168:171], v[128:131], a[40:43], v[168:171]\n"
"s_waitcnt vmcnt(5)\n"
"v_mfma_f32_16x16x32_bf16 v[168:171], v[128:131], a[44:47], v[168:171]\n"
"s_waitcnt vmcnt(4)\n"
"v_mfma_f32_16x16x32_bf16 v[168:171], v[132:135], a[48:51], v[168:171]\n"
"s_waitcnt vmcnt(3)\n"
"v_mfma_f32_16x16x32_bf16 v[168:171], v[132:135], a[52:55], v[168:171]\n"
"s_waitcnt vmcnt(2)\n"
"v_mfma_f32_16x16x32_bf16 v[168:171], v[132:135], a[56:59], v[168:171]\n"
"s_waitcnt vmcnt(1)\n"
"v_mfma_f32_16x16x32_bf16 v[168:171], v[132:135], a[60:63], v[168:171]\n"
"s_waitcnt vmcnt(0)\n"
"v_cmp_ne_u64 vcc, s[96:97], v[226:227]\n"
"s_cbranch_vccz .Lpok_5_%=\n"
"s_mov_b32 s90, 0x20000\n"
".Lpsp_5_%=:\n"
"global_load_dwordx2 v[226:227], v197, s[94:95] offset:1536 sc1\n"
"s_waitcnt vmcnt(0)\n"
"v_cmp_ne_u64 vcc, s[96:97], v[226:227]\n"
"s_cbranch_vccz .Lpok_5_%=\n"
"s_sleep 1\n"
"s_sub_u32 s90, s90, 1\n"
"s_cmp_lg_u32 s90, 0\n"
"s_cbranch_scc1 .Lpsp_5_%=\n"
".Lpok_5_%=:\n"
"s_mov_b32 s86, 262144\n"
"buffer_load_dwordx4 a[64:67], v244, s[56:59], s86 offen sc1\n"
"s_mov_b32 s86, 266240\n"
"buffer_load_dwordx4 a[68:71], v244, s[56:59], s86 offen sc1\n"
"s_mov_b32 s86, 270336\n"
"buffer_load_dwordx4 a[72:75], v244, s[56:59], s86 offen sc1\n"
"s_mov_b32 s86, 274432\n"
"buffer_load_dwordx4 a[76:79], v244, s[56:59], s86 offen sc1\n"
"s_mov_b32 s86, 327680\n"
"buffer_load_dwordx4 a[80:83], v244, s[56:59], s86 offen sc1\n"
"s_mov_b32 s86, 331776\n"
"buffer_load_dwordx4 a[84:87], v244, s[56:59], s86 offen sc1\n"
"s_mov_b32 s86, 335872\n"
"buffer_load_dwordx4 a[88:91], v244, s[56:59], s86 offen sc1\n"
"s_mov_b32 s86, 339968\n"
"buffer_load_dwordx4 a[92:95], v244, s[56:59], s86 offen sc1\n"
"s_mov_b32 s86, 393216\n"
"buffer_load_dwordx4 a[96:99], v244, s[56:59], s86 offen sc1\n"
"s_mov_b32 s86, 397312\n"
"buffer_load_dwordx4 a[100:103], v244, s[56:59], s86 offen sc1\n"
"s_mov_b32 s86, 401408\n"
"buffer_load_dwordx4 a[104:107], v244, s[56:59], s86 offen sc1\n"
"s_mov_b32 s86, 405504\n"
"buffer_load_dwordx4 a[108:111], v244, s[56:59], s86 offen sc1\n"
"s_nop 7\n"
"v_fmac_f32 v180, v164, v148\n"
"v_fmac_f32 v181, v165, v149\n"
"v_fmac_f32 v182, v166, v150\n"
"v_fmac_f32 v183, v167, v151\n"
"v_cvt_pk_bf16_f32 v164, v180, v181\n"
"v_cvt_pk_bf16_f32 v165, v182, v183\n"
"buffer_store_dwordx2 v[164:165], v245, s[64:67], 0 offen offset:0\n"
"v_fmac_f32 v184, v168, v152\n"
"v_fmac_f32 v185, v169, v153\n"
"v_fmac_f32 v186, v170, v154\n"
"v_fmac_f32 v187, v171, v155\n"
"v_cvt_pk_bf16_f32 v168, v184, v185\n"
"v_cvt_pk_bf16_f32 v169, v186, v187\n"
"buffer_store_dwordx2 v[168:169], v245, s[64:67], 0 offen offset:256\n"
"s_mov_b64 exec, -1\n"
"s_or_b32 s89, s87, s84\n"
"s_cmp_lg_u32 s89, 0\n"
"s_cbranch_scc1 .Lnoep_6_%=\n"
"s_add_u32 s89, s96, 0x80b583ec\n"
"s_addc_u32 s90, s97, 0x61c88646\n"
"v_mov_b32 v224, s89\n"
"v_mov_b32 v225, s90\n"
"v_mov_b32 v226, 0x80000\n"
"s_mov_b64 exec, 1\n"
"global_store_dwordx2 v226, v[224:225], s[94:95] sc1\n"
"s_mov_b64 exec, -1\n"
".Lnoep_6_%=:\n"
"s_waitcnt vmcnt(13)\n"
"v_mfma_f32_16x16x32_bf16 v[172:175], v[128:131], a[64:67], 0\n"
"s_waitcnt vmcnt(12)\n"
"v_mfma_f32_16x16x32_bf16 v[172:175], v[128:131], a[68:71], v[172:175]\n"
"s_waitcnt vmcnt(11)\n"
"v_mfma_f32_16x16x32_bf16 v[172:175], v[128:131], a[72:75], v[172:175]\n"
"s_waitcnt vmcnt(10)\n"
"v_mfma_f32_16x16x32_bf16 v[172:175], v[128:131], a[76:79], v[172:175]\n"
"s_waitcnt vmcnt(9)\n"
"v_mfma_f32_16x16x32_bf16 v[172:175], v[132:135], a[80:83], v[172:175]\n"
"s_waitcnt vmcnt(8)\n"
"v_mfma_f32_16x16x32_bf16 v[172:175], v[132:135], a[84:87], v[172:175]\n"
"s_waitcnt vmcnt(7)\n"
"v_mfma_f32_16x16x32_bf16 v[172:175], v[132:135], a[88:91], v[172:175]\n"
"s_waitcnt vmcnt(6)\n"
"v_mfma_f32_16x16x32_bf16 v[172:175], v[132:135], a[92:95], v[172:175]\n"
"s_waitcnt vmcnt(5)\n"
"v_mfma_f32_16x16x32_bf16 v[176:179], v[128:131], a[96:99], 0\n"
"s_waitcnt vmcnt(4)\n"
"v_mfma_f32_16x16x32_bf16 v[176:179], v[128:131], a[100:103], v[176:179]\n"
"s_waitcnt vmcnt(3)\n"
"v_mfma_f32_16x16x32_bf16 v[176:179], v[128:131], a[104:107], v[176:179]\n"
"s_waitcnt vmcnt(2)\n"
"v_mfma_f32_16x16x32_bf16 v[176:179], v[128:131], a[108:111], v[176:179]\n"
"s_nop 7\n"
"v_fmac_f32 v188, v172, v156\n"
"v_fmac_f32 v189, v173, v157\n"
"v_fmac_f32 v190, v174, v158\n"
"v_fmac_f32 v191, v175, v159\n"
"v_cvt_pk_bf16_f32 v172, v188, v189\n"
"v_cvt_pk_bf16_f32 v173, v190, v191\n"
"buffer_store_dwordx2 v[172:173], v245, s[64:67], 0 offen offset:512\n"
"v_fmac_f32 v192, v176, v160\n"
"v_fmac_f32 v193, v177, v161\n"
"v_fmac_f32 v194, v178, v162\n"
"v_fmac_f32 v195, v179, v163\n"
"v_cvt_pk_bf16_f32 v176, v192, v193\n"
"v_cvt_pk_bf16_f32 v177, v194, v195\n"
"s_mov_b32 exec_hi, 0\n"
"buffer_store_dwordx2 v[176:177], v245, s[64:67], 0 offen offset:768\n"
"s_mov_b64 exec, -1\n"
"s_endpgm\n"
".Lonce_%=:\n"
"s_cmp_eq_u32 s48, 3\n"
"s_cbranch_scc1 .Lonce3_%=\n"
"s_cmp_eq_u32 s48, 4\n"
"s_cbranch_scc1 .Lonce4_%=\n"
"s_lshl_b32 s89, s84, 7\n"
"s_lshl_b32 s90, s87, 3\n"
"s_add_u32 s89, s89, s90\n"
"v_mov_b32 v180, s89\n"
"v_mov_b32 v182, s96\n"
"v_mov_b32 v183, s97\n"
"v_mbcnt_lo_u32_b32 v181, -1, 0\n"
"v_mbcnt_hi_u32_b32 v181, -1, v181\n"
"v_and_b32 v181, 15, v181\n"
"s_lshr_b32 s89, s87, 2\n"
"s_lshl_b32 s89, s89, 7\n"
"v_lshl_add_u32 v181, v181, 3, s89\n"
"s_waitcnt vmcnt(16)\n"
"s_mov_b64 exec, 1\n"
"global_store_dwordx2 v180, v[182:183], s[94:95] offset:512 sc1\n"
"s_mov_b64 exec, -1\n"
"s_nop 1\n"
"s_branch .Loncer_%=\n"
".Lonce4_%=:\n"
"v_mbcnt_lo_u32_b32 v176, -1, 0\n"
"v_mbcnt_hi_u32_b32 v176, -1, v176\n"
"v_lshlrev_b32 v184, 4, v176\n"
"v_lshrrev_b32 v185, 8, v184\n"
"v_lshlrev_b32 v244, 10, v185\n"
"v_and_b32 v185, 0xf0, v184\n"
"v_add_u32 v244, v244, v185\n"
"s_lshl_b32 s89, s84, 8\n"
"v_add_u32 v244, s89, v244\n"
"v_bfe_u32 v185, v184, 4, 5\n"
"v_mul_u32_u24 v245, 896, v185\n"
"v_lshrrev_b32 v185, 9, v184\n"
"v_lshl_add_u32 v245, v185, 7, v245\n"
"s_and_b32 s89, s87, 3\n"
"s_lshl_b32 s89, s89, 5\n"
"s_lshl_b32 s90, s84, 3\n"
"s_add_u32 s89, s89, s90\n"
"v_add_u32 v245, s89, v245\n"
"s_lshr_b32 s89, s88, 5\n"
"s_lshl_b32 s89, s89, 2\n"
"s_add_u32 s89, s89, s84\n"
"s_mul_i32 s89, s89, 56\n"
"v_min_u32 v177, 55, v176\n"
"v_add_u32 v177, s89, v177\n"
"v_lshlrev_b32 v178, 7, v177\n"
"buffer_load_dword v254, v178, s[76:79], 0 offen\n"
"s_and_b32 s89, s87, 3\n"
"s_lshl_b32 s89, s89, 2\n"
"s_add_u32 s89, s89, s84\n"
"s_mul_i32 s89, s89, 1792\n"
"v_min_u32 v178, 13, v176\n"
"v_lshlrev_b32 v178, 7, v178\n"
"buffer_load_dword v254, v178, s[60:63], s89 offen\n"
"s_getpc_b64 s[90:91]\n"
".Lopc_%=:\n"
"s_sub_u32 s90, s90, .Lopc_%=-.Ltail_%=\n"
"s_subb_u32 s91, s91, 0\n"
"s_lshr_b32 s92, s88, 3\n"
"s_lshl_b32 s92, s92, 2\n"
"s_add_u32 s92, s92, s84\n"
"s_lshl_b32 s92, s92, 8\n"
"v_and_b32 v178, 1, v176\n"
"v_lshlrev_b32 v178, 7, v178\n"
"v_add_u32 v178, s92, v178\n"
"s_mov_b32 s92, .Lend_%=-.Ltail_%=-4\n"
"v_min_u32 v178, s92, v178\n"
"global_load_dword v254, v178, s[90:91]\n"
"s_lshl_b32 s89, s84, 7\n"
"s_lshl_b32 s90, s87, 3\n"
"s_add_u32 s89, s89, s90\n"
"v_mov_b32 v180, s89\n"
"v_mov_b32 v182, s96\n"
"v_mov_b32 v183, s97\n"
"v_mbcnt_lo_u32_b32 v181, -1, 0\n"
"v_mbcnt_hi_u32_b32 v181, -1, v181\n"
"v_and_b32 v181, 15, v181\n"
"s_lshr_b32 s89, s87, 2\n"
"s_lshl_b32 s89, s89, 7\n"
"v_lshl_add_u32 v181, v181, 3, s89\n"
"global_load_dwordx2 v[254:255], v181, s[94:95] offset:512 sc1\n"
"s_branch .Loncer_%=\n"
".Lonce3_%=:\n"
"s_lshl_b32 s89, s84, 7\n"
"s_lshl_b32 s90, s87, 3\n"
"s_add_u32 s89, s89, s90\n"
"v_mov_b32 v180, s89\n"
"v_mov_b32 v182, s96\n"
"v_mov_b32 v183, s97\n"
"v_mbcnt_lo_u32_b32 v181, -1, 0\n"
"v_mbcnt_hi_u32_b32 v181, -1, v181\n"
"v_and_b32 v181, 15, v181\n"
"s_lshr_b32 s89, s87, 2\n"
"s_lshl_b32 s89, s89, 7\n"
"v_lshl_add_u32 v181, v181, 3, s89\n"
"s_waitcnt vmcnt(16)\n"
"v_cmp_ne_u64 vcc, s[96:97], v[254:255]\n"
"s_cbranch_vccz .Lpok_7_%=\n"
"s_mov_b32 s90, 0x20000\n"
".Lpsp_7_%=:\n"
"global_load_dwordx2 v[254:255], v181, s[94:95] offset:512 sc1\n"
"s_waitcnt vmcnt(0)\n"
"v_cmp_ne_u64 vcc, s[96:97], v[254:255]\n"
"s_cbranch_vccz .Lpok_7_%=\n"
"s_sleep 1\n"
"s_sub_u32 s90, s90, 1\n"
"s_cmp_lg_u32 s90, 0\n"
"s_cbranch_scc1 .Lpsp_7_%=\n"
".Lpok_7_%=:\n"
"s_branch .Loncer_%=\n"
".Lend_%=:\n"
".Lredo_%=:\n"
"s_mov_b32 s54, 0x7fc00000\n"
"s_waitcnt vmcnt(0) lgkmcnt(0)\n"
"s_barrier\n"
"v_mbcnt_lo_u32_b32 v254, -1, 0\n"
"v_mbcnt_hi_u32_b32 v254, -1, v254\n"
"s_and_b32 s91, s84, 1\n"
"s_lshl_b32 s91, s91, 14\n"
"v_and_b32 v240, 15, v254\n"
"v_lshrrev_b32 v241, 4, v254\n"
"v_lshrrev_b32 v242, 3, v240\n"
"v_lshlrev_b32 v242, 12, v242\n"
"v_and_b32 v243, 7, v240\n"
"v_lshl_add_u32 v242, v243, 7, v242\n"
"v_add_u32 v242, s91, v242\n"
"v_lshrrev_b32 v243, 1, v240\n"
"v_xor_b32 v243, v243, v241\n"
"v_lshl_add_u32 v246, v243, 4, v242\n"
"v_xor_b32 v247, 64, v246\n"
"s_add_u32 s91, s91, 0x10000\n"
"v_bfe_u32 v240, v254, 2, 3\n"
"v_lshrrev_b32 v241, 5, v254\n"
"v_lshlrev_b32 v241, 12, v241\n"
"v_lshl_add_u32 v241, v240, 7, v241\n"
"v_and_b32 v242, 3, v254\n"
"v_lshl_add_u32 v241, v242, 3, v241\n"
"v_lshrrev_b32 v240, 1, v240\n"
"v_lshl_add_u32 v248, v240, 5, v241\n"
"v_add_u32 v248, s91, v248\n"
"v_xor_b32 v249, 32, v248\n"
"v_xor_b32 v250, 64, v248\n"
"v_xor_b32 v251, 96, v248\n"
"v_mov_b32 v192, 0\n"
"v_mov_b32 v193, 0\n"
"v_mov_b32 v194, 0\n"
"v_mov_b32 v195, 0\n"
"v_mov_b32 v196, 0\n"
"v_mov_b32 v197, 0\n"
"v_mov_b32 v198, 0\n"
"v_mov_b32 v199, 0\n"
"v_mov_b32 v200, 0\n"
"v_mov_b32 v201, 0\n"
"v_mov_b32 v202, 0\n"
"v_mov_b32 v203, 0\n"
"v_mov_b32 v204, 0\n"
"v_mov_b32 v205, 0\n"
"v_mov_b32 v206, 0\n"
"v_mov_b32 v207, 0\n"
"v_mov_b32 v176, 0\n"
"v_mov_b32 v177, 0\n"
"v_mov_b32 v178, 0\n"
"v_mov_b32 v179, 0\n"
"v_mov_b32 v240, 0\n"
"v_mov_b32 v241, 0\n"
"v_mov_b32 v242, 0\n"
"v_mov_b32 v243, 0\n"
"v_mbcnt_lo_u32_b32 v254, -1, 0\n"
"v_mbcnt_hi_u32_b32 v254, -1, v254\n"
"v_lshrrev_b32 v255, 4, v254\n"
"s_and_b32 s89, s84, 1\n"
"s_lshl_b32 s89, s89, 2\n"
"v_add_u32 v255, s89, v255\n"
"v_and_b32 v254, 7, v254\n"
"v_xor_b32 v254, v254, v255\n"
"v_and_b32 v252, 0xfffffe00, v253\n"
"v_lshl_add_u32 v252, v254, 4, v252\n"
"s_mov_b32 s44, 0\n"
"s_mov_b32 s45, 0x4000\n"
"s_mov_b32 s46, 0\n"
"s_mov_b32 s47, 0x4000\n"
"s_lshl_b32 s89, s84, 12\n"
"s_mov_b32 m0, s89\n"
"s_nop 0\n"
"s_nop 0\n"
"buffer_load_dwordx4 v252, s[36:39], s44 offen offset:0 nt lds\n"
"s_add_u32 m0, m0, 0x380\n"
"s_nop 0\n"
"buffer_load_dwordx4 v252, s[36:39], s44 offen offset:128 nt lds\n"
"s_add_u32 m0, m0, 0x380\n"
"s_nop 0\n"
"buffer_load_dwordx4 v252, s[36:39], s44 offen offset:256 nt lds\n"
"s_add_u32 m0, m0, 0x380\n"
"s_nop 0\n"
"buffer_load_dwordx4 v252, s[36:39], s44 offen offset:384 nt lds\n"
"s_add_u32 m0, m0, 0x3580\n"
"s_nop 0\n"
"buffer_load_dwordx4 v252, s[36:39], s45 offen offset:0 nt lds\n"
"s_add_u32 m0, m0, 0x380\n"
"s_nop 0\n"
"buffer_load_dwordx4 v252, s[36:39], s45 offen offset:128 nt lds\n"
"s_add_u32 m0, m0, 0x380\n"
"s_nop 0\n"
"buffer_load_dwordx4 v252, s[36:39], s45 offen offset:256 nt lds\n"
"s_add_u32 m0, m0, 0x380\n"
"s_nop 0\n"
"buffer_load_dwordx4 v252, s[36:39], s45 offen offset:384 nt lds\n"
"s_add_u32 s44, s44, 0x8000\n"
"s_add_u32 s45, s45, 0x8000\n"
"s_add_u32 m0, m0, 0x3580\n"
"s_nop 0\n"
"buffer_load_dwordx4 v252, s[36:39], s44 offen offset:0 nt lds\n"
"s_add_u32 m0, m0, 0x380\n"
"s_nop 0\n"
"buffer_load_dwordx4 v252, s[36:39], s44 offen offset:128 nt lds\n"
"s_add_u32 m0, m0, 0x380\n"
"s_nop 0\n"
"buffer_load_dwordx4 v252, s[36:39], s44 offen offset:256 nt lds\n"
"s_add_u32 m0, m0, 0x380\n"
"s_nop 0\n"
"buffer_load_dwordx4 v252, s[36:39], s44 offen offset:384 nt lds\n"
"s_add_u32 m0, m0, 0x3580\n"
"s_nop 0\n"
"buffer_load_dwordx4 v252, s[36:39], s45 offen offset:0 nt lds\n"
"s_add_u32 m0, m0, 0x380\n"
"s_nop 0\n"
"buffer_load_dwordx4 v252, s[36:39], s45 offen offset:128 nt lds\n"
"s_add_u32 m0, m0, 0x380\n"
"s_nop 0\n"
"buffer_load_dwordx4 v252, s[36:39], s45 offen offset:256 nt lds\n"
"s_add_u32 m0, m0, 0x380\n"
"s_nop 0\n"
"buffer_load_dwordx4 v252, s[36:39], s45 offen offset:384 nt lds\n"
"s_add_u32 s44, s44, 0x8000\n"
"s_add_u32 s45, s45, 0x8000\n"
"s_add_u32 m0, m0, 0x3580\n"
"s_nop 0\n"
"buffer_load_dwordx4 v253, s[40:43], s46 offen offset:0 nt lds\n"
"s_add_u32 m0, m0, 0x380\n"
"s_nop 0\n"
"buffer_load_dwordx4 v253, s[40:43], s46 offen offset:128 nt lds\n"
"s_add_u32 m0, m0, 0x380\n"
"s_nop 0\n"
"buffer_load_dwordx4 v253, s[40:43], s46 offen offset:256 nt lds\n"
"s_add_u32 m0, m0, 0x380\n"
"s_nop 0\n"
"buffer_load_dwordx4 v253, s[40:43], s46 offen offset:384 nt lds\n"
"s_add_u32 m0, m0, 0x3580\n"
"s_nop 0\n"
"buffer_load_dwordx4 v253, s[40:43], s47 offen offset:0 nt lds\n"
"s_add_u32 m0, m0, 0x380\n"
"s_nop 0\n"
"buffer_load_dwordx4 v253, s[40:43], s47 offen offset:128 nt lds\n"
"s_add_u32 m0, m0, 0x380\n"
"s_nop 0\n"
"buffer_load_dwordx4 v253, s[40:43], s47 offen offset:256 nt lds\n"
"s_add_u32 m0, m0, 0x380\n"
"s_nop 0\n"
"buffer_load_dwordx4 v253, s[40:43], s47 offen offset:384 nt lds\n"
"s_nop 1\n"
"v_mfma_f32_32x32x16_bf16 a[128:143], v[176:179], v[176:179], 0\n"
"v_mfma_f32_32x32x16_bf16 v[0:15], v[176:179], v[176:179], 0\n"
"v_mfma_f32_32x32x16_bf16 a[144:159], v[176:179], v[176:179], 0\n"
"v_mfma_f32_32x32x16_bf16 v[16:31], v[176:179], v[176:179], 0\n"
"v_mfma_f32_32x32x16_bf16 a[160:175], v[176:179], v[176:179], 0\n"
"v_mfma_f32_32x32x16_bf16 v[32:47], v[176:179], v[176:179], 0\n"
"v_mfma_f32_32x32x16_bf16 a[176:191], v[176:179], v[176:179], 0\n"
"v_mfma_f32_32x32x16_bf16 v[48:63], v[176:179], v[176:179], 0\n"
"v_mfma_f32_32x32x16_bf16 a[192:207], v[176:179], v[176:179], 0\n"
"v_mfma_f32_32x32x16_bf16 v[64:79], v[176:179], v[176:179], 0\n"
"v_mfma_f32_32x32x16_bf16 a[208:223], v[176:179], v[176:179], 0\n"
"v_mfma_f32_32x32x16_bf16 v[80:95], v[176:179], v[176:179], 0\n"
"v_mfma_f32_32x32x16_bf16 a[224:239], v[176:179], v[176:179], 0\n"
"v_mfma_f32_32x32x16_bf16 v[96:111], v[176:179], v[176:179], 0\n"
"v_mfma_f32_32x32x16_bf16 a[240:255], v[176:179], v[176:179], 0\n"
"v_mfma_f32_32x32x16_bf16 v[112:127], v[176:179], v[176:179], 0\n"
"s_waitcnt vmcnt(16) lgkmcnt(0)\n"
"s_barrier\n"
"ds_read_b128 v[208:211], v246 offset:0\n"
"ds_read_b128 v[212:215], v247 offset:0\n"
"ds_read_b128 v[216:219], v246 offset:1024\n"
"ds_read_b128 v[220:223], v247 offset:1024\n"
"ds_read_b128 v[224:227], v246 offset:2048\n"
"ds_read_b128 v[228:231], v247 offset:2048\n"
"ds_read_b128 v[232:235], v246 offset:3072\n"
"s_waitcnt lgkmcnt(6)\n"
"v_mfma_f32_16x16x32_bf16 v[128:131], v[208:211], a[0:3], v[192:195]\n"
"ds_read_b128 v[236:239], v247 offset:3072\n"
"v_mfma_f32_16x16x32_bf16 v[132:135], v[208:211], a[32:35], v[196:199]\n"
"v_mfma_f32_16x16x32_bf16 v[136:139], v[208:211], a[64:67], v[200:203]\n"
"v_mfma_f32_16x16x32_bf16 v[140:143], v[208:211], a[96:99], v[204:207]\n"
"s_waitcnt lgkmcnt(6)\n"
"v_mfma_f32_16x16x32_bf16 v[128:131], v[212:215], a[4:7], v[128:131]\n"
"ds_read_b128 v[208:211], v246 offset:8192\n"
"v_mfma_f32_16x16x32_bf16 v[132:135], v[212:215], a[36:39], v[132:135]\n"
"v_mfma_f32_16x16x32_bf16 v[136:139], v[212:215], a[68:71], v[136:139]\n"
"v_mfma_f32_16x16x32_bf16 v[140:143], v[212:215], a[100:103], v[140:143]\n"
"s_waitcnt lgkmcnt(6)\n"
"v_mfma_f32_16x16x32_bf16 v[128:131], v[216:219], a[8:11], v[128:131]\n"
"ds_read_b128 v[212:215], v247 offset:8192\n"
"v_mfma_f32_16x16x32_bf16 v[132:135], v[216:219], a[40:43], v[132:135]\n"
"v_mfma_f32_16x16x32_bf16 v[136:139], v[216:219], a[72:75], v[136:139]\n"
"v_mfma_f32_16x16x32_bf16 v[140:143], v[216:219], a[104:107], v[140:143]\n"
"s_waitcnt lgkmcnt(6)\n"
"v_mfma_f32_16x16x32_bf16 v[128:131], v[220:223], a[12:15], v[128:131]\n"
"ds_read_b128 v[216:219], v246 offset:9216\n"
"v_mfma_f32_16x16x32_bf16 v[132:135], v[220:223], a[44:47], v[132:135]\n"
"v_mfma_f32_16x16x32_bf16 v[136:139], v[220:223], a[76:79], v[136:139]\n"
"v_mfma_f32_16x16x32_bf16 v[140:143], v[220:223], a[108:111], v[140:143]\n"
"s_waitcnt lgkmcnt(6)\n"
"ds_read_b128 v[220:223], v247 offset:9216\n"
"v_mfma_f32_16x16x32_bf16 v[128:131], v[224:227], a[16:19], v[128:131]\n"
"v_mfma_f32_16x16x32_bf16 v[132:135], v[224:227], a[48:51], v[132:135]\n"
"v_mfma_f32_16x16x32_bf16 v[136:139], v[224:227], a[80:83], v[136:139]\n"
"v_mfma_f32_16x16x32_bf16 v[140:143], v[224:227], a[112:115], v[140:143]\n"
"s_waitcnt lgkmcnt(6)\n"
"ds_read_b128 v[224:227], v246 offset:10240\n"
"v_mfma_f32_16x16x32_bf16 v[128:131], v[228:231], a[20:23], v[128:131]\n"
"v_mfma_f32_16x16x32_bf16 v[132:135], v[228:231], a[52:55], v[132:135]\n"
"v_mfma_f32_16x16x32_bf16 v[136:139], v[228:231], a[84:87], v[136:139]\n"
"v_mfma_f32_16x16x32_bf16 v[140:143], v[228:231], a[116:119], v[140:143]\n"
"s_waitcnt lgkmcnt(6)\n"
"v_mfma_f32_16x16x32_bf16 v[128:131], v[232:235], a[24:27], v[128:131]\n"
"ds_read_b128 v[228:231], v247 offset:10240\n"
"v_mfma_f32_16x16x32_bf16 v[132:135], v[232:235], a[56:59], v[132:135]\n"
"v_mfma_f32_16x16x32_bf16 v[136:139], v[232:235], a[88:91], v[136:139]\n"
"v_mfma_f32_16x16x32_bf16 v[140:143], v[232:235], a[120:123], v[140:143]\n"
"s_waitcnt lgkmcnt(6)\n"
"ds_read_b128 v[232:235], v246 offset:11264\n"
"v_mfma_f32_16x16x32_bf16 v[128:131], v[236:239], a[28:31], v[128:131]\n"
"v_mfma_f32_16x16x32_bf16 v[132:135], v[236:239], a[60:63], v[132:135]\n"
"v_mfma_f32_16x16x32_bf16 v[136:139], v[236:239], a[92:95], v[136:139]\n"
"v_mfma_f32_16x16x32_bf16 v[140:143], v[236:239], a[124:127], v[140:143]\n"
"s_waitcnt lgkmcnt(6)\n"
"ds_read_b128 v[236:239], v247 offset:11264\n"
"v_mfma_f32_16x16x32_bf16 v[144:147], v[208:211], a[0:3], v[192:195]\n"
"v_mfma_f32_16x16x32_bf16 v[148:151], v[208:211], a[32:35], v[196:199]\n"
"v_mfma_f32_16x16x32_bf16 v[152:155], v[208:211], a[64:67], v[200:203]\n"
"v_mfma_f32_16x16x32_bf16 v[156:159], v[208:211], a[96:99], v[204:207]\n"
"s_waitcnt lgkmcnt(6)\n"
"v_mfma_f32_16x16x32_bf16 v[144:147], v[212:215], a[4:7], v[144:147]\n"
"s_add_u32 m0, m0, 0xfffeb580\n"
"v_mfma_f32_16x16x32_bf16 v[148:151], v[212:215], a[36:39], v[148:151]\n"
"s_add_u32 s46, s46, 0x8000\n"
"v_mfma_f32_16x16x32_bf16 v[152:155], v[212:215], a[68:71], v[152:155]\n"
"s_add_u32 s47, s47, 0x8000\n"
"v_mfma_f32_16x16x32_bf16 v[156:159], v[212:215], a[100:103], v[156:159]\n"
"s_waitcnt lgkmcnt(5)\n"
"v_mfma_f32_16x16x32_bf16 v[144:147], v[216:219], a[8:11], v[144:147]\n"
"v_mfma_f32_16x16x32_bf16 v[148:151], v[216:219], a[40:43], v[148:151]\n"
"v_mfma_f32_16x16x32_bf16 v[152:155], v[216:219], a[72:75], v[152:155]\n"
"s_waitcnt lgkmcnt(0)\n"
"s_barrier\n"
"v_mfma_f32_16x16x32_bf16 v[156:159], v[216:219], a[104:107], v[156:159]\n"
"v_mfma_f32_16x16x32_bf16 v[144:147], v[220:223], a[12:15], v[144:147]\n"
"v_mfma_f32_16x16x32_bf16 v[148:151], v[220:223], a[44:47], v[148:151]\n"
"v_mfma_f32_16x16x32_bf16 v[152:155], v[220:223], a[76:79], v[152:155]\n"
"buffer_load_dwordx4 v252, s[36:39], s44 offen offset:0 nt lds\n"
"v_mfma_f32_16x16x32_bf16 v[156:159], v[220:223], a[108:111], v[156:159]\n"
"s_add_u32 m0, m0, 0x380\n"
"v_mfma_f32_16x16x32_bf16 v[144:147], v[224:227], a[16:19], v[144:147]\n"
"v_mfma_f32_16x16x32_bf16 v[148:151], v[224:227], a[48:51], v[148:151]\n"
"v_mfma_f32_16x16x32_bf16 v[152:155], v[224:227], a[80:83], v[152:155]\n"
"v_mfma_f32_16x16x32_bf16 v[156:159], v[224:227], a[112:115], v[156:159]\n"
"v_mfma_f32_16x16x32_bf16 v[144:147], v[228:231], a[20:23], v[144:147]\n"
"v_mfma_f32_16x16x32_bf16 v[148:151], v[228:231], a[52:55], v[148:151]\n"
"buffer_load_dwordx4 v252, s[36:39], s44 offen offset:128 nt lds\n"
"v_mfma_f32_16x16x32_bf16 v[152:155], v[228:231], a[84:87], v[152:155]\n"
"s_add_u32 m0, m0, 0x380\n"
"v_mfma_f32_16x16x32_bf16 v[156:159], v[228:231], a[116:119], v[156:159]\n"
"v_mfma_f32_16x16x32_bf16 v[144:147], v[232:235], a[24:27], v[144:147]\n"
"v_mfma_f32_16x16x32_bf16 v[148:151], v[232:235], a[56:59], v[148:151]\n"
"buffer_load_dwordx4 v252, s[36:39], s44 offen offset:256 nt lds\n"
"v_mfma_f32_16x16x32_bf16 v[152:155], v[232:235], a[88:91], v[152:155]\n"
"s_add_u32 m0, m0, 0x380\n"
"v_mfma_f32_16x16x32_bf16 v[156:159], v[232:235], a[120:123], v[156:159]\n"
"v_mfma_f32_16x16x32_bf16 v[144:147], v[236:239], a[28:31], v[144:147]\n"
"v_mfma_f32_16x16x32_bf16 v[148:151], v[236:239], a[60:63], v[148:151]\n"
"v_mfma_f32_16x16x32_bf16 v[152:155], v[236:239], a[92:95], v[152:155]\n"
"v_mfma_f32_16x16x32_bf16 v[156:159], v[236:239], a[124:127], v[156:159]\n"
"s_nop 7\n"
"s_nop 0\n"
"buffer_load_dwordx4 v252, s[36:39], s44 offen offset:384 nt lds\n"
"s_nop 0\n"
"s_add_u32 m0, m0, 0x3580\n"
"s_nop 0\n"
"s_nop 4\n"
"v_max3_f32 v188, v128, v129, v130\n"
"v_max3_f32 v189, v132, v133, v134\n"
"v_max3_f32 v190, v136, v137, v138\n"
"v_max3_f32 v191, v140, v141, v142\n"
"v_max3_f32 v188, v131, v144, v188\n"
"v_max3_f32 v189, v135, v148, v189\n"
"v_max3_f32 v190, v139, v152, v190\n"
"v_max3_f32 v191, v143, v156, v191\n"
"v_max3_f32 v188, v145, v146, v188\n"
"v_max3_f32 v189, v149, v150, v189\n"
"v_max3_f32 v190, v153, v154, v190\n"
"v_max3_f32 v191, v157, v158, v191\n"
"v_max_f32 v188, v147, v188\n"
"v_max_f32 v189, v151, v189\n"
"v_max_f32 v190, v155, v190\n"
"v_max_f32 v191, v159, v191\n"
"v_mov_b32 v180, v188\n"
"v_mov_b32 v181, v189\n"
"v_mov_b32 v182, v190\n"
"v_mov_b32 v183, v191\n"
"v_permlane32_swap_b32 v188, v180\n"
"v_permlane32_swap_b32 v189, v181\n"
"v_permlane32_swap_b32 v190, v182\n"
"v_permlane32_swap_b32 v191, v183\n"
"v_max_f32 v188, v188, v180\n"
"v_max_f32 v189, v189, v181\n"
"v_max_f32 v190, v190, v182\n"
"v_max_f32 v191, v191, v183\n"
"v_mov_b32 v180, v188\n"
"v_mov_b32 v181, v189\n"
"v_mov_b32 v182, v190\n"
"v_mov_b32 v183, v191\n"
"v_permlane16_swap_b32 v188, v180\n"
"v_permlane16_swap_b32 v189, v181\n"
"v_permlane16_swap_b32 v190, v182\n"
"v_permlane16_swap_b32 v191, v183\n"
"v_max_f32 v188, v188, v180\n"
"v_max_f32 v189, v189, v181\n"
"v_max_f32 v190, v190, v182\n"
"v_max_f32 v191, v191, v183\n"
"v_sub_f32 v128, v128, v188\n"
"v_sub_f32 v129, v129, v188\n"
"v_sub_f32 v130, v130, v188\n"
"v_sub_f32 v131, v131, v188\n"
"v_sub_f32 v132, v132, v189\n"
"v_sub_f32 v133, v133, v189\n"
"v_sub_f32 v134, v134, v189\n"
"v_sub_f32 v135, v135, v189\n"
"v_sub_f32 v136, v136, v190\n"
"v_sub_f32 v137, v137, v190\n"
"v_sub_f32 v138, v138, v190\n"
"v_sub_f32 v139, v139, v190\n"
"v_sub_f32 v140, v140, v191\n"
"v_sub_f32 v141, v141, v191\n"
"v_sub_f32 v142, v142, v191\n"
"v_sub_f32 v143, v143, v191\n"
"v_sub_f32 v144, v144, v188\n"
"v_sub_f32 v145, v145, v188\n"
"v_sub_f32 v146, v146, v188\n"
"v_sub_f32 v147, v147, v188\n"
"v_sub_f32 v148, v148, v189\n"
"v_sub_f32 v149, v149, v189\n"
"v_sub_f32 v150, v150, v189\n"
"v_sub_f32 v151, v151, v189\n"
"v_sub_f32 v152, v152, v190\n"
"v_sub_f32 v153, v153, v190\n"
"v_sub_f32 v154, v154, v190\n"
"v_sub_f32 v155, v155, v190\n"
"v_sub_f32 v156, v156, v191\n"
"v_sub_f32 v157, v157, v191\n"
"v_sub_f32 v158, v158, v191\n"
"v_sub_f32 v159, v159, v191\n"
"v_sub_f32 v192, 0, v188\n"
"v_sub_f32 v193, 0, v188\n"
"v_sub_f32 v194, 0, v188\n"
"v_sub_f32 v195, 0, v188\n"
"v_sub_f32 v196, 0, v189\n"
"v_sub_f32 v197, 0, v189\n"
"v_sub_f32 v198, 0, v189\n"
"v_sub_f32 v199, 0, v189\n"
"v_sub_f32 v200, 0, v190\n"
"v_sub_f32 v201, 0, v190\n"
"v_sub_f32 v202, 0, v190\n"
"v_sub_f32 v203, 0, v190\n"
"v_sub_f32 v204, 0, v191\n"
"v_sub_f32 v205, 0, v191\n"
"v_sub_f32 v206, 0, v191\n"
"v_sub_f32 v207, 0, v191\n"
"s_nop 0\n"
"buffer_load_dwordx4 v252, s[36:39], s45 offen offset:0 nt lds\n"
"s_nop 0\n"
"v_exp_f32 v128, v128\n"
"s_nop 0\n"
"s_add_u32 m0, m0, 0x380\n"
"s_nop 0\n"
"v_add_f32 v240, v240, v128\n"
"s_nop 0\n"
"v_exp_f32 v129, v129\n"
"s_nop 0\n"
"v_exp_f32 v130, v130\n"
"s_nop 0\n"
"v_add_f32 v240, v240, v129\n"
"s_nop 0\n"
"v_cvt_pk_bf16_f32 v160, v128, v129\n"
"s_nop 0\n"
"v_exp_f32 v131, v131\n"
"s_nop 0\n"
"buffer_load_dwordx4 v252, s[36:39], s45 offen offset:128 nt lds\n"
"s_nop 0\n"
"v_add_f32 v240, v240, v130\n"
"s_nop 0\n"
"v_add_f32 v240, v240, v131\n"
"s_nop 0\n"
"s_add_u32 m0, m0, 0x380\n"
"v_exp_f32 v132, v132\n"
"s_nop 0\n"
"v_exp_f32 v133, v133\n"
"s_nop 0\n"
"v_cvt_pk_bf16_f32 v161, v130, v131\n"
"s_nop 0\n"
"v_add_f32 v241, v241, v132\n"
"s_nop 0\n"
"v_exp_f32 v134, v134\n"
"s_nop 0\n"
"v_exp_f32 v135, v135\n"
"s_nop 0\n"
"buffer_load_dwordx4 v252, s[36:39], s45 offen offset:256 nt lds\n"
"s_nop 0\n"
"v_add_f32 v241, v241, v133\n"
"s_nop 0\n"
"v_cvt_pk_bf16_f32 v164, v132, v133\n"
"s_nop 0\n"
"v_exp_f32 v136, v136\n"
"s_nop 0\n"
"v_add_f32 v241, v241, v134\n"
"s_nop 0\n"
"s_add_u32 m0, m0, 0x380\n"
"s_nop 0\n"
"v_exp_f32 v137, v137\n"
"s_nop 0\n"
"v_add_f32 v241, v241, v135\n"
"v_cvt_pk_bf16_f32 v165, v134, v135\n"
"s_nop 0\n"
"v_exp_f32 v138, v138\n"
"s_nop 0\n"
"buffer_load_dwordx4 v252, s[36:39], s45 offen offset:384 nt lds\n"
"s_nop 0\n"
"s_add_u32 m0, m0, 0x13580\n"
"v_add_f32 v242, v242, v136\n"
"v_add_f32 v242, v242, v137\n"
"s_nop 0\n"
"v_exp_f32 v139, v139\n"
"s_nop 0\n"
"v_cvt_pk_bf16_f32 v168, v136, v137\n"
"v_add_f32 v242, v242, v138\n"
"s_nop 0\n"
"v_exp_f32 v140, v140\n"
"s_nop 0\n"
"v_exp_f32 v141, v141\n"
"s_nop 0\n"
"s_waitcnt vmcnt(16) lgkmcnt(0)\n"
"s_barrier\n"
"v_add_f32 v242, v242, v139\n"
"s_nop 0\n"
"ds_read_b128 v[208:211], v246 offset:32768\n"
"v_cvt_pk_bf16_f32 v169, v138, v139\n"
"s_nop 0\n"
"s_add_u32 s44, s44, 0x8000\n"
"v_exp_f32 v142, v142\n"
"s_nop 0\n"
"s_add_u32 s45, s45, 0x8000\n"
"buffer_load_dwordx4 v253, s[40:43], s46 offen offset:0 nt lds\n"
"s_nop 0\n"
"ds_read_b128 v[212:215], v247 offset:32768\n"
"v_add_f32 v243, v243, v140\n"
"s_nop 0\n"
"s_add_u32 m0, m0, 0x380\n"
"v_exp_f32 v143, v143\n"
"s_nop 0\n"
"ds_read_b128 v[216:219], v246 offset:33792\n"
"v_add_f32 v243, v243, v141\n"
"s_nop 0\n"
"ds_read_b128 v[220:223], v247 offset:33792\n"
"v_cvt_pk_bf16_f32 v172, v140, v141\n"
"s_nop 0\n"
"v_exp_f32 v144, v144\n"
"s_nop 0\n"
"v_exp_f32 v145, v145\n"
"s_nop 0\n"
"ds_read_b128 v[224:227], v246 offset:34816\n"
"v_add_f32 v243, v243, v142\n"
"s_nop 0\n"
"buffer_load_dwordx4 v253, s[40:43], s46 offen offset:128 nt lds\n"
"s_nop 0\n"
"s_add_u32 m0, m0, 0x380\n"
"v_add_f32 v243, v243, v143\n"
"v_cvt_pk_bf16_f32 v173, v142, v143\n"
"s_nop 0\n"
"v_exp_f32 v146, v146\n"
"s_nop 0\n"
"ds_read_b128 v[228:231], v247 offset:34816\n"
"v_add_f32 v240, v240, v144\n"
"s_nop 0\n"
"v_exp_f32 v147, v147\n"
"s_nop 0\n"
"ds_read_b128 v[232:235], v246 offset:35840\n"
"v_add_f32 v240, v240, v145\n"
"s_nop 0\n"
"v_cvt_pk_bf16_f32 v162, v144, v145\n"
"v_add_f32 v240, v240, v146\n"
"s_nop 0\n"
"v_exp_f32 v148, v148\n"
"s_nop 0\n"
"v_exp_f32 v149, v149\n"
"s_nop 0\n"
"s_waitcnt lgkmcnt(6)\n"
"buffer_load_dwordx4 v253, s[40:43], s46 offen offset:256 nt lds\n"
"v_mfma_f32_16x16x32_bf16 v[128:131], v[208:211], a[0:3], v[192:195]\n"
"ds_read_b128 v[236:239], v247 offset:35840\n"
"v_add_f32 v240, v240, v147\n"
"v_mfma_f32_16x16x32_bf16 v[132:135], v[208:211], a[32:35], v[196:199]\n"
"s_add_u32 m0, m0, 0x380\n"
"v_cvt_pk_bf16_f32 v163, v146, v147\n"
"v_add_f32 v241, v241, v148\n"
"v_mfma_f32_16x16x32_bf16 v[136:139], v[208:211], a[64:67], v[200:203]\n"
"v_exp_f32 v150, v150\n"
"v_mfma_f32_16x16x32_bf16 v[140:143], v[208:211], a[96:99], v[204:207]\n"
"s_waitcnt lgkmcnt(6)\n"
"v_exp_f32 v151, v151\n"
"v_mfma_f32_16x16x32_bf16 v[128:131], v[212:215], a[4:7], v[128:131]\n"
"ds_read_b128 v[208:211], v246 offset:40960\n"
"v_add_f32 v241, v241, v149\n"
"v_mfma_f32_16x16x32_bf16 v[132:135], v[212:215], a[36:39], v[132:135]\n"
"v_exp_f32 v152, v152\n"
"v_mfma_f32_16x16x32_bf16 v[136:139], v[212:215], a[68:71], v[136:139]\n"
"v_cvt_pk_bf16_f32 v166, v148, v149\n"
"v_add_f32 v241, v241, v150\n"
"v_mfma_f32_16x16x32_bf16 v[140:143], v[212:215], a[100:103], v[140:143]\n"
"s_waitcnt lgkmcnt(6)\n"
"buffer_load_dwordx4 v253, s[40:43], s46 offen offset:384 nt lds\n"
"v_mfma_f32_16x16x32_bf16 v[128:131], v[216:219], a[8:11], v[128:131]\n"
"ds_read_b128 v[212:215], v247 offset:40960\n"
"v_add_f32 v241, v241, v151\n"
"v_mfma_f32_16x16x32_bf16 v[132:135], v[216:219], a[40:43], v[132:135]\n"
"s_add_u32 m0, m0, 0x3580\n"
"v_exp_f32 v153, v153\n"
"v_mfma_f32_16x16x32_bf16 v[136:139], v[216:219], a[72:75], v[136:139]\n"
"v_exp_f32 v154, v154\n"
"v_mfma_f32_16x16x32_bf16 v[140:143], v[216:219], a[104:107], v[140:143]\n"
"s_waitcnt lgkmcnt(6)\n"
"v_cvt_pk_bf16_f32 v167, v150, v151\n"
"v_add_f32 v242, v242, v152\n"
"v_mfma_f32_16x16x32_bf16 v[128:131], v[220:223], a[12:15], v[128:131]\n"
"ds_read_b128 v[216:219], v246 offset:41984\n"
"v_add_f32 v242, v242, v153\n"
"v_mfma_f32_16x16x32_bf16 v[132:135], v[220:223], a[44:47], v[132:135]\n"
"v_exp_f32 v155, v155\n"
"v_mfma_f32_16x16x32_bf16 v[136:139], v[220:223], a[76:79], v[136:139]\n"
"buffer_load_dwordx4 v253, s[40:43], s47 offen offset:0 nt lds\n"
"v_mfma_f32_16x16x32_bf16 v[140:143], v[220:223], a[108:111], v[140:143]\n"
"s_waitcnt lgkmcnt(6)\n"
"ds_read_b128 v[220:223], v247 offset:41984\n"
"v_mfma_f32_16x16x32_bf16 v[128:131], v[224:227], a[16:19], v[128:131]\n"
"s_add_u32 m0, m0, 0x380\n"
"v_cvt_pk_bf16_f32 v170, v152, v153\n"
"v_add_f32 v242, v242, v154\n"
"v_mfma_f32_16x16x32_bf16 v[132:135], v[224:227], a[48:51], v[132:135]\n"
"v_exp_f32 v156, v156\n"
"v_mfma_f32_16x16x32_bf16 v[136:139], v[224:227], a[80:83], v[136:139]\n"
"v_exp_f32 v157, v157\n"
"v_mfma_f32_16x16x32_bf16 v[140:143], v[224:227], a[112:115], v[140:143]\n"
"s_waitcnt lgkmcnt(6)\n"
"ds_read_b128 v[224:227], v246 offset:43008\n"
"v_mfma_f32_16x16x32_bf16 v[128:131], v[228:231], a[20:23], v[128:131]\n"
"v_exp_f32 v158, v158\n"
"v_mfma_f32_16x16x32_bf16 v[132:135], v[228:231], a[52:55], v[132:135]\n"
"v_add_f32 v242, v242, v155\n"
"v_cvt_pk_bf16_f32 v171, v154, v155\n"
"v_mfma_f32_16x16x32_bf16 v[136:139], v[228:231], a[84:87], v[136:139]\n"
"buffer_load_dwordx4 v253, s[40:43], s47 offen offset:128 nt lds\n"
"v_mfma_f32_16x16x32_bf16 v[140:143], v[228:231], a[116:119], v[140:143]\n"
"s_waitcnt lgkmcnt(6)\n"
"v_exp_f32 v159, v159\n"
"v_mfma_f32_16x16x32_bf16 v[128:131], v[232:235], a[24:27], v[128:131]\n"
"ds_read_b128 v[228:231], v247 offset:43008\n"
"v_add_f32 v243, v243, v156\n"
"v_mfma_f32_16x16x32_bf16 v[132:135], v[232:235], a[56:59], v[132:135]\n"
"s_add_u32 m0, m0, 0x380\n"
"v_add_f32 v243, v243, v157\n"
"v_cvt_pk_bf16_f32 v174, v156, v157\n"
"v_mfma_f32_16x16x32_bf16 v[136:139], v[232:235], a[88:91], v[136:139]\n"
"buffer_load_dwordx4 v253, s[40:43], s47 offen offset:256 nt lds\n"
"v_mfma_f32_16x16x32_bf16 v[140:143], v[232:235], a[120:123], v[140:143]\n"
"s_waitcnt lgkmcnt(6)\n"
"ds_read_b128 v[232:235], v246 offset:44032\n"
"v_mfma_f32_16x16x32_bf16 v[128:131], v[236:239], a[28:31], v[128:131]\n"
"s_add_u32 m0, m0, 0x380\n"
"v_add_f32 v243, v243, v158\n"
"v_add_f32 v243, v243, v159\n"
"v_mfma_f32_16x16x32_bf16 v[132:135], v[236:239], a[60:63], v[132:135]\n"
"v_cvt_pk_bf16_f32 v175, v158, v159\n"
"v_mfma_f32_16x16x32_bf16 v[136:139], v[236:239], a[92:95], v[136:139]\n"
"v_mfma_f32_16x16x32_bf16 v[140:143], v[236:239], a[124:127], v[140:143]\n"
"s_waitcnt lgkmcnt(6)\n"
"ds_read_b128 v[236:239], v247 offset:44032\n"
"v_mfma_f32_16x16x32_bf16 v[144:147], v[208:211], a[0:3], v[192:195]\n"
"buffer_load_dwordx4 v253, s[40:43], s47 offen offset:384 nt lds\n"
"v_mfma_f32_16x16x32_bf16 v[148:151], v[208:211], a[32:35], v[196:199]\n"
"v_mfma_f32_16x16x32_bf16 v[152:155], v[208:211], a[64:67], v[200:203]\n"
"v_mfma_f32_16x16x32_bf16 v[156:159], v[208:211], a[96:99], v[204:207]\n"
"s_waitcnt lgkmcnt(6)\n"
"s_mov_b32 s48, 15\n"
".p2align 6\n"
".Lloops_%=:\n"
"v_mfma_f32_16x16x32_bf16 v[144:147], v[212:215], a[4:7], v[144:147]\n"
"s_add_u32 m0, m0, 0xfffeb580\n"
"v_max_f32 v244, v128, v129\n"
"v_max_f32 v245, v130, v131\n"
"v_mfma_f32_16x16x32_bf16 v[148:151], v[212:215], a[36:39], v[148:151]\n"
"s_add_u32 s46, s46, 0x8000\n"
"v_max3_f32 v244, v132, v133, v244\n"
"v_max3_f32 v245, v134, v135, v245\n"
"v_mfma_f32_16x16x32_bf16 v[152:155], v[212:215], a[68:71], v[152:155]\n"
"s_add_u32 s47, s47, 0x8000\n"
"v_max3_f32 v244, v136, v137, v244\n"
"v_max3_f32 v245, v138, v139, v245\n"
"v_mfma_f32_16x16x32_bf16 v[156:159], v[212:215], a[100:103], v[156:159]\n"
"s_waitcnt lgkmcnt(5)\n"
"v_max3_f32 v244, v140, v141, v244\n"
"v_max3_f32 v245, v142, v143, v245\n"
"v_mfma_f32_16x16x32_bf16 v[144:147], v[216:219], a[8:11], v[144:147]\n"
"v_mfma_f32_16x16x32_bf16 v[148:151], v[216:219], a[40:43], v[148:151]\n"
"v_mfma_f32_16x16x32_bf16 v[152:155], v[216:219], a[72:75], v[152:155]\n"
"s_waitcnt vmcnt(16) lgkmcnt(0)\n"
"s_barrier\n"
"v_mfma_f32_16x16x32_bf16 v[156:159], v[216:219], a[104:107], v[156:159]\n"
"ds_read_b64_tr_b16 v[208:209], v248 offset:0\n"
"v_mfma_f32_16x16x32_bf16 v[144:147], v[220:223], a[12:15], v[144:147]\n"
"ds_read_b64_tr_b16 v[210:211], v248 offset:8192\n"
"v_mfma_f32_16x16x32_bf16 v[148:151], v[220:223], a[44:47], v[148:151]\n"
"ds_read_b64_tr_b16 v[212:213], v249 offset:0\n"
"v_mfma_f32_16x16x32_bf16 v[152:155], v[220:223], a[76:79], v[152:155]\n"
"buffer_load_dwordx4 v252, s[36:39], s44 offen offset:0 nt lds\n"
"v_mfma_f32_16x16x32_bf16 v[156:159], v[220:223], a[108:111], v[156:159]\n"
"s_add_u32 m0, m0, 0x380\n"
"ds_read_b64_tr_b16 v[214:215], v249 offset:8192\n"
"v_mfma_f32_16x16x32_bf16 v[144:147], v[224:227], a[16:19], v[144:147]\n"
"ds_read_b64_tr_b16 v[216:217], v250 offset:0\n"
"v_mfma_f32_16x16x32_bf16 v[148:151], v[224:227], a[48:51], v[148:151]\n"
"ds_read_b64_tr_b16 v[218:219], v250 offset:8192\n"
"v_mfma_f32_16x16x32_bf16 v[152:155], v[224:227], a[80:83], v[152:155]\n"
"ds_read_b64_tr_b16 v[220:221], v251 offset:0\n"
"v_mfma_f32_16x16x32_bf16 v[156:159], v[224:227], a[112:115], v[156:159]\n"
"ds_read_b64_tr_b16 v[222:223], v251 offset:8192\n"
"v_mfma_f32_16x16x32_bf16 v[144:147], v[228:231], a[20:23], v[144:147]\n"
"ds_read_b64_tr_b16 v[224:225], v248 offset:1024\n"
"v_mfma_f32_16x16x32_bf16 v[148:151], v[228:231], a[52:55], v[148:151]\n"
"buffer_load_dwordx4 v252, s[36:39], s44 offen offset:128 nt lds\n"
"v_mfma_f32_16x16x32_bf16 v[152:155], v[228:231], a[84:87], v[152:155]\n"
"s_add_u32 m0, m0, 0x380\n"
"ds_read_b64_tr_b16 v[226:227], v248 offset:9216\n"
"v_mfma_f32_16x16x32_bf16 v[156:159], v[228:231], a[116:119], v[156:159]\n"
"ds_read_b64_tr_b16 v[228:229], v249 offset:1024\n"
"v_mfma_f32_16x16x32_bf16 v[144:147], v[232:235], a[24:27], v[144:147]\n"
"ds_read_b64_tr_b16 v[230:231], v249 offset:9216\n"
"v_mfma_f32_16x16x32_bf16 v[148:151], v[232:235], a[56:59], v[148:151]\n"
"buffer_load_dwordx4 v252, s[36:39], s44 offen offset:256 nt lds\n"
"v_mfma_f32_16x16x32_bf16 v[152:155], v[232:235], a[88:91], v[152:155]\n"
"s_add_u32 m0, m0, 0x380\n"
"v_mfma_f32_16x16x32_bf16 v[156:159], v[232:235], a[120:123], v[156:159]\n"
"ds_read_b64_tr_b16 v[232:233], v250 offset:1024\n"
"v_mfma_f32_16x16x32_bf16 v[144:147], v[236:239], a[28:31], v[144:147]\n"
"ds_read_b64_tr_b16 v[234:235], v250 offset:9216\n"
"v_mfma_f32_16x16x32_bf16 v[148:151], v[236:239], a[60:63], v[148:151]\n"
"v_mfma_f32_16x16x32_bf16 v[152:155], v[236:239], a[92:95], v[152:155]\n"
"v_mfma_f32_16x16x32_bf16 v[156:159], v[236:239], a[124:127], v[156:159]\n"
"s_waitcnt lgkmcnt(12)\n"
"ds_read_b64_tr_b16 v[236:237], v251 offset:1024\n"
"v_mfma_f32_16x16x32_bf16 v[0:3], v[208:211], v[160:163], v[0:3]\n"
"ds_read_b64_tr_b16 v[238:239], v251 offset:9216\n"
"v_max3_f32 v244, v144, v145, v244\n"
"v_mfma_f32_16x16x32_bf16 v[4:7], v[208:211], v[164:167], v[4:7]\n"
"buffer_load_dwordx4 v252, s[36:39], s44 offen offset:384 nt lds\n"
"v_mfma_f32_16x16x32_bf16 a[128:131], v[208:211], v[168:171], a[128:131]\n"
"s_add_u32 m0, m0, 0x3580\n"
"v_max3_f32 v245, v146, v147, v245\n"
"v_max3_f32 v244, v148, v149, v244\n"
"v_mfma_f32_16x16x32_bf16 a[132:135], v[208:211], v[172:175], a[132:135]\n"
"s_waitcnt lgkmcnt(12)\n"
"ds_read_b64_tr_b16 v[208:209], v248 offset:2048\n"
"v_mfma_f32_16x16x32_bf16 a[140:143], v[212:215], v[172:175], a[140:143]\n"
"ds_read_b64_tr_b16 v[210:211], v248 offset:10240\n"
"v_max3_f32 v245, v150, v151, v245\n"
"v_mfma_f32_16x16x32_bf16 a[136:139], v[212:215], v[168:171], a[136:139]\n"
"v_max3_f32 v244, v152, v153, v244\n"
"v_max3_f32 v245, v154, v155, v245\n"
"v_mfma_f32_16x16x32_bf16 v[12:15], v[212:215], v[164:167], v[12:15]\n"
"v_max3_f32 v244, v156, v157, v244\n"
"v_max3_f32 v245, v158, v159, v245\n"
"v_mfma_f32_16x16x32_bf16 v[8:11], v[212:215], v[160:163], v[8:11]\n"
"s_waitcnt lgkmcnt(12)\n"
"v_max_f32 v244, v244, v245\n"
"v_mfma_f32_16x16x32_bf16 v[16:19], v[216:219], v[160:163], v[16:19]\n"
"ds_read_b64_tr_b16 v[212:213], v249 offset:2048\n"
"v_cmp_nge_f32 vcc, s50, v244\n"
"v_mfma_f32_16x16x32_bf16 v[20:23], v[216:219], v[164:167], v[20:23]\n"
"s_cbranch_vccnz .Lrare1_8_%=\n"
".Lret1_8_%=:\n"
"ds_read_b64_tr_b16 v[214:215], v249 offset:10240\n"
"v_mfma_f32_16x16x32_bf16 a[144:147], v[216:219], v[168:171], a[144:147]\n"
"buffer_load_dwordx4 v252, s[36:39], s45 offen offset:0 nt lds\n"
"v_mfma_f32_16x16x32_bf16 a[148:151], v[216:219], v[172:175], a[148:151]\n"
"s_waitcnt lgkmcnt(12)\n"
"v_exp_f32 v128, v128\n"
"v_mfma_f32_16x16x32_bf16 a[156:159], v[220:223], v[172:175], a[156:159]\n"
"s_add_u32 m0, m0, 0x380\n"
"ds_read_b64_tr_b16 v[216:217], v250 offset:2048\n"
"v_mfma_f32_16x16x32_bf16 a[152:155], v[220:223], v[168:171], a[152:155]\n"
"ds_read_b64_tr_b16 v[218:219], v250 offset:10240\n"
"v_add_f32 v240, v240, v128\n"
"v_mfma_f32_16x16x32_bf16 v[28:31], v[220:223], v[164:167], v[28:31]\n"
"v_exp_f32 v129, v129\n"
"v_mfma_f32_16x16x32_bf16 v[24:27], v[220:223], v[160:163], v[24:27]\n"
"s_waitcnt lgkmcnt(12)\n"
"v_exp_f32 v130, v130\n"
"v_mfma_f32_16x16x32_bf16 v[32:35], v[224:227], v[160:163], v[32:35]\n"
"ds_read_b64_tr_b16 v[220:221], v251 offset:2048\n"
"v_add_f32 v240, v240, v129\n"
"v_mfma_f32_16x16x32_bf16 v[36:39], v[224:227], v[164:167], v[36:39]\n"
"ds_read_b64_tr_b16 v[222:223], v251 offset:10240\n"
"v_cvt_pk_bf16_f32 v176, v128, v129\n"
"v_mfma_f32_16x16x32_bf16 a[160:163], v[224:227], v[168:171], a[160:163]\n"
"v_exp_f32 v131, v131\n"
"v_mfma_f32_16x16x32_bf16 a[164:167], v[224:227], v[172:175], a[164:167]\n"
"s_waitcnt lgkmcnt(12)\n"
"buffer_load_dwordx4 v252, s[36:39], s45 offen offset:128 nt lds\n"
"v_mfma_f32_16x16x32_bf16 a[172:175], v[228:231], v[172:175], a[172:175]\n"
"ds_read_b64_tr_b16 v[224:225], v248 offset:3072\n"
"v_add_f32 v240, v240, v130\n"
"v_mfma_f32_16x16x32_bf16 a[168:171], v[228:231], v[168:171], a[168:171]\n"
"ds_read_b64_tr_b16 v[226:227], v248 offset:11264\n"
"v_add_f32 v240, v240, v131\n"
"v_mfma_f32_16x16x32_bf16 v[44:47], v[228:231], v[164:167], v[44:47]\n"
"s_add_u32 m0, m0, 0x380\n"
"v_exp_f32 v132, v132\n"
"v_mfma_f32_16x16x32_bf16 v[40:43], v[228:231], v[160:163], v[40:43]\n"
"s_waitcnt lgkmcnt(12)\n"
"v_exp_f32 v133, v133\n"
"v_mfma_f32_16x16x32_bf16 v[48:51], v[232:235], v[160:163], v[48:51]\n"
"ds_read_b64_tr_b16 v[228:229], v249 offset:3072\n"
"v_cvt_pk_bf16_f32 v177, v130, v131\n"
"v_mfma_f32_16x16x32_bf16 v[52:55], v[232:235], v[164:167], v[52:55]\n"
"ds_read_b64_tr_b16 v[230:231], v249 offset:11264\n"
"v_add_f32 v241, v241, v132\n"
"v_mfma_f32_16x16x32_bf16 a[176:179], v[232:235], v[168:171], a[176:179]\n"
"v_exp_f32 v134, v134\n"
"v_mfma_f32_16x16x32_bf16 a[180:183], v[232:235], v[172:175], a[180:183]\n"
"s_waitcnt lgkmcnt(12)\n"
"v_exp_f32 v135, v135\n"
"v_mfma_f32_16x16x32_bf16 a[188:191], v[236:239], v[172:175], a[188:191]\n"
"buffer_load_dwordx4 v252, s[36:39], s45 offen offset:256 nt lds\n"
"v_mfma_f32_16x16x32_bf16 a[184:187], v[236:239], v[168:171], a[184:187]\n"
"ds_read_b64_tr_b16 v[232:233], v250 offset:3072\n"
"v_add_f32 v241, v241, v133\n"
"v_mfma_f32_16x16x32_bf16 v[60:63], v[236:239], v[164:167], v[60:63]\n"
"ds_read_b64_tr_b16 v[234:235], v250 offset:11264\n"
"v_cvt_pk_bf16_f32 v180, v132, v133\n"
"v_mfma_f32_16x16x32_bf16 v[56:59], v[236:239], v[160:163], v[56:59]\n"
"s_waitcnt lgkmcnt(12)\n"
"v_exp_f32 v136, v136\n"
"v_mfma_f32_16x16x32_bf16 v[64:67], v[208:211], v[160:163], v[64:67]\n"
"ds_read_b64_tr_b16 v[236:237], v251 offset:3072\n"
"v_add_f32 v241, v241, v134\n"
"v_mfma_f32_16x16x32_bf16 v[68:71], v[208:211], v[164:167], v[68:71]\n"
"s_add_u32 m0, m0, 0x380\n"
"ds_read_b64_tr_b16 v[238:239], v251 offset:11264\n"
"v_mfma_f32_16x16x32_bf16 a[192:195], v[208:211], v[168:171], a[192:195]\n"
"v_exp_f32 v137, v137\n"
"v_mfma_f32_16x16x32_bf16 a[196:199], v[208:211], v[172:175], a[196:199]\n"
"s_waitcnt lgkmcnt(12)\n"
"v_add_f32 v241, v241, v135\n"
"v_cvt_pk_bf16_f32 v181, v134, v135\n"
"v_mfma_f32_16x16x32_bf16 a[204:207], v[212:215], v[172:175], a[204:207]\n"
"v_exp_f32 v138, v138\n"
"v_mfma_f32_16x16x32_bf16 a[200:203], v[212:215], v[168:171], a[200:203]\n"
"buffer_load_dwordx4 v252, s[36:39], s45 offen offset:384 nt lds\n"
"v_mfma_f32_16x16x32_bf16 v[76:79], v[212:215], v[164:167], v[76:79]\n"
"s_add_u32 m0, m0, 0x3580\n"
"v_add_f32 v242, v242, v136\n"
"v_add_f32 v242, v242, v137\n"
"v_mfma_f32_16x16x32_bf16 v[72:75], v[212:215], v[160:163], v[72:75]\n"
"s_waitcnt lgkmcnt(10)\n"
"v_exp_f32 v139, v139\n"
"v_mfma_f32_16x16x32_bf16 v[80:83], v[216:219], v[160:163], v[80:83]\n"
"v_cvt_pk_bf16_f32 v184, v136, v137\n"
"v_add_f32 v242, v242, v138\n"
"v_mfma_f32_16x16x32_bf16 v[84:87], v[216:219], v[164:167], v[84:87]\n"
"v_exp_f32 v140, v140\n"
"v_mfma_f32_16x16x32_bf16 a[208:211], v[216:219], v[168:171], a[208:211]\n"
"v_exp_f32 v141, v141\n"
"v_mfma_f32_16x16x32_bf16 a[212:215], v[216:219], v[172:175], a[212:215]\n"
"s_waitcnt vmcnt(16) lgkmcnt(0)\n"
"s_barrier\n"
"v_add_f32 v242, v242, v139\n"
"v_mfma_f32_16x16x32_bf16 a[220:223], v[220:223], v[172:175], a[220:223]\n"
"ds_read_b128 v[208:211], v246 offset:0\n"
"v_cvt_pk_bf16_f32 v185, v138, v139\n"
"v_mfma_f32_16x16x32_bf16 a[216:219], v[220:223], v[168:171], a[216:219]\n"
"s_add_u32 s44, s44, 0x8000\n"
"v_exp_f32 v142, v142\n"
"v_mfma_f32_16x16x32_bf16 v[92:95], v[220:223], v[164:167], v[92:95]\n"
"s_add_u32 s45, s45, 0x8000\n"
"buffer_load_dwordx4 v253, s[40:43], s46 offen offset:0 nt lds\n"
"v_mfma_f32_16x16x32_bf16 v[88:91], v[220:223], v[160:163], v[88:91]\n"
"ds_read_b128 v[212:215], v247 offset:0\n"
"v_add_f32 v243, v243, v140\n"
"v_mfma_f32_16x16x32_bf16 v[96:99], v[224:227], v[160:163], v[96:99]\n"
"s_add_u32 m0, m0, 0x380\n"
"v_exp_f32 v143, v143\n"
"v_mfma_f32_16x16x32_bf16 v[100:103], v[224:227], v[164:167], v[100:103]\n"
"ds_read_b128 v[216:219], v246 offset:1024\n"
"v_add_f32 v243, v243, v141\n"
"v_mfma_f32_16x16x32_bf16 a[224:227], v[224:227], v[168:171], a[224:227]\n"
"ds_read_b128 v[220:223], v247 offset:1024\n"
"v_cvt_pk_bf16_f32 v188, v140, v141\n"
"v_mfma_f32_16x16x32_bf16 a[228:231], v[224:227], v[172:175], a[228:231]\n"
"v_exp_f32 v144, v144\n"
"v_mfma_f32_16x16x32_bf16 a[236:239], v[228:231], v[172:175], a[236:239]\n"
"v_exp_f32 v145, v145\n"
"v_mfma_f32_16x16x32_bf16 a[232:235], v[228:231], v[168:171], a[232:235]\n"
"ds_read_b128 v[224:227], v246 offset:2048\n"
"v_add_f32 v243, v243, v142\n"
"v_mfma_f32_16x16x32_bf16 v[108:111], v[228:231], v[164:167], v[108:111]\n"
"buffer_load_dwordx4 v253, s[40:43], s46 offen offset:128 nt lds\n"
"v_mfma_f32_16x16x32_bf16 v[104:107], v[228:231], v[160:163], v[104:107]\n"
"s_add_u32 m0, m0, 0x380\n"
"v_add_f32 v243, v243, v143\n"
"v_cvt_pk_bf16_f32 v189, v142, v143\n"
"v_mfma_f32_16x16x32_bf16 v[112:115], v[232:235], v[160:163], v[112:115]\n"
"v_exp_f32 v146, v146\n"
"v_mfma_f32_16x16x32_bf16 v[116:119], v[232:235], v[164:167], v[116:119]\n"
"ds_read_b128 v[228:231], v247 offset:2048\n"
"v_add_f32 v240, v240, v144\n"
"v_mfma_f32_16x16x32_bf16 a[240:243], v[232:235], v[168:171], a[240:243]\n"
"v_exp_f32 v147, v147\n"
"v_mfma_f32_16x16x32_bf16 a[244:247], v[232:235], v[172:175], a[244:247]\n"
"ds_read_b128 v[232:235], v246 offset:3072\n"
"v_add_f32 v240, v240, v145\n"
"v_mfma_f32_16x16x32_bf16 a[252:255], v[236:239], v[172:175], a[252:255]\n"
"v_cvt_pk_bf16_f32 v178, v144, v145\n"
"v_add_f32 v240, v240, v146\n"
"v_mfma_f32_16x16x32_bf16 a[248:251], v[236:239], v[168:171], a[248:251]\n"
"v_exp_f32 v148, v148\n"
"v_mfma_f32_16x16x32_bf16 v[124:127], v[236:239], v[164:167], v[124:127]\n"
"v_exp_f32 v149, v149\n"
"v_mfma_f32_16x16x32_bf16 v[120:123], v[236:239], v[160:163], v[120:123]\n"
"s_waitcnt lgkmcnt(6)\n"
"buffer_load_dwordx4 v253, s[40:43], s46 offen offset:256 nt lds\n"
"v_mfma_f32_16x16x32_bf16 v[128:131], v[208:211], a[0:3], v[192:195]\n"
"ds_read_b128 v[236:239], v247 offset:3072\n"
"v_add_f32 v240, v240, v147\n"
"v_mfma_f32_16x16x32_bf16 v[132:135], v[208:211], a[32:35], v[196:199]\n"
"s_add_u32 m0, m0, 0x380\n"
"v_cvt_pk_bf16_f32 v179, v146, v147\n"
"v_add_f32 v241, v241, v148\n"
"v_mfma_f32_16x16x32_bf16 v[136:139], v[208:211], a[64:67], v[200:203]\n"
"v_exp_f32 v150, v150\n"
"v_mfma_f32_16x16x32_bf16 v[140:143], v[208:211], a[96:99], v[204:207]\n"
"s_waitcnt lgkmcnt(6)\n"
"v_exp_f32 v151, v151\n"
"v_mfma_f32_16x16x32_bf16 v[128:131], v[212:215], a[4:7], v[128:131]\n"
"ds_read_b128 v[208:211], v246 offset:8192\n"
"v_add_f32 v241, v241, v149\n"
"v_mfma_f32_16x16x32_bf16 v[132:135], v[212:215], a[36:39], v[132:135]\n"
"v_exp_f32 v152, v152\n"
"v_mfma_f32_16x16x32_bf16 v[136:139], v[212:215], a[68:71], v[136:139]\n"
"v_cvt_pk_bf16_f32 v182, v148, v149\n"
"v_add_f32 v241, v241, v150\n"
"v_mfma_f32_16x16x32_bf16 v[140:143], v[212:215], a[100:103], v[140:143]\n"
"s_waitcnt lgkmcnt(6)\n"
"buffer_load_dwordx4 v253, s[40:43], s46 offen offset:384 nt lds\n"
"v_mfma_f32_16x16x32_bf16 v[128:131], v[216:219], a[8:11], v[128:131]\n"
"ds_read_b128 v[212:215], v247 offset:8192\n"
"v_add_f32 v241, v241, v151\n"
"v_mfma_f32_16x16x32_bf16 v[132:135], v[216:219], a[40:43], v[132:135]\n"
"s_add_u32 m0, m0, 0x3580\n"
"v_exp_f32 v153, v153\n"
"v_mfma_f32_16x16x32_bf16 v[136:139], v[216:219], a[72:75], v[136:139]\n"
"v_exp_f32 v154, v154\n"
"v_mfma_f32_16x16x32_bf16 v[140:143], v[216:219], a[104:107], v[140:143]\n"
"s_waitcnt lgkmcnt(6)\n"
"v_cvt_pk_bf16_f32 v183, v150, v151\n"
"v_add_f32 v242, v242, v152\n"
"v_mfma_f32_16x16x32_bf16 v[128:131], v[220:223], a[12:15], v[128:131]\n"
"ds_read_b128 v[216:219], v246 offset:9216\n"
"v_add_f32 v242, v242, v153\n"
"v_mfma_f32_16x16x32_bf16 v[132:135], v[220:223], a[44:47], v[132:135]\n"
"v_exp_f32 v155, v155\n"
"v_mfma_f32_16x16x32_bf16 v[136:139], v[220:223], a[76:79], v[136:139]\n"
"buffer_load_dwordx4 v253, s[40:43], s47 offen offset:0 nt lds\n"
"v_mfma_f32_16x16x32_bf16 v[140:143], v[220:223], a[108:111], v[140:143]\n"
"s_waitcnt lgkmcnt(6)\n"
"ds_read_b128 v[220:223], v247 offset:9216\n"
"v_mfma_f32_16x16x32_bf16 v[128:131], v[224:227], a[16:19], v[128:131]\n"
"s_add_u32 m0, m0, 0x380\n"
"v_cvt_pk_bf16_f32 v186, v152, v153\n"
"v_add_f32 v242, v242, v154\n"
"v_mfma_f32_16x16x32_bf16 v[132:135], v[224:227], a[48:51], v[132:135]\n"
"v_exp_f32 v156, v156\n"
"v_mfma_f32_16x16x32_bf16 v[136:139], v[224:227], a[80:83], v[136:139]\n"
"v_exp_f32 v157, v157\n"
"v_mfma_f32_16x16x32_bf16 v[140:143], v[224:227], a[112:115], v[140:143]\n"
"s_waitcnt lgkmcnt(6)\n"
"ds_read_b128 v[224:227], v246 offset:10240\n"
"v_mfma_f32_16x16x32_bf16 v[128:131], v[228:231], a[20:23], v[128:131]\n"
"v_exp_f32 v158, v158\n"
"v_mfma_f32_16x16x32_bf16 v[132:135], v[228:231], a[52:55], v[132:135]\n"
"v_add_f32 v242, v242, v155\n"
"v_cvt_pk_bf16_f32 v187, v154, v155\n"
"v_mfma_f32_16x16x32_bf16 v[136:139], v[228:231], a[84:87], v[136:139]\n"
"buffer_load_dwordx4 v253, s[40:43], s47 offen offset:128 nt lds\n"
"v_mfma_f32_16x16x32_bf16 v[140:143], v[228:231], a[116:119], v[140:143]\n"
"s_waitcnt lgkmcnt(6)\n"
"v_exp_f32 v159, v159\n"
"v_mfma_f32_16x16x32_bf16 v[128:131], v[232:235], a[24:27], v[128:131]\n"
"ds_read_b128 v[228:231], v247 offset:10240\n"
"v_add_f32 v243, v243, v156\n"
"v_mfma_f32_16x16x32_bf16 v[132:135], v[232:235], a[56:59], v[132:135]\n"
"s_add_u32 m0, m0, 0x380\n"
"v_add_f32 v243, v243, v157\n"
"v_cvt_pk_bf16_f32 v190, v156, v157\n"
"v_mfma_f32_16x16x32_bf16 v[136:139], v[232:235], a[88:91], v[136:139]\n"
"buffer_load_dwordx4 v253, s[40:43], s47 offen offset:256 nt lds\n"
"v_mfma_f32_16x16x32_bf16 v[140:143], v[232:235], a[120:123], v[140:143]\n"
"s_waitcnt lgkmcnt(6)\n"
"ds_read_b128 v[232:235], v246 offset:11264\n"
"v_mfma_f32_16x16x32_bf16 v[128:131], v[236:239], a[28:31], v[128:131]\n"
"s_add_u32 m0, m0, 0x380\n"
"v_add_f32 v243, v243, v158\n"
"v_add_f32 v243, v243, v159\n"
"v_mfma_f32_16x16x32_bf16 v[132:135], v[236:239], a[60:63], v[132:135]\n"
"v_cvt_pk_bf16_f32 v191, v158, v159\n"
"v_mfma_f32_16x16x32_bf16 v[136:139], v[236:239], a[92:95], v[136:139]\n"
"v_mfma_f32_16x16x32_bf16 v[140:143], v[236:239], a[124:127], v[140:143]\n"
"s_waitcnt lgkmcnt(6)\n"
"ds_read_b128 v[236:239], v247 offset:11264\n"
"v_mfma_f32_16x16x32_bf16 v[144:147], v[208:211], a[0:3], v[192:195]\n"
"buffer_load_dwordx4 v253, s[40:43], s47 offen offset:384 nt lds\n"
"v_mfma_f32_16x16x32_bf16 v[148:151], v[208:211], a[32:35], v[196:199]\n"
"s_cmp_lg_u32 s49, 0\n"
"s_cbranch_scc1 .Lrare2_9_%=\n"
".Lret2_9_%=:\n"
"v_mfma_f32_16x16x32_bf16 v[152:155], v[208:211], a[64:67], v[200:203]\n"
"v_mfma_f32_16x16x32_bf16 v[156:159], v[208:211], a[96:99], v[204:207]\n"
"s_waitcnt lgkmcnt(6)\n"
"v_mfma_f32_16x16x32_bf16 v[144:147], v[212:215], a[4:7], v[144:147]\n"
"s_add_u32 m0, m0, 0xfffeb580\n"
"v_max_f32 v244, v128, v129\n"
"v_max_f32 v245, v130, v131\n"
"v_mfma_f32_16x16x32_bf16 v[148:151], v[212:215], a[36:39], v[148:151]\n"
"s_add_u32 s46, s46, 0x8000\n"
"v_max3_f32 v244, v132, v133, v244\n"
"v_max3_f32 v245, v134, v135, v245\n"
"v_mfma_f32_16x16x32_bf16 v[152:155], v[212:215], a[68:71], v[152:155]\n"
"s_add_u32 s47, s47, 0x8000\n"
"v_max3_f32 v244, v136, v137, v244\n"
"v_max3_f32 v245, v138, v139, v245\n"
"v_mfma_f32_16x16x32_bf16 v[156:159], v[212:215], a[100:103], v[156:159]\n"
"s_waitcnt lgkmcnt(5)\n"
"v_max3_f32 v244, v140, v141, v244\n"
"v_max3_f32 v245, v142, v143, v245\n"
"v_mfma_f32_16x16x32_bf16 v[144:147], v[216:219], a[8:11], v[144:147]\n"
"v_mfma_f32_16x16x32_bf16 v[148:151], v[216:219], a[40:43], v[148:151]\n"
"v_mfma_f32_16x16x32_bf16 v[152:155], v[216:219], a[72:75], v[152:155]\n"
"s_waitcnt vmcnt(16) lgkmcnt(0)\n"
"s_barrier\n"
"v_mfma_f32_16x16x32_bf16 v[156:159], v[216:219], a[104:107], v[156:159]\n"
"ds_read_b64_tr_b16 v[208:209], v248 offset:32768\n"
"v_mfma_f32_16x16x32_bf16 v[144:147], v[220:223], a[12:15], v[144:147]\n"
"ds_read_b64_tr_b16 v[210:211], v248 offset:40960\n"
"v_mfma_f32_16x16x32_bf16 v[148:151], v[220:223], a[44:47], v[148:151]\n"
"ds_read_b64_tr_b16 v[212:213], v249 offset:32768\n"
"v_mfma_f32_16x16x32_bf16 v[152:155], v[220:223], a[76:79], v[152:155]\n"
"buffer_load_dwordx4 v252, s[36:39], s44 offen offset:0 nt lds\n"
"v_mfma_f32_16x16x32_bf16 v[156:159], v[220:223], a[108:111], v[156:159]\n"
"s_add_u32 m0, m0, 0x380\n"
"ds_read_b64_tr_b16 v[214:215], v249 offset:40960\n"
"v_mfma_f32_16x16x32_bf16 v[144:147], v[224:227], a[16:19], v[144:147]\n"
"ds_read_b64_tr_b16 v[216:217], v250 offset:32768\n"
"v_mfma_f32_16x16x32_bf16 v[148:151], v[224:227], a[48:51], v[148:151]\n"
"ds_read_b64_tr_b16 v[218:219], v250 offset:40960\n"
"v_mfma_f32_16x16x32_bf16 v[152:155], v[224:227], a[80:83], v[152:155]\n"
"ds_read_b64_tr_b16 v[220:221], v251 offset:32768\n"
"v_mfma_f32_16x16x32_bf16 v[156:159], v[224:227], a[112:115], v[156:159]\n"
"ds_read_b64_tr_b16 v[222:223], v251 offset:40960\n"
"v_mfma_f32_16x16x32_bf16 v[144:147], v[228:231], a[20:23], v[144:147]\n"
"ds_read_b64_tr_b16 v[224:225], v248 offset:33792\n"
"v_mfma_f32_16x16x32_bf16 v[148:151], v[228:231], a[52:55], v[148:151]\n"
"buffer_load_dwordx4 v252, s[36:39], s44 offen offset:128 nt lds\n"
"v_mfma_f32_16x16x32_bf16 v[152:155], v[228:231], a[84:87], v[152:155]\n"
"s_add_u32 m0, m0, 0x380\n"
"ds_read_b64_tr_b16 v[226:227], v248 offset:41984\n"
"v_mfma_f32_16x16x32_bf16 v[156:159], v[228:231], a[116:119], v[156:159]\n"
"ds_read_b64_tr_b16 v[228:229], v249 offset:33792\n"
"v_mfma_f32_16x16x32_bf16 v[144:147], v[232:235], a[24:27], v[144:147]\n"
"ds_read_b64_tr_b16 v[230:231], v249 offset:41984\n"
"v_mfma_f32_16x16x32_bf16 v[148:151], v[232:235], a[56:59], v[148:151]\n"
"buffer_load_dwordx4 v252, s[36:39], s44 offen offset:256 nt lds\n"
"v_mfma_f32_16x16x32_bf16 v[152:155], v[232:235], a[88:91], v[152:155]\n"
"s_add_u32 m0, m0, 0x380\n"
"v_mfma_f32_16x16x32_bf16 v[156:159], v[232:235], a[120:123], v[156:159]\n"
"ds_read_b64_tr_b16 v[232:233], v250 offset:33792\n"
"v_mfma_f32_16x16x32_bf16 v[144:147], v[236:239], a[28:31], v[144:147]\n"
"ds_read_b64_tr_b16 v[234:235], v250 offset:41984\n"
"v_mfma_f32_16x16x32_bf16 v[148:151], v[236:239], a[60:63], v[148:151]\n"
"v_mfma_f32_16x16x32_bf16 v[152:155], v[236:239], a[92:95], v[152:155]\n"
"v_mfma_f32_16x16x32_bf16 v[156:159], v[236:239], a[124:127], v[156:159]\n"
"s_waitcnt lgkmcnt(12)\n"
"ds_read_b64_tr_b16 v[236:237], v251 offset:33792\n"
"v_mfma_f32_16x16x32_bf16 v[0:3], v[208:211], v[176:179], v[0:3]\n"
"ds_read_b64_tr_b16 v[238:239], v251 offset:41984\n"
"v_max3_f32 v244, v144, v145, v244\n"
"v_mfma_f32_16x16x32_bf16 v[4:7], v[208:211], v[180:183], v[4:7]\n"
"buffer_load_dwordx4 v252, s[36:39], s44 offen offset:384 nt lds\n"
"v_mfma_f32_16x16x32_bf16 a[128:131], v[208:211], v[184:187], a[128:131]\n"
"s_add_u32 m0, m0, 0x3580\n"
"v_max3_f32 v245, v146, v147, v245\n"
"v_max3_f32 v244, v148, v149, v244\n"
"v_mfma_f32_16x16x32_bf16 a[132:135], v[208:211], v[188:191], a[132:135]\n"
"s_waitcnt lgkmcnt(12)\n"
"ds_read_b64_tr_b16 v[208:209], v248 offset:34816\n"
"v_mfma_f32_16x16x32_bf16 a[140:143], v[212:215], v[188:191], a[140:143]\n"
"ds_read_b64_tr_b16 v[210:211], v248 offset:43008\n"
"v_max3_f32 v245, v150, v151, v245\n"
"v_mfma_f32_16x16x32_bf16 a[136:139], v[212:215], v[184:187], a[136:139]\n"
"v_max3_f32 v244, v152, v153, v244\n"
"v_max3_f32 v245, v154, v155, v245\n"
"v_mfma_f32_16x16x32_bf16 v[12:15], v[212:215], v[180:183], v[12:15]\n"
"v_max3_f32 v244, v156, v157, v244\n"
"v_max3_f32 v245, v158, v159, v245\n"
"v_mfma_f32_16x16x32_bf16 v[8:11], v[212:215], v[176:179], v[8:11]\n"
"s_waitcnt lgkmcnt(12)\n"
"v_max_f32 v244, v244, v245\n"
"v_mfma_f32_16x16x32_bf16 v[16:19], v[216:219], v[176:179], v[16:19]\n"
"ds_read_b64_tr_b16 v[212:213], v249 offset:34816\n"
"v_cmp_nge_f32 vcc, s50, v244\n"
"v_mfma_f32_16x16x32_bf16 v[20:23], v[216:219], v[180:183], v[20:23]\n"
"s_cbranch_vccnz .Lrare1_10_%=\n"
".Lret1_10_%=:\n"
"ds_read_b64_tr_b16 v[214:215], v249 offset:43008\n"
"v_mfma_f32_16x16x32_bf16 a[144:147], v[216:219], v[184:187], a[144:147]\n"
"buffer_load_dwordx4 v252, s[36:39], s45 offen offset:0 nt lds\n"
"v_mfma_f32_16x16x32_bf16 a[148:151], v[216:219], v[188:191], a[148:151]\n"
"s_waitcnt lgkmcnt(12)\n"
"v_exp_f32 v128, v128\n"
"v_mfma_f32_16x16x32_bf16 a[156:159], v[220:223], v[188:191], a[156:159]\n"
"s_add_u32 m0, m0, 0x380\n"
"ds_read_b64_tr_b16 v[216:217], v250 offset:34816\n"
"v_mfma_f32_16x16x32_bf16 a[152:155], v[220:223], v[184:187], a[152:155]\n"
"ds_read_b64_tr_b16 v[218:219], v250 offset:43008\n"
"v_add_f32 v240, v240, v128\n"
"v_mfma_f32_16x16x32_bf16 v[28:31], v[220:223], v[180:183], v[28:31]\n"
"v_exp_f32 v129, v129\n"
"v_mfma_f32_16x16x32_bf16 v[24:27], v[220:223], v[176:179], v[24:27]\n"
"s_waitcnt lgkmcnt(12)\n"
"v_exp_f32 v130, v130\n"
"v_mfma_f32_16x16x32_bf16 v[32:35], v[224:227], v[176:179], v[32:35]\n"
"ds_read_b64_tr_b16 v[220:221], v251 offset:34816\n"
"v_add_f32 v240, v240, v129\n"
"v_mfma_f32_16x16x32_bf16 v[36:39], v[224:227], v[180:183], v[36:39]\n"
"ds_read_b64_tr_b16 v[222:223], v251 offset:43008\n"
"v_cvt_pk_bf16_f32 v160, v128, v129\n"
"v_mfma_f32_16x16x32_bf16 a[160:163], v[224:227], v[184:187], a[160:163]\n"
"v_exp_f32 v131, v131\n"
"v_mfma_f32_16x16x32_bf16 a[164:167], v[224:227], v[188:191], a[164:167]\n"
"s_waitcnt lgkmcnt(12)\n"
"buffer_load_dwordx4 v252, s[36:39], s45 offen offset:128 nt lds\n"
"v_mfma_f32_16x16x32_bf16 a[172:175], v[228:231], v[188:191], a[172:175]\n"
"ds_read_b64_tr_b16 v[224:225], v248 offset:35840\n"
"v_add_f32 v240, v240, v130\n"
"v_mfma_f32_16x16x32_bf16 a[168:171], v[228:231], v[184:187], a[168:171]\n"
"ds_read_b64_tr_b16 v[226:227], v248 offset:44032\n"
"v_add_f32 v240, v240, v131\n"
"v_mfma_f32_16x16x32_bf16 v[44:47], v[228:231], v[180:183], v[44:47]\n"
"s_add_u32 m0, m0, 0x380\n"
"v_exp_f32 v132, v132\n"
"v_mfma_f32_16x16x32_bf16 v[40:43], v[228:231], v[176:179], v[40:43]\n"
"s_waitcnt lgkmcnt(12)\n"
"v_exp_f32 v133, v133\n"
"v_mfma_f32_16x16x32_bf16 v[48:51], v[232:235], v[176:179], v[48:51]\n"
"ds_read_b64_tr_b16 v[228:229], v249 offset:35840\n"
"v_cvt_pk_bf16_f32 v161, v130, v131\n"
"v_mfma_f32_16x16x32_bf16 v[52:55], v[232:235], v[180:183], v[52:55]\n"
"ds_read_b64_tr_b16 v[230:231], v249 offset:44032\n"
"v_add_f32 v241, v241, v132\n"
"v_mfma_f32_16x16x32_bf16 a[176:179], v[232:235], v[184:187], a[176:179]\n"
"v_exp_f32 v134, v134\n"
"v_mfma_f32_16x16x32_bf16 a[180:183], v[232:235], v[188:191], a[180:183]\n"
"s_waitcnt lgkmcnt(12)\n"
"v_exp_f32 v135, v135\n"
"v_mfma_f32_16x16x32_bf16 a[188:191], v[236:239], v[188:191], a[188:191]\n"
"buffer_load_dwordx4 v252, s[36:39], s45 offen offset:256 nt lds\n"
"v_mfma_f32_16x16x32_bf16 a[184:187], v[236:239], v[184:187], a[184:187]\n"
"ds_read_b64_tr_b16 v[232:233], v250 offset:35840\n"
"v_add_f32 v241, v241, v133\n"
"v_mfma_f32_16x16x32_bf16 v[60:63], v[236:239], v[180:183], v[60:63]\n"
"ds_read_b64_tr_b16 v[234:235], v250 offset:44032\n"
"v_cvt_pk_bf16_f32 v164, v132, v133\n"
"v_mfma_f32_16x16x32_bf16 v[56:59], v[236:239], v[176:179], v[56:59]\n"
"s_waitcnt lgkmcnt(12)\n"
"v_exp_f32 v136, v136\n"
"v_mfma_f32_16x16x32_bf16 v[64:67], v[208:211], v[176:179], v[64:67]\n"
"ds_read_b64_tr_b16 v[236:237], v251 offset:35840\n"
"v_add_f32 v241, v241, v134\n"
"v_mfma_f32_16x16x32_bf16 v[68:71], v[208:211], v[180:183], v[68:71]\n"
"s_add_u32 m0, m0, 0x380\n"
"ds_read_b64_tr_b16 v[238:239], v251 offset:44032\n"
"v_mfma_f32_16x16x32_bf16 a[192:195], v[208:211], v[184:187], a[192:195]\n"
"v_exp_f32 v137, v137\n"
"v_mfma_f32_16x16x32_bf16 a[196:199], v[208:211], v[188:191], a[196:199]\n"
"s_waitcnt lgkmcnt(12)\n"
"v_add_f32 v241, v241, v135\n"
"v_cvt_pk_bf16_f32 v165, v134, v135\n"
"v_mfma_f32_16x16x32_bf16 a[204:207], v[212:215], v[188:191], a[204:207]\n"
"v_exp_f32 v138, v138\n"
"v_mfma_f32_16x16x32_bf16 a[200:203], v[212:215], v[184:187], a[200:203]\n"
"buffer_load_dwordx4 v252, s[36:39], s45 offen offset:384 nt lds\n"
"v_mfma_f32_16x16x32_bf16 v[76:79], v[212:215], v[180:183], v[76:79]\n"
"s_add_u32 m0, m0, 0x13580\n"
"v_add_f32 v242, v242, v136\n"
"v_add_f32 v242, v242, v137\n"
"v_mfma_f32_16x16x32_bf16 v[72:75], v[212:215], v[176:179], v[72:75]\n"
"s_waitcnt lgkmcnt(10)\n"
"v_exp_f32 v139, v139\n"
"v_mfma_f32_16x16x32_bf16 v[80:83], v[216:219], v[176:179], v[80:83]\n"
"v_cvt_pk_bf16_f32 v168, v136, v137\n"
"v_add_f32 v242, v242, v138\n"
"v_mfma_f32_16x16x32_bf16 v[84:87], v[216:219], v[180:183], v[84:87]\n"
"v_exp_f32 v140, v140\n"
"v_mfma_f32_16x16x32_bf16 a[208:211], v[216:219], v[184:187], a[208:211]\n"
"v_exp_f32 v141, v141\n"
"v_mfma_f32_16x16x32_bf16 a[212:215], v[216:219], v[188:191], a[212:215]\n"
"s_waitcnt vmcnt(16) lgkmcnt(0)\n"
"s_barrier\n"
"v_add_f32 v242, v242, v139\n"
"v_mfma_f32_16x16x32_bf16 a[220:223], v[220:223], v[188:191], a[220:223]\n"
"ds_read_b128 v[208:211], v246 offset:32768\n"
"v_cvt_pk_bf16_f32 v169, v138, v139\n"
"v_mfma_f32_16x16x32_bf16 a[216:219], v[220:223], v[184:187], a[216:219]\n"
"s_add_u32 s44, s44, 0x8000\n"
"v_exp_f32 v142, v142\n"
"v_mfma_f32_16x16x32_bf16 v[92:95], v[220:223], v[180:183], v[92:95]\n"
"s_add_u32 s45, s45, 0x8000\n"
"buffer_load_dwordx4 v253, s[40:43], s46 offen offset:0 nt lds\n"
"v_mfma_f32_16x16x32_bf16 v[88:91], v[220:223], v[176:179], v[88:91]\n"
"ds_read_b128 v[212:215], v247 offset:32768\n"
"v_add_f32 v243, v243, v140\n"
"v_mfma_f32_16x16x32_bf16 v[96:99], v[224:227], v[176:179], v[96:99]\n"
"s_add_u32 m0, m0, 0x380\n"
"v_exp_f32 v143, v143\n"
"v_mfma_f32_16x16x32_bf16 v[100:103], v[224:227], v[180:183], v[100:103]\n"
"ds_read_b128 v[216:219], v246 offset:33792\n"
"v_add_f32 v243, v243, v141\n"
"v_mfma_f32_16x16x32_bf16 a[224:227], v[224:227], v[184:187], a[224:227]\n"
"ds_read_b128 v[220:223], v247 offset:33792\n"
"v_cvt_pk_bf16_f32 v172, v140, v141\n"
"v_mfma_f32_16x16x32_bf16 a[228:231], v[224:227], v[188:191], a[228:231]\n"
"v_exp_f32 v144, v144\n"
"v_mfma_f32_16x16x32_bf16 a[236:239], v[228:231], v[188:191], a[236:239]\n"
"v_exp_f32 v145, v145\n"
"v_mfma_f32_16x16x32_bf16 a[232:235], v[228:231], v[184:187], a[232:235]\n"
"ds_read_b128 v[224:227], v246 offset:34816\n"
"v_add_f32 v243, v243, v142\n"
"v_mfma_f32_16x16x32_bf16 v[108:111], v[228:231], v[180:183], v[108:111]\n"
"buffer_load_dwordx4 v253, s[40:43], s46 offen offset:128 nt lds\n"
"v_mfma_f32_16x16x32_bf16 v[104:107], v[228:231], v[176:179], v[104:107]\n"
"s_add_u32 m0, m0, 0x380\n"
"v_add_f32 v243, v243, v143\n"
"v_cvt_pk_bf16_f32 v173, v142, v143\n"
"v_mfma_f32_16x16x32_bf16 v[112:115], v[232:235], v[176:179], v[112:115]\n"
"v_exp_f32 v146, v146\n"
"v_mfma_f32_16x16x32_bf16 v[116:119], v[232:235], v[180:183], v[116:119]\n"
"ds_read_b128 v[228:231], v247 offset:34816\n"
"v_add_f32 v240, v240, v144\n"
"v_mfma_f32_16x16x32_bf16 a[240:243], v[232:235], v[184:187], a[240:243]\n"
"v_exp_f32 v147, v147\n"
"v_mfma_f32_16x16x32_bf16 a[244:247], v[232:235], v[188:191], a[244:247]\n"
"ds_read_b128 v[232:235], v246 offset:35840\n"
"v_add_f32 v240, v240, v145\n"
"v_mfma_f32_16x16x32_bf16 a[252:255], v[236:239], v[188:191], a[252:255]\n"
"v_cvt_pk_bf16_f32 v162, v144, v145\n"
"v_add_f32 v240, v240, v146\n"
"v_mfma_f32_16x16x32_bf16 a[248:251], v[236:239], v[184:187], a[248:251]\n"
"v_exp_f32 v148, v148\n"
"v_mfma_f32_16x16x32_bf16 v[124:127], v[236:239], v[180:183], v[124:127]\n"
"v_exp_f32 v149, v149\n"
"v_mfma_f32_16x16x32_bf16 v[120:123], v[236:239], v[176:179], v[120:123]\n"
"s_waitcnt lgkmcnt(6)\n"
"buffer_load_dwordx4 v253, s[40:43], s46 offen offset:256 nt lds\n"
"v_mfma_f32_16x16x32_bf16 v[128:131], v[208:211], a[0:3], v[192:195]\n"
"ds_read_b128 v[236:239], v247 offset:35840\n"
"v_add_f32 v240, v240, v147\n"
"v_mfma_f32_16x16x32_bf16 v[132:135], v[208:211], a[32:35], v[196:199]\n"
"s_add_u32 m0, m0, 0x380\n"
"v_cvt_pk_bf16_f32 v163, v146, v147\n"
"v_add_f32 v241, v241, v148\n"
"v_mfma_f32_16x16x32_bf16 v[136:139], v[208:211], a[64:67], v[200:203]\n"
"v_exp_f32 v150, v150\n"
"v_mfma_f32_16x16x32_bf16 v[140:143], v[208:211], a[96:99], v[204:207]\n"
"s_waitcnt lgkmcnt(6)\n"
"v_exp_f32 v151, v151\n"
"v_mfma_f32_16x16x32_bf16 v[128:131], v[212:215], a[4:7], v[128:131]\n"
"ds_read_b128 v[208:211], v246 offset:40960\n"
"v_add_f32 v241, v241, v149\n"
"v_mfma_f32_16x16x32_bf16 v[132:135], v[212:215], a[36:39], v[132:135]\n"
"v_exp_f32 v152, v152\n"
"v_mfma_f32_16x16x32_bf16 v[136:139], v[212:215], a[68:71], v[136:139]\n"
"v_cvt_pk_bf16_f32 v166, v148, v149\n"
"v_add_f32 v241, v241, v150\n"
"v_mfma_f32_16x16x32_bf16 v[140:143], v[212:215], a[100:103], v[140:143]\n"
"s_waitcnt lgkmcnt(6)\n"
"buffer_load_dwordx4 v253, s[40:43], s46 offen offset:384 nt lds\n"
"v_mfma_f32_16x16x32_bf16 v[128:131], v[216:219], a[8:11], v[128:131]\n"
"ds_read_b128 v[212:215], v247 offset:40960\n"
"v_add_f32 v241, v241, v151\n"
"v_mfma_f32_16x16x32_bf16 v[132:135], v[216:219], a[40:43], v[132:135]\n"
"s_add_u32 m0, m0, 0x3580\n"
"v_exp_f32 v153, v153\n"
"v_mfma_f32_16x16x32_bf16 v[136:139], v[216:219], a[72:75], v[136:139]\n"
"v_exp_f32 v154, v154\n"
"v_mfma_f32_16x16x32_bf16 v[140:143], v[216:219], a[104:107], v[140:143]\n"
"s_waitcnt lgkmcnt(6)\n"
"v_cvt_pk_bf16_f32 v167, v150, v151\n"
"v_add_f32 v242, v242, v152\n"
"v_mfma_f32_16x16x32_bf16 v[128:131], v[220:223], a[12:15], v[128:131]\n"
"ds_read_b128 v[216:219], v246 offset:41984\n"
"v_add_f32 v242, v242, v153\n"
"v_mfma_f32_16x16x32_bf16 v[132:135], v[220:223], a[44:47], v[132:135]\n"
"v_exp_f32 v155, v155\n"
"v_mfma_f32_16x16x32_bf16 v[136:139], v[220:223], a[76:79], v[136:139]\n"
"buffer_load_dwordx4 v253, s[40:43], s47 offen offset:0 nt lds\n"
"v_mfma_f32_16x16x32_bf16 v[140:143], v[220:223], a[108:111], v[140:143]\n"
"s_waitcnt lgkmcnt(6)\n"
"ds_read_b128 v[220:223], v247 offset:41984\n"
"v_mfma_f32_16x16x32_bf16 v[128:131], v[224:227], a[16:19], v[128:131]\n"
"s_add_u32 m0, m0, 0x380\n"
"v_cvt_pk_bf16_f32 v170, v152, v153\n"
"v_add_f32 v242, v242, v154\n"
"v_mfma_f32_16x16x32_bf16 v[132:135], v[224:227], a[48:51], v[132:135]\n"
"v_exp_f32 v156, v156\n"
"v_mfma_f32_16x16x32_bf16 v[136:139], v[224:227], a[80:83], v[136:139]\n"
"v_exp_f32 v157, v157\n"
"v_mfma_f32_16x16x32_bf16 v[140:143], v[224:227], a[112:115], v[140:143]\n"
"s_waitcnt lgkmcnt(6)\n"
"ds_read_b128 v[224:227], v246 offset:43008\n"
"v_mfma_f32_16x16x32_bf16 v[128:131], v[228:231], a[20:23], v[128:131]\n"
"v_exp_f32 v158, v158\n"
"v_mfma_f32_16x16x32_bf16 v[132:135], v[228:231], a[52:55], v[132:135]\n"
"v_add_f32 v242, v242, v155\n"
"v_cvt_pk_bf16_f32 v171, v154, v155\n"
"v_mfma_f32_16x16x32_bf16 v[136:139], v[228:231], a[84:87], v[136:139]\n"
"buffer_load_dwordx4 v253, s[40:43], s47 offen offset:128 nt lds\n"
"v_mfma_f32_16x16x32_bf16 v[140:143], v[228:231], a[116:119], v[140:143]\n"
"s_waitcnt lgkmcnt(6)\n"
"v_exp_f32 v159, v159\n"
"v_mfma_f32_16x16x32_bf16 v[128:131], v[232:235], a[24:27], v[128:131]\n"
"ds_read_b128 v[228:231], v247 offset:43008\n"
"v_add_f32 v243, v243, v156\n"
"v_mfma_f32_16x16x32_bf16 v[132:135], v[232:235], a[56:59], v[132:135]\n"
"s_add_u32 m0, m0, 0x380\n"
"v_add_f32 v243, v243, v157\n"
"v_cvt_pk_bf16_f32 v174, v156, v157\n"
"v_mfma_f32_16x16x32_bf16 v[136:139], v[232:235], a[88:91], v[136:139]\n"
"buffer_load_dwordx4 v253, s[40:43], s47 offen offset:256 nt lds\n"
"v_mfma_f32_16x16x32_bf16 v[140:143], v[232:235], a[120:123], v[140:143]\n"
"s_waitcnt lgkmcnt(6)\n"
"ds_read_b128 v[232:235], v246 offset:44032\n"
"v_mfma_f32_16x16x32_bf16 v[128:131], v[236:239], a[28:31], v[128:131]\n"
"s_add_u32 m0, m0, 0x380\n"
"v_add_f32 v243, v243, v158\n"
"v_add_f32 v243, v243, v159\n"
"v_mfma_f32_16x16x32_bf16 v[132:135], v[236:239], a[60:63], v[132:135]\n"
"v_cvt_pk_bf16_f32 v175, v158, v159\n"
"v_mfma_f32_16x16x32_bf16 v[136:139], v[236:239], a[92:95], v[136:139]\n"
"v_mfma_f32_16x16x32_bf16 v[140:143], v[236:239], a[124:127], v[140:143]\n"
"s_waitcnt lgkmcnt(6)\n"
"ds_read_b128 v[236:239], v247 offset:44032\n"
"v_mfma_f32_16x16x32_bf16 v[144:147], v[208:211], a[0:3], v[192:195]\n"
"buffer_load_dwordx4 v253, s[40:43], s47 offen offset:384 nt lds\n"
"v_mfma_f32_16x16x32_bf16 v[148:151], v[208:211], a[32:35], v[196:199]\n"
"s_cmp_lg_u32 s49, 0\n"
"s_cbranch_scc1 .Lrare2_11_%=\n"
".Lret2_11_%=:\n"
"v_mfma_f32_16x16x32_bf16 v[152:155], v[208:211], a[64:67], v[200:203]\n"
"s_sub_u32 s48, s48, 1\n"
"v_mfma_f32_16x16x32_bf16 v[156:159], v[208:211], a[96:99], v[204:207]\n"
"s_waitcnt lgkmcnt(6)\n"
"s_cmp_lg_u32 s48, 0\n"
"s_cbranch_scc1 .Lloops_%=\n"
"v_mfma_f32_16x16x32_bf16 v[144:147], v[212:215], a[4:7], v[144:147]\n"
"v_max_f32 v244, v128, v129\n"
"v_max_f32 v245, v130, v131\n"
"v_mfma_f32_16x16x32_bf16 v[148:151], v[212:215], a[36:39], v[148:151]\n"
"v_max3_f32 v244, v132, v133, v244\n"
"v_max3_f32 v245, v134, v135, v245\n"
"v_mfma_f32_16x16x32_bf16 v[152:155], v[212:215], a[68:71], v[152:155]\n"
"v_max3_f32 v244, v136, v137, v244\n"
"v_max3_f32 v245, v138, v139, v245\n"
"v_mfma_f32_16x16x32_bf16 v[156:159], v[212:215], a[100:103], v[156:159]\n"
"s_waitcnt lgkmcnt(5)\n"
"v_max3_f32 v244, v140, v141, v244\n"
"v_max3_f32 v245, v142, v143, v245\n"
"v_mfma_f32_16x16x32_bf16 v[144:147], v[216:219], a[8:11], v[144:147]\n"
"v_mfma_f32_16x16x32_bf16 v[148:151], v[216:219], a[40:43], v[148:151]\n"
"v_mfma_f32_16x16x32_bf16 v[152:155], v[216:219], a[72:75], v[152:155]\n"
"s_waitcnt vmcnt(16) lgkmcnt(0)\n"
"s_barrier\n"
"v_mfma_f32_16x16x32_bf16 v[156:159], v[216:219], a[104:107], v[156:159]\n"
"ds_read_b64_tr_b16 v[208:209], v248 offset:0\n"
"v_mfma_f32_16x16x32_bf16 v[144:147], v[220:223], a[12:15], v[144:147]\n"
"ds_read_b64_tr_b16 v[210:211], v248 offset:8192\n"
"v_mfma_f32_16x16x32_bf16 v[148:151], v[220:223], a[44:47], v[148:151]\n"
"ds_read_b64_tr_b16 v[212:213], v249 offset:0\n"
"v_mfma_f32_16x16x32_bf16 v[152:155], v[220:223], a[76:79], v[152:155]\n"
"v_mfma_f32_16x16x32_bf16 v[156:159], v[220:223], a[108:111], v[156:159]\n"
"ds_read_b64_tr_b16 v[214:215], v249 offset:8192\n"
"v_mfma_f32_16x16x32_bf16 v[144:147], v[224:227], a[16:19], v[144:147]\n"
"ds_read_b64_tr_b16 v[216:217], v250 offset:0\n"
"v_mfma_f32_16x16x32_bf16 v[148:151], v[224:227], a[48:51], v[148:151]\n"
"ds_read_b64_tr_b16 v[218:219], v250 offset:8192\n"
"v_mfma_f32_16x16x32_bf16 v[152:155], v[224:227], a[80:83], v[152:155]\n"
"ds_read_b64_tr_b16 v[220:221], v251 offset:0\n"
"v_mfma_f32_16x16x32_bf16 v[156:159], v[224:227], a[112:115], v[156:159]\n"
"ds_read_b64_tr_b16 v[222:223], v251 offset:8192\n"
"v_mfma_f32_16x16x32_bf16 v[144:147], v[228:231], a[20:23], v[144:147]\n"
"ds_read_b64_tr_b16 v[224:225], v248 offset:1024\n"
"v_mfma_f32_16x16x32_bf16 v[148:151], v[228:231], a[52:55], v[148:151]\n"
"v_mfma_f32_16x16x32_bf16 v[152:155], v[228:231], a[84:87], v[152:155]\n"
"ds_read_b64_tr_b16 v[226:227], v248 offset:9216\n"
"v_mfma_f32_16x16x32_bf16 v[156:159], v[228:231], a[116:119], v[156:159]\n"
"ds_read_b64_tr_b16 v[228:229], v249 offset:1024\n"
"v_mfma_f32_16x16x32_bf16 v[144:147], v[232:235], a[24:27], v[144:147]\n"
"ds_read_b64_tr_b16 v[230:231], v249 offset:9216\n"
"v_mfma_f32_16x16x32_bf16 v[148:151], v[232:235], a[56:59], v[148:151]\n"
"v_mfma_f32_16x16x32_bf16 v[152:155], v[232:235], a[88:91], v[152:155]\n"
"v_mfma_f32_16x16x32_bf16 v[156:159], v[232:235], a[120:123], v[156:159]\n"
"ds_read_b64_tr_b16 v[232:233], v250 offset:1024\n"
"v_mfma_f32_16x16x32_bf16 v[144:147], v[236:239], a[28:31], v[144:147]\n"
"ds_read_b64_tr_b16 v[234:235], v250 offset:9216\n"
"v_mfma_f32_16x16x32_bf16 v[148:151], v[236:239], a[60:63], v[148:151]\n"
"v_mfma_f32_16x16x32_bf16 v[152:155], v[236:239], a[92:95], v[152:155]\n"
"v_mfma_f32_16x16x32_bf16 v[156:159], v[236:239], a[124:127], v[156:159]\n"
"s_waitcnt lgkmcnt(12)\n"
"ds_read_b64_tr_b16 v[236:237], v251 offset:1024\n"
"v_mfma_f32_16x16x32_bf16 v[0:3], v[208:211], v[160:163], v[0:3]\n"
"ds_read_b64_tr_b16 v[238:239], v251 offset:9216\n"
"v_max3_f32 v244, v144, v145, v244\n"
"v_mfma_f32_16x16x32_bf16 v[4:7], v[208:211], v[164:167], v[4:7]\n"
"v_mfma_f32_16x16x32_bf16 a[128:131], v[208:211], v[168:171], a[128:131]\n"
"v_max3_f32 v245, v146, v147, v245\n"
"v_max3_f32 v244, v148, v149, v244\n"
"v_mfma_f32_16x16x32_bf16 a[132:135], v[208:211], v[172:175], a[132:135]\n"
"s_waitcnt lgkmcnt(12)\n"
"ds_read_b64_tr_b16 v[208:209], v248 offset:2048\n"
"v_mfma_f32_16x16x32_bf16 a[140:143], v[212:215], v[172:175], a[140:143]\n"
"ds_read_b64_tr_b16 v[210:211], v248 offset:10240\n"
"v_max3_f32 v245, v150, v151, v245\n"
"v_mfma_f32_16x16x32_bf16 a[136:139], v[212:215], v[168:171], a[136:139]\n"
"v_max3_f32 v244, v152, v153, v244\n"
"v_max3_f32 v245, v154, v155, v245\n"
"v_mfma_f32_16x16x32_bf16 v[12:15], v[212:215], v[164:167], v[12:15]\n"
"v_max3_f32 v244, v156, v157, v244\n"
"v_max3_f32 v245, v158, v159, v245\n"
"v_mfma_f32_16x16x32_bf16 v[8:11], v[212:215], v[160:163], v[8:11]\n"
"s_waitcnt lgkmcnt(12)\n"
"v_max_f32 v244, v244, v245\n"
"v_mfma_f32_16x16x32_bf16 v[16:19], v[216:219], v[160:163], v[16:19]\n"
"ds_read_b64_tr_b16 v[212:213], v249 offset:2048\n"
"v_cmp_nge_f32 vcc, s50, v244\n"
"v_mfma_f32_16x16x32_bf16 v[20:23], v[216:219], v[164:167], v[20:23]\n"
"s_cbranch_vccnz .Lrare1_12_%=\n"
".Lret1_12_%=:\n"
"ds_read_b64_tr_b16 v[214:215], v249 offset:10240\n"
"v_mfma_f32_16x16x32_bf16 a[144:147], v[216:219], v[168:171], a[144:147]\n"
"v_mfma_f32_16x16x32_bf16 a[148:151], v[216:219], v[172:175], a[148:151]\n"
"s_waitcnt lgkmcnt(12)\n"
"v_exp_f32 v128, v128\n"
"v_mfma_f32_16x16x32_bf16 a[156:159], v[220:223], v[172:175], a[156:159]\n"
"ds_read_b64_tr_b16 v[216:217], v250 offset:2048\n"
"v_mfma_f32_16x16x32_bf16 a[152:155], v[220:223], v[168:171], a[152:155]\n"
"ds_read_b64_tr_b16 v[218:219], v250 offset:10240\n"
"v_add_f32 v240, v240, v128\n"
"v_mfma_f32_16x16x32_bf16 v[28:31], v[220:223], v[164:167], v[28:31]\n"
"v_exp_f32 v129, v129\n"
"v_mfma_f32_16x16x32_bf16 v[24:27], v[220:223], v[160:163], v[24:27]\n"
"s_waitcnt lgkmcnt(12)\n"
"v_exp_f32 v130, v130\n"
"v_mfma_f32_16x16x32_bf16 v[32:35], v[224:227], v[160:163], v[32:35]\n"
"ds_read_b64_tr_b16 v[220:221], v251 offset:2048\n"
"v_add_f32 v240, v240, v129\n"
"v_mfma_f32_16x16x32_bf16 v[36:39], v[224:227], v[164:167], v[36:39]\n"
"ds_read_b64_tr_b16 v[222:223], v251 offset:10240\n"
"v_cvt_pk_bf16_f32 v176, v128, v129\n"
"v_mfma_f32_16x16x32_bf16 a[160:163], v[224:227], v[168:171], a[160:163]\n"
"v_exp_f32 v131, v131\n"
"v_mfma_f32_16x16x32_bf16 a[164:167], v[224:227], v[172:175], a[164:167]\n"
"s_waitcnt lgkmcnt(12)\n"
"v_mfma_f32_16x16x32_bf16 a[172:175], v[228:231], v[172:175], a[172:175]\n"
"ds_read_b64_tr_b16 v[224:225], v248 offset:3072\n"
"v_add_f32 v240, v240, v130\n"
"v_mfma_f32_16x16x32_bf16 a[168:171], v[228:231], v[168:171], a[168:171]\n"
"ds_read_b64_tr_b16 v[226:227], v248 offset:11264\n"
"v_add_f32 v240, v240, v131\n"
"v_mfma_f32_16x16x32_bf16 v[44:47], v[228:231], v[164:167], v[44:47]\n"
"v_exp_f32 v132, v132\n"
"v_mfma_f32_16x16x32_bf16 v[40:43], v[228:231], v[160:163], v[40:43]\n"
"s_waitcnt lgkmcnt(12)\n"
"v_exp_f32 v133, v133\n"
"v_mfma_f32_16x16x32_bf16 v[48:51], v[232:235], v[160:163], v[48:51]\n"
"ds_read_b64_tr_b16 v[228:229], v249 offset:3072\n"
"v_cvt_pk_bf16_f32 v177, v130, v131\n"
"v_mfma_f32_16x16x32_bf16 v[52:55], v[232:235], v[164:167], v[52:55]\n"
"ds_read_b64_tr_b16 v[230:231], v249 offset:11264\n"
"v_add_f32 v241, v241, v132\n"
"v_mfma_f32_16x16x32_bf16 a[176:179], v[232:235], v[168:171], a[176:179]\n"
"v_exp_f32 v134, v134\n"
"v_mfma_f32_16x16x32_bf16 a[180:183], v[232:235], v[172:175], a[180:183]\n"
"s_waitcnt lgkmcnt(12)\n"
"v_exp_f32 v135, v135\n"
"v_mfma_f32_16x16x32_bf16 a[188:191], v[236:239], v[172:175], a[188:191]\n"
"v_mfma_f32_16x16x32_bf16 a[184:187], v[236:239], v[168:171], a[184:187]\n"
"ds_read_b64_tr_b16 v[232:233], v250 offset:3072\n"
"v_add_f32 v241, v241, v133\n"
"v_mfma_f32_16x16x32_bf16 v[60:63], v[236:239], v[164:167], v[60:63]\n"
"ds_read_b64_tr_b16 v[234:235], v250 offset:11264\n"
"v_cvt_pk_bf16_f32 v180, v132, v133\n"
"v_mfma_f32_16x16x32_bf16 v[56:59], v[236:239], v[160:163], v[56:59]\n"
"s_waitcnt lgkmcnt(12)\n"
"v_exp_f32 v136, v136\n"
"v_mfma_f32_16x16x32_bf16 v[64:67], v[208:211], v[160:163], v[64:67]\n"
"ds_read_b64_tr_b16 v[236:237], v251 offset:3072\n"
"v_add_f32 v241, v241, v134\n"
"v_mfma_f32_16x16x32_bf16 v[68:71], v[208:211], v[164:167], v[68:71]\n"
"ds_read_b64_tr_b16 v[238:239], v251 offset:11264\n"
"v_mfma_f32_16x16x32_bf16 a[192:195], v[208:211], v[168:171], a[192:195]\n"
"v_exp_f32 v137, v137\n"
"v_mfma_f32_16x16x32_bf16 a[196:199], v[208:211], v[172:175], a[196:199]\n"
"s_waitcnt lgkmcnt(12)\n"
"v_add_f32 v241, v241, v135\n"
"v_cvt_pk_bf16_f32 v181, v134, v135\n"
"v_mfma_f32_16x16x32_bf16 a[204:207], v[212:215], v[172:175], a[204:207]\n"
"v_exp_f32 v138, v138\n"
"v_mfma_f32_16x16x32_bf16 a[200:203], v[212:215], v[168:171], a[200:203]\n"
"v_mfma_f32_16x16x32_bf16 v[76:79], v[212:215], v[164:167], v[76:79]\n"
"v_add_f32 v242, v242, v136\n"
"v_add_f32 v242, v242, v137\n"
"v_mfma_f32_16x16x32_bf16 v[72:75], v[212:215], v[160:163], v[72:75]\n"
"s_waitcnt lgkmcnt(10)\n"
"v_exp_f32 v139, v139\n"
"v_mfma_f32_16x16x32_bf16 v[80:83], v[216:219], v[160:163], v[80:83]\n"
"v_cvt_pk_bf16_f32 v184, v136, v137\n"
"v_add_f32 v242, v242, v138\n"
"v_mfma_f32_16x16x32_bf16 v[84:87], v[216:219], v[164:167], v[84:87]\n"
"v_exp_f32 v140, v140\n"
"v_mfma_f32_16x16x32_bf16 a[208:211], v[216:219], v[168:171], a[208:211]\n"
"v_exp_f32 v141, v141\n"
"v_mfma_f32_16x16x32_bf16 a[212:215], v[216:219], v[172:175], a[212:215]\n"
"s_waitcnt vmcnt(8) lgkmcnt(0)\n"
"s_barrier\n"
"v_add_f32 v242, v242, v139\n"
"v_mfma_f32_16x16x32_bf16 a[220:223], v[220:223], v[172:175], a[220:223]\n"
"v_cvt_pk_bf16_f32 v185, v138, v139\n"
"v_mfma_f32_16x16x32_bf16 a[216:219], v[220:223], v[168:171], a[216:219]\n"
"v_exp_f32 v142, v142\n"
"v_mfma_f32_16x16x32_bf16 v[92:95], v[220:223], v[164:167], v[92:95]\n"
"v_mfma_f32_16x16x32_bf16 v[88:91], v[220:223], v[160:163], v[88:91]\n"
"v_add_f32 v243, v243, v140\n"
"v_mfma_f32_16x16x32_bf16 v[96:99], v[224:227], v[160:163], v[96:99]\n"
"v_exp_f32 v143, v143\n"
"v_mfma_f32_16x16x32_bf16 v[100:103], v[224:227], v[164:167], v[100:103]\n"
"v_add_f32 v243, v243, v141\n"
"v_mfma_f32_16x16x32_bf16 a[224:227], v[224:227], v[168:171], a[224:227]\n"
"v_cvt_pk_bf16_f32 v188, v140, v141\n"
"v_mfma_f32_16x16x32_bf16 a[228:231], v[224:227], v[172:175], a[228:231]\n"
"v_exp_f32 v144, v144\n"
"v_mfma_f32_16x16x32_bf16 a[236:239], v[228:231], v[172:175], a[236:239]\n"
"v_exp_f32 v145, v145\n"
"v_mfma_f32_16x16x32_bf16 a[232:235], v[228:231], v[168:171], a[232:235]\n"
"v_add_f32 v243, v243, v142\n"
"v_mfma_f32_16x16x32_bf16 v[108:111], v[228:231], v[164:167], v[108:111]\n"
"v_mfma_f32_16x16x32_bf16 v[104:107], v[228:231], v[160:163], v[104:107]\n"
"v_add_f32 v243, v243, v143\n"
"v_cvt_pk_bf16_f32 v189, v142, v143\n"
"v_mfma_f32_16x16x32_bf16 v[112:115], v[232:235], v[160:163], v[112:115]\n"
"v_exp_f32 v146, v146\n"
"v_mfma_f32_16x16x32_bf16 v[116:119], v[232:235], v[164:167], v[116:119]\n"
"v_add_f32 v240, v240, v144\n"
"v_mfma_f32_16x16x32_bf16 a[240:243], v[232:235], v[168:171], a[240:243]\n"
"v_exp_f32 v147, v147\n"
"v_mfma_f32_16x16x32_bf16 a[244:247], v[232:235], v[172:175], a[244:247]\n"
"v_add_f32 v240, v240, v145\n"
"v_mfma_f32_16x16x32_bf16 a[252:255], v[236:239], v[172:175], a[252:255]\n"
"v_cvt_pk_bf16_f32 v178, v144, v145\n"
"v_add_f32 v240, v240, v146\n"
"v_mfma_f32_16x16x32_bf16 a[248:251], v[236:239], v[168:171], a[248:251]\n"
"v_exp_f32 v148, v148\n"
"v_mfma_f32_16x16x32_bf16 v[124:127], v[236:239], v[164:167], v[124:127]\n"
"v_exp_f32 v149, v149\n"
"v_mfma_f32_16x16x32_bf16 v[120:123], v[236:239], v[160:163], v[120:123]\n"
"s_nop 0\n"
"v_add_f32 v240, v240, v147\n"
"s_nop 0\n"
"v_cvt_pk_bf16_f32 v179, v146, v147\n"
"v_add_f32 v241, v241, v148\n"
"s_nop 0\n"
"v_exp_f32 v150, v150\n"
"s_nop 0\n"
"v_exp_f32 v151, v151\n"
"s_nop 0\n"
"v_add_f32 v241, v241, v149\n"
"s_nop 0\n"
"v_exp_f32 v152, v152\n"
"s_nop 0\n"
"v_cvt_pk_bf16_f32 v182, v148, v149\n"
"v_add_f32 v241, v241, v150\n"
"s_nop 0\n"
"v_add_f32 v241, v241, v151\n"
"s_nop 0\n"
"v_exp_f32 v153, v153\n"
"s_nop 0\n"
"v_exp_f32 v154, v154\n"
"s_nop 0\n"
"v_cvt_pk_bf16_f32 v183, v150, v151\n"
"v_add_f32 v242, v242, v152\n"
"s_nop 0\n"
"v_add_f32 v242, v242, v153\n"
"s_nop 0\n"
"v_exp_f32 v155, v155\n"
"s_nop 0\n"
"v_cvt_pk_bf16_f32 v186, v152, v153\n"
"v_add_f32 v242, v242, v154\n"
"s_nop 0\n"
"v_exp_f32 v156, v156\n"
"s_nop 0\n"
"v_exp_f32 v157, v157\n"
"s_nop 0\n"
"v_exp_f32 v158, v158\n"
"s_nop 0\n"
"v_add_f32 v242, v242, v155\n"
"v_cvt_pk_bf16_f32 v187, v154, v155\n"
"s_nop 0\n"
"v_exp_f32 v159, v159\n"
"s_nop 0\n"
"v_add_f32 v243, v243, v156\n"
"s_nop 0\n"
"v_add_f32 v243, v243, v157\n"
"v_cvt_pk_bf16_f32 v190, v156, v157\n"
"s_nop 0\n"
"v_add_f32 v243, v243, v158\n"
"v_add_f32 v243, v243, v159\n"
"s_nop 0\n"
"v_cvt_pk_bf16_f32 v191, v158, v159\n"
"s_nop 0\n"
"s_cmp_lg_u32 s49, 0\n"
"s_cbranch_scc1 .Lrare2_13_%=\n"
".Lret2_13_%=:\n"
"s_waitcnt vmcnt(0) lgkmcnt(0)\n"
"s_barrier\n"
"ds_read_b64_tr_b16 v[208:209], v248 offset:32768\n"
"ds_read_b64_tr_b16 v[210:211], v248 offset:40960\n"
"ds_read_b64_tr_b16 v[212:213], v249 offset:32768\n"
"ds_read_b64_tr_b16 v[214:215], v249 offset:40960\n"
"ds_read_b64_tr_b16 v[216:217], v250 offset:32768\n"
"ds_read_b64_tr_b16 v[218:219], v250 offset:40960\n"
"ds_read_b64_tr_b16 v[220:221], v251 offset:32768\n"
"ds_read_b64_tr_b16 v[222:223], v251 offset:40960\n"
"ds_read_b64_tr_b16 v[224:225], v248 offset:33792\n"
"ds_read_b64_tr_b16 v[226:227], v248 offset:41984\n"
"ds_read_b64_tr_b16 v[228:229], v249 offset:33792\n"
"ds_read_b64_tr_b16 v[230:231], v249 offset:41984\n"
"ds_read_b64_tr_b16 v[232:233], v250 offset:33792\n"
"ds_read_b64_tr_b16 v[234:235], v250 offset:41984\n"
"s_waitcnt lgkmcnt(12)\n"
"ds_read_b64_tr_b16 v[236:237], v251 offset:33792\n"
"v_mfma_f32_16x16x32_bf16 v[0:3], v[208:211], v[176:179], v[0:3]\n"
"ds_read_b64_tr_b16 v[238:239], v251 offset:41984\n"
"v_mfma_f32_16x16x32_bf16 v[4:7], v[208:211], v[180:183], v[4:7]\n"
"v_mfma_f32_16x16x32_bf16 a[128:131], v[208:211], v[184:187], a[128:131]\n"
"v_mfma_f32_16x16x32_bf16 a[132:135], v[208:211], v[188:191], a[132:135]\n"
"s_waitcnt lgkmcnt(12)\n"
"ds_read_b64_tr_b16 v[208:209], v248 offset:34816\n"
"v_mfma_f32_16x16x32_bf16 a[140:143], v[212:215], v[188:191], a[140:143]\n"
"ds_read_b64_tr_b16 v[210:211], v248 offset:43008\n"
"v_mfma_f32_16x16x32_bf16 a[136:139], v[212:215], v[184:187], a[136:139]\n"
"v_mfma_f32_16x16x32_bf16 v[12:15], v[212:215], v[180:183], v[12:15]\n"
"v_mfma_f32_16x16x32_bf16 v[8:11], v[212:215], v[176:179], v[8:11]\n"
"s_waitcnt lgkmcnt(12)\n"
"v_mfma_f32_16x16x32_bf16 v[16:19], v[216:219], v[176:179], v[16:19]\n"
"ds_read_b64_tr_b16 v[212:213], v249 offset:34816\n"
"v_mfma_f32_16x16x32_bf16 v[20:23], v[216:219], v[180:183], v[20:23]\n"
"ds_read_b64_tr_b16 v[214:215], v249 offset:43008\n"
"v_mfma_f32_16x16x32_bf16 a[144:147], v[216:219], v[184:187], a[144:147]\n"
"v_mfma_f32_16x16x32_bf16 a[148:151], v[216:219], v[188:191], a[148:151]\n"
"s_waitcnt lgkmcnt(12)\n"
"v_mfma_f32_16x16x32_bf16 a[156:159], v[220:223], v[188:191], a[156:159]\n"
"ds_read_b64_tr_b16 v[216:217], v250 offset:34816\n"
"v_mfma_f32_16x16x32_bf16 a[152:155], v[220:223], v[184:187], a[152:155]\n"
"ds_read_b64_tr_b16 v[218:219], v250 offset:43008\n"
"v_mfma_f32_16x16x32_bf16 v[28:31], v[220:223], v[180:183], v[28:31]\n"
"v_mfma_f32_16x16x32_bf16 v[24:27], v[220:223], v[176:179], v[24:27]\n"
"s_waitcnt lgkmcnt(12)\n"
"v_mfma_f32_16x16x32_bf16 v[32:35], v[224:227], v[176:179], v[32:35]\n"
"ds_read_b64_tr_b16 v[220:221], v251 offset:34816\n"
"v_mfma_f32_16x16x32_bf16 v[36:39], v[224:227], v[180:183], v[36:39]\n"
"ds_read_b64_tr_b16 v[222:223], v251 offset:43008\n"
"v_mfma_f32_16x16x32_bf16 a[160:163], v[224:227], v[184:187], a[160:163]\n"
"v_mfma_f32_16x16x32_bf16 a[164:167], v[224:227], v[188:191], a[164:167]\n"
"s_waitcnt lgkmcnt(12)\n"
"v_mfma_f32_16x16x32_bf16 a[172:175], v[228:231], v[188:191], a[172:175]\n"
"ds_read_b64_tr_b16 v[224:225], v248 offset:35840\n"
"v_mfma_f32_16x16x32_bf16 a[168:171], v[228:231], v[184:187], a[168:171]\n"
"ds_read_b64_tr_b16 v[226:227], v248 offset:44032\n"
"v_mfma_f32_16x16x32_bf16 v[44:47], v[228:231], v[180:183], v[44:47]\n"
"v_mfma_f32_16x16x32_bf16 v[40:43], v[228:231], v[176:179], v[40:43]\n"
"s_waitcnt lgkmcnt(12)\n"
"v_mfma_f32_16x16x32_bf16 v[48:51], v[232:235], v[176:179], v[48:51]\n"
"ds_read_b64_tr_b16 v[228:229], v249 offset:35840\n"
"v_mfma_f32_16x16x32_bf16 v[52:55], v[232:235], v[180:183], v[52:55]\n"
"ds_read_b64_tr_b16 v[230:231], v249 offset:44032\n"
"v_mfma_f32_16x16x32_bf16 a[176:179], v[232:235], v[184:187], a[176:179]\n"
"v_mfma_f32_16x16x32_bf16 a[180:183], v[232:235], v[188:191], a[180:183]\n"
"s_waitcnt lgkmcnt(12)\n"
"v_mfma_f32_16x16x32_bf16 a[188:191], v[236:239], v[188:191], a[188:191]\n"
"v_mfma_f32_16x16x32_bf16 a[184:187], v[236:239], v[184:187], a[184:187]\n"
"ds_read_b64_tr_b16 v[232:233], v250 offset:35840\n"
"v_mfma_f32_16x16x32_bf16 v[60:63], v[236:239], v[180:183], v[60:63]\n"
"ds_read_b64_tr_b16 v[234:235], v250 offset:44032\n"
"v_mfma_f32_16x16x32_bf16 v[56:59], v[236:239], v[176:179], v[56:59]\n"
"s_waitcnt lgkmcnt(12)\n"
"v_mfma_f32_16x16x32_bf16 v[64:67], v[208:211], v[176:179], v[64:67]\n"
"ds_read_b64_tr_b16 v[236:237], v251 offset:35840\n"
"v_mfma_f32_16x16x32_bf16 v[68:71], v[208:211], v[180:183], v[68:71]\n"
"ds_read_b64_tr_b16 v[238:239], v251 offset:44032\n"
"v_mfma_f32_16x16x32_bf16 a[192:195], v[208:211], v[184:187], a[192:195]\n"
"v_mfma_f32_16x16x32_bf16 a[196:199], v[208:211], v[188:191], a[196:199]\n"
"s_waitcnt lgkmcnt(12)\n"
"v_mfma_f32_16x16x32_bf16 a[204:207], v[212:215], v[188:191], a[204:207]\n"
"v_mfma_f32_16x16x32_bf16 a[200:203], v[212:215], v[184:187], a[200:203]\n"
"v_mfma_f32_16x16x32_bf16 v[76:79], v[212:215], v[180:183], v[76:79]\n"
"v_mfma_f32_16x16x32_bf16 v[72:75], v[212:215], v[176:179], v[72:75]\n"
"s_waitcnt lgkmcnt(10)\n"
"v_mfma_f32_16x16x32_bf16 v[80:83], v[216:219], v[176:179], v[80:83]\n"
"v_mfma_f32_16x16x32_bf16 v[84:87], v[216:219], v[180:183], v[84:87]\n"
"v_mfma_f32_16x16x32_bf16 a[208:211], v[216:219], v[184:187], a[208:211]\n"
"v_mfma_f32_16x16x32_bf16 a[212:215], v[216:219], v[188:191], a[212:215]\n"
"s_waitcnt lgkmcnt(0)\n"
"s_barrier\n"
"v_mfma_f32_16x16x32_bf16 a[220:223], v[220:223], v[188:191], a[220:223]\n"
"v_mfma_f32_16x16x32_bf16 a[216:219], v[220:223], v[184:187], a[216:219]\n"
"v_mfma_f32_16x16x32_bf16 v[92:95], v[220:223], v[180:183], v[92:95]\n"
"v_mfma_f32_16x16x32_bf16 v[88:91], v[220:223], v[176:179], v[88:91]\n"
"v_mfma_f32_16x16x32_bf16 v[96:99], v[224:227], v[176:179], v[96:99]\n"
"v_mfma_f32_16x16x32_bf16 v[100:103], v[224:227], v[180:183], v[100:103]\n"
"v_mfma_f32_16x16x32_bf16 a[224:227], v[224:227], v[184:187], a[224:227]\n"
"v_mfma_f32_16x16x32_bf16 a[228:231], v[224:227], v[188:191], a[228:231]\n"
"v_mfma_f32_16x16x32_bf16 a[236:239], v[228:231], v[188:191], a[236:239]\n"
"v_mfma_f32_16x16x32_bf16 a[232:235], v[228:231], v[184:187], a[232:235]\n"
"v_mfma_f32_16x16x32_bf16 v[108:111], v[228:231], v[180:183], v[108:111]\n"
"v_mfma_f32_16x16x32_bf16 v[104:107], v[228:231], v[176:179], v[104:107]\n"
"v_mfma_f32_16x16x32_bf16 v[112:115], v[232:235], v[176:179], v[112:115]\n"
"v_mfma_f32_16x16x32_bf16 v[116:119], v[232:235], v[180:183], v[116:119]\n"
"v_mfma_f32_16x16x32_bf16 a[240:243], v[232:235], v[184:187], a[240:243]\n"
"v_mfma_f32_16x16x32_bf16 a[244:247], v[232:235], v[188:191], a[244:247]\n"
"v_mfma_f32_16x16x32_bf16 a[252:255], v[236:239], v[188:191], a[252:255]\n"
"v_mfma_f32_16x16x32_bf16 a[248:251], v[236:239], v[184:187], a[248:251]\n"
"v_mfma_f32_16x16x32_bf16 v[124:127], v[236:239], v[180:183], v[124:127]\n"
"v_mfma_f32_16x16x32_bf16 v[120:123], v[236:239], v[176:179], v[120:123]\n"
"s_waitcnt vmcnt(0) lgkmcnt(0)\n"
"s_barrier\n"
"s_mov_b32 s89, 384\n"
"v_lshrrev_b32 v246, 9, v253\n"
"v_mad_u32_u24 v246, v246, s89, v253\n"
"v_mbcnt_lo_u32_b32 v252, -1, 0\n"
"v_mbcnt_hi_u32_b32 v252, -1, v252\n"
"v_lshlrev_b32 v252, 4, v252\n"
"s_lshl_b32 s89, s84, 14\n"
"s_add_u32 s89, s89, 32768\n"
"v_add_u32 v254, s89, v252\n"
"v_xor_b32 v255, 0x4000, v254\n"
"s_lshl_b32 s89, s84, 12\n"
"s_add_u32 s89, s89, 98304\n"
"v_add_u32 v247, s89, v252\n"
"v_mov_b32 v128, v192\n"
"v_mov_b32 v129, v196\n"
"v_mov_b32 v130, v200\n"
"v_mov_b32 v131, v204\n"
"ds_write_b128 v254, v[128:131]\n"
"s_waitcnt lgkmcnt(0)\n"
"s_barrier\n"
"ds_read_b128 v[132:135], v255\n"
"s_waitcnt lgkmcnt(0)\n"
"s_barrier\n"
"v_min_f32 v136, v128, v134\n"
"v_sub_f32 v136, v136, v128\n"
"v_exp_f32 v136, v136\n"
"s_nop 0\n"
"v_mul_f32 v240, v240, v136\n"
"v_mul_f32 v0, v0, v136\n"
"v_mul_f32 v1, v1, v136\n"
"v_mul_f32 v2, v2, v136\n"
"v_mul_f32 v3, v3, v136\n"
"v_mul_f32 v8, v8, v136\n"
"v_mul_f32 v9, v9, v136\n"
"v_mul_f32 v10, v10, v136\n"
"v_mul_f32 v11, v11, v136\n"
"v_mul_f32 v16, v16, v136\n"
"v_mul_f32 v17, v17, v136\n"
"v_mul_f32 v18, v18, v136\n"
"v_mul_f32 v19, v19, v136\n"
"v_mul_f32 v24, v24, v136\n"
"v_mul_f32 v25, v25, v136\n"
"v_mul_f32 v26, v26, v136\n"
"v_mul_f32 v27, v27, v136\n"
"v_mul_f32 v32, v32, v136\n"
"v_mul_f32 v33, v33, v136\n"
"v_mul_f32 v34, v34, v136\n"
"v_mul_f32 v35, v35, v136\n"
"v_mul_f32 v40, v40, v136\n"
"v_mul_f32 v41, v41, v136\n"
"v_mul_f32 v42, v42, v136\n"
"v_mul_f32 v43, v43, v136\n"
"v_mul_f32 v48, v48, v136\n"
"v_mul_f32 v49, v49, v136\n"
"v_mul_f32 v50, v50, v136\n"
"v_mul_f32 v51, v51, v136\n"
"v_mul_f32 v56, v56, v136\n"
"v_mul_f32 v57, v57, v136\n"
"v_mul_f32 v58, v58, v136\n"
"v_mul_f32 v59, v59, v136\n"
"v_mul_f32 v64, v64, v136\n"
"v_mul_f32 v65, v65, v136\n"
"v_mul_f32 v66, v66, v136\n"
"v_mul_f32 v67, v67, v136\n"
"v_mul_f32 v72, v72, v136\n"
"v_mul_f32 v73, v73, v136\n"
"v_mul_f32 v74, v74, v136\n"
"v_mul_f32 v75, v75, v136\n"
"v_mul_f32 v80, v80, v136\n"
"v_mul_f32 v81, v81, v136\n"
"v_mul_f32 v82, v82, v136\n"
"v_mul_f32 v83, v83, v136\n"
"v_mul_f32 v88, v88, v136\n"
"v_mul_f32 v89, v89, v136\n"
"v_mul_f32 v90, v90, v136\n"
"v_mul_f32 v91, v91, v136\n"
"v_mul_f32 v96, v96, v136\n"
"v_mul_f32 v97, v97, v136\n"
"v_mul_f32 v98, v98, v136\n"
"v_mul_f32 v99, v99, v136\n"
"v_mul_f32 v104, v104, v136\n"
"v_mul_f32 v105, v105, v136\n"
"v_mul_f32 v106, v106, v136\n"
"v_mul_f32 v107, v107, v136\n"
"v_mul_f32 v112, v112, v136\n"
"v_mul_f32 v113, v113, v136\n"
"v_mul_f32 v114, v114, v136\n"
"v_mul_f32 v115, v115, v136\n"
"v_mul_f32 v120, v120, v136\n"
"v_mul_f32 v121, v121, v136\n"
"v_mul_f32 v122, v122, v136\n"
"v_mul_f32 v123, v123, v136\n"
"v_min_f32 v136, v129, v135\n"
"v_sub_f32 v136, v136, v129\n"
"v_exp_f32 v136, v136\n"
"s_nop 0\n"
"v_mul_f32 v241, v241, v136\n"
"v_mul_f32 v4, v4, v136\n"
"v_mul_f32 v5, v5, v136\n"
"v_mul_f32 v6, v6, v136\n"
"v_mul_f32 v7, v7, v136\n"
"v_mul_f32 v12, v12, v136\n"
"v_mul_f32 v13, v13, v136\n"
"v_mul_f32 v14, v14, v136\n"
"v_mul_f32 v15, v15, v136\n"
"v_mul_f32 v20, v20, v136\n"
"v_mul_f32 v21, v21, v136\n"
"v_mul_f32 v22, v22, v136\n"
"v_mul_f32 v23, v23, v136\n"
"v_mul_f32 v28, v28, v136\n"
"v_mul_f32 v29, v29, v136\n"
"v_mul_f32 v30, v30, v136\n"
"v_mul_f32 v31, v31, v136\n"
"v_mul_f32 v36, v36, v136\n"
"v_mul_f32 v37, v37, v136\n"
"v_mul_f32 v38, v38, v136\n"
"v_mul_f32 v39, v39, v136\n"
"v_mul_f32 v44, v44, v136\n"
"v_mul_f32 v45, v45, v136\n"
"v_mul_f32 v46, v46, v136\n"
"v_mul_f32 v47, v47, v136\n"
"v_mul_f32 v52, v52, v136\n"
"v_mul_f32 v53, v53, v136\n"
"v_mul_f32 v54, v54, v136\n"
"v_mul_f32 v55, v55, v136\n"
"v_mul_f32 v60, v60, v136\n"
"v_mul_f32 v61, v61, v136\n"
"v_mul_f32 v62, v62, v136\n"
"v_mul_f32 v63, v63, v136\n"
"v_mul_f32 v68, v68, v136\n"
"v_mul_f32 v69, v69, v136\n"
"v_mul_f32 v70, v70, v136\n"
"v_mul_f32 v71, v71, v136\n"
"v_mul_f32 v76, v76, v136\n"
"v_mul_f32 v77, v77, v136\n"
"v_mul_f32 v78, v78, v136\n"
"v_mul_f32 v79, v79, v136\n"
"v_mul_f32 v84, v84, v136\n"
"v_mul_f32 v85, v85, v136\n"
"v_mul_f32 v86, v86, v136\n"
"v_mul_f32 v87, v87, v136\n"
"v_mul_f32 v92, v92, v136\n"
"v_mul_f32 v93, v93, v136\n"
"v_mul_f32 v94, v94, v136\n"
"v_mul_f32 v95, v95, v136\n"
"v_mul_f32 v100, v100, v136\n"
"v_mul_f32 v101, v101, v136\n"
"v_mul_f32 v102, v102, v136\n"
"v_mul_f32 v103, v103, v136\n"
"v_mul_f32 v108, v108, v136\n"
"v_mul_f32 v109, v109, v136\n"
"v_mul_f32 v110, v110, v136\n"
"v_mul_f32 v111, v111, v136\n"
"v_mul_f32 v116, v116, v136\n"
"v_mul_f32 v117, v117, v136\n"
"v_mul_f32 v118, v118, v136\n"
"v_mul_f32 v119, v119, v136\n"
"v_mul_f32 v124, v124, v136\n"
"v_mul_f32 v125, v125, v136\n"
"v_mul_f32 v126, v126, v136\n"
"v_mul_f32 v127, v127, v136\n"
"v_min_f32 v136, v130, v132\n"
"v_sub_f32 v136, v136, v130\n"
"v_exp_f32 v136, v136\n"
"s_nop 0\n"
"v_mul_f32 v242, v242, v136\n"
"v_accvgpr_read_b32 v137, a128\n"
"v_mul_f32 v137, v137, v136\n"
"v_accvgpr_write_b32 a128, v137\n"
"v_accvgpr_read_b32 v137, a129\n"
"v_mul_f32 v137, v137, v136\n"
"v_accvgpr_write_b32 a129, v137\n"
"v_accvgpr_read_b32 v137, a130\n"
"v_mul_f32 v137, v137, v136\n"
"v_accvgpr_write_b32 a130, v137\n"
"v_accvgpr_read_b32 v137, a131\n"
"v_mul_f32 v137, v137, v136\n"
"v_accvgpr_write_b32 a131, v137\n"
"v_accvgpr_read_b32 v137, a136\n"
"v_mul_f32 v137, v137, v136\n"
"v_accvgpr_write_b32 a136, v137\n"
"v_accvgpr_read_b32 v137, a137\n"
"v_mul_f32 v137, v137, v136\n"
"v_accvgpr_write_b32 a137, v137\n"
"v_accvgpr_read_b32 v137, a138\n"
"v_mul_f32 v137, v137, v136\n"
"v_accvgpr_write_b32 a138, v137\n"
"v_accvgpr_read_b32 v137, a139\n"
"v_mul_f32 v137, v137, v136\n"
"v_accvgpr_write_b32 a139, v137\n"
"v_accvgpr_read_b32 v137, a144\n"
"v_mul_f32 v137, v137, v136\n"
"v_accvgpr_write_b32 a144, v137\n"
"v_accvgpr_read_b32 v137, a145\n"
"v_mul_f32 v137, v137, v136\n"
"v_accvgpr_write_b32 a145, v137\n"
"v_accvgpr_read_b32 v137, a146\n"
"v_mul_f32 v137, v137, v136\n"
"v_accvgpr_write_b32 a146, v137\n"
"v_accvgpr_read_b32 v137, a147\n"
"v_mul_f32 v137, v137, v136\n"
"v_accvgpr_write_b32 a147, v137\n"
"v_accvgpr_read_b32 v137, a152\n"
"v_mul_f32 v137, v137, v136\n"
"v_accvgpr_write_b32 a152, v137\n"
"v_accvgpr_read_b32 v137, a153\n"
"v_mul_f32 v137, v137, v136\n"
"v_accvgpr_write_b32 a153, v137\n"
"v_accvgpr_read_b32 v137, a154\n"
"v_mul_f32 v137, v137, v136\n"
"v_accvgpr_write_b32 a154, v137\n"
"v_accvgpr_read_b32 v137, a155\n"
"v_mul_f32 v137, v137, v136\n"
"v_accvgpr_write_b32 a155, v137\n"
"v_accvgpr_read_b32 v137, a160\n"
"v_mul_f32 v137, v137, v136\n"
"v_accvgpr_write_b32 a160, v137\n"
"v_accvgpr_read_b32 v137, a161\n"
"v_mul_f32 v137, v137, v136\n"
"v_accvgpr_write_b32 a161, v137\n"
"v_accvgpr_read_b32 v137, a162\n"
"v_mul_f32 v137, v137, v136\n"
"v_accvgpr_write_b32 a162, v137\n"
"v_accvgpr_read_b32 v137, a163\n"
"v_mul_f32 v137, v137, v136\n"
"v_accvgpr_write_b32 a163, v137\n"
"v_accvgpr_read_b32 v137, a168\n"
"v_mul_f32 v137, v137, v136\n"
"v_accvgpr_write_b32 a168, v137\n"
"v_accvgpr_read_b32 v137, a169\n"
"v_mul_f32 v137, v137, v136\n"
"v_accvgpr_write_b32 a169, v137\n"
"v_accvgpr_read_b32 v137, a170\n"
"v_mul_f32 v137, v137, v136\n"
"v_accvgpr_write_b32 a170, v137\n"
"v_accvgpr_read_b32 v137, a171\n"
"v_mul_f32 v137, v137, v136\n"
"v_accvgpr_write_b32 a171, v137\n"
"v_accvgpr_read_b32 v137, a176\n"
"v_mul_f32 v137, v137, v136\n"
"v_accvgpr_write_b32 a176, v137\n"
"v_accvgpr_read_b32 v137, a177\n"
"v_mul_f32 v137, v137, v136\n"
"v_accvgpr_write_b32 a177, v137\n"
"v_accvgpr_read_b32 v137, a178\n"
"v_mul_f32 v137, v137, v136\n"
"v_accvgpr_write_b32 a178, v137\n"
"v_accvgpr_read_b32 v137, a179\n"
"v_mul_f32 v137, v137, v136\n"
"v_accvgpr_write_b32 a179, v137\n"
"v_accvgpr_read_b32 v137, a184\n"
"v_mul_f32 v137, v137, v136\n"
"v_accvgpr_write_b32 a184, v137\n"
"v_accvgpr_read_b32 v137, a185\n"
"v_mul_f32 v137, v137, v136\n"
"v_accvgpr_write_b32 a185, v137\n"
"v_accvgpr_read_b32 v137, a186\n"
"v_mul_f32 v137, v137, v136\n"
"v_accvgpr_write_b32 a186, v137\n"
"v_accvgpr_read_b32 v137, a187\n"
"v_mul_f32 v137, v137, v136\n"
"v_accvgpr_write_b32 a187, v137\n"
"v_accvgpr_read_b32 v137, a192\n"
"v_mul_f32 v137, v137, v136\n"
"v_accvgpr_write_b32 a192, v137\n"
"v_accvgpr_read_b32 v137, a193\n"
"v_mul_f32 v137, v137, v136\n"
"v_accvgpr_write_b32 a193, v137\n"
"v_accvgpr_read_b32 v137, a194\n"
"v_mul_f32 v137, v137, v136\n"
"v_accvgpr_write_b32 a194, v137\n"
"v_accvgpr_read_b32 v137, a195\n"
"v_mul_f32 v137, v137, v136\n"
"v_accvgpr_write_b32 a195, v137\n"
"v_accvgpr_read_b32 v137, a200\n"
"v_mul_f32 v137, v137, v136\n"
"v_accvgpr_write_b32 a200, v137\n"
"v_accvgpr_read_b32 v137, a201\n"
"v_mul_f32 v137, v137, v136\n"
"v_accvgpr_write_b32 a201, v137\n"
"v_accvgpr_read_b32 v137, a202\n"
"v_mul_f32 v137, v137, v136\n"
"v_accvgpr_write_b32 a202, v137\n"
"v_accvgpr_read_b32 v137, a203\n"
"v_mul_f32 v137, v137, v136\n"
"v_accvgpr_write_b32 a203, v137\n"
"v_accvgpr_read_b32 v137, a208\n"
"v_mul_f32 v137, v137, v136\n"
"v_accvgpr_write_b32 a208, v137\n"
"v_accvgpr_read_b32 v137, a209\n"
"v_mul_f32 v137, v137, v136\n"
"v_accvgpr_write_b32 a209, v137\n"
"v_accvgpr_read_b32 v137, a210\n"
"v_mul_f32 v137, v137, v136\n"
"v_accvgpr_write_b32 a210, v137\n"
"v_accvgpr_read_b32 v137, a211\n"
"v_mul_f32 v137, v137, v136\n"
"v_accvgpr_write_b32 a211, v137\n"
"v_accvgpr_read_b32 v137, a216\n"
"v_mul_f32 v137, v137, v136\n"
"v_accvgpr_write_b32 a216, v137\n"
"v_accvgpr_read_b32 v137, a217\n"
"v_mul_f32 v137, v137, v136\n"
"v_accvgpr_write_b32 a217, v137\n"
"v_accvgpr_read_b32 v137, a218\n"
"v_mul_f32 v137, v137, v136\n"
"v_accvgpr_write_b32 a218, v137\n"
"v_accvgpr_read_b32 v137, a219\n"
"v_mul_f32 v137, v137, v136\n"
"v_accvgpr_write_b32 a219, v137\n"
"v_accvgpr_read_b32 v137, a224\n"
"v_mul_f32 v137, v137, v136\n"
"v_accvgpr_write_b32 a224, v137\n"
"v_accvgpr_read_b32 v137, a225\n"
"v_mul_f32 v137, v137, v136\n"
"v_accvgpr_write_b32 a225, v137\n"
"v_accvgpr_read_b32 v137, a226\n"
"v_mul_f32 v137, v137, v136\n"
"v_accvgpr_write_b32 a226, v137\n"
"v_accvgpr_read_b32 v137, a227\n"
"v_mul_f32 v137, v137, v136\n"
"v_accvgpr_write_b32 a227, v137\n"
"v_accvgpr_read_b32 v137, a232\n"
"v_mul_f32 v137, v137, v136\n"
"v_accvgpr_write_b32 a232, v137\n"
"v_accvgpr_read_b32 v137, a233\n"
"v_mul_f32 v137, v137, v136\n"
"v_accvgpr_write_b32 a233, v137\n"
"v_accvgpr_read_b32 v137, a234\n"
"v_mul_f32 v137, v137, v136\n"
"v_accvgpr_write_b32 a234, v137\n"
"v_accvgpr_read_b32 v137, a235\n"
"v_mul_f32 v137, v137, v136\n"
"v_accvgpr_write_b32 a235, v137\n"
"v_accvgpr_read_b32 v137, a240\n"
"v_mul_f32 v137, v137, v136\n"
"v_accvgpr_write_b32 a240, v137\n"
"v_accvgpr_read_b32 v137, a241\n"
"v_mul_f32 v137, v137, v136\n"
"v_accvgpr_write_b32 a241, v137\n"
"v_accvgpr_read_b32 v137, a242\n"
"v_mul_f32 v137, v137, v136\n"
"v_accvgpr_write_b32 a242, v137\n"
"v_accvgpr_read_b32 v137, a243\n"
"v_mul_f32 v137, v137, v136\n"
"v_accvgpr_write_b32 a243, v137\n"
"v_accvgpr_read_b32 v137, a248\n"
"v_mul_f32 v137, v137, v136\n"
"v_accvgpr_write_b32 a248, v137\n"
"v_accvgpr_read_b32 v137, a249\n"
"v_mul_f32 v137, v137, v136\n"
"v_accvgpr_write_b32 a249, v137\n"
"v_accvgpr_read_b32 v137, a250\n"
"v_mul_f32 v137, v137, v136\n"
"v_accvgpr_write_b32 a250, v137\n"
"v_accvgpr_read_b32 v137, a251\n"
"v_mul_f32 v137, v137, v136\n"
"v_accvgpr_write_b32 a251, v137\n"
"v_min_f32 v136, v131, v133\n"
"v_sub_f32 v136, v136, v131\n"
"v_exp_f32 v136, v136\n"
"s_nop 0\n"
"v_mul_f32 v243, v243, v136\n"
"v_accvgpr_read_b32 v137, a132\n"
"v_mul_f32 v137, v137, v136\n"
"v_accvgpr_write_b32 a132, v137\n"
"v_accvgpr_read_b32 v137, a133\n"
"v_mul_f32 v137, v137, v136\n"
"v_accvgpr_write_b32 a133, v137\n"
"v_accvgpr_read_b32 v137, a134\n"
"v_mul_f32 v137, v137, v136\n"
"v_accvgpr_write_b32 a134, v137\n"
"v_accvgpr_read_b32 v137, a135\n"
"v_mul_f32 v137, v137, v136\n"
"v_accvgpr_write_b32 a135, v137\n"
"v_accvgpr_read_b32 v137, a140\n"
"v_mul_f32 v137, v137, v136\n"
"v_accvgpr_write_b32 a140, v137\n"
"v_accvgpr_read_b32 v137, a141\n"
"v_mul_f32 v137, v137, v136\n"
"v_accvgpr_write_b32 a141, v137\n"
"v_accvgpr_read_b32 v137, a142\n"
"v_mul_f32 v137, v137, v136\n"
"v_accvgpr_write_b32 a142, v137\n"
"v_accvgpr_read_b32 v137, a143\n"
"v_mul_f32 v137, v137, v136\n"
"v_accvgpr_write_b32 a143, v137\n"
"v_accvgpr_read_b32 v137, a148\n"
"v_mul_f32 v137, v137, v136\n"
"v_accvgpr_write_b32 a148, v137\n"
"v_accvgpr_read_b32 v137, a149\n"
"v_mul_f32 v137, v137, v136\n"
"v_accvgpr_write_b32 a149, v137\n"
"v_accvgpr_read_b32 v137, a150\n"
"v_mul_f32 v137, v137, v136\n"
"v_accvgpr_write_b32 a150, v137\n"
"v_accvgpr_read_b32 v137, a151\n"
"v_mul_f32 v137, v137, v136\n"
"v_accvgpr_write_b32 a151, v137\n"
"v_accvgpr_read_b32 v137, a156\n"
"v_mul_f32 v137, v137, v136\n"
"v_accvgpr_write_b32 a156, v137\n"
"v_accvgpr_read_b32 v137, a157\n"
"v_mul_f32 v137, v137, v136\n"
"v_accvgpr_write_b32 a157, v137\n"
"v_accvgpr_read_b32 v137, a158\n"
"v_mul_f32 v137, v137, v136\n"
"v_accvgpr_write_b32 a158, v137\n"
"v_accvgpr_read_b32 v137, a159\n"
"v_mul_f32 v137, v137, v136\n"
"v_accvgpr_write_b32 a159, v137\n"
"v_accvgpr_read_b32 v137, a164\n"
"v_mul_f32 v137, v137, v136\n"
"v_accvgpr_write_b32 a164, v137\n"
"v_accvgpr_read_b32 v137, a165\n"
"v_mul_f32 v137, v137, v136\n"
"v_accvgpr_write_b32 a165, v137\n"
"v_accvgpr_read_b32 v137, a166\n"
"v_mul_f32 v137, v137, v136\n"
"v_accvgpr_write_b32 a166, v137\n"
"v_accvgpr_read_b32 v137, a167\n"
"v_mul_f32 v137, v137, v136\n"
"v_accvgpr_write_b32 a167, v137\n"
"v_accvgpr_read_b32 v137, a172\n"
"v_mul_f32 v137, v137, v136\n"
"v_accvgpr_write_b32 a172, v137\n"
"v_accvgpr_read_b32 v137, a173\n"
"v_mul_f32 v137, v137, v136\n"
"v_accvgpr_write_b32 a173, v137\n"
"v_accvgpr_read_b32 v137, a174\n"
"v_mul_f32 v137, v137, v136\n"
"v_accvgpr_write_b32 a174, v137\n"
"v_accvgpr_read_b32 v137, a175\n"
"v_mul_f32 v137, v137, v136\n"
"v_accvgpr_write_b32 a175, v137\n"
"v_accvgpr_read_b32 v137, a180\n"
"v_mul_f32 v137, v137, v136\n"
"v_accvgpr_write_b32 a180, v137\n"
"v_accvgpr_read_b32 v137, a181\n"
"v_mul_f32 v137, v137, v136\n"
"v_accvgpr_write_b32 a181, v137\n"
"v_accvgpr_read_b32 v137, a182\n"
"v_mul_f32 v137, v137, v136\n"
"v_accvgpr_write_b32 a182, v137\n"
"v_accvgpr_read_b32 v137, a183\n"
"v_mul_f32 v137, v137, v136\n"
"v_accvgpr_write_b32 a183, v137\n"
"v_accvgpr_read_b32 v137, a188\n"
"v_mul_f32 v137, v137, v136\n"
"v_accvgpr_write_b32 a188, v137\n"
"v_accvgpr_read_b32 v137, a189\n"
"v_mul_f32 v137, v137, v136\n"
"v_accvgpr_write_b32 a189, v137\n"
"v_accvgpr_read_b32 v137, a190\n"
"v_mul_f32 v137, v137, v136\n"
"v_accvgpr_write_b32 a190, v137\n"
"v_accvgpr_read_b32 v137, a191\n"
"v_mul_f32 v137, v137, v136\n"
"v_accvgpr_write_b32 a191, v137\n"
"v_accvgpr_read_b32 v137, a196\n"
"v_mul_f32 v137, v137, v136\n"
"v_accvgpr_write_b32 a196, v137\n"
"v_accvgpr_read_b32 v137, a197\n"
"v_mul_f32 v137, v137, v136\n"
"v_accvgpr_write_b32 a197, v137\n"
"v_accvgpr_read_b32 v137, a198\n"
"v_mul_f32 v137, v137, v136\n"
"v_accvgpr_write_b32 a198, v137\n"
"v_accvgpr_read_b32 v137, a199\n"
"v_mul_f32 v137, v137, v136\n"
"v_accvgpr_write_b32 a199, v137\n"
"v_accvgpr_read_b32 v137, a204\n"
"v_mul_f32 v137, v137, v136\n"
"v_accvgpr_write_b32 a204, v137\n"
"v_accvgpr_read_b32 v137, a205\n"
"v_mul_f32 v137, v137, v136\n"
"v_accvgpr_write_b32 a205, v137\n"
"v_accvgpr_read_b32 v137, a206\n"
"v_mul_f32 v137, v137, v136\n"
"v_accvgpr_write_b32 a206, v137\n"
"v_accvgpr_read_b32 v137, a207\n"
"v_mul_f32 v137, v137, v136\n"
"v_accvgpr_write_b32 a207, v137\n"
"v_accvgpr_read_b32 v137, a212\n"
"v_mul_f32 v137, v137, v136\n"
"v_accvgpr_write_b32 a212, v137\n"
"v_accvgpr_read_b32 v137, a213\n"
"v_mul_f32 v137, v137, v136\n"
"v_accvgpr_write_b32 a213, v137\n"
"v_accvgpr_read_b32 v137, a214\n"
"v_mul_f32 v137, v137, v136\n"
"v_accvgpr_write_b32 a214, v137\n"
"v_accvgpr_read_b32 v137, a215\n"
"v_mul_f32 v137, v137, v136\n"
"v_accvgpr_write_b32 a215, v137\n"
"v_accvgpr_read_b32 v137, a220\n"
"v_mul_f32 v137, v137, v136\n"
"v_accvgpr_write_b32 a220, v137\n"
"v_accvgpr_read_b32 v137, a221\n"
"v_mul_f32 v137, v137, v136\n"
"v_accvgpr_write_b32 a221, v137\n"
"v_accvgpr_read_b32 v137, a222\n"
"v_mul_f32 v137, v137, v136\n"
"v_accvgpr_write_b32 a222, v137\n"
"v_accvgpr_read_b32 v137, a223\n"
"v_mul_f32 v137, v137, v136\n"
"v_accvgpr_write_b32 a223, v137\n"
"v_accvgpr_read_b32 v137, a228\n"
"v_mul_f32 v137, v137, v136\n"
"v_accvgpr_write_b32 a228, v137\n"
"v_accvgpr_read_b32 v137, a229\n"
"v_mul_f32 v137, v137, v136\n"
"v_accvgpr_write_b32 a229, v137\n"
"v_accvgpr_read_b32 v137, a230\n"
"v_mul_f32 v137, v137, v136\n"
"v_accvgpr_write_b32 a230, v137\n"
"v_accvgpr_read_b32 v137, a231\n"
"v_mul_f32 v137, v137, v136\n"
"v_accvgpr_write_b32 a231, v137\n"
"v_accvgpr_read_b32 v137, a236\n"
"v_mul_f32 v137, v137, v136\n"
"v_accvgpr_write_b32 a236, v137\n"
"v_accvgpr_read_b32 v137, a237\n"
"v_mul_f32 v137, v137, v136\n"
"v_accvgpr_write_b32 a237, v137\n"
"v_accvgpr_read_b32 v137, a238\n"
"v_mul_f32 v137, v137, v136\n"
"v_accvgpr_write_b32 a238, v137\n"
"v_accvgpr_read_b32 v137, a239\n"
"v_mul_f32 v137, v137, v136\n"
"v_accvgpr_write_b32 a239, v137\n"
"v_accvgpr_read_b32 v137, a244\n"
"v_mul_f32 v137, v137, v136\n"
"v_accvgpr_write_b32 a244, v137\n"
"v_accvgpr_read_b32 v137, a245\n"
"v_mul_f32 v137, v137, v136\n"
"v_accvgpr_write_b32 a245, v137\n"
"v_accvgpr_read_b32 v137, a246\n"
"v_mul_f32 v137, v137, v136\n"
"v_accvgpr_write_b32 a246, v137\n"
"v_accvgpr_read_b32 v137, a247\n"
"v_mul_f32 v137, v137, v136\n"
"v_accvgpr_write_b32 a247, v137\n"
"v_accvgpr_read_b32 v137, a252\n"
"v_mul_f32 v137, v137, v136\n"
"v_accvgpr_write_b32 a252, v137\n"
"v_accvgpr_read_b32 v137, a253\n"
"v_mul_f32 v137, v137, v136\n"
"v_accvgpr_write_b32 a253, v137\n"
"v_accvgpr_read_b32 v137, a254\n"
"v_mul_f32 v137, v137, v136\n"
"v_accvgpr_write_b32 a254, v137\n"
"v_accvgpr_read_b32 v137, a255\n"
"v_mul_f32 v137, v137, v136\n"
"v_accvgpr_write_b32 a255, v137\n"
"s_add_u32 m0, m0, 0xfffe3580\n"
"s_mov_b32 s85, 0x0\n"
"buffer_load_dwordx4 v246, s[76:79], s85 offen offset:0 lds\n"
"s_add_u32 m0, m0, 0x4000\n"
"s_mov_b32 s85, 0x7000\n"
"buffer_load_dwordx4 v246, s[76:79], s85 offen offset:0 lds\n"
"s_add_u32 m0, m0, 0xffffc400\n"
"s_mov_b32 s85, 0xe000\n"
"buffer_load_dwordx4 v246, s[76:79], s85 offen offset:0 lds\n"
"s_add_u32 m0, m0, 0x4000\n"
"s_mov_b32 s85, 0x15000\n"
"buffer_load_dwordx4 v246, s[76:79], s85 offen offset:0 lds\n"
"s_add_u32 m0, m0, 0xffffc400\n"
"s_mov_b32 s85, 0x1c000\n"
"buffer_load_dwordx4 v246, s[76:79], s85 offen offset:0 lds\n"
"s_add_u32 m0, m0, 0x4000\n"
"s_mov_b32 s85, 0x23000\n"
"buffer_load_dwordx4 v246, s[76:79], s85 offen offset:0 lds\n"
"s_add_u32 m0, m0, 0xffffc400\n"
"s_mov_b32 s85, 0x2a000\n"
"buffer_load_dwordx4 v246, s[76:79], s85 offen offset:0 lds\n"
"s_add_u32 m0, m0, 0x4000\n"
"s_mov_b32 s85, 0x31000\n"
"buffer_load_dwordx4 v246, s[76:79], s85 offen offset:0 lds\n"
"s_add_u32 m0, m0, 0x1b400\n"
"s_mov_b32 s85, 0x80\n"
"buffer_load_dwordx4 v246, s[76:79], s85 offen offset:0 lds\n"
"s_add_u32 m0, m0, 0x4000\n"
"s_mov_b32 s85, 0x7080\n"
"buffer_load_dwordx4 v246, s[76:79], s85 offen offset:0 lds\n"
"s_add_u32 m0, m0, 0xffffc400\n"
"s_mov_b32 s85, 0xe080\n"
"buffer_load_dwordx4 v246, s[76:79], s85 offen offset:0 lds\n"
"s_add_u32 m0, m0, 0x4000\n"
"s_mov_b32 s85, 0x15080\n"
"buffer_load_dwordx4 v246, s[76:79], s85 offen offset:0 lds\n"
"s_add_u32 m0, m0, 0xffffc400\n"
"s_mov_b32 s85, 0x1c080\n"
"buffer_load_dwordx4 v246, s[76:79], s85 offen offset:0 lds\n"
"s_add_u32 m0, m0, 0x4000\n"
"s_mov_b32 s85, 0x23080\n"
"buffer_load_dwordx4 v246, s[76:79], s85 offen offset:0 lds\n"
"s_add_u32 m0, m0, 0xffffc400\n"
"s_mov_b32 s85, 0x2a080\n"
"buffer_load_dwordx4 v246, s[76:79], s85 offen offset:0 lds\n"
"s_add_u32 m0, m0, 0x4000\n"
"s_mov_b32 s85, 0x31080\n"
"buffer_load_dwordx4 v246, s[76:79], s85 offen offset:0 lds\n"
"s_add_u32 m0, m0, 0xffff3400\n"
"s_mov_b32 s85, 0x100\n"
"buffer_load_dwordx4 v246, s[76:79], s85 offen offset:0 lds\n"
"s_add_u32 m0, m0, 0x4000\n"
"s_mov_b32 s85, 0x7100\n"
"buffer_load_dwordx4 v246, s[76:79], s85 offen offset:0 lds\n"
"s_add_u32 m0, m0, 0xffffc400\n"
"s_mov_b32 s85, 0xe100\n"
"buffer_load_dwordx4 v246, s[76:79], s85 offen offset:0 lds\n"
"s_add_u32 m0, m0, 0x4000\n"
"s_mov_b32 s85, 0x15100\n"
"buffer_load_dwordx4 v246, s[76:79], s85 offen offset:0 lds\n"
"s_add_u32 m0, m0, 0xffffc400\n"
"s_mov_b32 s85, 0x1c100\n"
"buffer_load_dwordx4 v246, s[76:79], s85 offen offset:0 lds\n"
"s_add_u32 m0, m0, 0x4000\n"
"s_mov_b32 s85, 0x23100\n"
"buffer_load_dwordx4 v246, s[76:79], s85 offen offset:0 lds\n"
"s_add_u32 m0, m0, 0xffffc400\n"
"s_mov_b32 s85, 0x2a100\n"
"buffer_load_dwordx4 v246, s[76:79], s85 offen offset:0 lds\n"
"v_mbcnt_lo_u32_b32 v192, -1, 0\n"
"v_mbcnt_hi_u32_b32 v192, -1, v192\n"
"v_and_b32 v192, 1, v192\n"
"v_lshlrev_b32 v192, 4, v192\n"
"s_mov_b32 s89, 0x3f80\n"
"v_lshlrev_b32_e64 v192, v192, s89\n"
"s_mov_b32 s90, 0x300003\n"
"s_mov_b32 s91, 0x30000300\n"
"v_cndmask_b32 v128, 0, v192, s[90:91]\n"
"s_lshl_b64 s[90:91], s[90:91], 2\n"
"v_cndmask_b32 v129, 0, v192, s[90:91]\n"
"v_mov_b32 v130, 0\n"
"v_mov_b32 v131, 0\n"
"v_mov_b32 v132, 0\n"
"v_mov_b32 v133, 0\n"
"v_mov_b32 v134, v128\n"
"v_mov_b32 v135, v129\n"
"v_lshrrev_b32 v193, 8, v252\n"
"v_lshlrev_b32 v244, 10, v193\n"
"v_and_b32 v193, 0xf0, v252\n"
"v_add_u32 v244, v244, v193\n"
"s_lshl_b32 s89, s84, 8\n"
"v_add_u32 v244, s89, v244\n"
"v_bfe_u32 v193, v252, 4, 5\n"
"v_mul_u32_u24 v245, 896, v193\n"
"v_lshrrev_b32 v193, 9, v252\n"
"v_lshl_add_u32 v245, v193, 7, v245\n"
"s_and_b32 s89, s87, 3\n"
"s_lshl_b32 s89, s89, 5\n"
"s_lshl_b32 s90, s84, 3\n"
"s_add_u32 s89, s89, s90\n"
"v_add_u32 v245, s89, v245\n"
"buffer_load_dwordx2 v[200:201], v245, s[60:63], 0 offen offset:0\n"
"buffer_load_dwordx2 v[202:203], v245, s[60:63], 0 offen offset:256\n"
"buffer_load_dwordx2 v[204:205], v245, s[60:63], 0 offen offset:512\n"
"buffer_load_dwordx2 v[206:207], v245, s[60:63], 0 offen offset:768\n"
"v_accvgpr_read_b32 v136, a128\n"
"v_accvgpr_read_b32 v137, a129\n"
"v_accvgpr_read_b32 v138, a130\n"
"v_accvgpr_read_b32 v139, a131\n"
"v_accvgpr_read_b32 v140, a136\n"
"v_accvgpr_read_b32 v141, a137\n"
"v_accvgpr_read_b32 v142, a138\n"
"v_accvgpr_read_b32 v143, a139\n"
"v_cvt_pk_bf16_f32 v136, v136, v137\n"
"v_cvt_pk_bf16_f32 v137, v138, v139\n"
"v_cvt_pk_bf16_f32 v138, v140, v141\n"
"v_cvt_pk_bf16_f32 v139, v142, v143\n"
"ds_write_b128 v254, v[136:139] offset:0\n"
"v_accvgpr_read_b32 v144, a132\n"
"v_accvgpr_read_b32 v145, a133\n"
"v_accvgpr_read_b32 v146, a134\n"
"v_accvgpr_read_b32 v147, a135\n"
"v_accvgpr_read_b32 v148, a140\n"
"v_accvgpr_read_b32 v149, a141\n"
"v_accvgpr_read_b32 v150, a142\n"
"v_accvgpr_read_b32 v151, a143\n"
"v_cvt_pk_bf16_f32 v144, v144, v145\n"
"v_cvt_pk_bf16_f32 v145, v146, v147\n"
"v_cvt_pk_bf16_f32 v146, v148, v149\n"
"v_cvt_pk_bf16_f32 v147, v150, v151\n"
"ds_write_b128 v254, v[144:147] offset:1024\n"
"v_accvgpr_read_b32 v152, a144\n"
"v_accvgpr_read_b32 v153, a145\n"
"v_accvgpr_read_b32 v154, a146\n"
"v_accvgpr_read_b32 v155, a147\n"
"v_accvgpr_read_b32 v156, a152\n"
"v_accvgpr_read_b32 v157, a153\n"
"v_accvgpr_read_b32 v158, a154\n"
"v_accvgpr_read_b32 v159, a155\n"
"v_cvt_pk_bf16_f32 v152, v152, v153\n"
"v_cvt_pk_bf16_f32 v153, v154, v155\n"
"v_cvt_pk_bf16_f32 v154, v156, v157\n"
"v_cvt_pk_bf16_f32 v155, v158, v159\n"
"ds_write_b128 v254, v[152:155] offset:2048\n"
"v_accvgpr_read_b32 v160, a148\n"
"v_accvgpr_read_b32 v161, a149\n"
"v_accvgpr_read_b32 v162, a150\n"
"v_accvgpr_read_b32 v163, a151\n"
"v_accvgpr_read_b32 v164, a156\n"
"v_accvgpr_read_b32 v165, a157\n"
"v_accvgpr_read_b32 v166, a158\n"
"v_accvgpr_read_b32 v167, a159\n"
"v_cvt_pk_bf16_f32 v160, v160, v161\n"
"v_cvt_pk_bf16_f32 v161, v162, v163\n"
"v_cvt_pk_bf16_f32 v162, v164, v165\n"
"v_cvt_pk_bf16_f32 v163, v166, v167\n"
"ds_write_b128 v254, v[160:163] offset:3072\n"
"v_accvgpr_read_b32 v168, a160\n"
"v_accvgpr_read_b32 v169, a161\n"
"v_accvgpr_read_b32 v170, a162\n"
"v_accvgpr_read_b32 v171, a163\n"
"v_accvgpr_read_b32 v172, a168\n"
"v_accvgpr_read_b32 v173, a169\n"
"v_accvgpr_read_b32 v174, a170\n"
"v_accvgpr_read_b32 v175, a171\n"
"v_cvt_pk_bf16_f32 v168, v168, v169\n"
"v_cvt_pk_bf16_f32 v169, v170, v171\n"
"v_cvt_pk_bf16_f32 v170, v172, v173\n"
"v_cvt_pk_bf16_f32 v171, v174, v175\n"
"ds_write_b128 v254, v[168:171] offset:4096\n"
"v_accvgpr_read_b32 v136, a164\n"
"v_accvgpr_read_b32 v137, a165\n"
"v_accvgpr_read_b32 v138, a166\n"
"v_accvgpr_read_b32 v139, a167\n"
"v_accvgpr_read_b32 v140, a172\n"
"v_accvgpr_read_b32 v141, a173\n"
"v_accvgpr_read_b32 v142, a174\n"
"v_accvgpr_read_b32 v143, a175\n"
"v_cvt_pk_bf16_f32 v136, v136, v137\n"
"v_cvt_pk_bf16_f32 v137, v138, v139\n"
"v_cvt_pk_bf16_f32 v138, v140, v141\n"
"v_cvt_pk_bf16_f32 v139, v142, v143\n"
"ds_write_b128 v254, v[136:139] offset:5120\n"
"v_accvgpr_read_b32 v144, a176\n"
"v_accvgpr_read_b32 v145, a177\n"
"v_accvgpr_read_b32 v146, a178\n"
"v_accvgpr_read_b32 v147, a179\n"
"v_accvgpr_read_b32 v148, a184\n"
"v_accvgpr_read_b32 v149, a185\n"
"v_accvgpr_read_b32 v150, a186\n"
"v_accvgpr_read_b32 v151, a187\n"
"v_cvt_pk_bf16_f32 v144, v144, v145\n"
"v_cvt_pk_bf16_f32 v145, v146, v147\n"
"v_cvt_pk_bf16_f32 v146, v148, v149\n"
"v_cvt_pk_bf16_f32 v147, v150, v151\n"
"ds_write_b128 v254, v[144:147] offset:6144\n"
"v_accvgpr_read_b32 v152, a180\n"
"v_accvgpr_read_b32 v153, a181\n"
"v_accvgpr_read_b32 v154, a182\n"
"v_accvgpr_read_b32 v155, a183\n"
"v_accvgpr_read_b32 v156, a188\n"
"v_accvgpr_read_b32 v157, a189\n"
"v_accvgpr_read_b32 v158, a190\n"
"v_accvgpr_read_b32 v159, a191\n"
"v_cvt_pk_bf16_f32 v152, v152, v153\n"
"v_cvt_pk_bf16_f32 v153, v154, v155\n"
"v_cvt_pk_bf16_f32 v154, v156, v157\n"
"v_cvt_pk_bf16_f32 v155, v158, v159\n"
"ds_write_b128 v254, v[152:155] offset:7168\n"
"v_accvgpr_read_b32 v160, a192\n"
"v_accvgpr_read_b32 v161, a193\n"
"v_accvgpr_read_b32 v162, a194\n"
"v_accvgpr_read_b32 v163, a195\n"
"v_accvgpr_read_b32 v164, a200\n"
"v_accvgpr_read_b32 v165, a201\n"
"v_accvgpr_read_b32 v166, a202\n"
"v_accvgpr_read_b32 v167, a203\n"
"v_cvt_pk_bf16_f32 v160, v160, v161\n"
"v_cvt_pk_bf16_f32 v161, v162, v163\n"
"v_cvt_pk_bf16_f32 v162, v164, v165\n"
"v_cvt_pk_bf16_f32 v163, v166, v167\n"
"ds_write_b128 v254, v[160:163] offset:8192\n"
"v_accvgpr_read_b32 v168, a196\n"
"v_accvgpr_read_b32 v169, a197\n"
"v_accvgpr_read_b32 v170, a198\n"
"v_accvgpr_read_b32 v171, a199\n"
"v_accvgpr_read_b32 v172, a204\n"
"v_accvgpr_read_b32 v173, a205\n"
"v_accvgpr_read_b32 v174, a206\n"
"v_accvgpr_read_b32 v175, a207\n"
"v_cvt_pk_bf16_f32 v168, v168, v169\n"
"v_cvt_pk_bf16_f32 v169, v170, v171\n"
"v_cvt_pk_bf16_f32 v170, v172, v173\n"
"v_cvt_pk_bf16_f32 v171, v174, v175\n"
"ds_write_b128 v254, v[168:171] offset:9216\n"
"v_accvgpr_read_b32 v136, a208\n"
"v_accvgpr_read_b32 v137, a209\n"
"v_accvgpr_read_b32 v138, a210\n"
"v_accvgpr_read_b32 v139, a211\n"
"v_accvgpr_read_b32 v140, a216\n"
"v_accvgpr_read_b32 v141, a217\n"
"v_accvgpr_read_b32 v142, a218\n"
"v_accvgpr_read_b32 v143, a219\n"
"v_cvt_pk_bf16_f32 v136, v136, v137\n"
"v_cvt_pk_bf16_f32 v137, v138, v139\n"
"v_cvt_pk_bf16_f32 v138, v140, v141\n"
"v_cvt_pk_bf16_f32 v139, v142, v143\n"
"ds_write_b128 v254, v[136:139] offset:10240\n"
"v_accvgpr_read_b32 v144, a212\n"
"v_accvgpr_read_b32 v145, a213\n"
"v_accvgpr_read_b32 v146, a214\n"
"v_accvgpr_read_b32 v147, a215\n"
"v_accvgpr_read_b32 v148, a220\n"
"v_accvgpr_read_b32 v149, a221\n"
"v_accvgpr_read_b32 v150, a222\n"
"v_accvgpr_read_b32 v151, a223\n"
"v_cvt_pk_bf16_f32 v144, v144, v145\n"
"v_cvt_pk_bf16_f32 v145, v146, v147\n"
"v_cvt_pk_bf16_f32 v146, v148, v149\n"
"v_cvt_pk_bf16_f32 v147, v150, v151\n"
"ds_write_b128 v254, v[144:147] offset:11264\n"
"v_accvgpr_read_b32 v152, a224\n"
"v_accvgpr_read_b32 v153, a225\n"
"v_accvgpr_read_b32 v154, a226\n"
"v_accvgpr_read_b32 v155, a227\n"
"v_accvgpr_read_b32 v156, a232\n"
"v_accvgpr_read_b32 v157, a233\n"
"v_accvgpr_read_b32 v158, a234\n"
"v_accvgpr_read_b32 v159, a235\n"
"v_cvt_pk_bf16_f32 v152, v152, v153\n"
"v_cvt_pk_bf16_f32 v153, v154, v155\n"
"v_cvt_pk_bf16_f32 v154, v156, v157\n"
"v_cvt_pk_bf16_f32 v155, v158, v159\n"
"ds_write_b128 v254, v[152:155] offset:12288\n"
"v_accvgpr_read_b32 v160, a228\n"
"v_accvgpr_read_b32 v161, a229\n"
"v_accvgpr_read_b32 v162, a230\n"
"v_accvgpr_read_b32 v163, a231\n"
"v_accvgpr_read_b32 v164, a236\n"
"v_accvgpr_read_b32 v165, a237\n"
"v_accvgpr_read_b32 v166, a238\n"
"v_accvgpr_read_b32 v167, a239\n"
"v_cvt_pk_bf16_f32 v160, v160, v161\n"
"v_cvt_pk_bf16_f32 v161, v162, v163\n"
"v_cvt_pk_bf16_f32 v162, v164, v165\n"
"v_cvt_pk_bf16_f32 v163, v166, v167\n"
"ds_write_b128 v254, v[160:163] offset:13312\n"
"v_accvgpr_read_b32 v168, a240\n"
"v_accvgpr_read_b32 v169, a241\n"
"v_accvgpr_read_b32 v170, a242\n"
"v_accvgpr_read_b32 v171, a243\n"
"v_accvgpr_read_b32 v172, a248\n"
"v_accvgpr_read_b32 v173, a249\n"
"v_accvgpr_read_b32 v174, a250\n"
"v_accvgpr_read_b32 v175, a251\n"
"v_cvt_pk_bf16_f32 v168, v168, v169\n"
"v_cvt_pk_bf16_f32 v169, v170, v171\n"
"v_cvt_pk_bf16_f32 v170, v172, v173\n"
"v_cvt_pk_bf16_f32 v171, v174, v175\n"
"ds_write_b128 v254, v[168:171] offset:14336\n"
"v_accvgpr_read_b32 v136, a244\n"
"v_accvgpr_read_b32 v137, a245\n"
"v_accvgpr_read_b32 v138, a246\n"
"v_accvgpr_read_b32 v139, a247\n"
"v_accvgpr_read_b32 v140, a252\n"
"v_accvgpr_read_b32 v141, a253\n"
"v_accvgpr_read_b32 v142, a254\n"
"v_accvgpr_read_b32 v143, a255\n"
"v_cvt_pk_bf16_f32 v136, v136, v137\n"
"v_cvt_pk_bf16_f32 v137, v138, v139\n"
"v_cvt_pk_bf16_f32 v138, v140, v141\n"
"v_cvt_pk_bf16_f32 v139, v142, v143\n"
"ds_write_b128 v254, v[136:139] offset:15360\n"
"ds_write_b128 v247, v[240:243] offset:19456\n"
"s_waitcnt vmcnt(0) lgkmcnt(0)\n"
"s_barrier\n"
"s_branch .Ljoin_%=\n"
".Lrare1_8_%=:\n"
"s_getpc_b64 s[52:53]\n"
".Lpc1_8_%=:\n"
"s_add_u32 s52, s52, .Lret1_8_%=-.Lpc1_8_%=\n"
"s_addc_u32 s53, s53, -1\n"
"s_branch .Lrarebody1_%=\n"
".Lrare2_9_%=:\n"
"s_getpc_b64 s[52:53]\n"
".Lpc2_9_%=:\n"
"s_add_u32 s52, s52, .Lret2_9_%=-.Lpc2_9_%=\n"
"s_addc_u32 s53, s53, -1\n"
"s_branch .Lrarebody2_%=\n"
".Lrare1_10_%=:\n"
"s_getpc_b64 s[52:53]\n"
".Lpc1_10_%=:\n"
"s_add_u32 s52, s52, .Lret1_10_%=-.Lpc1_10_%=\n"
"s_addc_u32 s53, s53, -1\n"
"s_branch .Lrarebody1_%=\n"
".Lrare2_11_%=:\n"
"s_getpc_b64 s[52:53]\n"
".Lpc2_11_%=:\n"
"s_add_u32 s52, s52, .Lret2_11_%=-.Lpc2_11_%=\n"
"s_addc_u32 s53, s53, -1\n"
"s_branch .Lrarebody2_%=\n"
".Lrare1_12_%=:\n"
"s_getpc_b64 s[52:53]\n"
".Lpc1_12_%=:\n"
"s_add_u32 s52, s52, .Lret1_12_%=-.Lpc1_12_%=\n"
"s_addc_u32 s53, s53, -1\n"
"s_branch .Lrarebody1_%=\n"
".Lrare2_13_%=:\n"
"s_getpc_b64 s[52:53]\n"
".Lpc2_13_%=:\n"
"s_add_u32 s52, s52, .Lret2_13_%=-.Lpc2_13_%=\n"
"s_addc_u32 s53, s53, -1\n"
"s_branch .Lrarebody2_%=\n"
".Lrarebody1_%=:\n"
"s_nop 7\n"
"v_max3_f32 v244, v128, v129, v130\n"
"v_max3_f32 v244, v131, v144, v244\n"
"v_max3_f32 v244, v145, v146, v244\n"
"v_max_f32 v244, v147, v244\n"
"v_mov_b32 v245, v244\n"
"s_nop 1\n"
"v_permlane32_swap_b32 v244, v245\n"
"s_nop 1\n"
"v_max_f32 v244, v244, v245\n"
"v_mov_b32 v245, v244\n"
"s_nop 1\n"
"v_permlane16_swap_b32 v244, v245\n"
"s_nop 1\n"
"v_max_f32 v244, v244, v245\n"
"v_max_f32 v244, 0, v244\n"
"v_sub_f32 v128, v128, v244\n"
"v_sub_f32 v129, v129, v244\n"
"v_sub_f32 v130, v130, v244\n"
"v_sub_f32 v131, v131, v244\n"
"v_sub_f32 v144, v144, v244\n"
"v_sub_f32 v145, v145, v244\n"
"v_sub_f32 v146, v146, v244\n"
"v_sub_f32 v147, v147, v244\n"
"v_sub_f32 v192, v192, v244\n"
"v_sub_f32 v193, v193, v244\n"
"v_sub_f32 v194, v194, v244\n"
"v_sub_f32 v195, v195, v244\n"
"v_exp_f32 v245, -v244\n"
"s_nop 0\n"
"v_mul_f32 v240, v240, v245\n"
"v_mbcnt_lo_u32_b32 v244, -1, 0\n"
"v_mbcnt_hi_u32_b32 v244, -1, v244\n"
"v_lshlrev_b32 v244, 2, v244\n"
"v_add_u32 v244, s51, v244\n"
"global_store_dword v244, v245, s[94:95] offset:0 sc1\n"
"s_waitcnt vmcnt(0)\n"
"v_max3_f32 v244, v132, v133, v134\n"
"v_max3_f32 v244, v135, v148, v244\n"
"v_max3_f32 v244, v149, v150, v244\n"
"v_max_f32 v244, v151, v244\n"
"v_mov_b32 v245, v244\n"
"s_nop 1\n"
"v_permlane32_swap_b32 v244, v245\n"
"s_nop 1\n"
"v_max_f32 v244, v244, v245\n"
"v_mov_b32 v245, v244\n"
"s_nop 1\n"
"v_permlane16_swap_b32 v244, v245\n"
"s_nop 1\n"
"v_max_f32 v244, v244, v245\n"
"v_max_f32 v244, 0, v244\n"
"v_sub_f32 v132, v132, v244\n"
"v_sub_f32 v133, v133, v244\n"
"v_sub_f32 v134, v134, v244\n"
"v_sub_f32 v135, v135, v244\n"
"v_sub_f32 v148, v148, v244\n"
"v_sub_f32 v149, v149, v244\n"
"v_sub_f32 v150, v150, v244\n"
"v_sub_f32 v151, v151, v244\n"
"v_sub_f32 v196, v196, v244\n"
"v_sub_f32 v197, v197, v244\n"
"v_sub_f32 v198, v198, v244\n"
"v_sub_f32 v199, v199, v244\n"
"v_exp_f32 v245, -v244\n"
"s_nop 0\n"
"v_mul_f32 v241, v241, v245\n"
"v_mbcnt_lo_u32_b32 v244, -1, 0\n"
"v_mbcnt_hi_u32_b32 v244, -1, v244\n"
"v_lshlrev_b32 v244, 2, v244\n"
"v_add_u32 v244, s51, v244\n"
"global_store_dword v244, v245, s[94:95] offset:256 sc1\n"
"s_waitcnt vmcnt(0)\n"
"v_max3_f32 v244, v136, v137, v138\n"
"v_max3_f32 v244, v139, v152, v244\n"
"v_max3_f32 v244, v153, v154, v244\n"
"v_max_f32 v244, v155, v244\n"
"v_mov_b32 v245, v244\n"
"s_nop 1\n"
"v_permlane32_swap_b32 v244, v245\n"
"s_nop 1\n"
"v_max_f32 v244, v244, v245\n"
"v_mov_b32 v245, v244\n"
"s_nop 1\n"
"v_permlane16_swap_b32 v244, v245\n"
"s_nop 1\n"
"v_max_f32 v244, v244, v245\n"
"v_max_f32 v244, 0, v244\n"
"v_sub_f32 v136, v136, v244\n"
"v_sub_f32 v137, v137, v244\n"
"v_sub_f32 v138, v138, v244\n"
"v_sub_f32 v139, v139, v244\n"
"v_sub_f32 v152, v152, v244\n"
"v_sub_f32 v153, v153, v244\n"
"v_sub_f32 v154, v154, v244\n"
"v_sub_f32 v155, v155, v244\n"
"v_sub_f32 v200, v200, v244\n"
"v_sub_f32 v201, v201, v244\n"
"v_sub_f32 v202, v202, v244\n"
"v_sub_f32 v203, v203, v244\n"
"v_exp_f32 v245, -v244\n"
"s_nop 0\n"
"v_mul_f32 v242, v242, v245\n"
"v_mbcnt_lo_u32_b32 v244, -1, 0\n"
"v_mbcnt_hi_u32_b32 v244, -1, v244\n"
"v_lshlrev_b32 v244, 2, v244\n"
"v_add_u32 v244, s51, v244\n"
"global_store_dword v244, v245, s[94:95] offset:512 sc1\n"
"s_waitcnt vmcnt(0)\n"
"v_max3_f32 v244, v140, v141, v142\n"
"v_max3_f32 v244, v143, v156, v244\n"
"v_max3_f32 v244, v157, v158, v244\n"
"v_max_f32 v244, v159, v244\n"
"v_mov_b32 v245, v244\n"
"s_nop 1\n"
"v_permlane32_swap_b32 v244, v245\n"
"s_nop 1\n"
"v_max_f32 v244, v244, v245\n"
"v_mov_b32 v245, v244\n"
"s_nop 1\n"
"v_permlane16_swap_b32 v244, v245\n"
"s_nop 1\n"
"v_max_f32 v244, v244, v245\n"
"v_max_f32 v244, 0, v244\n"
"v_sub_f32 v140, v140, v244\n"
"v_sub_f32 v141, v141, v244\n"
"v_sub_f32 v142, v142, v244\n"
"v_sub_f32 v143, v143, v244\n"
"v_sub_f32 v156, v156, v244\n"
"v_sub_f32 v157, v157, v244\n"
"v_sub_f32 v158, v158, v244\n"
"v_sub_f32 v159, v159, v244\n"
"v_sub_f32 v204, v204, v244\n"
"v_sub_f32 v205, v205, v244\n"
"v_sub_f32 v206, v206, v244\n"
"v_sub_f32 v207, v207, v244\n"
"v_exp_f32 v245, -v244\n"
"s_nop 0\n"
"v_mul_f32 v243, v243, v245\n"
"v_mbcnt_lo_u32_b32 v244, -1, 0\n"
"v_mbcnt_hi_u32_b32 v244, -1, v244\n"
"v_lshlrev_b32 v244, 2, v244\n"
"v_add_u32 v244, s51, v244\n"
"global_store_dword v244, v245, s[94:95] offset:768 sc1\n"
"s_waitcnt vmcnt(0)\n"
"s_mov_b32 s49, 1\n"
"s_setpc_b64 s[52:53]\n"
".Lrarebody2_%=:\n"
"s_nop 7\n"
"v_mbcnt_lo_u32_b32 v244, -1, 0\n"
"v_mbcnt_hi_u32_b32 v244, -1, v244\n"
"v_lshlrev_b32 v244, 2, v244\n"
"v_add_u32 v244, s51, v244\n"
"global_load_dword v245, v244, s[94:95] offset:0 sc1\n"
"s_waitcnt vmcnt(0)\n"
"v_mul_f32 v0, v0, v245\n"
"v_mul_f32 v1, v1, v245\n"
"v_mul_f32 v2, v2, v245\n"
"v_mul_f32 v3, v3, v245\n"
"v_mul_f32 v8, v8, v245\n"
"v_mul_f32 v9, v9, v245\n"
"v_mul_f32 v10, v10, v245\n"
"v_mul_f32 v11, v11, v245\n"
"v_mul_f32 v16, v16, v245\n"
"v_mul_f32 v17, v17, v245\n"
"v_mul_f32 v18, v18, v245\n"
"v_mul_f32 v19, v19, v245\n"
"v_mul_f32 v24, v24, v245\n"
"v_mul_f32 v25, v25, v245\n"
"v_mul_f32 v26, v26, v245\n"
"v_mul_f32 v27, v27, v245\n"
"v_mul_f32 v32, v32, v245\n"
"v_mul_f32 v33, v33, v245\n"
"v_mul_f32 v34, v34, v245\n"
"v_mul_f32 v35, v35, v245\n"
"v_mul_f32 v40, v40, v245\n"
"v_mul_f32 v41, v41, v245\n"
"v_mul_f32 v42, v42, v245\n"
"v_mul_f32 v43, v43, v245\n"
"v_mul_f32 v48, v48, v245\n"
"v_mul_f32 v49, v49, v245\n"
"v_mul_f32 v50, v50, v245\n"
"v_mul_f32 v51, v51, v245\n"
"v_mul_f32 v56, v56, v245\n"
"v_mul_f32 v57, v57, v245\n"
"v_mul_f32 v58, v58, v245\n"
"v_mul_f32 v59, v59, v245\n"
"v_mul_f32 v64, v64, v245\n"
"v_mul_f32 v65, v65, v245\n"
"v_mul_f32 v66, v66, v245\n"
"v_mul_f32 v67, v67, v245\n"
"v_mul_f32 v72, v72, v245\n"
"v_mul_f32 v73, v73, v245\n"
"v_mul_f32 v74, v74, v245\n"
"v_mul_f32 v75, v75, v245\n"
"v_mul_f32 v80, v80, v245\n"
"v_mul_f32 v81, v81, v245\n"
"v_mul_f32 v82, v82, v245\n"
"v_mul_f32 v83, v83, v245\n"
"v_mul_f32 v88, v88, v245\n"
"v_mul_f32 v89, v89, v245\n"
"v_mul_f32 v90, v90, v245\n"
"v_mul_f32 v91, v91, v245\n"
"v_mul_f32 v96, v96, v245\n"
"v_mul_f32 v97, v97, v245\n"
"v_mul_f32 v98, v98, v245\n"
"v_mul_f32 v99, v99, v245\n"
"v_mul_f32 v104, v104, v245\n"
"v_mul_f32 v105, v105, v245\n"
"v_mul_f32 v106, v106, v245\n"
"v_mul_f32 v107, v107, v245\n"
"v_mul_f32 v112, v112, v245\n"
"v_mul_f32 v113, v113, v245\n"
"v_mul_f32 v114, v114, v245\n"
"v_mul_f32 v115, v115, v245\n"
"v_mul_f32 v120, v120, v245\n"
"v_mul_f32 v121, v121, v245\n"
"v_mul_f32 v122, v122, v245\n"
"v_mul_f32 v123, v123, v245\n"
"v_mbcnt_lo_u32_b32 v244, -1, 0\n"
"v_mbcnt_hi_u32_b32 v244, -1, v244\n"
"v_lshlrev_b32 v244, 2, v244\n"
"v_add_u32 v244, s51, v244\n"
"global_load_dword v245, v244, s[94:95] offset:256 sc1\n"
"s_waitcnt vmcnt(0)\n"
"v_mul_f32 v4, v4, v245\n"
"v_mul_f32 v5, v5, v245\n"
"v_mul_f32 v6, v6, v245\n"
"v_mul_f32 v7, v7, v245\n"
"v_mul_f32 v12, v12, v245\n"
"v_mul_f32 v13, v13, v245\n"
"v_mul_f32 v14, v14, v245\n"
"v_mul_f32 v15, v15, v245\n"
"v_mul_f32 v20, v20, v245\n"
"v_mul_f32 v21, v21, v245\n"
"v_mul_f32 v22, v22, v245\n"
"v_mul_f32 v23, v23, v245\n"
"v_mul_f32 v28, v28, v245\n"
"v_mul_f32 v29, v29, v245\n"
"v_mul_f32 v30, v30, v245\n"
"v_mul_f32 v31, v31, v245\n"
"v_mul_f32 v36, v36, v245\n"
"v_mul_f32 v37, v37, v245\n"
"v_mul_f32 v38, v38, v245\n"
"v_mul_f32 v39, v39, v245\n"
"v_mul_f32 v44, v44, v245\n"
"v_mul_f32 v45, v45, v245\n"
"v_mul_f32 v46, v46, v245\n"
"v_mul_f32 v47, v47, v245\n"
"v_mul_f32 v52, v52, v245\n"
"v_mul_f32 v53, v53, v245\n"
"v_mul_f32 v54, v54, v245\n"
"v_mul_f32 v55, v55, v245\n"
"v_mul_f32 v60, v60, v245\n"
"v_mul_f32 v61, v61, v245\n"
"v_mul_f32 v62, v62, v245\n"
"v_mul_f32 v63, v63, v245\n"
"v_mul_f32 v68, v68, v245\n"
"v_mul_f32 v69, v69, v245\n"
"v_mul_f32 v70, v70, v245\n"
"v_mul_f32 v71, v71, v245\n"
"v_mul_f32 v76, v76, v245\n"
"v_mul_f32 v77, v77, v245\n"
"v_mul_f32 v78, v78, v245\n"
"v_mul_f32 v79, v79, v245\n"
"v_mul_f32 v84, v84, v245\n"
"v_mul_f32 v85, v85, v245\n"
"v_mul_f32 v86, v86, v245\n"
"v_mul_f32 v87, v87, v245\n"
"v_mul_f32 v92, v92, v245\n"
"v_mul_f32 v93, v93, v245\n"
"v_mul_f32 v94, v94, v245\n"
"v_mul_f32 v95, v95, v245\n"
"v_mul_f32 v100, v100, v245\n"
"v_mul_f32 v101, v101, v245\n"
"v_mul_f32 v102, v102, v245\n"
"v_mul_f32 v103, v103, v245\n"
"v_mul_f32 v108, v108, v245\n"
"v_mul_f32 v109, v109, v245\n"
"v_mul_f32 v110, v110, v245\n"
"v_mul_f32 v111, v111, v245\n"
"v_mul_f32 v116, v116, v245\n"
"v_mul_f32 v117, v117, v245\n"
"v_mul_f32 v118, v118, v245\n"
"v_mul_f32 v119, v119, v245\n"
"v_mul_f32 v124, v124, v245\n"
"v_mul_f32 v125, v125, v245\n"
"v_mul_f32 v126, v126, v245\n"
"v_mul_f32 v127, v127, v245\n"
"v_mbcnt_lo_u32_b32 v244, -1, 0\n"
"v_mbcnt_hi_u32_b32 v244, -1, v244\n"
"v_lshlrev_b32 v244, 2, v244\n"
"v_add_u32 v244, s51, v244\n"
"global_load_dword v245, v244, s[94:95] offset:512 sc1\n"
"s_waitcnt vmcnt(0)\n"
"v_accvgpr_read_b32 v244, a128\n"
"v_mul_f32 v244, v244, v245\n"
"v_accvgpr_write_b32 a128, v244\n"
"v_accvgpr_read_b32 v244, a129\n"
"v_mul_f32 v244, v244, v245\n"
"v_accvgpr_write_b32 a129, v244\n"
"v_accvgpr_read_b32 v244, a130\n"
"v_mul_f32 v244, v244, v245\n"
"v_accvgpr_write_b32 a130, v244\n"
"v_accvgpr_read_b32 v244, a131\n"
"v_mul_f32 v244, v244, v245\n"
"v_accvgpr_write_b32 a131, v244\n"
"v_accvgpr_read_b32 v244, a136\n"
"v_mul_f32 v244, v244, v245\n"
"v_accvgpr_write_b32 a136, v244\n"
"v_accvgpr_read_b32 v244, a137\n"
"v_mul_f32 v244, v244, v245\n"
"v_accvgpr_write_b32 a137, v244\n"
"v_accvgpr_read_b32 v244, a138\n"
"v_mul_f32 v244, v244, v245\n"
"v_accvgpr_write_b32 a138, v244\n"
"v_accvgpr_read_b32 v244, a139\n"
"v_mul_f32 v244, v244, v245\n"
"v_accvgpr_write_b32 a139, v244\n"
"v_accvgpr_read_b32 v244, a144\n"
"v_mul_f32 v244, v244, v245\n"
"v_accvgpr_write_b32 a144, v244\n"
"v_accvgpr_read_b32 v244, a145\n"
"v_mul_f32 v244, v244, v245\n"
"v_accvgpr_write_b32 a145, v244\n"
"v_accvgpr_read_b32 v244, a146\n"
"v_mul_f32 v244, v244, v245\n"
"v_accvgpr_write_b32 a146, v244\n"
"v_accvgpr_read_b32 v244, a147\n"
"v_mul_f32 v244, v244, v245\n"
"v_accvgpr_write_b32 a147, v244\n"
"v_accvgpr_read_b32 v244, a152\n"
"v_mul_f32 v244, v244, v245\n"
"v_accvgpr_write_b32 a152, v244\n"
"v_accvgpr_read_b32 v244, a153\n"
"v_mul_f32 v244, v244, v245\n"
"v_accvgpr_write_b32 a153, v244\n"
"v_accvgpr_read_b32 v244, a154\n"
"v_mul_f32 v244, v244, v245\n"
"v_accvgpr_write_b32 a154, v244\n"
"v_accvgpr_read_b32 v244, a155\n"
"v_mul_f32 v244, v244, v245\n"
"v_accvgpr_write_b32 a155, v244\n"
"v_accvgpr_read_b32 v244, a160\n"
"v_mul_f32 v244, v244, v245\n"
"v_accvgpr_write_b32 a160, v244\n"
"v_accvgpr_read_b32 v244, a161\n"
"v_mul_f32 v244, v244, v245\n"
"v_accvgpr_write_b32 a161, v244\n"
"v_accvgpr_read_b32 v244, a162\n"
"v_mul_f32 v244, v244, v245\n"
"v_accvgpr_write_b32 a162, v244\n"
"v_accvgpr_read_b32 v244, a163\n"
"v_mul_f32 v244, v244, v245\n"
"v_accvgpr_write_b32 a163, v244\n"
"v_accvgpr_read_b32 v244, a168\n"
"v_mul_f32 v244, v244, v245\n"
"v_accvgpr_write_b32 a168, v244\n"
"v_accvgpr_read_b32 v244, a169\n"
"v_mul_f32 v244, v244, v245\n"
"v_accvgpr_write_b32 a169, v244\n"
"v_accvgpr_read_b32 v244, a170\n"
"v_mul_f32 v244, v244, v245\n"
"v_accvgpr_write_b32 a170, v244\n"
"v_accvgpr_read_b32 v244, a171\n"
"v_mul_f32 v244, v244, v245\n"
"v_accvgpr_write_b32 a171, v244\n"
"v_accvgpr_read_b32 v244, a176\n"
"v_mul_f32 v244, v244, v245\n"
"v_accvgpr_write_b32 a176, v244\n"
"v_accvgpr_read_b32 v244, a177\n"
"v_mul_f32 v244, v244, v245\n"
"v_accvgpr_write_b32 a177, v244\n"
"v_accvgpr_read_b32 v244, a178\n"
"v_mul_f32 v244, v244, v245\n"
"v_accvgpr_write_b32 a178, v244\n"
"v_accvgpr_read_b32 v244, a179\n"
"v_mul_f32 v244, v244, v245\n"
"v_accvgpr_write_b32 a179, v244\n"
"v_accvgpr_read_b32 v244, a184\n"
"v_mul_f32 v244, v244, v245\n"
"v_accvgpr_write_b32 a184, v244\n"
"v_accvgpr_read_b32 v244, a185\n"
"v_mul_f32 v244, v244, v245\n"
"v_accvgpr_write_b32 a185, v244\n"
"v_accvgpr_read_b32 v244, a186\n"
"v_mul_f32 v244, v244, v245\n"
"v_accvgpr_write_b32 a186, v244\n"
"v_accvgpr_read_b32 v244, a187\n"
"v_mul_f32 v244, v244, v245\n"
"v_accvgpr_write_b32 a187, v244\n"
"v_accvgpr_read_b32 v244, a192\n"
"v_mul_f32 v244, v244, v245\n"
"v_accvgpr_write_b32 a192, v244\n"
"v_accvgpr_read_b32 v244, a193\n"
"v_mul_f32 v244, v244, v245\n"
"v_accvgpr_write_b32 a193, v244\n"
"v_accvgpr_read_b32 v244, a194\n"
"v_mul_f32 v244, v244, v245\n"
"v_accvgpr_write_b32 a194, v244\n"
"v_accvgpr_read_b32 v244, a195\n"
"v_mul_f32 v244, v244, v245\n"
"v_accvgpr_write_b32 a195, v244\n"
"v_accvgpr_read_b32 v244, a200\n"
"v_mul_f32 v244, v244, v245\n"
"v_accvgpr_write_b32 a200, v244\n"
"v_accvgpr_read_b32 v244, a201\n"
"v_mul_f32 v244, v244, v245\n"
"v_accvgpr_write_b32 a201, v244\n"
"v_accvgpr_read_b32 v244, a202\n"
"v_mul_f32 v244, v244, v245\n"
"v_accvgpr_write_b32 a202, v244\n"
"v_accvgpr_read_b32 v244, a203\n"
"v_mul_f32 v244, v244, v245\n"
"v_accvgpr_write_b32 a203, v244\n"
"v_accvgpr_read_b32 v244, a208\n"
"v_mul_f32 v244, v244, v245\n"
"v_accvgpr_write_b32 a208, v244\n"
"v_accvgpr_read_b32 v244, a209\n"
"v_mul_f32 v244, v244, v245\n"
"v_accvgpr_write_b32 a209, v244\n"
"v_accvgpr_read_b32 v244, a210\n"
"v_mul_f32 v244, v244, v245\n"
"v_accvgpr_write_b32 a210, v244\n"
"v_accvgpr_read_b32 v244, a211\n"
"v_mul_f32 v244, v244, v245\n"
"v_accvgpr_write_b32 a211, v244\n"
"v_accvgpr_read_b32 v244, a216\n"
"v_mul_f32 v244, v244, v245\n"
"v_accvgpr_write_b32 a216, v244\n"
"v_accvgpr_read_b32 v244, a217\n"
"v_mul_f32 v244, v244, v245\n"
"v_accvgpr_write_b32 a217, v244\n"
"v_accvgpr_read_b32 v244, a218\n"
"v_mul_f32 v244, v244, v245\n"
"v_accvgpr_write_b32 a218, v244\n"
"v_accvgpr_read_b32 v244, a219\n"
"v_mul_f32 v244, v244, v245\n"
"v_accvgpr_write_b32 a219, v244\n"
"v_accvgpr_read_b32 v244, a224\n"
"v_mul_f32 v244, v244, v245\n"
"v_accvgpr_write_b32 a224, v244\n"
"v_accvgpr_read_b32 v244, a225\n"
"v_mul_f32 v244, v244, v245\n"
"v_accvgpr_write_b32 a225, v244\n"
"v_accvgpr_read_b32 v244, a226\n"
"v_mul_f32 v244, v244, v245\n"
"v_accvgpr_write_b32 a226, v244\n"
"v_accvgpr_read_b32 v244, a227\n"
"v_mul_f32 v244, v244, v245\n"
"v_accvgpr_write_b32 a227, v244\n"
"v_accvgpr_read_b32 v244, a232\n"
"v_mul_f32 v244, v244, v245\n"
"v_accvgpr_write_b32 a232, v244\n"
"v_accvgpr_read_b32 v244, a233\n"
"v_mul_f32 v244, v244, v245\n"
"v_accvgpr_write_b32 a233, v244\n"
"v_accvgpr_read_b32 v244, a234\n"
"v_mul_f32 v244, v244, v245\n"
"v_accvgpr_write_b32 a234, v244\n"
"v_accvgpr_read_b32 v244, a235\n"
"v_mul_f32 v244, v244, v245\n"
"v_accvgpr_write_b32 a235, v244\n"
"v_accvgpr_read_b32 v244, a240\n"
"v_mul_f32 v244, v244, v245\n"
"v_accvgpr_write_b32 a240, v244\n"
"v_accvgpr_read_b32 v244, a241\n"
"v_mul_f32 v244, v244, v245\n"
"v_accvgpr_write_b32 a241, v244\n"
"v_accvgpr_read_b32 v244, a242\n"
"v_mul_f32 v244, v244, v245\n"
"v_accvgpr_write_b32 a242, v244\n"
"v_accvgpr_read_b32 v244, a243\n"
"v_mul_f32 v244, v244, v245\n"
"v_accvgpr_write_b32 a243, v244\n"
"v_accvgpr_read_b32 v244, a248\n"
"v_mul_f32 v244, v244, v245\n"
"v_accvgpr_write_b32 a248, v244\n"
"v_accvgpr_read_b32 v244, a249\n"
"v_mul_f32 v244, v244, v245\n"
"v_accvgpr_write_b32 a249, v244\n"
"v_accvgpr_read_b32 v244, a250\n"
"v_mul_f32 v244, v244, v245\n"
"v_accvgpr_write_b32 a250, v244\n"
"v_accvgpr_read_b32 v244, a251\n"
"v_mul_f32 v244, v244, v245\n"
"v_accvgpr_write_b32 a251, v244\n"
"v_mbcnt_lo_u32_b32 v244, -1, 0\n"
"v_mbcnt_hi_u32_b32 v244, -1, v244\n"
"v_lshlrev_b32 v244, 2, v244\n"
"v_add_u32 v244, s51, v244\n"
"global_load_dword v245, v244, s[94:95] offset:768 sc1\n"
"s_waitcnt vmcnt(0)\n"
"v_accvgpr_read_b32 v244, a132\n"
"v_mul_f32 v244, v244, v245\n"
"v_accvgpr_write_b32 a132, v244\n"
"v_accvgpr_read_b32 v244, a133\n"
"v_mul_f32 v244, v244, v245\n"
"v_accvgpr_write_b32 a133, v244\n"
"v_accvgpr_read_b32 v244, a134\n"
"v_mul_f32 v244, v244, v245\n"
"v_accvgpr_write_b32 a134, v244\n"
"v_accvgpr_read_b32 v244, a135\n"
"v_mul_f32 v244, v244, v245\n"
"v_accvgpr_write_b32 a135, v244\n"
"v_accvgpr_read_b32 v244, a140\n"
"v_mul_f32 v244, v244, v245\n"
"v_accvgpr_write_b32 a140, v244\n"
"v_accvgpr_read_b32 v244, a141\n"
"v_mul_f32 v244, v244, v245\n"
"v_accvgpr_write_b32 a141, v244\n"
"v_accvgpr_read_b32 v244, a142\n"
"v_mul_f32 v244, v244, v245\n"
"v_accvgpr_write_b32 a142, v244\n"
"v_accvgpr_read_b32 v244, a143\n"
"v_mul_f32 v244, v244, v245\n"
"v_accvgpr_write_b32 a143, v244\n"
"v_accvgpr_read_b32 v244, a148\n"
"v_mul_f32 v244, v244, v245\n"
"v_accvgpr_write_b32 a148, v244\n"
"v_accvgpr_read_b32 v244, a149\n"
"v_mul_f32 v244, v244, v245\n"
"v_accvgpr_write_b32 a149, v244\n"
"v_accvgpr_read_b32 v244, a150\n"
"v_mul_f32 v244, v244, v245\n"
"v_accvgpr_write_b32 a150, v244\n"
"v_accvgpr_read_b32 v244, a151\n"
"v_mul_f32 v244, v244, v245\n"
"v_accvgpr_write_b32 a151, v244\n"
"v_accvgpr_read_b32 v244, a156\n"
"v_mul_f32 v244, v244, v245\n"
"v_accvgpr_write_b32 a156, v244\n"
"v_accvgpr_read_b32 v244, a157\n"
"v_mul_f32 v244, v244, v245\n"
"v_accvgpr_write_b32 a157, v244\n"
"v_accvgpr_read_b32 v244, a158\n"
"v_mul_f32 v244, v244, v245\n"
"v_accvgpr_write_b32 a158, v244\n"
"v_accvgpr_read_b32 v244, a159\n"
"v_mul_f32 v244, v244, v245\n"
"v_accvgpr_write_b32 a159, v244\n"
"v_accvgpr_read_b32 v244, a164\n"
"v_mul_f32 v244, v244, v245\n"
"v_accvgpr_write_b32 a164, v244\n"
"v_accvgpr_read_b32 v244, a165\n"
"v_mul_f32 v244, v244, v245\n"
"v_accvgpr_write_b32 a165, v244\n"
"v_accvgpr_read_b32 v244, a166\n"
"v_mul_f32 v244, v244, v245\n"
"v_accvgpr_write_b32 a166, v244\n"
"v_accvgpr_read_b32 v244, a167\n"
"v_mul_f32 v244, v244, v245\n"
"v_accvgpr_write_b32 a167, v244\n"
"v_accvgpr_read_b32 v244, a172\n"
"v_mul_f32 v244, v244, v245\n"
"v_accvgpr_write_b32 a172, v244\n"
"v_accvgpr_read_b32 v244, a173\n"
"v_mul_f32 v244, v244, v245\n"
"v_accvgpr_write_b32 a173, v244\n"
"v_accvgpr_read_b32 v244, a174\n"
"v_mul_f32 v244, v244, v245\n"
"v_accvgpr_write_b32 a174, v244\n"
"v_accvgpr_read_b32 v244, a175\n"
"v_mul_f32 v244, v244, v245\n"
"v_accvgpr_write_b32 a175, v244\n"
"v_accvgpr_read_b32 v244, a180\n"
"v_mul_f32 v244, v244, v245\n"
"v_accvgpr_write_b32 a180, v244\n"
"v_accvgpr_read_b32 v244, a181\n"
"v_mul_f32 v244, v244, v245\n"
"v_accvgpr_write_b32 a181, v244\n"
"v_accvgpr_read_b32 v244, a182\n"
"v_mul_f32 v244, v244, v245\n"
"v_accvgpr_write_b32 a182, v244\n"
"v_accvgpr_read_b32 v244, a183\n"
"v_mul_f32 v244, v244, v245\n"
"v_accvgpr_write_b32 a183, v244\n"
"v_accvgpr_read_b32 v244, a188\n"
"v_mul_f32 v244, v244, v245\n"
"v_accvgpr_write_b32 a188, v244\n"
"v_accvgpr_read_b32 v244, a189\n"
"v_mul_f32 v244, v244, v245\n"
"v_accvgpr_write_b32 a189, v244\n"
"v_accvgpr_read_b32 v244, a190\n"
"v_mul_f32 v244, v244, v245\n"
"v_accvgpr_write_b32 a190, v244\n"
"v_accvgpr_read_b32 v244, a191\n"
"v_mul_f32 v244, v244, v245\n"
"v_accvgpr_write_b32 a191, v244\n"
"v_accvgpr_read_b32 v244, a196\n"
"v_mul_f32 v244, v244, v245\n"
"v_accvgpr_write_b32 a196, v244\n"
"v_accvgpr_read_b32 v244, a197\n"
"v_mul_f32 v244, v244, v245\n"
"v_accvgpr_write_b32 a197, v244\n"
"v_accvgpr_read_b32 v244, a198\n"
"v_mul_f32 v244, v244, v245\n"
"v_accvgpr_write_b32 a198, v244\n"
"v_accvgpr_read_b32 v244, a199\n"
"v_mul_f32 v244, v244, v245\n"
"v_accvgpr_write_b32 a199, v244\n"
"v_accvgpr_read_b32 v244, a204\n"
"v_mul_f32 v244, v244, v245\n"
"v_accvgpr_write_b32 a204, v244\n"
"v_accvgpr_read_b32 v244, a205\n"
"v_mul_f32 v244, v244, v245\n"
"v_accvgpr_write_b32 a205, v244\n"
"v_accvgpr_read_b32 v244, a206\n"
"v_mul_f32 v244, v244, v245\n"
"v_accvgpr_write_b32 a206, v244\n"
"v_accvgpr_read_b32 v244, a207\n"
"v_mul_f32 v244, v244, v245\n"
"v_accvgpr_write_b32 a207, v244\n"
"v_accvgpr_read_b32 v244, a212\n"
"v_mul_f32 v244, v244, v245\n"
"v_accvgpr_write_b32 a212, v244\n"
"v_accvgpr_read_b32 v244, a213\n"
"v_mul_f32 v244, v244, v245\n"
"v_accvgpr_write_b32 a213, v244\n"
"v_accvgpr_read_b32 v244, a214\n"
"v_mul_f32 v244, v244, v245\n"
"v_accvgpr_write_b32 a214, v244\n"
"v_accvgpr_read_b32 v244, a215\n"
"v_mul_f32 v244, v244, v245\n"
"v_accvgpr_write_b32 a215, v244\n"
"v_accvgpr_read_b32 v244, a220\n"
"v_mul_f32 v244, v244, v245\n"
"v_accvgpr_write_b32 a220, v244\n"
"v_accvgpr_read_b32 v244, a221\n"
"v_mul_f32 v244, v244, v245\n"
"v_accvgpr_write_b32 a221, v244\n"
"v_accvgpr_read_b32 v244, a222\n"
"v_mul_f32 v244, v244, v245\n"
"v_accvgpr_write_b32 a222, v244\n"
"v_accvgpr_read_b32 v244, a223\n"
"v_mul_f32 v244, v244, v245\n"
"v_accvgpr_write_b32 a223, v244\n"
"v_accvgpr_read_b32 v244, a228\n"
"v_mul_f32 v244, v244, v245\n"
"v_accvgpr_write_b32 a228, v244\n"
"v_accvgpr_read_b32 v244, a229\n"
"v_mul_f32 v244, v244, v245\n"
"v_accvgpr_write_b32 a229, v244\n"
"v_accvgpr_read_b32 v244, a230\n"
"v_mul_f32 v244, v244, v245\n"
"v_accvgpr_write_b32 a230, v244\n"
"v_accvgpr_read_b32 v244, a231\n"
"v_mul_f32 v244, v244, v245\n"
"v_accvgpr_write_b32 a231, v244\n"
"v_accvgpr_read_b32 v244, a236\n"
"v_mul_f32 v244, v244, v245\n"
"v_accvgpr_write_b32 a236, v244\n"
"v_accvgpr_read_b32 v244, a237\n"
"v_mul_f32 v244, v244, v245\n"
"v_accvgpr_write_b32 a237, v244\n"
"v_accvgpr_read_b32 v244, a238\n"
"v_mul_f32 v244, v244, v245\n"
"v_accvgpr_write_b32 a238, v244\n"
"v_accvgpr_read_b32 v244, a239\n"
"v_mul_f32 v244, v244, v245\n"
"v_accvgpr_write_b32 a239, v244\n"
"v_accvgpr_read_b32 v244, a244\n"
"v_mul_f32 v244, v244, v245\n"
"v_accvgpr_write_b32 a244, v244\n"
"v_accvgpr_read_b32 v244, a245\n"
"v_mul_f32 v244, v244, v245\n"
"v_accvgpr_write_b32 a245, v244\n"
"v_accvgpr_read_b32 v244, a246\n"
"v_mul_f32 v244, v244, v245\n"
"v_accvgpr_write_b32 a246, v244\n"
"v_accvgpr_read_b32 v244, a247\n"
"v_mul_f32 v244, v244, v245\n"
"v_accvgpr_write_b32 a247, v244\n"
"v_accvgpr_read_b32 v244, a252\n"
"v_mul_f32 v244, v244, v245\n"
"v_accvgpr_write_b32 a252, v244\n"
"v_accvgpr_read_b32 v244, a253\n"
"v_mul_f32 v244, v244, v245\n"
"v_accvgpr_write_b32 a253, v244\n"
"v_accvgpr_read_b32 v244, a254\n"
"v_mul_f32 v244, v244, v245\n"
"v_accvgpr_write_b32 a254, v244\n"
"v_accvgpr_read_b32 v244, a255\n"
"v_mul_f32 v244, v244, v245\n"
"v_accvgpr_write_b32 a255, v244\n"
"s_mov_b32 s49, 0\n"
"s_nop 1\n"
"s_setpc_b64 s[52:53]\n"
    :: [tid] "v"(threadIdx.x), [bid] "s"(blockIdx.x),
       [ka] "s"(__builtin_amdgcn_kernarg_segment_ptr()),
       [lds] "s"((unsigned)(size_t)lds)
    : "v1", "v2", "v3", "v4", "v5", "v6", "v7", "v8", "v9", "v10", "v11", "v12", "v13", "v14", "v15", "v16", "v17", "v18", "v19", "v20", "v21", "v22", "v23", "v24", "v25", "v26", "v27", "v28", "v29", "v30", "v31", "v32", "v33", "v34", "v35", "v36", "v37", "v38", "v39", "v40", "v41", "v42", "v43", "v44", "v45", "v46", "v47", "v48", "v49", "v50", "v51", "v52", "v53", "v54", "v55", "v56", "v57", "v58", "v59", "v60", "v61", "v62", "v63", "v64", "v65", "v66", "v67", "v68", "v69", "v70", "v71", "v72", "v73", "v74", "v75", "v76", "v77", "v78", "v79", "v80", "v81", "v82", "v83", "v84", "v85", "v86", "v87", "v88", "v89", "v90", "v91", "v92", "v93", "v94", "v95", "v96", "v97", "v98", "v99", "v100", "v101", "v102", "v103", "v104", "v105", "v106", "v107", "v108", "v109", "v110", "v111", "v112", "v113", "v114", "v115", "v116", "v117", "v118", "v119", "v120", "v121", "v122", "v123", "v124", "v125", "v126", "v127", "v128", "v129", "v130", "v131", "v132", "v133", "v134", "v135", "v136", "v137", "v138", "v139", "v140", "v141", "v142", "v143", "v144", "v145", "v146", "v147", "v148", "v149", "v150", "v151", "v152", "v153", "v154", "v155", "v156", "v157", "v158", "v159", "v160", "v161", "v162", "v163", "v164", "v165", "v166", "v167", "v168", "v169", "v170", "v171", "v172", "v173", "v174", "v175", "v176", "v177", "v178", "v179", "v180", "v181", "v182", "v183", "v184", "v185", "v186", "v187", "v188", "v189", "v190", "v191", "v192", "v193", "v194", "v195", "v196", "v197", "v198", "v199", "v200", "v201", "v202", "v203", "v204", "v205", "v206", "v207", "v208", "v209", "v210", "v211", "v212", "v213", "v214", "v215", "v216", "v217", "v218", "v219", "v220", "v221", "v222", "v223", "v224", "v225", "v226", "v227", "v228", "v229", "v230", "v231", "v232", "v233", "v234", "v235", "v236", "v237", "v238", "v239", "v240", "v241", "v242", "v243", "v244", "v245", "v246", "v247", "v248", "v249", "v250", "v251", "v252", "v253", "v254", "v255", "a0", "a1", "a2", "a3", "a4", "a5", "a6", "a7", "a8", "a9", "a10", "a11", "a12", "a13", "a14", "a15", "a16", "a17", "a18", "a19", "a20", "a21", "a22", "a23", "a24", "a25", "a26", "a27", "a28", "a29", "a30", "a31", "a32", "a33", "a34", "a35", "a36", "a37", "a38", "a39", "a40", "a41", "a42", "a43", "a44", "a45", "a46", "a47", "a48", "a49", "a50", "a51", "a52", "a53", "a54", "a55", "a56", "a57", "a58", "a59", "a60", "a61", "a62", "a63", "a64", "a65", "a66", "a67", "a68", "a69", "a70", "a71", "a72", "a73", "a74", "a75", "a76", "a77", "a78", "a79", "a80", "a81", "a82", "a83", "a84", "a85", "a86", "a87", "a88", "a89", "a90", "a91", "a92", "a93", "a94", "a95", "a96", "a97", "a98", "a99", "a100", "a101", "a102", "a103", "a104", "a105", "a106", "a107", "a108", "a109", "a110", "a111", "a112", "a113", "a114", "a115", "a116", "a117", "a118", "a119", "a120", "a121", "a122", "a123", "a124", "a125", "a126", "a127", "a128", "a129", "a130", "a131", "a132", "a133", "a134", "a135", "a136", "a137", "a138", "a139", "a140", "a141", "a142", "a143", "a144", "a145", "a146", "a147", "a148", "a149", "a150", "a151", "a152", "a153", "a154", "a155", "a156", "a157", "a158", "a159", "a160", "a161", "a162", "a163", "a164", "a165", "a166", "a167", "a168", "a169", "a170", "a171", "a172", "a173", "a174", "a175", "a176", "a177", "a178", "a179", "a180", "a181", "a182", "a183", "a184", "a185", "a186", "a187", "a188", "a189", "a190", "a191", "a192", "a193", "a194", "a195", "a196", "a197", "a198", "a199", "a200", "a201", "a202", "a203", "a204", "a205", "a206", "a207", "a208", "a209", "a210", "a211", "a212", "a213", "a214", "a215", "a216", "a217", "a218", "a219", "a220", "a221", "a222", "a223", "a224", "a225", "a226", "a227", "a228", "a229", "a230", "a231", "a232", "a233", "a234", "a235", "a236", "a237", "a238", "a239", "a240", "a241", "a242", "a243", "a244", "a245", "a246", "a247", "a248", "a249", "a250", "a251", "a252", "a253", "a254", "a255", "s8", "s9", "s10", "s11", "s12", "s13", "s14", "s15", "s16", "s17", "s18", "s19", "s20", "s21", "s22", "s23", "s24", "s25", "s26", "s27", "s28", "s29", "s30", "s31", "s32", "s33", "s34", "s35", "s36", "s37", "s38", "s39", "s40", "s41", "s42", "s43", "s44", "s45", "s46", "s47", "s48", "s49", "s50", "s51", "s52", "s53", "s54", "s55", "s56", "s57", "s58", "s59", "s60", "s61", "s62", "s63", "s64", "s65", "s66", "s67", "s68", "s69", "s70", "s71", "s72", "s73", "s74", "s75", "s76", "s77", "s78", "s79", "s80", "s81", "s82", "s83", "s84", "s85", "s86", "s87", "s88", "s89", "s90", "s91", "s92", "s93", "s94", "s95", "s96", "s97", "s98", "s99", "s100", "s101", "memory", "scc", "vcc");
}

extern "C" void kernel_launch(void* const* d_in, const int* in_sizes, int n_in, void* d_out, int out_size, void* d_ws, size_t ws_size,
                              hipStream_t stream) {
  (void)in_sizes; (void)n_in; (void)out_size; (void)ws_size;
  hipLaunchKernelGGL(fused_main, dim3(256), dim3(256), 0, stream, d_in[0], d_in[1], d_in[2], d_in[3], d_in[4], d_in[6], d_in[7], d_in[8], (void*)((char*)d_ws + 0), d_out);
}
```
